```python
import torch

def _softmax(x, axis=-1):
    return torch.softmax(x, dim=axis)
def _gelu(x):
    return torch.nn.functional.gelu(x, approximate="tanh")
def _silu(x):
    return torch.nn.functional.silu(x)
def _sigmoid(x):
    return torch.sigmoid(x)
def _layernorm(x, axis=-1, eps=1e-5):
    m = x.mean(dim=axis, keepdim=True)
    v = x.var(dim=axis, keepdim=True, unbiased=False)
    return (x - m) / torch.sqrt(v + eps)
def _scatter_add(tgt, idx, src, axis=0):
    return tgt.clone().index_add_(axis, idx, src)
def _segment_sum(src, seg, n):
    out = torch.zeros((n,) + src.shape[1:], dtype=src.dtype)
    return out.index_add_(0, seg, src)
def _pad(x, pw):
    flat = tuple(v for pair in reversed(pw) for v in pair)
    return torch.nn.functional.pad(x, flat)

kernel_name = 'concat_heads__gqa_block_cached__mha_block_cached__near_ridge'

def setup_inputs():

    _w = (lambda a: (lambda t: t.to(torch.bfloat16) if t.is_floating_point() else t)(torch.as_tensor(a)))
    def _A_setup():
        B, Sq, D = 8, 144, 576
        Hq, Hkv, Sk, Dh = 32, 16, 1024, 128
        assert Hq % Hkv == 0, (Hq, Hkv)
        return {
            "x": _w((lambda *s: torch.randn(*s, dtype=torch.bfloat16) * 0.8)(B, Sq, D)),
            "k_cache": _w((lambda *s: torch.randn(*s, dtype=torch.bfloat16) * 0.8)(B, Hkv, Sk, Dh)),
            "v_cache": _w((lambda *s: torch.randn(*s, dtype=torch.bfloat16) * 0.05)(B, Hkv, Sk, Dh)),
            "wq": _w((lambda *s: torch.randn(*s, dtype=torch.bfloat16) * 0.8)(D, Hq * Dh) * (D ** -0.5)),
            "wo": _w((lambda *s: torch.randn(*s, dtype=torch.bfloat16) * 0.05)(Hq * Dh, D) * ((Hq * Dh) ** -0.5)),
        }
    _A_in = _A_setup()

    def _B_setup():
        B, Sq, D = 8, 144, 576
        H, Sk, Dh = 32, 1024, 128
        return {
            "x": _w((lambda *s: torch.randn(*s, dtype=torch.bfloat16) * 0.8)(B, Sq, D)),
            "k_cache": _w((lambda *s: torch.randn(*s, dtype=torch.bfloat16) * 0.8)(B, H, Sk, Dh)),
            "v_cache": _w((lambda *s: torch.randn(*s, dtype=torch.bfloat16) * 0.05)(B, H, Sk, Dh)),
            "wq": _w((lambda *s: torch.randn(*s, dtype=torch.bfloat16) * 0.8)(D, H * Dh) * (D ** -0.5)),
            "wo": _w((lambda *s: torch.randn(*s, dtype=torch.bfloat16) * 0.05)(H * Dh, D) * ((H * Dh) ** -0.5)),
        }
    _B_in = _B_setup()

    return {
        "A_x": _A_in["x"],
        "A_k_cache": _A_in["k_cache"],
        "A_v_cache": _A_in["v_cache"],
        "A_wq": _A_in["wq"],
        "A_wo": _A_in["wo"],
        "B_x": _B_in["x"],
        "B_k_cache": _B_in["k_cache"],
        "B_v_cache": _B_in["v_cache"],
        "B_wq": _B_in["wq"],
        "B_wo": _B_in["wo"],
    }

def reference(A_x, A_k_cache, A_v_cache, A_wq, A_wo, B_x, B_k_cache, B_v_cache, B_wq, B_wo):
    def _A_ref(x, k_cache, v_cache, wq, wo):
        B, Sq, D = x.shape
        Hq, Dh = 32, 128
        Hkv = k_cache.shape[1]
        rep = Hq // Hkv
        q = (lambda x, *d: x.permute(*d))((x @ wq).reshape(B, Sq, Hq, Dh), 0, 2, 1, 3)
        k = (lambda x, n, axis: torch.repeat_interleave(x, n, dim=axis))(k_cache, rep, axis=1)
        v = (lambda x, n, axis: torch.repeat_interleave(x, n, dim=axis))(v_cache, rep, axis=1)
        scores = torch.einsum("bhqd,bhkd->bhqk", q, k) * (Dh ** -0.5)
        p = _softmax(scores, axis=-1)
        o = torch.einsum("bhqk,bhkd->bhqd", p, v)
        return (lambda x, *d: x.permute(*d))(o, 0, 2, 1, 3).reshape(B, Sq, Hq * Dh) @ wo

    def _B_ref(x, k_cache, v_cache, wq, wo):
        B, Sq, D = x.shape
        H, Dh = 32, 128
        q = (lambda x, *d: x.permute(*d))((x @ wq).reshape(B, Sq, H, Dh), 0, 2, 1, 3)
        scores = torch.einsum("bhqd,bhkd->bhqk", q, k_cache) * (Dh ** -0.5)
        p = _softmax(scores, axis=-1)
        o = torch.einsum("bhqk,bhkd->bhqd", p, v_cache)
        return (lambda x, *d: x.permute(*d))(o, 0, 2, 1, 3).reshape(B, Sq, H * Dh) @ wo

    a = _A_ref(A_x, A_k_cache, A_v_cache, A_wq, A_wo)
    b = _B_ref(B_x, B_k_cache, B_v_cache, B_wq, B_wo)
    return torch.concatenate([a, b], axis=-1)
```

```cpp
#include <hip/hip_runtime.h>
#include <hip/hip_bf16.h>
typedef __hip_bfloat16 bf16;
__global__ void qproj(const bf16* x, const bf16* w, float* q) {
  int n = blockIdx.x * 256 + threadIdx.x; int m = blockIdx.y;
  float acc = 0.f;
  for (int c = 0; c < 576; ++c) acc += __bfloat162float(x[m * 576 + c]) * __bfloat162float(w[(size_t)c * 4096 + n]);
  q[(size_t)m * 4096 + n] = __bfloat162float(__float2bfloat16(acc));
}
__global__ void attn(const float* q, const bf16* k, const bf16* v, float* o, int hkv) {
  int m = blockIdx.x, h = blockIdx.y; int b = m / 144; int kvh = h / (32 / hkv);
  __shared__ float qs[128]; __shared__ float p[1024]; __shared__ float red[256];
  int t = threadIdx.x;
  if (t < 128) qs[t] = q[(size_t)m * 4096 + h * 128 + t];
  __syncthreads();
  const bf16* kb = k + ((size_t)(b * hkv + kvh)) * 1024 * 128;
  const bf16* vb = v + ((size_t)(b * hkv + kvh)) * 1024 * 128;
  float mx = -1e30f;
  for (int j = t; j < 1024; j += 256) { float s = 0; for (int d = 0; d < 128; ++d) s += qs[d] * __bfloat162float(kb[j * 128 + d]); s *= 0.08838834764831845f; p[j] = s; mx = fmaxf(mx, s); }
  red[t] = mx; __syncthreads();
  for (int s = 128; s > 0; s >>= 1) { if (t < s) red[t] = fmaxf(red[t], red[t + s]); __syncthreads(); }
  mx = red[0]; __syncthreads();
  float sum = 0; for (int j = t; j < 1024; j += 256) { float e = __expf(p[j] - mx); p[j] = e; sum += e; }
  red[t] = sum; __syncthreads();
  for (int s = 128; s > 0; s >>= 1) { if (t < s) red[t] += red[t + s]; __syncthreads(); }
  sum = red[0];
  if (t < 128) { float acc = 0; for (int j = 0; j < 1024; ++j) acc += p[j] * __bfloat162float(vb[j * 128 + t]); o[(size_t)m * 4096 + h * 128 + t] = __bfloat162float(__float2bfloat16(acc / sum)); }
}
__global__ void oproj(const float* o, const bf16* w, bf16* out, int coff) {
  int n = blockIdx.x * 64 + threadIdx.x; int m = blockIdx.y;
  float acc = 0.f;
  for (int c = 0; c < 4096; ++c) acc += o[(size_t)m * 4096 + c] * __bfloat162float(w[(size_t)c * 576 + n]);
  out[(size_t)m * 1152 + coff + n] = __float2bfloat16(acc);
}
extern "C" void kernel_launch(void* const* d_in, const int* in_sizes, int n_in, void* d_out, int out_size, void* d_ws, size_t ws_size, hipStream_t stream) {
  float* q = (float*)d_ws; float* o = q + 1152 * 4096;
  for (int p = 0; p < 2; ++p) {
    const bf16* x = (const bf16*)d_in[5 * p + 0]; const bf16* k = (const bf16*)d_in[5 * p + 1]; const bf16* v = (const bf16*)d_in[5 * p + 2];
    const bf16* wq = (const bf16*)d_in[5 * p + 3]; const bf16* wo = (const bf16*)d_in[5 * p + 4];
    qproj<<<dim3(16, 1152), 256, 0, stream>>>(x, wq, q);
    attn<<<dim3(1152, 32), 256, 0, stream>>>(q, k, v, o, p == 0 ? 16 : 32);
    oproj<<<dim3(9, 1152), 64, 0, stream>>>(o, wo, (bf16*)d_out, p * 576);
  }
}
```

```cpp
#include <hip/hip_runtime.h>
#include <hip/hip_bf16.h>
#include <stdint.h>
typedef unsigned u32x4 __attribute__((ext_vector_type(4)));
typedef float f32x4 __attribute__((ext_vector_type(4)));
typedef __bf16 bf16x8 __attribute__((ext_vector_type(8)));
typedef unsigned short u16;
#define GP __attribute__((address_space(1)))
typedef const GP u16* gcp;
#define DI __device__ __forceinline__
DI u32x4 mk_srd(gcp p, unsigned bytes) {
  uint64_t a = (uint64_t)p;
  u32x4 r = {(unsigned)a, (unsigned)(a >> 32) & 0xffffu, bytes, 0x20000u};
  return r;
}
DI float bf2f(u16 v) { return __uint_as_float(((unsigned)v) << 16); }
DI u16 f2bf(float f) { __bf16 b = (__bf16)f; return __builtin_bit_cast(u16, b); }
DI unsigned perm4(unsigned i) { return (0x78u >> (2u * (i & 3u))) & 3u; }

__global__ __launch_bounds__(512, 2) void k1_attn(const u16* xA, const u16* kA, const u16* vA, const u16* wA, const u16* woA,
                                                  const u16* xB, const u16* kB, const u16* vB, const u16* wB, const u16* woB, u16* ofrag_, u16* out_ ) {
  __shared__ __attribute__((aligned(1024))) char smem[163840];
  unsigned mode = 0u, wv = __builtin_amdgcn_readfirstlane(threadIdx.x >> 6), wg = blockIdx.x;
  const __attribute__((address_space(4))) uint64_t* ka = (const __attribute__((address_space(4))) uint64_t*)__builtin_amdgcn_kernarg_segment_ptr();
  for (;;) {
  unsigned l;
  asm volatile("v_mbcnt_lo_u32_b32 %0, -1, 0\n v_mbcnt_hi_u32_b32 %0, -1, %0" : "=v"(l), "+s"(ka), "+s"(wg), "+s"(wv));
  uint64_t r0, r1, r2, r3, r4, r5, r6; u32x4 srdw, srdx;
  {
  __builtin_assume(l < 64u);
  const unsigned xcd = wg & 7u, jx = wg >> 3, jj = jx & 15u, isA = jx >> 4;
  const unsigned pair = (xcd & 3u) * 4u + (jj & 3u), b = (xcd >> 2) * 4u + (jj >> 2);
  const unsigned h = wv >> 2, w = (wv & 3u) ^ (h * 2u);
  const unsigned head = 2u * pair + h;
  const unsigned kvh = isA ? (b * 16u + pair) : (b * 32u + head);
  const unsigned P = isA ? 0u : 1u, pb = P * 8u + b;
  r0 = ka[P * 5u], r1 = ka[P * 5u + 1u], r2 = ka[P * 5u + 2u], r3 = ka[P * 5u + 3u], r4 = ka[P * 5u + 4u], r5 = ka[10], r6 = ka[11];

  const unsigned so = isA ? h * 4096u : 0u;
  const unsigned rt = 2u * ((jj & 3u) + (jj >> 2)), rho = rt >= 9u ? rt - 9u : rt;
  const unsigned wrot = ((jj & 3u) + (jj >> 2)) & 3u, xrot = 1040u << ((wrot + 3u) & 3u), osoff = head * 36864u;
  const unsigned hb = h * 65536u, kvs = isA ? 16384u : 8192u, d1 = isA ? 65536u : 0u, rb = isA ? 0u : hb;
  const unsigned kq = xcd & 3u, nb = jj & 3u;
  const unsigned os1 = (8u * kq + 2u * ((nb + 1u + b % 3u) & 3u) + h) * 36864u, os2 = (8u * kq + 2u * ((nb + 1u + (b + 1u) % 3u) & 3u) + h) * 36864u,
                 os3 = (8u * kq + 2u * ((nb + 1u + (b + 2u) % 3u) & 3u) + h) * 36864u;
  const unsigned kq64 = kq * 64u, nb256 = nb * 256u, pbase = (pb * 4u + nb) * 262144u;
  const unsigned hg = h ? 64u + pb * 4u + nb : pb * 4u + kq, me = h ? kq : nb, peer = h ? (kq + l) & 3u : (nb + 1u + (l + 2u + b) % 3u) & 3u;
  const bool hl = l - 1u < 3u;
  const unsigned gin = hl ? (16u << 20) + (hg * 4u + peer) * 128u + me * 8u : 0x7ffffff0u, gout = hl ? (16u << 20) + (hg * 4u + me) * 128u + peer * 8u : 0x7ffffff0u;
  const unsigned g = l >> 4, r = l & 15u, rv = l >> 2;
  const unsigned sb = (unsigned)(size_t)smem;
  const unsigned kc = sb + (r & 7u) * 64u + ((g ^ perm4(r >> 2)) * 16u);
  const unsigned kb = kc + ((r >> 2) & 1u) * 768u + (r >> 3) * 4096u + rb, xb = kc + (r >> 3) * 1024u;
  const unsigned vbe = sb + rb + (rv >> 3) * 4096u + ((rv >> 2) & 1u) * 1024u + (rv & 3u) * 64u + ((((l >> 1) & 1u) ^ perm4(g)) * 16u) + (l & 1u) * 8u;
  const unsigned wbe = sb + hb + g * 2048u + ((l >> 2) & 3u) * 64u + ((((l >> 1) & 1u) ^ ((g & 1u) * 3u)) * 16u) + (l & 1u) * 8u;
  const unsigned s1 = l >> 5, r7 = (l >> 2) & 7u, c4 = l & 3u;
  const unsigned g0 = (8u * w + (r7 & 3u)) * 256u + g * 64u + ((c4 ^ perm4(2u * w)) * 16u) + 1024u, g1 = g0 ^ 32u, l16 = l * 16u;
  const unsigned xoff = (32u * w + 16u * h + r7) * 1152u + s1 * 64u + ((c4 ^ perm4(r7 >> 2)) * 16u);
  const unsigned xo9 = (128u + 8u * h + r7) * 1152u + s1 * 64u + ((c4 ^ perm4(2u * h + (r7 >> 2))) * 16u);
  const unsigned role = w + mode;
  const unsigned ux = b * 165888u, uo = pb * 1179648u, ut = b * 144u * 2304u + P * 1152u, nk = 262144u + 3072u - so * 2u, nv = nk - 2048u, nw = 580u * 8192u - head * 256u;
  const uint64_t uk = (uint64_t)(kvh * 262144u + so * 2u) - 3072u, uv = uk + 2048u, uw = (uint64_t)(head * 256u) - 32768u;
  const unsigned ew0 = (g0 >> 8) * 7936u + g0, ew1 = (ew0 ^ 32u) + 31744u, ex1 = (xoff ^ 48u) + 8192u, mw = hb + 32768u + w * 2048u, mx = (hb >> 5) + 131072u + w * 4096u, m9 = (hb >> 6) + 57344u;
  const unsigned tv = w ? ((72u * l16 + (hb >> 9) * 324u) | (l16 >= 576u ? 0x40000000u : 0u)) : ((l16 >> 5) << 13) + ((l16 & 16u) << 3) + 32768u, to = tv | 0x40000000u;
  const unsigned ts = w ? ((wrot & 1u) ? 0u : 82944u) + (((wrot + 3u) & 2u) ? 256u : 128u) : (((wrot + 3u) & 3u) + 1u) * 262144u, t1 = 0x40000u;
  asm volatile("" : "+s"(r0), "+s"(r3) : "s"(ux), "s"(nw), "s"(uw), "s"(role), "s"(mw), "s"(mx), "s"(m9), "v"(ew0), "v"(ew1), "v"(ex1), "v"(xoff), "v"(xo9), "v"(tv), "v"(to), "s"(ts), "s"(t1));
  srdw = mk_srd((gcp)(r3 + uw), nw), srdx = mk_srd((gcp)(r0 + ux), 165888u);
  asm volatile("s_cmp_ge_u32 %[role], 4\n s_cbranch_scc1 LEA_%=\n s_mov_b32 m0, %[mw]\n s_nop 0\n"
    "buffer_load_dwordx4 %[ew0], %[srdw], 0 offen lds\n buffer_load_dwordx4 %[ew1], %[srdw], 0 offen offset:1024 lds\n s_mov_b32 m0, %[mx]\n s_nop 0\n"
    "buffer_load_dwordx4 %[xoff], %[srdx], 0 offen lds\n buffer_load_dwordx4 %[ex1], %[srdx], 0 offen offset:1024 lds\n s_cmp_gt_u32 %[role], 1\n s_cbranch_scc1 LEW_%=\n s_cmp_lg_u32 %[role], 0\n s_cbranch_scc1 LEX_%=\n"
    "s_mov_b32 m0, %[m9]\n s_nop 0\n buffer_load_dwordx4 %[xo9], %[srdx], 0 offen lds\n buffer_load_dword v201, %[tv], %[srdw], %[ts] offen\n s_branch LED_%=\n"
    "LEX_%=:\n buffer_load_dword v201, %[tv], %[srdx], %[ts] offen\n LED_%=:\n buffer_load_dword v201, %[to], %[srdx], 0 offen\n buffer_load_dword v201, %[to], %[srdx], 0 offen\n buffer_load_dword v201, %[to], %[srdx], 0 offen\n"
    "LEW_%=:\n s_add_u32 m0, %[mw], 8192\n s_nop 0\n buffer_load_dwordx4 %[ew0], %[srdw], %[t1] offen lds\n buffer_load_dwordx4 %[ew1], %[srdw], %[t1] offen offset:1024 lds\n LEA_%=:\n"
    : : [srdw] "s"(srdw), [srdx] "s"(srdx), [role] "s"(role), [mw] "s"(mw), [mx] "s"(mx), [m9] "s"(m9),
      [ew0] "v"(ew0), [ew1] "v"(ew1), [ex1] "v"(ex1), [xoff] "v"(xoff), [xo9] "v"(xo9), [tv] "v"(tv), [to] "v"(to), [ts] "s"(ts), [t1] "s"(t1) : "memory", "scc", "v201");
  asm volatile("" : "+v"(l), "+s"(wg), "+s"(wv));
  }
  __builtin_assume(l < 64u);
  const unsigned xcd = wg & 7u, jx = wg >> 3, jj = jx & 15u, isA = jx >> 4;
  const unsigned pair = (xcd & 3u) * 4u + (jj & 3u), b = (xcd >> 2) * 4u + (jj >> 2);
  const unsigned h = wv >> 2, w = (wv & 3u) ^ (h * 2u);
  const unsigned head = 2u * pair + h;
  const unsigned kvh = isA ? (b * 16u + pair) : (b * 32u + head);
  const unsigned P = isA ? 0u : 1u, pb = P * 8u + b;

  const unsigned so = isA ? h * 4096u : 0u;
  const unsigned rt = 2u * ((jj & 3u) + (jj >> 2)), rho = rt >= 9u ? rt - 9u : rt;
  const unsigned wrot = ((jj & 3u) + (jj >> 2)) & 3u, xrot = 1040u << ((wrot + 3u) & 3u), osoff = head * 36864u;
  const unsigned hb = h * 65536u, kvs = isA ? 16384u : 8192u, d1 = isA ? 65536u : 0u, rb = isA ? 0u : hb;
  const unsigned kq = xcd & 3u, nb = jj & 3u;
  const unsigned os1 = (8u * kq + 2u * ((nb + 1u + b % 3u) & 3u) + h) * 36864u, os2 = (8u * kq + 2u * ((nb + 1u + (b + 1u) % 3u) & 3u) + h) * 36864u,
                 os3 = (8u * kq + 2u * ((nb + 1u + (b + 2u) % 3u) & 3u) + h) * 36864u;
  const unsigned kq64 = kq * 64u, nb256 = nb * 256u, pbase = (pb * 4u + nb) * 262144u;
  const unsigned hg = h ? 64u + pb * 4u + nb : pb * 4u + kq, me = h ? kq : nb, peer = h ? (kq + l) & 3u : (nb + 1u + (l + 2u + b) % 3u) & 3u;
  const bool hl = l - 1u < 3u;
  const unsigned gin = hl ? (16u << 20) + (hg * 4u + peer) * 128u + me * 8u : 0x7ffffff0u, gout = hl ? (16u << 20) + (hg * 4u + me) * 128u + peer * 8u : 0x7ffffff0u;
  const unsigned g = l >> 4, r = l & 15u, rv = l >> 2;
  const unsigned sb = (unsigned)(size_t)smem;
  const unsigned kc = sb + (r & 7u) * 64u + ((g ^ perm4(r >> 2)) * 16u);
  const unsigned kb = kc + ((r >> 2) & 1u) * 768u + (r >> 3) * 4096u + rb, xb = kc + (r >> 3) * 1024u;
  const unsigned vbe = sb + rb + (rv >> 3) * 4096u + ((rv >> 2) & 1u) * 1024u + (rv & 3u) * 64u + ((((l >> 1) & 1u) ^ perm4(g)) * 16u) + (l & 1u) * 8u;
  const unsigned wbe = sb + hb + g * 2048u + ((l >> 2) & 3u) * 64u + ((((l >> 1) & 1u) ^ ((g & 1u) * 3u)) * 16u) + (l & 1u) * 8u;
  const unsigned s1 = l >> 5, r7 = (l >> 2) & 7u, c4 = l & 3u;
  const unsigned g0 = (8u * w + (r7 & 3u)) * 256u + g * 64u + ((c4 ^ perm4(2u * w)) * 16u) + 1024u, g1 = g0 ^ 32u, l16 = l * 16u;
  const unsigned xoff = (32u * w + 16u * h + r7) * 1152u + s1 * 64u + ((c4 ^ perm4(r7 >> 2)) * 16u);
  const unsigned xo9 = (128u + 8u * h + r7) * 1152u + s1 * 64u + ((c4 ^ perm4(2u * h + (r7 >> 2))) * 16u);
  const unsigned role = w + mode;
  const unsigned ux = b * 165888u, uo = pb * 1179648u, ut = b * 144u * 2304u + P * 1152u, nk = 262144u + 3072u - so * 2u, nv = nk - 2048u, nw = 580u * 8192u - head * 256u;
  const uint64_t uk = (uint64_t)(kvh * 262144u + so * 2u) - 3072u, uv = uk + 2048u, uw = (uint64_t)(head * 256u) - 32768u;
  u32x4 srdk = mk_srd((gcp)(r1 + uk), nk), srdv = mk_srd((gcp)(r2 + uv), nv), srdo = mk_srd((gcp)(r5 + uo), 9u * 128u * 1024u);
  const uint64_t pwo = r4 - 4 * 1152, ppart = r5 + (32u << 20), pout = r6 + ut;
  unsigned flag;
  asm volatile(
"LS_%=:\n"
"s_mov_b64 s[76:77], %[pwo]\n"
"s_mov_b32 s78, 4723200\n"
"s_mov_b32 s79, 0x20000\n"
"s_mov_b64 s[80:81], %[ppart]\n"
"s_mov_b32 s82, 16842752\n"
"s_mov_b32 s83, s79\n"
"s_mov_b64 s[84:85], %[pout]\n"
"s_mov_b32 s86, 330624\n"
"s_mov_b32 s87, s79\n"
"s_mov_b32 %[flag], 0\n"
"s_cmp_eq_u32 %[role], 1\n"
"s_cbranch_scc1 LR1_%=\n"
"s_cmp_eq_u32 %[role], 2\n"
"s_cbranch_scc1 LR2_%=\n"
"s_cmp_eq_u32 %[role], 3\n"
"s_cbranch_scc1 LR3_%=\n"
"s_cmp_lg_u32 %[role], 0\n"
"s_cbranch_scc1 LFD_%=\n"
".macro KB0 p0,p1\n"
"s_barrier\n"
"s_cmp_lt_u32 s61, 0x36000\n"
"s_cbranch_scc0 7f\n"
"s_sleep 3\n"
"7:\n"
"s_sleep 1\n"
"s_setprio 1\n"
"v_mfma_f32_16x16x32_bf16 v[16:19], v[160:163], v[224:227], v[16:19]\n"
"ds_read_b128 v[128:131], %[kb] offset:\\p0+2048\n"
"v_add_f32 v240, v240, v192\n"
"v_add_f32_e64 v241, v241, v196\n"
"v_mfma_f32_16x16x32_bf16 v[48:51], v[160:163], v[228:231], v[48:51]\n"
"ds_read_b128 v[132:135], %[kb] offset:\\p0+2304\n"
"v_add_f32 v242, v242, v208\n"
"v_add_f32 v240, v240, v193\n"
"v_mfma_f32_16x16x32_bf16 v[20:23], v[164:167], v[224:227], v[20:23]\n"
"ds_read_b128 v[136:139], %[kb] offset:\\p0+2560\n"
"v_add_f32 v241, v241, v197\n"
"v_add_f32 v242, v242, v209\n"
"v_mfma_f32_16x16x32_bf16 v[52:55], v[164:167], v[228:231], v[52:55]\n"
"ds_read_b128 v[140:143], %[kb] offset:\\p0+2816\n"
"v_add_f32 v240, v240, v194\n"
"v_add_f32 v241, v241, v198\n"
"v_mfma_f32_16x16x32_bf16 v[24:27], v[168:171], v[224:227], v[24:27]\n"
"ds_read_b128 v[144:147], %[kb] offset:\\p0+10240\n"
"v_add_f32 v242, v242, v210\n"
"v_add_f32 v240, v240, v195\n"
"v_mfma_f32_16x16x32_bf16 v[56:59], v[168:171], v[228:231], v[56:59]\n"
"ds_read_b128 v[148:151], %[kb] offset:\\p0+10496\n"
"v_add_f32 v241, v241, v199\n"
"v_add_f32 v242, v242, v211\n"
"v_mfma_f32_16x16x32_bf16 v[28:31], v[172:175], v[224:227], v[28:31]\n"
"ds_read_b128 v[152:155], %[kb] offset:\\p0+10752\n"
"v_add_f32 v240, v240, v200\n"
"v_add_f32 v241, v241, v204\n"
"v_mfma_f32_16x16x32_bf16 v[60:63], v[172:175], v[228:231], v[60:63]\n"
"ds_read_b128 v[156:159], %[kb] offset:\\p0+11008\n"
"v_add_f32 v240, v240, v201\n"
"v_add_f32 v241, v241, v205\n"
"v_mfma_f32_16x16x32_bf16 v[32:35], v[176:179], v[224:227], v[32:35]\n"
"ds_read_b64_tr_b16 v[160:161], %[vbe] offset:\\p0+0\n"
"v_add_f32 v240, v240, v202\n"
"v_add_f32 v241, v241, v206\n"
"v_mfma_f32_16x16x32_bf16 v[64:67], v[176:179], v[228:231], v[64:67]\n"
"ds_read_b64_tr_b16 v[162:163], %[vbe] offset:\\p0+8192\n"
"v_add_f32 v240, v240, v203\n"
"v_add_f32 v241, v241, v207\n"
"v_mfma_f32_16x16x32_bf16 v[36:39], v[180:183], v[224:227], v[36:39]\n"
"ds_read_b64_tr_b16 v[164:165], v243 offset:\\p0+0\n"
"v_mfma_f32_16x16x32_bf16 v[68:71], v[180:183], v[228:231], v[68:71]\n"
"ds_read_b64_tr_b16 v[166:167], v243 offset:\\p0+8192\n"
"v_mfma_f32_16x16x32_bf16 v[40:43], v[184:187], v[224:227], v[40:43]\n"
"ds_read_b64_tr_b16 v[168:169], %[vbe] offset:\\p0+256\n"
"v_mfma_f32_16x16x32_bf16 v[72:75], v[184:187], v[228:231], v[72:75]\n"
"ds_read_b64_tr_b16 v[170:171], %[vbe] offset:\\p0+8448\n"
"v_mfma_f32_16x16x32_bf16 v[44:47], v[188:191], v[224:227], v[44:47]\n"
"ds_read_b64_tr_b16 v[172:173], v243 offset:\\p0+256\n"
"v_mfma_f32_16x16x32_bf16 v[76:79], v[188:191], v[228:231], v[76:79]\n"
"ds_read_b64_tr_b16 v[174:175], v243 offset:\\p0+8448\n"
"s_waitcnt lgkmcnt(8)\n"
"v_mfma_f32_16x16x32_bf16 v[208:211], v[128:131], v[80:83], v[220:223]\n"
"ds_read_b64_tr_b16 v[176:177], %[vbe] offset:\\p0+512\n"
"v_mfma_f32_16x16x32_bf16 v[208:211], v[132:135], v[84:87], v[208:211]\n"
"ds_read_b64_tr_b16 v[178:179], %[vbe] offset:\\p0+8704\n"
"v_mfma_f32_16x16x32_bf16 v[208:211], v[136:139], v[88:91], v[208:211]\n"
"ds_read_b64_tr_b16 v[180:181], v243 offset:\\p0+512\n"
"v_mfma_f32_16x16x32_bf16 v[208:211], v[140:143], v[92:95], v[208:211]\n"
"ds_read_b64_tr_b16 v[182:183], v243 offset:\\p0+8704\n"
"v_mfma_f32_16x16x32_bf16 v[192:195], v[128:131], v[96:99], v[212:215]\n"
"ds_read_b64_tr_b16 v[184:185], %[vbe] offset:\\p0+768\n"
"v_mfma_f32_16x16x32_bf16 v[192:195], v[132:135], v[100:103], v[192:195]\n"
"ds_read_b64_tr_b16 v[186:187], %[vbe] offset:\\p0+8960\n"
"v_mfma_f32_16x16x32_bf16 v[192:195], v[136:139], v[104:107], v[192:195]\n"
"ds_read_b64_tr_b16 v[188:189], v243 offset:\\p0+768\n"
"v_mfma_f32_16x16x32_bf16 v[192:195], v[140:143], v[108:111], v[192:195]\n"
"ds_read_b64_tr_b16 v[190:191], v243 offset:\\p0+8960\n"
"v_mfma_f32_16x16x32_bf16 v[196:199], v[128:131], v[112:115], v[216:219]\n"
"v_mfma_f32_16x16x32_bf16 v[196:199], v[132:135], v[116:119], v[196:199]\n"
"v_mfma_f32_16x16x32_bf16 v[196:199], v[136:139], v[120:123], v[196:199]\n"
"v_mfma_f32_16x16x32_bf16 v[196:199], v[140:143], v[124:127], v[196:199]\n"
"v_mfma_f32_16x16x32_bf16 v[200:203], v[144:147], v[96:99], v[212:215]\n"
"v_mfma_f32_16x16x32_bf16 v[200:203], v[148:151], v[100:103], v[200:203]\n"
"v_mfma_f32_16x16x32_bf16 v[200:203], v[152:155], v[104:107], v[200:203]\n"
"v_mfma_f32_16x16x32_bf16 v[200:203], v[156:159], v[108:111], v[200:203]\n"
"v_mfma_f32_16x16x32_bf16 v[204:207], v[144:147], v[112:115], v[216:219]\n"
"v_mfma_f32_16x16x32_bf16 v[204:207], v[148:151], v[116:119], v[204:207]\n"
"v_mfma_f32_16x16x32_bf16 v[204:207], v[152:155], v[120:123], v[204:207]\n"
"v_mfma_f32_16x16x32_bf16 v[204:207], v[156:159], v[124:127], v[204:207]\n"
"s_waitcnt lgkmcnt(0)\n"
"s_barrier\n"
"s_mov_b32 m0, \\p1\n"
"s_setprio 0\n"
"buffer_load_dwordx4 %[g0], %[srdv], s61 offen nt lds\n"
"buffer_load_dwordx4 %[g1], %[srdv], s61 offen offset:1024 nt lds\n"
"buffer_load_dwordx4 %[g0], %[srdk], s61 offen offset:2048 nt lds\n"
"s_add_u32 s61, s61, 0x2000\n"
"s_cmp_eq_u32 s61, 0x40000\n"
"v_exp_f32 v208, v208\n"
"v_exp_f32 v209, v209\n"
"v_exp_f32 v210, v210\n"
"v_exp_f32_e64 v211, v211\n"
"v_cvt_pk_bf16_f32 v232, v208, v209\n"
"v_cvt_pk_bf16_f32 v233, v210, v211\n"
"ds_write_b64 v247, v[232:233] offset:4096\n"
"v_exp_f32 v192, v192\n"
"v_exp_f32 v193, v193\n"
"v_exp_f32 v194, v194\n"
"v_exp_f32 v195, v195\n"
"v_cvt_pk_bf16_f32 v224, v192, v193\n"
"v_cvt_pk_bf16_f32 v225, v194, v195\n"
"v_exp_f32 v196, v196\n"
"v_exp_f32 v197, v197\n"
"v_exp_f32 v198, v198\n"
"v_exp_f32 v199, v199\n"
"v_cvt_pk_bf16_f32 v228, v196, v197\n"
"v_cvt_pk_bf16_f32 v229, v198, v199\n"
"v_exp_f32 v200, v200\n"
"v_exp_f32 v201, v201\n"
"v_exp_f32 v202, v202\n"
"v_exp_f32 v203, v203\n"
"v_cvt_pk_bf16_f32 v226, v200, v201\n"
"v_cvt_pk_bf16_f32 v227, v202, v203\n"
"v_exp_f32 v204, v204\n"
"v_exp_f32 v205, v205\n"
"v_exp_f32 v206, v206\n"
"v_exp_f32 v207, v207\n"
"v_cvt_pk_bf16_f32 v230, v204, v205\n"
"v_cvt_pk_bf16_f32 v231, v206, v207\n"
"s_waitcnt vmcnt(9) lgkmcnt(0)\n"
"s_cbranch_scc1 LQ0_%=\n"
".endm\n"
".macro KA0 p0,p1,p2\n"
"s_barrier\n"
"s_sleep 3\n"
"s_setprio 1\n"
"v_mfma_f32_16x16x32_bf16 v[16:19], v[160:163], v[224:227], v[16:19]\n"
"ds_read_b128 v[128:131], v244 offset:\\p0+2048\n"
"v_add_f32 v240, v240, v192\n"
"v_add_f32_e64 v241, v241, v196\n"
"v_mfma_f32_16x16x32_bf16 v[48:51], v[160:163], v[228:231], v[48:51]\n"
"ds_read_b128 v[132:135], v244 offset:\\p0+2304\n"
"v_add_f32 v242, v242, v208\n"
"v_add_f32 v240, v240, v193\n"
"v_mfma_f32_16x16x32_bf16 v[20:23], v[164:167], v[224:227], v[20:23]\n"
"ds_read_b128 v[136:139], v244 offset:\\p0+2560\n"
"v_add_f32 v241, v241, v197\n"
"v_add_f32 v242, v242, v209\n"
"v_mfma_f32_16x16x32_bf16 v[52:55], v[164:167], v[228:231], v[52:55]\n"
"ds_read_b128 v[140:143], v244 offset:\\p0+2816\n"
"v_add_f32 v240, v240, v194\n"
"v_add_f32 v241, v241, v198\n"
"v_mfma_f32_16x16x32_bf16 v[24:27], v[168:171], v[224:227], v[24:27]\n"
"ds_read_b128 v[144:147], v244 offset:\\p0+10240\n"
"v_add_f32 v242, v242, v210\n"
"v_add_f32 v240, v240, v195\n"
"v_mfma_f32_16x16x32_bf16 v[56:59], v[168:171], v[228:231], v[56:59]\n"
"ds_read_b128 v[148:151], v244 offset:\\p0+10496\n"
"v_add_f32 v241, v241, v199\n"
"v_add_f32 v242, v242, v211\n"
"v_mfma_f32_16x16x32_bf16 v[28:31], v[172:175], v[224:227], v[28:31]\n"
"ds_read_b128 v[152:155], v244 offset:\\p0+10752\n"
"v_add_f32 v240, v240, v200\n"
"v_add_f32 v241, v241, v204\n"
"v_mfma_f32_16x16x32_bf16 v[60:63], v[172:175], v[228:231], v[60:63]\n"
"ds_read_b128 v[156:159], v244 offset:\\p0+11008\n"
"v_add_f32 v240, v240, v201\n"
"v_add_f32 v241, v241, v205\n"
"v_mfma_f32_16x16x32_bf16 v[32:35], v[176:179], v[224:227], v[32:35]\n"
"ds_read_b64_tr_b16 v[160:161], v245 offset:\\p0+0\n"
"v_add_f32 v240, v240, v202\n"
"v_add_f32 v241, v241, v206\n"
"v_mfma_f32_16x16x32_bf16 v[64:67], v[176:179], v[228:231], v[64:67]\n"
"ds_read_b64_tr_b16 v[162:163], v245 offset:\\p0+8192\n"
"v_add_f32 v240, v240, v203\n"
"v_add_f32 v241, v241, v207\n"
"v_mfma_f32_16x16x32_bf16 v[36:39], v[180:183], v[224:227], v[36:39]\n"
"ds_read_b64_tr_b16 v[164:165], v246 offset:\\p0+0\n"
"v_mfma_f32_16x16x32_bf16 v[68:71], v[180:183], v[228:231], v[68:71]\n"
"ds_read_b64_tr_b16 v[166:167], v246 offset:\\p0+8192\n"
"v_mfma_f32_16x16x32_bf16 v[40:43], v[184:187], v[224:227], v[40:43]\n"
"ds_read_b64_tr_b16 v[168:169], v245 offset:\\p0+256\n"
"v_mfma_f32_16x16x32_bf16 v[72:75], v[184:187], v[228:231], v[72:75]\n"
"ds_read_b64_tr_b16 v[170:171], v245 offset:\\p0+8448\n"
"v_mfma_f32_16x16x32_bf16 v[44:47], v[188:191], v[224:227], v[44:47]\n"
"ds_read_b64_tr_b16 v[172:173], v246 offset:\\p0+256\n"
"v_mfma_f32_16x16x32_bf16 v[76:79], v[188:191], v[228:231], v[76:79]\n"
"ds_read_b64_tr_b16 v[174:175], v246 offset:\\p0+8448\n"
"s_waitcnt lgkmcnt(8)\n"
"v_mfma_f32_16x16x32_bf16 v[208:211], v[128:131], v[80:83], v[220:223]\n"
"ds_read_b64_tr_b16 v[176:177], v245 offset:\\p0+512\n"
"v_mfma_f32_16x16x32_bf16 v[208:211], v[132:135], v[84:87], v[208:211]\n"
"ds_read_b64_tr_b16 v[178:179], v245 offset:\\p0+8704\n"
"v_mfma_f32_16x16x32_bf16 v[208:211], v[136:139], v[88:91], v[208:211]\n"
"ds_read_b64_tr_b16 v[180:181], v246 offset:\\p0+512\n"
"v_mfma_f32_16x16x32_bf16 v[208:211], v[140:143], v[92:95], v[208:211]\n"
"ds_read_b64_tr_b16 v[182:183], v246 offset:\\p0+8704\n"
"v_mfma_f32_16x16x32_bf16 v[192:195], v[128:131], v[96:99], v[212:215]\n"
"ds_read_b64_tr_b16 v[184:185], v245 offset:\\p0+768\n"
"v_mfma_f32_16x16x32_bf16 v[192:195], v[132:135], v[100:103], v[192:195]\n"
"ds_read_b64_tr_b16 v[186:187], v245 offset:\\p0+8960\n"
"v_mfma_f32_16x16x32_bf16 v[192:195], v[136:139], v[104:107], v[192:195]\n"
"ds_read_b64_tr_b16 v[188:189], v246 offset:\\p0+768\n"
"v_mfma_f32_16x16x32_bf16 v[192:195], v[140:143], v[108:111], v[192:195]\n"
"ds_read_b64_tr_b16 v[190:191], v246 offset:\\p0+8960\n"
"v_mfma_f32_16x16x32_bf16 v[196:199], v[128:131], v[112:115], v[216:219]\n"
"v_mfma_f32_16x16x32_bf16 v[196:199], v[132:135], v[116:119], v[196:199]\n"
"v_mfma_f32_16x16x32_bf16 v[196:199], v[136:139], v[120:123], v[196:199]\n"
"v_mfma_f32_16x16x32_bf16 v[196:199], v[140:143], v[124:127], v[196:199]\n"
"v_mfma_f32_16x16x32_bf16 v[200:203], v[144:147], v[96:99], v[212:215]\n"
"v_mfma_f32_16x16x32_bf16 v[200:203], v[148:151], v[100:103], v[200:203]\n"
"v_mfma_f32_16x16x32_bf16 v[200:203], v[152:155], v[104:107], v[200:203]\n"
"v_mfma_f32_16x16x32_bf16 v[200:203], v[156:159], v[108:111], v[200:203]\n"
"v_mfma_f32_16x16x32_bf16 v[204:207], v[144:147], v[112:115], v[216:219]\n"
"v_mfma_f32_16x16x32_bf16 v[204:207], v[148:151], v[116:119], v[204:207]\n"
"v_mfma_f32_16x16x32_bf16 v[204:207], v[152:155], v[120:123], v[204:207]\n"
"v_mfma_f32_16x16x32_bf16 v[204:207], v[156:159], v[124:127], v[204:207]\n"
"s_waitcnt lgkmcnt(0)\n"
"s_barrier\n"
"s_mov_b32 m0, \\p1\n"
"s_setprio 0\n"
"buffer_load_dwordx4 %[g0], %[srdv], s61 offen nt lds\n"
"buffer_load_dwordx4 %[g1], %[srdv], s61 offen offset:1024 nt lds\n"
"v_exp_f32 v208, v208\n"
"v_exp_f32 v209, v209\n"
"v_exp_f32 v210, v210\n"
"v_exp_f32_e64 v211, v211\n"
"v_cvt_pk_bf16_f32 v232, v208, v209\n"
"v_cvt_pk_bf16_f32 v233, v210, v211\n"
"ds_write_b64 v247, v[232:233] offset:4096\n"
"v_exp_f32 v192, v192\n"
"v_exp_f32 v193, v193\n"
"v_exp_f32 v194, v194\n"
"v_exp_f32 v195, v195\n"
"v_cvt_pk_bf16_f32 v224, v192, v193\n"
"v_cvt_pk_bf16_f32 v225, v194, v195\n"
"v_exp_f32 v196, v196\n"
"v_exp_f32 v197, v197\n"
"v_exp_f32 v198, v198\n"
"v_exp_f32 v199, v199\n"
"v_cvt_pk_bf16_f32 v228, v196, v197\n"
"v_cvt_pk_bf16_f32 v229, v198, v199\n"
"v_exp_f32 v200, v200\n"
"v_exp_f32 v201, v201\n"
"v_exp_f32 v202, v202\n"
"v_exp_f32 v203, v203\n"
"v_cvt_pk_bf16_f32 v226, v200, v201\n"
"v_cvt_pk_bf16_f32 v227, v202, v203\n"
"v_exp_f32 v204, v204\n"
"v_exp_f32 v205, v205\n"
"v_exp_f32 v206, v206\n"
"v_exp_f32 v207, v207\n"
"v_cvt_pk_bf16_f32 v230, v204, v205\n"
"v_cvt_pk_bf16_f32 v231, v206, v207\n"
"s_waitcnt vmcnt(8) lgkmcnt(0)\n"
"s_barrier\n"
"s_sleep 3\n"
"s_setprio 1\n"
"v_mfma_f32_16x16x32_bf16 v[16:19], v[160:163], v[224:227], v[16:19]\n"
"ds_read_b128 v[128:131], %[kb] offset:\\p2+2048\n"
"v_add_f32 v240, v240, v192\n"
"v_add_f32 v241, v241, v196\n"
"v_mfma_f32_16x16x32_bf16 v[48:51], v[160:163], v[228:231], v[48:51]\n"
"ds_read_b128 v[132:135], %[kb] offset:\\p2+2304\n"
"v_add_f32 v242, v242, v208\n"
"v_add_f32 v240, v240, v193\n"
"v_mfma_f32_16x16x32_bf16 v[20:23], v[164:167], v[224:227], v[20:23]\n"
"ds_read_b128 v[136:139], %[kb] offset:\\p2+2560\n"
"v_add_f32 v241, v241, v197\n"
"v_add_f32 v242, v242, v209\n"
"v_mfma_f32_16x16x32_bf16 v[52:55], v[164:167], v[228:231], v[52:55]\n"
"ds_read_b128 v[140:143], %[kb] offset:\\p2+2816\n"
"v_add_f32 v240, v240, v194\n"
"v_add_f32 v241, v241, v198\n"
"v_mfma_f32_16x16x32_bf16 v[24:27], v[168:171], v[224:227], v[24:27]\n"
"ds_read_b128 v[144:147], %[kb] offset:\\p2+10240\n"
"v_add_f32 v242, v242, v210\n"
"v_add_f32 v240, v240, v195\n"
"v_mfma_f32_16x16x32_bf16 v[56:59], v[168:171], v[228:231], v[56:59]\n"
"ds_read_b128 v[148:151], %[kb] offset:\\p2+10496\n"
"v_add_f32 v241, v241, v199\n"
"v_add_f32 v242, v242, v211\n"
"v_mfma_f32_16x16x32_bf16 v[28:31], v[172:175], v[224:227], v[28:31]\n"
"ds_read_b128 v[152:155], %[kb] offset:\\p2+10752\n"
"v_add_f32 v240, v240, v200\n"
"v_add_f32 v241, v241, v204\n"
"v_mfma_f32_16x16x32_bf16 v[60:63], v[172:175], v[228:231], v[60:63]\n"
"ds_read_b128 v[156:159], %[kb] offset:\\p2+11008\n"
"v_add_f32 v240, v240, v201\n"
"v_add_f32 v241, v241, v205\n"
"v_mfma_f32_16x16x32_bf16 v[32:35], v[176:179], v[224:227], v[32:35]\n"
"ds_read_b64_tr_b16 v[160:161], %[vbe] offset:\\p2+0\n"
"v_add_f32 v240, v240, v202\n"
"v_add_f32 v241, v241, v206\n"
"v_mfma_f32_16x16x32_bf16 v[64:67], v[176:179], v[228:231], v[64:67]\n"
"ds_read_b64_tr_b16 v[162:163], %[vbe] offset:\\p2+8192\n"
"v_add_f32 v240, v240, v203\n"
"v_add_f32 v241, v241, v207\n"
"v_mfma_f32_16x16x32_bf16 v[36:39], v[180:183], v[224:227], v[36:39]\n"
"ds_read_b64_tr_b16 v[164:165], v243 offset:\\p2+0\n"
"v_mfma_f32_16x16x32_bf16 v[68:71], v[180:183], v[228:231], v[68:71]\n"
"ds_read_b64_tr_b16 v[166:167], v243 offset:\\p2+8192\n"
"v_mfma_f32_16x16x32_bf16 v[40:43], v[184:187], v[224:227], v[40:43]\n"
"ds_read_b64_tr_b16 v[168:169], %[vbe] offset:\\p2+256\n"
"v_mfma_f32_16x16x32_bf16 v[72:75], v[184:187], v[228:231], v[72:75]\n"
"ds_read_b64_tr_b16 v[170:171], %[vbe] offset:\\p2+8448\n"
"v_mfma_f32_16x16x32_bf16 v[44:47], v[188:191], v[224:227], v[44:47]\n"
"ds_read_b64_tr_b16 v[172:173], v243 offset:\\p2+256\n"
"v_mfma_f32_16x16x32_bf16 v[76:79], v[188:191], v[228:231], v[76:79]\n"
"ds_read_b64_tr_b16 v[174:175], v243 offset:\\p2+8448\n"
"s_waitcnt lgkmcnt(8)\n"
"v_mfma_f32_16x16x32_bf16 v[208:211], v[128:131], v[80:83], v[220:223]\n"
"ds_read_b64_tr_b16 v[176:177], %[vbe] offset:\\p2+512\n"
"v_mfma_f32_16x16x32_bf16 v[208:211], v[132:135], v[84:87], v[208:211]\n"
"ds_read_b64_tr_b16 v[178:179], %[vbe] offset:\\p2+8704\n"
"v_mfma_f32_16x16x32_bf16 v[208:211], v[136:139], v[88:91], v[208:211]\n"
"ds_read_b64_tr_b16 v[180:181], v243 offset:\\p2+512\n"
"v_mfma_f32_16x16x32_bf16 v[208:211], v[140:143], v[92:95], v[208:211]\n"
"ds_read_b64_tr_b16 v[182:183], v243 offset:\\p2+8704\n"
"v_mfma_f32_16x16x32_bf16 v[192:195], v[128:131], v[96:99], v[212:215]\n"
"ds_read_b64_tr_b16 v[184:185], %[vbe] offset:\\p2+768\n"
"v_mfma_f32_16x16x32_bf16 v[192:195], v[132:135], v[100:103], v[192:195]\n"
"ds_read_b64_tr_b16 v[186:187], %[vbe] offset:\\p2+8960\n"
"v_mfma_f32_16x16x32_bf16 v[192:195], v[136:139], v[104:107], v[192:195]\n"
"ds_read_b64_tr_b16 v[188:189], v243 offset:\\p2+768\n"
"v_mfma_f32_16x16x32_bf16 v[192:195], v[140:143], v[108:111], v[192:195]\n"
"ds_read_b64_tr_b16 v[190:191], v243 offset:\\p2+8960\n"
"v_mfma_f32_16x16x32_bf16 v[196:199], v[128:131], v[112:115], v[216:219]\n"
"v_mfma_f32_16x16x32_bf16 v[196:199], v[132:135], v[116:119], v[196:199]\n"
"v_mfma_f32_16x16x32_bf16 v[196:199], v[136:139], v[120:123], v[196:199]\n"
"v_mfma_f32_16x16x32_bf16 v[196:199], v[140:143], v[124:127], v[196:199]\n"
"v_mfma_f32_16x16x32_bf16 v[200:203], v[144:147], v[96:99], v[212:215]\n"
"v_mfma_f32_16x16x32_bf16 v[200:203], v[148:151], v[100:103], v[200:203]\n"
"v_mfma_f32_16x16x32_bf16 v[200:203], v[152:155], v[104:107], v[200:203]\n"
"v_mfma_f32_16x16x32_bf16 v[200:203], v[156:159], v[108:111], v[200:203]\n"
"v_mfma_f32_16x16x32_bf16 v[204:207], v[144:147], v[112:115], v[216:219]\n"
"v_mfma_f32_16x16x32_bf16 v[204:207], v[148:151], v[116:119], v[204:207]\n"
"v_mfma_f32_16x16x32_bf16 v[204:207], v[152:155], v[120:123], v[204:207]\n"
"v_mfma_f32_16x16x32_bf16 v[204:207], v[156:159], v[124:127], v[204:207]\n"
"s_waitcnt lgkmcnt(0)\n"
"s_barrier\n"
"s_mov_b32 m0, \\p1\n"
"s_setprio 0\n"
"buffer_load_dwordx4 %[g0], %[srdk], s61 offen offset:2048 nt lds\n"
"s_add_u32 s61, s61, 0x4000\n"
"s_cmp_eq_u32 s61, 0x40000\n"
"v_exp_f32 v208, v208\n"
"v_exp_f32 v209, v209\n"
"v_exp_f32 v210, v210\n"
"v_exp_f32_e64 v211, v211\n"
"v_cvt_pk_bf16_f32 v232, v208, v209\n"
"v_cvt_pk_bf16_f32 v233, v210, v211\n"
"ds_write_b64 v247, v[232:233] offset:4096\n"
"v_exp_f32 v192, v192\n"
"v_exp_f32 v193, v193\n"
"v_exp_f32 v194, v194\n"
"v_exp_f32 v195, v195\n"
"v_cvt_pk_bf16_f32 v224, v192, v193\n"
"v_cvt_pk_bf16_f32 v225, v194, v195\n"
"v_exp_f32 v196, v196\n"
"v_exp_f32 v197, v197\n"
"v_exp_f32 v198, v198\n"
"v_exp_f32 v199, v199\n"
"v_cvt_pk_bf16_f32 v228, v196, v197\n"
"v_cvt_pk_bf16_f32 v229, v198, v199\n"
"v_exp_f32 v200, v200\n"
"v_exp_f32 v201, v201\n"
"v_exp_f32 v202, v202\n"
"v_exp_f32 v203, v203\n"
"v_cvt_pk_bf16_f32 v226, v200, v201\n"
"v_cvt_pk_bf16_f32 v227, v202, v203\n"
"v_exp_f32 v204, v204\n"
"v_exp_f32 v205, v205\n"
"v_exp_f32 v206, v206\n"
"v_exp_f32 v207, v207\n"
"v_cvt_pk_bf16_f32 v230, v204, v205\n"
"v_cvt_pk_bf16_f32 v231, v206, v207\n"
"s_waitcnt lgkmcnt(0)\n"
"s_nop 0\n"
".endm\n"
".p2align 3\n"
"LR0_%=:\n"
"s_mov_b32 s62, 0\n"
"s_mov_b32 s63, 0\n"
"s_mov_b32 s61, 0\n"
"s_lshr_b32 s60, %[hb], 5\n"
"s_lshr_b32 s64, %[hb], 6\n"
"v_lshrrev_b32 v198, 8, %[g0]\n"
"v_mul_u32_u24 v198, 7936, v198\n"
"v_add_u32 v198, %[g0], v198\n"
"v_xor_b32 v195, 32, v198\n"
"v_add_u32 v195, 31744, v195\n"
"v_xor_b32_e64 v193, 48, %[xoff]\n"
"s_add_u32 m0, %[hb], 32768\n"
"v_add_u32 v193, 8192, v193\n"
"s_add_u32 s62, s62, 0x40000\n"
"s_add_u32 m0, s60, 131072\n"
"s_nop 0\n"
"s_add_u32 m0, s64, 57344\n"
"s_nop 0\n"
"s_add_u32 s63, s63, 0x80\n"
"v_lshrrev_b32 v199, 5, %[l16]\n"
"v_lshlrev_b32 v199, 13, v199\n"
"v_and_b32_e64 v200, 16, %[l16]\n"
"v_lshl_add_u32 v199, v200, 3, v199\n"
"v_add_u32 v199, 32768, v199\n"
"s_cmp_eq_u32 %[wrot], 1\n"
"s_cselect_b32 s68, 0, 0x40000000\n"
"s_mov_b32 s69, 262144\n"
"v_or_b32_e64 v200, s68, v199\n"
"s_cmp_eq_u32 %[wrot], 2\n"
"s_cselect_b32 s68, 0, 0x40000000\n"
"s_mov_b32 s69, 524288\n"
"v_or_b32 v200, s68, v199\n"
"s_cmp_eq_u32 %[wrot], 3\n"
"s_cselect_b32 s68, 0, 0x40000000\n"
"s_mov_b32 s69, 786432\n"
"v_or_b32 v200, s68, v199\n"
"s_cmp_eq_u32 %[wrot], 0\n"
"s_cselect_b32 s68, 0, 0x40000000\n"
"s_mov_b32 s69, 1048576\n"
"v_or_b32 v200, s68, v199\n"
"s_mul_i32 s91, %[xrot], 6\n"
"s_add_u32 m0, %[hb], 40960\n"
"s_nop 0\n"
"s_add_u32 s62, s62, 0x40000\n"
"buffer_load_dword v197, %[gin], s[80:83], 0 offen offset:4 sc1\n"
"s_add_u32 s52, %[hb], 0\n"
"s_add_u32 s53, %[hb], 16384\n"
"s_add_u32 s54, %[hb], 32768\n"
"s_add_u32 s55, %[hb], 49152\n"
"s_lshr_b32 s90, %[hb], 3\n"
"s_add_u32 s90, s90, 131072\n"
"v_xor_b32 v192, 32, %[wbe]\n"
"v_xor_b32 v243, 32, %[vbe]\n"
"v_add_u32 v194, 131072, %[xb]\n"
"v_add_u32_e64 v194, s60, v194\n"
"v_xor_b32 v196, 2048, v194\n"
"v_add_u32 v244, %[d1], %[kb]\n"
"v_add_u32 v245, %[d1], %[vbe]\n"
"v_add_u32 v246, %[d1], v243\n"
"s_lshr_b32 s65, %[hb], 3\n"
"s_add_u32 s65, s65, 147456\n"
"v_add_u32_e64 v247, s65, %[l16]\n"
"s_mov_b32 s66, 0x3e0293ee\n"
"s_mov_b32 s67, s66\n"
"s_waitcnt vmcnt(7)\n"
"s_barrier\n"
"ds_read_b128 v[96:99], v194 offset:0\n"
"ds_read_b128 v[100:103], v196 offset:0\n"
"ds_read_b128 v[104:107], %[xb] offset:57344\n"
"ds_read_b64_tr_b16 v[128:129], %[wbe] offset:32768\n"
"ds_read_b64_tr_b16 v[130:131], v192 offset:33792\n"
"ds_read_b64_tr_b16 v[132:133], v192 offset:32768\n"
"ds_read_b64_tr_b16 v[134:135], %[wbe] offset:33792\n"
"ds_read_b64_tr_b16 v[136:137], %[wbe] offset:33024\n"
"ds_read_b64_tr_b16 v[138:139], v192 offset:34048\n"
"ds_read_b64_tr_b16 v[140:141], v192 offset:33024\n"
"ds_read_b64_tr_b16 v[142:143], %[wbe] offset:34048\n"
"ds_read_b64_tr_b16 v[144:145], %[wbe] offset:33280\n"
"ds_read_b64_tr_b16 v[146:147], v192 offset:34304\n"
"ds_read_b64_tr_b16 v[148:149], v192 offset:33280\n"
"ds_read_b64_tr_b16 v[150:151], %[wbe] offset:34304\n"
"ds_read_b64_tr_b16 v[152:153], %[wbe] offset:33536\n"
"ds_read_b64_tr_b16 v[154:155], v192 offset:34560\n"
"ds_read_b64_tr_b16 v[156:157], v192 offset:33536\n"
"s_add_u32 m0, s60, 147456\n"
"ds_read_b64_tr_b16 v[158:159], %[wbe] offset:34560\n"
"buffer_load_dwordx4 %[xoff], %[srdx], s63 offen lds\n"
"buffer_load_dwordx4 v193, %[srdx], s63 offen offset:1024 lds\n"
"s_add_u32 s63, s63, 0x80\n"
"s_add_u32 m0, %[hb], 49152\n"
"s_nop 0\n"
"buffer_load_dwordx4 v198, %[srdw], s62 offen lds\n"
"buffer_load_dwordx4 v195, %[srdw], s62 offen offset:1024 lds\n"
"s_add_u32 s62, s62, 0x40000\n"
"s_cmp_eq_u32 %[wrot], 1\n"
"s_cselect_b32 s68, 0, 0x40000000\n"
"s_mov_b32 s69, 1310720\n"
"v_or_b32 v200, s68, v199\n"
"buffer_load_dword v201, v200, %[srdw], s69 offen\n"
"s_cmp_eq_u32 %[wrot], 2\n"
"s_cselect_b32 s68, 0, 0x40000000\n"
"s_mov_b32 s69, 1572864\n"
"v_or_b32 v200, s68, v199\n"
"buffer_load_dword v201, v200, %[srdw], s69 offen\n"
"s_waitcnt vmcnt(7) lgkmcnt(0)\n"
"s_barrier\n"
"v_mfma_f32_16x16x32_bf16 v[16:19], v[128:131], v[96:99], 0\n"
"ds_read_b128 v[108:111], v194 offset:512\n"
"ds_read_b128 v[112:115], v196 offset:512\n"
"s_add_u32 m0, %[hb], 32768\n"
"v_mfma_f32_16x16x32_bf16 v[48:51], v[128:131], v[100:103], 0\n"
"buffer_load_dwordx4 v198, %[srdw], s62 offen lds\n"
"v_mfma_f32_16x16x32_bf16 v[80:83], v[128:131], v[104:107], 0\n"
"ds_read_b128 v[116:119], %[xb] offset:57856\n"
"v_mfma_f32_16x16x32_bf16 v[20:23], v[132:135], v[96:99], 0\n"
"ds_read_b64_tr_b16 v[160:161], %[wbe] offset:40960\n"
"ds_read_b64_tr_b16 v[162:163], v192 offset:41984\n"
"v_mfma_f32_16x16x32_bf16 v[52:55], v[132:135], v[100:103], 0\n"
"ds_read_b64_tr_b16 v[164:165], v192 offset:40960\n"
"v_mfma_f32_16x16x32_bf16 v[84:87], v[132:135], v[104:107], 0\n"
"buffer_load_dwordx4 v195, %[srdw], s62 offen offset:1024 lds\n"
"s_add_u32 s62, s62, 0x40000\n"
"v_mfma_f32_16x16x32_bf16 v[24:27], v[136:139], v[96:99], 0\n"
"ds_read_b64_tr_b16 v[166:167], %[wbe] offset:41984\n"
"v_mfma_f32_16x16x32_bf16 v[56:59], v[136:139], v[100:103], 0\n"
"ds_read_b64_tr_b16 v[168:169], %[wbe] offset:41216\n"
"ds_read_b64_tr_b16 v[170:171], v192 offset:42240\n"
"v_mfma_f32_16x16x32_bf16 v[28:31], v[140:143], v[96:99], 0\n"
"ds_read_b64_tr_b16 v[172:173], v192 offset:41216\n"
"v_mfma_f32_16x16x32_bf16 v[60:63], v[140:143], v[100:103], 0\n"
"s_cmp_eq_u32 %[wrot], 3\n"
"s_cselect_b32 s68, 0, 0x40000000\n"
"s_mov_b32 s69, 1835008\n"
"v_or_b32 v200, s68, v199\n"
"buffer_load_dword v201, v200, %[srdw], s69 offen\n"
"v_mfma_f32_16x16x32_bf16 v[32:35], v[144:147], v[96:99], 0\n"
"ds_read_b64_tr_b16 v[174:175], %[wbe] offset:42240\n"
"v_mfma_f32_16x16x32_bf16 v[64:67], v[144:147], v[100:103], 0\n"
"ds_read_b64_tr_b16 v[176:177], %[wbe] offset:41472\n"
"ds_read_b64_tr_b16 v[178:179], v192 offset:42496\n"
"v_mfma_f32_16x16x32_bf16 v[36:39], v[148:151], v[96:99], 0\n"
"ds_read_b64_tr_b16 v[180:181], v192 offset:41472\n"
"v_mfma_f32_16x16x32_bf16 v[68:71], v[148:151], v[100:103], 0\n"
"ds_read_b64_tr_b16 v[182:183], %[wbe] offset:42496\n"
"v_mfma_f32_16x16x32_bf16 v[40:43], v[152:155], v[96:99], 0\n"
"ds_read_b64_tr_b16 v[184:185], %[wbe] offset:41728\n"
"ds_read_b64_tr_b16 v[186:187], v192 offset:42752\n"
"v_mfma_f32_16x16x32_bf16 v[72:75], v[152:155], v[100:103], 0\n"
"ds_read_b64_tr_b16 v[188:189], v192 offset:41728\n"
"v_mfma_f32_16x16x32_bf16 v[44:47], v[156:159], v[96:99], 0\n"
"ds_read_b64_tr_b16 v[190:191], %[wbe] offset:42752\n"
"v_mfma_f32_16x16x32_bf16 v[76:79], v[156:159], v[100:103], 0\n"
"s_waitcnt vmcnt(5) lgkmcnt(0)\n"
"s_barrier\n"
"v_mfma_f32_16x16x32_bf16 v[16:19], v[160:163], v[108:111], v[16:19]\n"
"ds_read_b128 v[96:99], v194 offset:16384\n"
"ds_read_b128 v[100:103], v196 offset:16384\n"
"s_add_u32 m0, s60, 131072\n"
"v_mfma_f32_16x16x32_bf16 v[48:51], v[160:163], v[112:115], v[48:51]\n"
"buffer_load_dwordx4 %[xoff], %[srdx], s63 offen lds\n"
"v_mfma_f32_16x16x32_bf16 v[80:83], v[160:163], v[116:119], v[80:83]\n"
"ds_read_b128 v[104:107], %[xb] offset:59392\n"
"ds_read_b64_tr_b16 v[128:129], %[wbe] offset:49152\n"
"v_mfma_f32_16x16x32_bf16 v[20:23], v[164:167], v[108:111], v[20:23]\n"
"ds_read_b64_tr_b16 v[130:131], v192 offset:50176\n"
"v_mfma_f32_16x16x32_bf16 v[52:55], v[164:167], v[112:115], v[52:55]\n"
"buffer_load_dwordx4 v193, %[srdx], s63 offen offset:1024 lds\n"
"s_add_u32 s63, s63, 0x80\n"
"v_mfma_f32_16x16x32_bf16 v[84:87], v[164:167], v[116:119], v[84:87]\n"
"ds_read_b64_tr_b16 v[132:133], v192 offset:49152\n"
"ds_read_b64_tr_b16 v[134:135], %[wbe] offset:50176\n"
"v_mfma_f32_16x16x32_bf16 v[24:27], v[168:171], v[108:111], v[24:27]\n"
"ds_read_b64_tr_b16 v[136:137], %[wbe] offset:49408\n"
"s_add_u32 m0, %[hb], 40960\n"
"v_mfma_f32_16x16x32_bf16 v[56:59], v[168:171], v[112:115], v[56:59]\n"
"buffer_load_dwordx4 v198, %[srdw], s62 offen lds\n"
"v_mfma_f32_16x16x32_bf16 v[28:31], v[172:175], v[108:111], v[28:31]\n"
"ds_read_b64_tr_b16 v[138:139], v192 offset:50432\n"
"ds_read_b64_tr_b16 v[140:141], v192 offset:49408\n"
"v_mfma_f32_16x16x32_bf16 v[60:63], v[172:175], v[112:115], v[60:63]\n"
"ds_read_b64_tr_b16 v[142:143], %[wbe] offset:50432\n"
"ds_read_b64_tr_b16 v[144:145], %[wbe] offset:49664\n"
"v_mfma_f32_16x16x32_bf16 v[32:35], v[176:179], v[108:111], v[32:35]\n"
"buffer_load_dwordx4 v195, %[srdw], s62 offen offset:1024 lds\n"
"s_add_u32 s62, s62, 0x40000\n"
"v_mfma_f32_16x16x32_bf16 v[64:67], v[176:179], v[112:115], v[64:67]\n"
"ds_read_b64_tr_b16 v[146:147], v192 offset:50688\n"
"v_mfma_f32_16x16x32_bf16 v[36:39], v[180:183], v[108:111], v[36:39]\n"
"ds_read_b64_tr_b16 v[148:149], v192 offset:49664\n"
"ds_read_b64_tr_b16 v[150:151], %[wbe] offset:50688\n"
"v_mfma_f32_16x16x32_bf16 v[68:71], v[180:183], v[112:115], v[68:71]\n"
"s_cmp_eq_u32 %[wrot], 0\n"
"s_cselect_b32 s68, 0, 0x40000000\n"
"s_mov_b32 s69, 2097152\n"
"v_or_b32 v200, s68, v199\n"
"buffer_load_dword v201, v200, %[srdw], s69 offen\n"
"v_mfma_f32_16x16x32_bf16 v[40:43], v[184:187], v[108:111], v[40:43]\n"
"ds_read_b64_tr_b16 v[152:153], %[wbe] offset:49920\n"
"v_mfma_f32_16x16x32_bf16 v[72:75], v[184:187], v[112:115], v[72:75]\n"
"ds_read_b64_tr_b16 v[154:155], v192 offset:50944\n"
"ds_read_b64_tr_b16 v[156:157], v192 offset:49920\n"
"v_mfma_f32_16x16x32_bf16 v[44:47], v[188:191], v[108:111], v[44:47]\n"
"ds_read_b64_tr_b16 v[158:159], %[wbe] offset:50944\n"
"v_mfma_f32_16x16x32_bf16 v[76:79], v[188:191], v[112:115], v[76:79]\n"
"s_waitcnt vmcnt(6) lgkmcnt(0)\n"
"s_barrier\n"
"v_mfma_f32_16x16x32_bf16 v[16:19], v[128:131], v[96:99], v[16:19]\n"
"ds_read_b128 v[108:111], v194 offset:16896\n"
"ds_read_b128 v[112:115], v196 offset:16896\n"
"s_add_u32 m0, %[hb], 49152\n"
"v_mfma_f32_16x16x32_bf16 v[48:51], v[128:131], v[100:103], v[48:51]\n"
"buffer_load_dwordx4 v198, %[srdw], s62 offen lds\n"
"v_mfma_f32_16x16x32_bf16 v[80:83], v[128:131], v[104:107], v[80:83]\n"
"ds_read_b128 v[116:119], %[xb] offset:59904\n"
"v_mfma_f32_16x16x32_bf16 v[20:23], v[132:135], v[96:99], v[20:23]\n"
"ds_read_b64_tr_b16 v[160:161], %[wbe] offset:32768\n"
"ds_read_b64_tr_b16 v[162:163], v192 offset:33792\n"
"v_mfma_f32_16x16x32_bf16 v[52:55], v[132:135], v[100:103], v[52:55]\n"
"ds_read_b64_tr_b16 v[164:165], v192 offset:32768\n"
"v_mfma_f32_16x16x32_bf16 v[84:87], v[132:135], v[104:107], v[84:87]\n"
"buffer_load_dwordx4 v195, %[srdw], s62 offen offset:1024 lds\n"
"s_add_u32 s62, s62, 0x40000\n"
"v_mfma_f32_16x16x32_bf16 v[24:27], v[136:139], v[96:99], v[24:27]\n"
"ds_read_b64_tr_b16 v[166:167], %[wbe] offset:33792\n"
"v_mfma_f32_16x16x32_bf16 v[56:59], v[136:139], v[100:103], v[56:59]\n"
"ds_read_b64_tr_b16 v[168:169], %[wbe] offset:33024\n"
"ds_read_b64_tr_b16 v[170:171], v192 offset:34048\n"
"v_mfma_f32_16x16x32_bf16 v[28:31], v[140:143], v[96:99], v[28:31]\n"
"ds_read_b64_tr_b16 v[172:173], v192 offset:33024\n"
"v_mfma_f32_16x16x32_bf16 v[60:63], v[140:143], v[100:103], v[60:63]\n"
"s_cmp_eq_u32 %[wrot], 1\n"
"s_cselect_b32 s68, 0, 0x40000000\n"
"s_mov_b32 s69, 2359296\n"
"v_or_b32 v200, s68, v199\n"
"buffer_load_dword v201, v200, %[srdw], s69 offen\n"
"v_mfma_f32_16x16x32_bf16 v[32:35], v[144:147], v[96:99], v[32:35]\n"
"ds_read_b64_tr_b16 v[174:175], %[wbe] offset:34048\n"
"v_mfma_f32_16x16x32_bf16 v[64:67], v[144:147], v[100:103], v[64:67]\n"
"ds_read_b64_tr_b16 v[176:177], %[wbe] offset:33280\n"
"ds_read_b64_tr_b16 v[178:179], v192 offset:34304\n"
"v_mfma_f32_16x16x32_bf16 v[36:39], v[148:151], v[96:99], v[36:39]\n"
"ds_read_b64_tr_b16 v[180:181], v192 offset:33280\n"
"v_mfma_f32_16x16x32_bf16 v[68:71], v[148:151], v[100:103], v[68:71]\n"
"ds_read_b64_tr_b16 v[182:183], %[wbe] offset:34304\n"
"v_mfma_f32_16x16x32_bf16 v[40:43], v[152:155], v[96:99], v[40:43]\n"
"ds_read_b64_tr_b16 v[184:185], %[wbe] offset:33536\n"
"ds_read_b64_tr_b16 v[186:187], v192 offset:34560\n"
"v_mfma_f32_16x16x32_bf16 v[72:75], v[152:155], v[100:103], v[72:75]\n"
"ds_read_b64_tr_b16 v[188:189], v192 offset:33536\n"
"v_mfma_f32_16x16x32_bf16 v[44:47], v[156:159], v[96:99], v[44:47]\n"
"ds_read_b64_tr_b16 v[190:191], %[wbe] offset:34560\n"
"v_mfma_f32_16x16x32_bf16 v[76:79], v[156:159], v[100:103], v[76:79]\n"
"s_waitcnt vmcnt(4) lgkmcnt(0)\n"
"s_barrier\n"
"v_mfma_f32_16x16x32_bf16 v[16:19], v[160:163], v[108:111], v[16:19]\n"
"ds_read_b128 v[96:99], v194 offset:0\n"
"ds_read_b128 v[100:103], v196 offset:0\n"
"s_add_u32 m0, s60, 147456\n"
"v_mfma_f32_16x16x32_bf16 v[48:51], v[160:163], v[112:115], v[48:51]\n"
"buffer_load_dwordx4 %[xoff], %[srdx], s63 offen lds\n"
"v_mfma_f32_16x16x32_bf16 v[80:83], v[160:163], v[116:119], v[80:83]\n"
"ds_read_b128 v[104:107], %[xb] offset:57344\n"
"ds_read_b64_tr_b16 v[128:129], %[wbe] offset:40960\n"
"v_mfma_f32_16x16x32_bf16 v[20:23], v[164:167], v[108:111], v[20:23]\n"
"ds_read_b64_tr_b16 v[130:131], v192 offset:41984\n"
"v_mfma_f32_16x16x32_bf16 v[52:55], v[164:167], v[112:115], v[52:55]\n"
"buffer_load_dwordx4 v193, %[srdx], s63 offen offset:1024 lds\n"
"s_add_u32 s63, s63, 0x80\n"
"v_mfma_f32_16x16x32_bf16 v[84:87], v[164:167], v[116:119], v[84:87]\n"
"ds_read_b64_tr_b16 v[132:133], v192 offset:40960\n"
"ds_read_b64_tr_b16 v[134:135], %[wbe] offset:41984\n"
"v_mfma_f32_16x16x32_bf16 v[24:27], v[168:171], v[108:111], v[24:27]\n"
"ds_read_b64_tr_b16 v[136:137], %[wbe] offset:41216\n"
"s_add_u32 m0, %[hb], 32768\n"
"v_mfma_f32_16x16x32_bf16 v[56:59], v[168:171], v[112:115], v[56:59]\n"
"buffer_load_dwordx4 v198, %[srdw], s62 offen lds\n"
"v_mfma_f32_16x16x32_bf16 v[28:31], v[172:175], v[108:111], v[28:31]\n"
"ds_read_b64_tr_b16 v[138:139], v192 offset:42240\n"
"ds_read_b64_tr_b16 v[140:141], v192 offset:41216\n"
"v_mfma_f32_16x16x32_bf16 v[60:63], v[172:175], v[112:115], v[60:63]\n"
"ds_read_b64_tr_b16 v[142:143], %[wbe] offset:42240\n"
"ds_read_b64_tr_b16 v[144:145], %[wbe] offset:41472\n"
"v_mfma_f32_16x16x32_bf16 v[32:35], v[176:179], v[108:111], v[32:35]\n"
"buffer_load_dwordx4 v195, %[srdw], s62 offen offset:1024 lds\n"
"s_add_u32 s62, s62, 0x40000\n"
"v_mfma_f32_16x16x32_bf16 v[64:67], v[176:179], v[112:115], v[64:67]\n"
"ds_read_b64_tr_b16 v[146:147], v192 offset:42496\n"
"v_mfma_f32_16x16x32_bf16 v[36:39], v[180:183], v[108:111], v[36:39]\n"
"ds_read_b64_tr_b16 v[148:149], v192 offset:41472\n"
"ds_read_b64_tr_b16 v[150:151], %[wbe] offset:42496\n"
"v_mfma_f32_16x16x32_bf16 v[68:71], v[180:183], v[112:115], v[68:71]\n"
"s_cmp_eq_u32 %[wrot], 2\n"
"s_cselect_b32 s68, 0, 0x40000000\n"
"s_mov_b32 s69, 2621440\n"
"v_or_b32 v200, s68, v199\n"
"buffer_load_dword v201, v200, %[srdw], s69 offen\n"
"v_mfma_f32_16x16x32_bf16 v[40:43], v[184:187], v[108:111], v[40:43]\n"
"ds_read_b64_tr_b16 v[152:153], %[wbe] offset:41728\n"
"v_mfma_f32_16x16x32_bf16 v[72:75], v[184:187], v[112:115], v[72:75]\n"
"ds_read_b64_tr_b16 v[154:155], v192 offset:42752\n"
"ds_read_b64_tr_b16 v[156:157], v192 offset:41728\n"
"v_mfma_f32_16x16x32_bf16 v[44:47], v[188:191], v[108:111], v[44:47]\n"
"ds_read_b64_tr_b16 v[158:159], %[wbe] offset:42752\n"
"v_mfma_f32_16x16x32_bf16 v[76:79], v[188:191], v[112:115], v[76:79]\n"
"s_waitcnt vmcnt(6) lgkmcnt(0)\n"
"s_barrier\n"
"v_mfma_f32_16x16x32_bf16 v[16:19], v[128:131], v[96:99], v[16:19]\n"
"ds_read_b128 v[108:111], v194 offset:512\n"
"ds_read_b128 v[112:115], v196 offset:512\n"
"s_add_u32 m0, %[hb], 40960\n"
"v_mfma_f32_16x16x32_bf16 v[48:51], v[128:131], v[100:103], v[48:51]\n"
"buffer_load_dwordx4 v198, %[srdw], s62 offen lds\n"
"v_mfma_f32_16x16x32_bf16 v[80:83], v[128:131], v[104:107], v[80:83]\n"
"ds_read_b128 v[116:119], %[xb] offset:57856\n"
"ds_read_b64_tr_b16 v[160:161], %[wbe] offset:49152\n"
"v_mfma_f32_16x16x32_bf16 v[20:23], v[132:135], v[96:99], v[20:23]\n"
"ds_read_b64_tr_b16 v[162:163], v192 offset:50176\n"
"v_mfma_f32_16x16x32_bf16 v[52:55], v[132:135], v[100:103], v[52:55]\n"
"buffer_load_dwordx4 v195, %[srdw], s62 offen offset:1024 lds\n"
"s_add_u32 s62, s62, 0x40000\n"
"v_mfma_f32_16x16x32_bf16 v[84:87], v[132:135], v[104:107], v[84:87]\n"
"ds_read_b64_tr_b16 v[164:165], v192 offset:49152\n"
"ds_read_b64_tr_b16 v[166:167], %[wbe] offset:50176\n"
"v_mfma_f32_16x16x32_bf16 v[24:27], v[136:139], v[96:99], v[24:27]\n"
"ds_read_b64_tr_b16 v[168:169], %[wbe] offset:49408\n"
"v_mfma_f32_16x16x32_bf16 v[56:59], v[136:139], v[100:103], v[56:59]\n"
"s_cmp_eq_u32 %[wrot], 3\n"
"s_cselect_b32 s68, 0, 0x40000000\n"
"s_mov_b32 s69, 2883584\n"
"v_or_b32 v200, s68, v199\n"
"buffer_load_dword v201, v200, %[srdw], s69 offen\n"
"v_mfma_f32_16x16x32_bf16 v[28:31], v[140:143], v[96:99], v[28:31]\n"
"ds_read_b64_tr_b16 v[170:171], v192 offset:50432\n"
"ds_read_b64_tr_b16 v[172:173], v192 offset:49408\n"
"v_mfma_f32_16x16x32_bf16 v[60:63], v[140:143], v[100:103], v[60:63]\n"
"ds_read_b64_tr_b16 v[174:175], %[wbe] offset:50432\n"
"ds_read_b64_tr_b16 v[176:177], %[wbe] offset:49664\n"
"v_mfma_f32_16x16x32_bf16 v[32:35], v[144:147], v[96:99], v[32:35]\n"
"s_bitcmp0_b32 %[xrot], 4\n"
"s_cbranch_scc1 LK0_4_%=\n"
"s_mov_b32 m0, s52\n"
"s_nop 0\n"
"buffer_load_dwordx4 %[g0], %[srdv], s61 offen nt lds\n"
"buffer_load_dwordx4 %[g1], %[srdv], s61 offen offset:1024 nt lds\n"
"buffer_load_dwordx4 %[g0], %[srdk], s61 offen offset:2048 nt lds\n"
"s_add_u32 s61, s61, %[kvs]\n"
"LK0_4_%=:\n"
"v_mfma_f32_16x16x32_bf16 v[64:67], v[144:147], v[100:103], v[64:67]\n"
"ds_read_b64_tr_b16 v[178:179], v192 offset:50688\n"
"v_mfma_f32_16x16x32_bf16 v[36:39], v[148:151], v[96:99], v[36:39]\n"
"ds_read_b64_tr_b16 v[180:181], v192 offset:49664\n"
"ds_read_b64_tr_b16 v[182:183], %[wbe] offset:50688\n"
"v_mfma_f32_16x16x32_bf16 v[68:71], v[148:151], v[100:103], v[68:71]\n"
"v_add_u32 v197, 1, v197\n"
"buffer_store_dword v197, %[gout], s[80:83], 0 offen sc1\n"
"v_mfma_f32_16x16x32_bf16 v[40:43], v[152:155], v[96:99], v[40:43]\n"
"ds_read_b64_tr_b16 v[184:185], %[wbe] offset:49920\n"
"v_mfma_f32_16x16x32_bf16 v[72:75], v[152:155], v[100:103], v[72:75]\n"
"ds_read_b64_tr_b16 v[186:187], v192 offset:50944\n"
"ds_read_b64_tr_b16 v[188:189], v192 offset:49920\n"
"v_mfma_f32_16x16x32_bf16 v[44:47], v[156:159], v[96:99], v[44:47]\n"
"ds_read_b64_tr_b16 v[190:191], %[wbe] offset:50944\n"
"v_mfma_f32_16x16x32_bf16 v[76:79], v[156:159], v[100:103], v[76:79]\n"
"s_waitcnt vmcnt(8)\n"
"s_bitcmp1_b32 s91, 5\n"
"s_cbranch_scc1 LJ0_5_%=\n"
"s_waitcnt vmcnt(5)\n"
"LJ0_5_%=:\n"
"s_waitcnt lgkmcnt(0)\n"
"s_barrier\n"
"v_mfma_f32_16x16x32_bf16 v[16:19], v[160:163], v[108:111], v[16:19]\n"
"ds_read_b128 v[96:99], v194 offset:16384\n"
"ds_read_b128 v[100:103], v196 offset:16384\n"
"s_add_u32 m0, s60, 131072\n"
"v_mfma_f32_16x16x32_bf16 v[48:51], v[160:163], v[112:115], v[48:51]\n"
"buffer_load_dwordx4 %[xoff], %[srdx], s63 offen lds\n"
"v_mfma_f32_16x16x32_bf16 v[80:83], v[160:163], v[116:119], v[80:83]\n"
"ds_read_b128 v[104:107], %[xb] offset:59392\n"
"ds_read_b64_tr_b16 v[128:129], %[wbe] offset:32768\n"
"v_mfma_f32_16x16x32_bf16 v[20:23], v[164:167], v[108:111], v[20:23]\n"
"buffer_load_dwordx4 v193, %[srdx], s63 offen offset:1024 lds\n"
"v_mfma_f32_16x16x32_bf16 v[52:55], v[164:167], v[112:115], v[52:55]\n"
"ds_read_b64_tr_b16 v[130:131], v192 offset:33792\n"
"ds_read_b64_tr_b16 v[132:133], v192 offset:32768\n"
"s_add_u32 m0, s64, 57344\n"
"v_mfma_f32_16x16x32_bf16 v[84:87], v[164:167], v[116:119], v[84:87]\n"
"buffer_load_dwordx4 %[xo9], %[srdx], s63 offen lds\n"
"s_add_u32 s63, s63, 0x80\n"
"v_mfma_f32_16x16x32_bf16 v[24:27], v[168:171], v[108:111], v[24:27]\n"
"ds_read_b64_tr_b16 v[134:135], %[wbe] offset:33792\n"
"ds_read_b64_tr_b16 v[136:137], %[wbe] offset:33024\n"
"s_add_u32 m0, %[hb], 49152\n"
"v_mfma_f32_16x16x32_bf16 v[56:59], v[168:171], v[112:115], v[56:59]\n"
"buffer_load_dwordx4 v198, %[srdw], s62 offen lds\n"
"v_mfma_f32_16x16x32_bf16 v[28:31], v[172:175], v[108:111], v[28:31]\n"
"ds_read_b64_tr_b16 v[138:139], v192 offset:34048\n"
"ds_read_b64_tr_b16 v[140:141], v192 offset:33024\n"
"v_mfma_f32_16x16x32_bf16 v[60:63], v[172:175], v[112:115], v[60:63]\n"
"ds_read_b64_tr_b16 v[142:143], %[wbe] offset:34048\n"
"ds_read_b64_tr_b16 v[144:145], %[wbe] offset:33280\n"
"v_mfma_f32_16x16x32_bf16 v[32:35], v[176:179], v[108:111], v[32:35]\n"
"buffer_load_dwordx4 v195, %[srdw], s62 offen offset:1024 lds\n"
"s_add_u32 s62, s62, 0x40000\n"
"v_mfma_f32_16x16x32_bf16 v[64:67], v[176:179], v[112:115], v[64:67]\n"
"ds_read_b64_tr_b16 v[146:147], v192 offset:34304\n"
"ds_read_b64_tr_b16 v[148:149], v192 offset:33280\n"
"v_mfma_f32_16x16x32_bf16 v[36:39], v[180:183], v[108:111], v[36:39]\n"
"s_cmp_eq_u32 %[wrot], 0\n"
"s_cselect_b32 s68, 0, 0x40000000\n"
"s_mov_b32 s69, 3145728\n"
"v_or_b32 v200, s68, v199\n"
"buffer_load_dword v201, v200, %[srdw], s69 offen\n"
"v_mfma_f32_16x16x32_bf16 v[68:71], v[180:183], v[112:115], v[68:71]\n"
"ds_read_b64_tr_b16 v[150:151], %[wbe] offset:34304\n"
"ds_read_b64_tr_b16 v[152:153], %[wbe] offset:33536\n"
"v_mfma_f32_16x16x32_bf16 v[40:43], v[184:187], v[108:111], v[40:43]\n"
"s_bitcmp0_b32 %[xrot], 5\n"
"s_cbranch_scc1 LK0_5_%=\n"
"s_mov_b32 m0, s52\n"
"s_nop 0\n"
"buffer_load_dwordx4 %[g0], %[srdv], s61 offen nt lds\n"
"buffer_load_dwordx4 %[g1], %[srdv], s61 offen offset:1024 nt lds\n"
"buffer_load_dwordx4 %[g0], %[srdk], s61 offen offset:2048 nt lds\n"
"s_add_u32 s61, s61, %[kvs]\n"
"LK0_5_%=:\n"
"v_mfma_f32_16x16x32_bf16 v[72:75], v[184:187], v[112:115], v[72:75]\n"
"ds_read_b64_tr_b16 v[154:155], v192 offset:34560\n"
"ds_read_b64_tr_b16 v[156:157], v192 offset:33536\n"
"v_mfma_f32_16x16x32_bf16 v[44:47], v[188:191], v[108:111], v[44:47]\n"
"ds_read_b64_tr_b16 v[158:159], %[wbe] offset:34560\n"
"v_mfma_f32_16x16x32_bf16 v[76:79], v[188:191], v[112:115], v[76:79]\n"
"s_waitcnt vmcnt(11)\n"
"s_bitcmp1_b32 s91, 6\n"
"s_cbranch_scc1 LJ0_6_%=\n"
"s_waitcnt vmcnt(8)\n"
"LJ0_6_%=:\n"
"s_waitcnt lgkmcnt(0)\n"
"s_barrier\n"
"v_mfma_f32_16x16x32_bf16 v[16:19], v[128:131], v[96:99], v[16:19]\n"
"ds_read_b128 v[108:111], v194 offset:16896\n"
"ds_read_b128 v[112:115], v196 offset:16896\n"
"s_add_u32 m0, %[hb], 32768\n"
"v_mfma_f32_16x16x32_bf16 v[48:51], v[128:131], v[100:103], v[48:51]\n"
"buffer_load_dwordx4 v198, %[srdw], s62 offen lds\n"
"v_mfma_f32_16x16x32_bf16 v[80:83], v[128:131], v[104:107], v[80:83]\n"
"ds_read_b128 v[116:119], %[xb] offset:59904\n"
"v_mfma_f32_16x16x32_bf16 v[20:23], v[132:135], v[96:99], v[20:23]\n"
"ds_read_b64_tr_b16 v[160:161], %[wbe] offset:40960\n"
"ds_read_b64_tr_b16 v[162:163], v192 offset:41984\n"
"v_mfma_f32_16x16x32_bf16 v[52:55], v[132:135], v[100:103], v[52:55]\n"
"ds_read_b64_tr_b16 v[164:165], v192 offset:40960\n"
"v_mfma_f32_16x16x32_bf16 v[84:87], v[132:135], v[104:107], v[84:87]\n"
"buffer_load_dwordx4 v195, %[srdw], s62 offen offset:1024 lds\n"
"s_add_u32 s62, s62, 0x40000\n"
"v_mfma_f32_16x16x32_bf16 v[24:27], v[136:139], v[96:99], v[24:27]\n"
"ds_read_b64_tr_b16 v[166:167], %[wbe] offset:41984\n"
"ds_read_b64_tr_b16 v[168:169], %[wbe] offset:41216\n"
"v_mfma_f32_16x16x32_bf16 v[56:59], v[136:139], v[100:103], v[56:59]\n"
"ds_read_b64_tr_b16 v[170:171], v192 offset:42240\n"
"v_mfma_f32_16x16x32_bf16 v[28:31], v[140:143], v[96:99], v[28:31]\n"
"ds_read_b64_tr_b16 v[172:173], v192 offset:41216\n"
"ds_read_b64_tr_b16 v[174:175], %[wbe] offset:42240\n"
"v_mfma_f32_16x16x32_bf16 v[60:63], v[140:143], v[100:103], v[60:63]\n"
"s_cmp_eq_u32 %[wrot], 1\n"
"s_cselect_b32 s68, 0, 0x40000000\n"
"s_mov_b32 s69, 3407872\n"
"v_or_b32_e64 v200, s68, v199\n"
"buffer_load_dword v201, v200, %[srdw], s69 offen\n"
"v_mfma_f32_16x16x32_bf16 v[32:35], v[144:147], v[96:99], v[32:35]\n"
"ds_read_b64_tr_b16 v[176:177], %[wbe] offset:41472\n"
"v_mfma_f32_16x16x32_bf16 v[64:67], v[144:147], v[100:103], v[64:67]\n"
"ds_read_b64_tr_b16 v[178:179], v192 offset:42496\n"
"ds_read_b64_tr_b16 v[180:181], v192 offset:41472\n"
"v_mfma_f32_16x16x32_bf16 v[36:39], v[148:151], v[96:99], v[36:39]\n"
"ds_read_b64_tr_b16 v[182:183], %[wbe] offset:42496\n"
"v_mfma_f32_16x16x32_bf16 v[68:71], v[148:151], v[100:103], v[68:71]\n"
"s_bitcmp0_b32 %[xrot], 6\n"
"s_cbranch_scc1 LK0_6_%=\n"
"s_mov_b32 m0, s52\n"
"s_nop 0\n"
"buffer_load_dwordx4 %[g0], %[srdv], s61 offen nt lds\n"
"buffer_load_dwordx4 %[g1], %[srdv], s61 offen offset:1024 nt lds\n"
"buffer_load_dwordx4 %[g0], %[srdk], s61 offen offset:2048 nt lds\n"
"s_add_u32 s61, s61, %[kvs]\n"
"LK0_6_%=:\n"
"v_mfma_f32_16x16x32_bf16 v[40:43], v[152:155], v[96:99], v[40:43]\n"
"ds_read_b64_tr_b16 v[184:185], %[wbe] offset:41728\n"
"ds_read_b64_tr_b16 v[186:187], v192 offset:42752\n"
"v_mfma_f32_16x16x32_bf16 v[72:75], v[152:155], v[100:103], v[72:75]\n"
"ds_read_b64_tr_b16 v[188:189], v192 offset:41728\n"
"v_mfma_f32_16x16x32_bf16 v[44:47], v[156:159], v[96:99], v[44:47]\n"
"ds_read_b64_tr_b16 v[190:191], %[wbe] offset:42752\n"
"v_mfma_f32_16x16x32_bf16 v[76:79], v[156:159], v[100:103], v[76:79]\n"
"s_waitcnt vmcnt(7)\n"
"s_bitcmp1_b32 s91, 7\n"
"s_cbranch_scc1 LJ0_7_%=\n"
"s_waitcnt vmcnt(4)\n"
"LJ0_7_%=:\n"
"s_waitcnt lgkmcnt(0)\n"
"s_barrier\n"
"v_mfma_f32_16x16x32_bf16 v[16:19], v[160:163], v[108:111], v[16:19]\n"
"ds_read_b128 v[96:99], v194 offset:0\n"
"ds_read_b128 v[100:103], v196 offset:0\n"
"s_add_u32 m0, s60, 147456\n"
"v_mfma_f32_16x16x32_bf16 v[48:51], v[160:163], v[112:115], v[48:51]\n"
"buffer_load_dwordx4 %[xoff], %[srdx], s63 offen lds\n"
"v_mfma_f32_16x16x32_bf16 v[80:83], v[160:163], v[116:119], v[80:83]\n"
"ds_read_b128 v[104:107], %[xb] offset:57344\n"
"ds_read_b64_tr_b16 v[128:129], %[wbe] offset:49152\n"
"v_mfma_f32_16x16x32_bf16 v[20:23], v[164:167], v[108:111], v[20:23]\n"
"buffer_load_dwordx4 v193, %[srdx], s63 offen offset:1024 lds\n"
"s_add_u32 s63, s63, 0x80\n"
"v_mfma_f32_16x16x32_bf16 v[52:55], v[164:167], v[112:115], v[52:55]\n"
"ds_read_b64_tr_b16 v[130:131], v192 offset:50176\n"
"ds_read_b64_tr_b16 v[132:133], v192 offset:49152\n"
"v_mfma_f32_16x16x32_bf16 v[84:87], v[164:167], v[116:119], v[84:87]\n"
"ds_read_b64_tr_b16 v[134:135], %[wbe] offset:50176\n"
"s_add_u32 m0, %[hb], 40960\n"
"v_mfma_f32_16x16x32_bf16 v[24:27], v[168:171], v[108:111], v[24:27]\n"
"buffer_load_dwordx4 v198, %[srdw], s62 offen lds\n"
"v_mfma_f32_16x16x32_bf16 v[56:59], v[168:171], v[112:115], v[56:59]\n"
"ds_read_b64_tr_b16 v[136:137], %[wbe] offset:49408\n"
"ds_read_b64_tr_b16 v[138:139], v192 offset:50432\n"
"v_mfma_f32_16x16x32_bf16 v[28:31], v[172:175], v[108:111], v[28:31]\n"
"ds_read_b64_tr_b16 v[140:141], v192 offset:49408\n"
"ds_read_b64_tr_b16 v[142:143], %[wbe] offset:50432\n"
"v_mfma_f32_16x16x32_bf16 v[60:63], v[172:175], v[112:115], v[60:63]\n"
"buffer_load_dwordx4 v195, %[srdw], s62 offen offset:1024 lds\n"
"s_add_u32 s62, s62, 0x40000\n"
"v_mfma_f32_16x16x32_bf16 v[32:35], v[176:179], v[108:111], v[32:35]\n"
"ds_read_b64_tr_b16 v[144:145], %[wbe] offset:49664\n"
"ds_read_b64_tr_b16 v[146:147], v192 offset:50688\n"
"v_mfma_f32_16x16x32_bf16 v[64:67], v[176:179], v[112:115], v[64:67]\n"
"s_cmp_eq_u32 %[wrot], 2\n"
"s_cselect_b32 s68, 0, 0x40000000\n"
"s_mov_b32 s69, 3670016\n"
"v_or_b32_e64 v200, s68, v199\n"
"buffer_load_dword v201, v200, %[srdw], s69 offen\n"
"v_mfma_f32_16x16x32_bf16 v[36:39], v[180:183], v[108:111], v[36:39]\n"
"ds_read_b64_tr_b16 v[148:149], v192 offset:49664\n"
"v_mfma_f32_16x16x32_bf16 v[68:71], v[180:183], v[112:115], v[68:71]\n"
"ds_read_b64_tr_b16 v[150:151], %[wbe] offset:50688\n"
"ds_read_b64_tr_b16 v[152:153], %[wbe] offset:49920\n"
"v_mfma_f32_16x16x32_bf16 v[40:43], v[184:187], v[108:111], v[40:43]\n"
"s_bitcmp0_b32 %[xrot], 7\n"
"s_cbranch_scc1 LK0_7_%=\n"
"s_mov_b32 m0, s52\n"
"s_nop 0\n"
"buffer_load_dwordx4 %[g0], %[srdv], s61 offen nt lds\n"
"buffer_load_dwordx4 %[g1], %[srdv], s61 offen offset:1024 nt lds\n"
"buffer_load_dwordx4 %[g0], %[srdk], s61 offen offset:2048 nt lds\n"
"s_add_u32 s61, s61, %[kvs]\n"
"LK0_7_%=:\n"
"v_mfma_f32_16x16x32_bf16 v[72:75], v[184:187], v[112:115], v[72:75]\n"
"ds_read_b64_tr_b16 v[154:155], v192 offset:50944\n"
"ds_read_b64_tr_b16 v[156:157], v192 offset:49920\n"
"v_mfma_f32_16x16x32_bf16 v[44:47], v[188:191], v[108:111], v[44:47]\n"
"ds_read_b64_tr_b16 v[158:159], %[wbe] offset:50944\n"
"v_mfma_f32_16x16x32_bf16 v[76:79], v[188:191], v[112:115], v[76:79]\n"
"s_waitcnt vmcnt(9)\n"
"s_bitcmp1_b32 s91, 8\n"
"s_cbranch_scc1 LJ0_8_%=\n"
"s_waitcnt vmcnt(6)\n"
"LJ0_8_%=:\n"
"s_waitcnt lgkmcnt(0)\n"
"s_barrier\n"
"v_mfma_f32_16x16x32_bf16 v[16:19], v[128:131], v[96:99], v[16:19]\n"
"ds_read_b128 v[108:111], v194 offset:512\n"
"ds_read_b128 v[112:115], v196 offset:512\n"
"s_add_u32 m0, %[hb], 49152\n"
"v_mfma_f32_16x16x32_bf16 v[48:51], v[128:131], v[100:103], v[48:51]\n"
"buffer_load_dwordx4 v198, %[srdw], s62 offen lds\n"
"v_mfma_f32_16x16x32_bf16 v[80:83], v[128:131], v[104:107], v[80:83]\n"
"ds_read_b128 v[116:119], %[xb] offset:57856\n"
"v_mfma_f32_16x16x32_bf16 v[20:23], v[132:135], v[96:99], v[20:23]\n"
"ds_read_b64_tr_b16 v[160:161], %[wbe] offset:32768\n"
"ds_read_b64_tr_b16 v[162:163], v192 offset:33792\n"
"v_mfma_f32_16x16x32_bf16 v[52:55], v[132:135], v[100:103], v[52:55]\n"
"ds_read_b64_tr_b16 v[164:165], v192 offset:32768\n"
"v_mfma_f32_16x16x32_bf16 v[84:87], v[132:135], v[104:107], v[84:87]\n"
"buffer_load_dwordx4 v195, %[srdw], s62 offen offset:1024 lds\n"
"s_add_u32 s62, s62, 0x40000\n"
"v_mfma_f32_16x16x32_bf16 v[24:27], v[136:139], v[96:99], v[24:27]\n"
"ds_read_b64_tr_b16 v[166:167], %[wbe] offset:33792\n"
"v_mfma_f32_16x16x32_bf16 v[56:59], v[136:139], v[100:103], v[56:59]\n"
"ds_read_b64_tr_b16 v[168:169], %[wbe] offset:33024\n"
"ds_read_b64_tr_b16 v[170:171], v192 offset:34048\n"
"v_mfma_f32_16x16x32_bf16 v[28:31], v[140:143], v[96:99], v[28:31]\n"
"ds_read_b64_tr_b16 v[172:173], v192 offset:33024\n"
"v_mfma_f32_16x16x32_bf16 v[60:63], v[140:143], v[100:103], v[60:63]\n"
"s_cmp_eq_u32 %[wrot], 3\n"
"s_cselect_b32 s68, 0, 0x40000000\n"
"s_mov_b32 s69, 3932160\n"
"v_or_b32_e64 v200, s68, v199\n"
"buffer_load_dword v201, v200, %[srdw], s69 offen\n"
"v_mfma_f32_16x16x32_bf16 v[32:35], v[144:147], v[96:99], v[32:35]\n"
"ds_read_b64_tr_b16 v[174:175], %[wbe] offset:34048\n"
"v_mfma_f32_16x16x32_bf16 v[64:67], v[144:147], v[100:103], v[64:67]\n"
"ds_read_b64_tr_b16 v[176:177], %[wbe] offset:33280\n"
"ds_read_b64_tr_b16 v[178:179], v192 offset:34304\n"
"v_mfma_f32_16x16x32_bf16 v[36:39], v[148:151], v[96:99], v[36:39]\n"
"ds_read_b64_tr_b16 v[180:181], v192 offset:33280\n"
"v_mfma_f32_16x16x32_bf16 v[68:71], v[148:151], v[100:103], v[68:71]\n"
"ds_read_b64_tr_b16 v[182:183], %[wbe] offset:34304\n"
"v_mfma_f32_16x16x32_bf16 v[40:43], v[152:155], v[96:99], v[40:43]\n"
"ds_read_b64_tr_b16 v[184:185], %[wbe] offset:33536\n"
"ds_read_b64_tr_b16 v[186:187], v192 offset:34560\n"
"v_mfma_f32_16x16x32_bf16 v[72:75], v[152:155], v[100:103], v[72:75]\n"
"ds_read_b64_tr_b16 v[188:189], v192 offset:33536\n"
"v_mfma_f32_16x16x32_bf16 v[44:47], v[156:159], v[96:99], v[44:47]\n"
"ds_read_b64_tr_b16 v[190:191], %[wbe] offset:34560\n"
"v_mfma_f32_16x16x32_bf16 v[76:79], v[156:159], v[100:103], v[76:79]\n"
"s_waitcnt vmcnt(7)\n"
"s_bitcmp1_b32 s91, 9\n"
"s_cbranch_scc1 LJ0_9_%=\n"
"s_waitcnt vmcnt(4)\n"
"LJ0_9_%=:\n"
"s_waitcnt lgkmcnt(0)\n"
"s_barrier\n"
"v_mfma_f32_16x16x32_bf16 v[16:19], v[160:163], v[108:111], v[16:19]\n"
"ds_read_b128 v[96:99], v194 offset:16384\n"
"ds_read_b128 v[100:103], v196 offset:16384\n"
"s_add_u32 m0, s60, 131072\n"
"v_mfma_f32_16x16x32_bf16 v[48:51], v[160:163], v[112:115], v[48:51]\n"
"buffer_load_dwordx4 %[xoff], %[srdx], s63 offen lds\n"
"v_mfma_f32_16x16x32_bf16 v[80:83], v[160:163], v[116:119], v[80:83]\n"
"ds_read_b128 v[104:107], %[xb] offset:59392\n"
"ds_read_b64_tr_b16 v[128:129], %[wbe] offset:40960\n"
"v_mfma_f32_16x16x32_bf16 v[20:23], v[164:167], v[108:111], v[20:23]\n"
"ds_read_b64_tr_b16 v[130:131], v192 offset:41984\n"
"v_mfma_f32_16x16x32_bf16 v[52:55], v[164:167], v[112:115], v[52:55]\n"
"buffer_load_dwordx4 v193, %[srdx], s63 offen offset:1024 lds\n"
"s_add_u32 s63, s63, 0x80\n"
"v_mfma_f32_16x16x32_bf16 v[84:87], v[164:167], v[116:119], v[84:87]\n"
"ds_read_b64_tr_b16 v[132:133], v192 offset:40960\n"
"ds_read_b64_tr_b16 v[134:135], %[wbe] offset:41984\n"
"v_mfma_f32_16x16x32_bf16 v[24:27], v[168:171], v[108:111], v[24:27]\n"
"ds_read_b64_tr_b16 v[136:137], %[wbe] offset:41216\n"
"s_add_u32 m0, %[hb], 32768\n"
"v_mfma_f32_16x16x32_bf16 v[56:59], v[168:171], v[112:115], v[56:59]\n"
"buffer_load_dwordx4 v198, %[srdw], s62 offen lds\n"
"v_mfma_f32_16x16x32_bf16 v[28:31], v[172:175], v[108:111], v[28:31]\n"
"ds_read_b64_tr_b16 v[138:139], v192 offset:42240\n"
"ds_read_b64_tr_b16 v[140:141], v192 offset:41216\n"
"v_mfma_f32_16x16x32_bf16 v[60:63], v[172:175], v[112:115], v[60:63]\n"
"ds_read_b64_tr_b16 v[142:143], %[wbe] offset:42240\n"
"ds_read_b64_tr_b16 v[144:145], %[wbe] offset:41472\n"
"v_mfma_f32_16x16x32_bf16 v[32:35], v[176:179], v[108:111], v[32:35]\n"
"buffer_load_dwordx4 v195, %[srdw], s62 offen offset:1024 lds\n"
"s_add_u32 s62, s62, 0x40000\n"
"v_mfma_f32_16x16x32_bf16 v[64:67], v[176:179], v[112:115], v[64:67]\n"
"ds_read_b64_tr_b16 v[146:147], v192 offset:42496\n"
"v_mfma_f32_16x16x32_bf16 v[36:39], v[180:183], v[108:111], v[36:39]\n"
"ds_read_b64_tr_b16 v[148:149], v192 offset:41472\n"
"ds_read_b64_tr_b16 v[150:151], %[wbe] offset:42496\n"
"v_mfma_f32_16x16x32_bf16 v[68:71], v[180:183], v[112:115], v[68:71]\n"
"s_cmp_eq_u32 %[wrot], 0\n"
"s_cselect_b32 s68, 0, 0x40000000\n"
"s_mov_b32 s69, 4194304\n"
"v_or_b32 v200, s68, v199\n"
"buffer_load_dword v201, v200, %[srdw], s69 offen\n"
"v_mfma_f32_16x16x32_bf16 v[40:43], v[184:187], v[108:111], v[40:43]\n"
"ds_read_b64_tr_b16 v[152:153], %[wbe] offset:41728\n"
"v_mfma_f32_16x16x32_bf16 v[72:75], v[184:187], v[112:115], v[72:75]\n"
"ds_read_b64_tr_b16 v[154:155], v192 offset:42752\n"
"ds_read_b64_tr_b16 v[156:157], v192 offset:41728\n"
"v_mfma_f32_16x16x32_bf16 v[44:47], v[188:191], v[108:111], v[44:47]\n"
"ds_read_b64_tr_b16 v[158:159], %[wbe] offset:42752\n"
"v_mfma_f32_16x16x32_bf16 v[76:79], v[188:191], v[112:115], v[76:79]\n"
"s_waitcnt vmcnt(6) lgkmcnt(0)\n"
"s_barrier\n"
"v_mfma_f32_16x16x32_bf16 v[16:19], v[128:131], v[96:99], v[16:19]\n"
"ds_read_b128 v[108:111], v194 offset:16896\n"
"ds_read_b128 v[112:115], v196 offset:16896\n"
"s_add_u32 m0, %[hb], 40960\n"
"v_mfma_f32_16x16x32_bf16 v[48:51], v[128:131], v[100:103], v[48:51]\n"
"buffer_load_dwordx4 v198, %[srdw], s62 offen lds\n"
"v_mfma_f32_16x16x32_bf16 v[80:83], v[128:131], v[104:107], v[80:83]\n"
"ds_read_b128 v[116:119], %[xb] offset:59904\n"
"v_mfma_f32_16x16x32_bf16 v[20:23], v[132:135], v[96:99], v[20:23]\n"
"ds_read_b64_tr_b16 v[160:161], %[wbe] offset:49152\n"
"ds_read_b64_tr_b16 v[162:163], v192 offset:50176\n"
"v_mfma_f32_16x16x32_bf16 v[52:55], v[132:135], v[100:103], v[52:55]\n"
"ds_read_b64_tr_b16 v[164:165], v192 offset:49152\n"
"v_mfma_f32_16x16x32_bf16 v[84:87], v[132:135], v[104:107], v[84:87]\n"
"buffer_load_dwordx4 v195, %[srdw], s62 offen offset:1024 lds\n"
"s_add_u32 s62, s62, 0x40000\n"
"v_mfma_f32_16x16x32_bf16 v[24:27], v[136:139], v[96:99], v[24:27]\n"
"ds_read_b64_tr_b16 v[166:167], %[wbe] offset:50176\n"
"ds_read_b64_tr_b16 v[168:169], %[wbe] offset:49408\n"
"v_mfma_f32_16x16x32_bf16 v[56:59], v[136:139], v[100:103], v[56:59]\n"
"ds_read_b64_tr_b16 v[170:171], v192 offset:50432\n"
"v_mfma_f32_16x16x32_bf16 v[28:31], v[140:143], v[96:99], v[28:31]\n"
"ds_read_b64_tr_b16 v[172:173], v192 offset:49408\n"
"ds_read_b64_tr_b16 v[174:175], %[wbe] offset:50432\n"
"v_mfma_f32_16x16x32_bf16 v[60:63], v[140:143], v[100:103], v[60:63]\n"
"s_cmp_eq_u32 %[wrot], 1\n"
"s_cselect_b32 s68, 0, 0x40000000\n"
"s_mov_b32 s69, 4456448\n"
"v_or_b32 v200, s68, v199\n"
"buffer_load_dword v201, v200, %[srdw], s69 offen\n"
"v_mfma_f32_16x16x32_bf16 v[32:35], v[144:147], v[96:99], v[32:35]\n"
"ds_read_b64_tr_b16 v[176:177], %[wbe] offset:49664\n"
"v_mfma_f32_16x16x32_bf16 v[64:67], v[144:147], v[100:103], v[64:67]\n"
"ds_read_b64_tr_b16 v[178:179], v192 offset:50688\n"
"ds_read_b64_tr_b16 v[180:181], v192 offset:49664\n"
"v_mfma_f32_16x16x32_bf16 v[36:39], v[148:151], v[96:99], v[36:39]\n"
"ds_read_b64_tr_b16 v[182:183], %[wbe] offset:50688\n"
"v_mfma_f32_16x16x32_bf16 v[68:71], v[148:151], v[100:103], v[68:71]\n"
"s_bitcmp0_b32 %[xrot], 10\n"
"s_cbranch_scc1 LK0_10_%=\n"
"s_mov_b32 m0, s53\n"
"s_nop 0\n"
"buffer_load_dwordx4 %[g0], %[srdv], s61 offen nt lds\n"
"buffer_load_dwordx4 %[g1], %[srdv], s61 offen offset:1024 nt lds\n"
"buffer_load_dwordx4 %[g0], %[srdk], s61 offen offset:2048 nt lds\n"
"s_add_u32 s61, s61, %[kvs]\n"
"LK0_10_%=:\n"
"v_mfma_f32_16x16x32_bf16 v[40:43], v[152:155], v[96:99], v[40:43]\n"
"ds_read_b64_tr_b16 v[184:185], %[wbe] offset:49920\n"
"ds_read_b64_tr_b16 v[186:187], v192 offset:50944\n"
"v_mfma_f32_16x16x32_bf16 v[72:75], v[152:155], v[100:103], v[72:75]\n"
"ds_read_b64_tr_b16 v[188:189], v192 offset:49920\n"
"v_mfma_f32_16x16x32_bf16 v[44:47], v[156:159], v[96:99], v[44:47]\n"
"ds_read_b64_tr_b16 v[190:191], %[wbe] offset:50944\n"
"v_mfma_f32_16x16x32_bf16 v[76:79], v[156:159], v[100:103], v[76:79]\n"
"s_waitcnt vmcnt(7)\n"
"s_bitcmp1_b32 s91, 11\n"
"s_cbranch_scc1 LJ0_11_%=\n"
"s_waitcnt vmcnt(4)\n"
"LJ0_11_%=:\n"
"s_waitcnt lgkmcnt(0)\n"
"s_barrier\n"
"v_mfma_f32_16x16x32_bf16 v[16:19], v[160:163], v[108:111], v[16:19]\n"
"ds_read_b128 v[96:99], v194 offset:0\n"
"ds_read_b128 v[100:103], v196 offset:0\n"
"s_add_u32 m0, s60, 147456\n"
"v_mfma_f32_16x16x32_bf16 v[48:51], v[160:163], v[112:115], v[48:51]\n"
"buffer_load_dwordx4 %[xoff], %[srdx], s63 offen lds\n"
"v_mfma_f32_16x16x32_bf16 v[80:83], v[160:163], v[116:119], v[80:83]\n"
"ds_read_b128 v[104:107], %[xb] offset:57344\n"
"ds_read_b64_tr_b16 v[128:129], %[wbe] offset:32768\n"
"v_mfma_f32_16x16x32_bf16 v[20:23], v[164:167], v[108:111], v[20:23]\n"
"ds_read_b64_tr_b16 v[130:131], v192 offset:33792\n"
"v_mfma_f32_16x16x32_bf16 v[52:55], v[164:167], v[112:115], v[52:55]\n"
"buffer_load_dwordx4 v193, %[srdx], s63 offen offset:1024 lds\n"
"s_add_u32 s63, s63, 0x80\n"
"v_mfma_f32_16x16x32_bf16 v[84:87], v[164:167], v[116:119], v[84:87]\n"
"ds_read_b64_tr_b16 v[132:133], v192 offset:32768\n"
"ds_read_b64_tr_b16 v[134:135], %[wbe] offset:33792\n"
"v_mfma_f32_16x16x32_bf16 v[24:27], v[168:171], v[108:111], v[24:27]\n"
"ds_read_b64_tr_b16 v[136:137], %[wbe] offset:33024\n"
"s_add_u32 m0, %[hb], 49152\n"
"v_mfma_f32_16x16x32_bf16 v[56:59], v[168:171], v[112:115], v[56:59]\n"
"buffer_load_dwordx4 v198, %[srdw], s62 offen lds\n"
"v_mfma_f32_16x16x32_bf16 v[28:31], v[172:175], v[108:111], v[28:31]\n"
"ds_read_b64_tr_b16 v[138:139], v192 offset:34048\n"
"ds_read_b64_tr_b16 v[140:141], v192 offset:33024\n"
"v_mfma_f32_16x16x32_bf16 v[60:63], v[172:175], v[112:115], v[60:63]\n"
"ds_read_b64_tr_b16 v[142:143], %[wbe] offset:34048\n"
"ds_read_b64_tr_b16 v[144:145], %[wbe] offset:33280\n"
"v_mfma_f32_16x16x32_bf16 v[32:35], v[176:179], v[108:111], v[32:35]\n"
"buffer_load_dwordx4 v195, %[srdw], s62 offen offset:1024 lds\n"
"s_add_u32 s62, s62, 0x40000\n"
"v_mfma_f32_16x16x32_bf16 v[64:67], v[176:179], v[112:115], v[64:67]\n"
"ds_read_b64_tr_b16 v[146:147], v192 offset:34304\n"
"v_mfma_f32_16x16x32_bf16 v[36:39], v[180:183], v[108:111], v[36:39]\n"
"ds_read_b64_tr_b16 v[148:149], v192 offset:33280\n"
"ds_read_b64_tr_b16 v[150:151], %[wbe] offset:34304\n"
"v_mfma_f32_16x16x32_bf16 v[68:71], v[180:183], v[112:115], v[68:71]\n"
"s_bitcmp0_b32 %[xrot], 11\n"
"s_cbranch_scc1 LK0_11_%=\n"
"s_mov_b32 m0, s53\n"
"s_nop 0\n"
"buffer_load_dwordx4 %[g0], %[srdv], s61 offen nt lds\n"
"buffer_load_dwordx4 %[g1], %[srdv], s61 offen offset:1024 nt lds\n"
"buffer_load_dwordx4 %[g0], %[srdk], s61 offen offset:2048 nt lds\n"
"s_add_u32 s61, s61, %[kvs]\n"
"LK0_11_%=:\n"
"v_mfma_f32_16x16x32_bf16 v[40:43], v[184:187], v[108:111], v[40:43]\n"
"ds_read_b64_tr_b16 v[152:153], %[wbe] offset:33536\n"
"v_mfma_f32_16x16x32_bf16 v[72:75], v[184:187], v[112:115], v[72:75]\n"
"ds_read_b64_tr_b16 v[154:155], v192 offset:34560\n"
"ds_read_b64_tr_b16 v[156:157], v192 offset:33536\n"
"v_mfma_f32_16x16x32_bf16 v[44:47], v[188:191], v[108:111], v[44:47]\n"
"ds_read_b64_tr_b16 v[158:159], %[wbe] offset:34560\n"
"v_mfma_f32_16x16x32_bf16 v[76:79], v[188:191], v[112:115], v[76:79]\n"
"s_waitcnt vmcnt(8)\n"
"s_bitcmp1_b32 s91, 12\n"
"s_cbranch_scc1 LJ0_12_%=\n"
"s_waitcnt vmcnt(5)\n"
"LJ0_12_%=:\n"
"s_waitcnt lgkmcnt(0)\n"
"s_barrier\n"
"v_mfma_f32_16x16x32_bf16 v[16:19], v[128:131], v[96:99], v[16:19]\n"
"ds_read_b128 v[108:111], v194 offset:512\n"
"ds_read_b128 v[112:115], v196 offset:512\n"
"s_add_u32 m0, %[hb], 32768\n"
"v_mfma_f32_16x16x32_bf16 v[48:51], v[128:131], v[100:103], v[48:51]\n"
"buffer_load_dwordx4 v198, %[srdw], s62 offen lds\n"
"v_mfma_f32_16x16x32_bf16 v[80:83], v[128:131], v[104:107], v[80:83]\n"
"ds_read_b128 v[116:119], %[xb] offset:57856\n"
"v_mfma_f32_16x16x32_bf16 v[20:23], v[132:135], v[96:99], v[20:23]\n"
"ds_read_b64_tr_b16 v[160:161], %[wbe] offset:40960\n"
"ds_read_b64_tr_b16 v[162:163], v192 offset:41984\n"
"v_mfma_f32_16x16x32_bf16 v[52:55], v[132:135], v[100:103], v[52:55]\n"
"ds_read_b64_tr_b16 v[164:165], v192 offset:40960\n"
"v_mfma_f32_16x16x32_bf16 v[84:87], v[132:135], v[104:107], v[84:87]\n"
"buffer_load_dwordx4 v195, %[srdw], s62 offen offset:1024 lds\n"
"s_add_u32 s62, s62, 0x40000\n"
"v_mfma_f32_16x16x32_bf16 v[24:27], v[136:139], v[96:99], v[24:27]\n"
"ds_read_b64_tr_b16 v[166:167], %[wbe] offset:41984\n"
"v_mfma_f32_16x16x32_bf16 v[56:59], v[136:139], v[100:103], v[56:59]\n"
"ds_read_b64_tr_b16 v[168:169], %[wbe] offset:41216\n"
"ds_read_b64_tr_b16 v[170:171], v192 offset:42240\n"
"v_mfma_f32_16x16x32_bf16 v[28:31], v[140:143], v[96:99], v[28:31]\n"
"ds_read_b64_tr_b16 v[172:173], v192 offset:41216\n"
"v_mfma_f32_16x16x32_bf16 v[60:63], v[140:143], v[100:103], v[60:63]\n"
"s_bitcmp0_b32 %[xrot], 12\n"
"s_cbranch_scc1 LK0_12_%=\n"
"s_mov_b32 m0, s53\n"
"s_nop 0\n"
"buffer_load_dwordx4 %[g0], %[srdv], s61 offen nt lds\n"
"buffer_load_dwordx4 %[g1], %[srdv], s61 offen offset:1024 nt lds\n"
"buffer_load_dwordx4 %[g0], %[srdk], s61 offen offset:2048 nt lds\n"
"s_add_u32 s61, s61, %[kvs]\n"
"LK0_12_%=:\n"
"v_mfma_f32_16x16x32_bf16 v[32:35], v[144:147], v[96:99], v[32:35]\n"
"ds_read_b64_tr_b16 v[174:175], %[wbe] offset:42240\n"
"v_mfma_f32_16x16x32_bf16 v[64:67], v[144:147], v[100:103], v[64:67]\n"
"ds_read_b64_tr_b16 v[176:177], %[wbe] offset:41472\n"
"ds_read_b64_tr_b16 v[178:179], v192 offset:42496\n"
"v_mfma_f32_16x16x32_bf16 v[36:39], v[148:151], v[96:99], v[36:39]\n"
"ds_read_b64_tr_b16 v[180:181], v192 offset:41472\n"
"v_mfma_f32_16x16x32_bf16 v[68:71], v[148:151], v[100:103], v[68:71]\n"
"ds_read_b64_tr_b16 v[182:183], %[wbe] offset:42496\n"
"v_mfma_f32_16x16x32_bf16 v[40:43], v[152:155], v[96:99], v[40:43]\n"
"ds_read_b64_tr_b16 v[184:185], %[wbe] offset:41728\n"
"ds_read_b64_tr_b16 v[186:187], v192 offset:42752\n"
"v_mfma_f32_16x16x32_bf16 v[72:75], v[152:155], v[100:103], v[72:75]\n"
"ds_read_b64_tr_b16 v[188:189], v192 offset:41728\n"
"v_mfma_f32_16x16x32_bf16 v[44:47], v[156:159], v[96:99], v[44:47]\n"
"ds_read_b64_tr_b16 v[190:191], %[wbe] offset:42752\n"
"v_mfma_f32_16x16x32_bf16 v[76:79], v[156:159], v[100:103], v[76:79]\n"
"s_waitcnt vmcnt(5)\n"
"s_bitcmp1_b32 s91, 13\n"
"s_cbranch_scc1 LJ0_13_%=\n"
"s_waitcnt vmcnt(2)\n"
"LJ0_13_%=:\n"
"s_waitcnt lgkmcnt(0)\n"
"s_barrier\n"
"v_mfma_f32_16x16x32_bf16 v[16:19], v[160:163], v[108:111], v[16:19]\n"
"ds_read_b128 v[96:99], v194 offset:16384\n"
"ds_read_b128 v[100:103], v196 offset:16384\n"
"s_add_u32 m0, s60, 131072\n"
"v_mfma_f32_16x16x32_bf16 v[48:51], v[160:163], v[112:115], v[48:51]\n"
"buffer_load_dwordx4 %[xoff], %[srdx], s63 offen lds\n"
"v_mfma_f32_16x16x32_bf16 v[80:83], v[160:163], v[116:119], v[80:83]\n"
"ds_read_b128 v[104:107], %[xb] offset:59392\n"
"ds_read_b64_tr_b16 v[128:129], %[wbe] offset:49152\n"
"v_mfma_f32_16x16x32_bf16 v[20:23], v[164:167], v[108:111], v[20:23]\n"
"buffer_load_dwordx4 v193, %[srdx], s63 offen offset:1024 lds\n"
"v_mfma_f32_16x16x32_bf16 v[52:55], v[164:167], v[112:115], v[52:55]\n"
"ds_read_b64_tr_b16 v[130:131], v192 offset:50176\n"
"ds_read_b64_tr_b16 v[132:133], v192 offset:49152\n"
"v_mfma_f32_16x16x32_bf16 v[84:87], v[164:167], v[116:119], v[84:87]\n"
"ds_read_b64_tr_b16 v[134:135], %[wbe] offset:50176\n"
"s_add_u32 m0, s64, 57344\n"
"v_mfma_f32_16x16x32_bf16 v[24:27], v[168:171], v[108:111], v[24:27]\n"
"buffer_load_dwordx4 %[xo9], %[srdx], s63 offen lds\n"
"s_add_u32 s63, s63, 0x80\n"
"v_mfma_f32_16x16x32_bf16 v[56:59], v[168:171], v[112:115], v[56:59]\n"
"ds_read_b64_tr_b16 v[136:137], %[wbe] offset:49408\n"
"ds_read_b64_tr_b16 v[138:139], v192 offset:50432\n"
"v_mfma_f32_16x16x32_bf16 v[28:31], v[172:175], v[108:111], v[28:31]\n"
"ds_read_b64_tr_b16 v[140:141], v192 offset:49408\n"
"ds_read_b64_tr_b16 v[142:143], %[wbe] offset:50432\n"
"s_add_u32 m0, %[hb], 40960\n"
"v_mfma_f32_16x16x32_bf16 v[60:63], v[172:175], v[112:115], v[60:63]\n"
"buffer_load_dwordx4 v198, %[srdw], s62 offen lds\n"
"v_mfma_f32_16x16x32_bf16 v[32:35], v[176:179], v[108:111], v[32:35]\n"
"ds_read_b64_tr_b16 v[144:145], %[wbe] offset:49664\n"
"ds_read_b64_tr_b16 v[146:147], v192 offset:50688\n"
"v_mfma_f32_16x16x32_bf16 v[64:67], v[176:179], v[112:115], v[64:67]\n"
"buffer_load_dwordx4 v195, %[srdw], s62 offen offset:1024 lds\n"
"s_add_u32 s62, s62, 0x40000\n"
"v_mfma_f32_16x16x32_bf16 v[36:39], v[180:183], v[108:111], v[36:39]\n"
"ds_read_b64_tr_b16 v[148:149], v192 offset:49664\n"
"v_mfma_f32_16x16x32_bf16 v[68:71], v[180:183], v[112:115], v[68:71]\n"
"ds_read_b64_tr_b16 v[150:151], %[wbe] offset:50688\n"
"ds_read_b64_tr_b16 v[152:153], %[wbe] offset:49920\n"
"v_mfma_f32_16x16x32_bf16 v[40:43], v[184:187], v[108:111], v[40:43]\n"
"s_bitcmp0_b32 %[xrot], 13\n"
"s_cbranch_scc1 LK0_13_%=\n"
"s_mov_b32 m0, s53\n"
"s_nop 0\n"
"buffer_load_dwordx4 %[g0], %[srdv], s61 offen nt lds\n"
"buffer_load_dwordx4 %[g1], %[srdv], s61 offen offset:1024 nt lds\n"
"buffer_load_dwordx4 %[g0], %[srdk], s61 offen offset:2048 nt lds\n"
"s_add_u32 s61, s61, %[kvs]\n"
"LK0_13_%=:\n"
"v_mfma_f32_16x16x32_bf16 v[72:75], v[184:187], v[112:115], v[72:75]\n"
"ds_read_b64_tr_b16 v[154:155], v192 offset:50944\n"
"ds_read_b64_tr_b16 v[156:157], v192 offset:49920\n"
"v_mfma_f32_16x16x32_bf16 v[44:47], v[188:191], v[108:111], v[44:47]\n"
"ds_read_b64_tr_b16 v[158:159], %[wbe] offset:50944\n"
"v_mfma_f32_16x16x32_bf16 v[76:79], v[188:191], v[112:115], v[76:79]\n"
"s_waitcnt vmcnt(8)\n"
"s_bitcmp1_b32 s91, 14\n"
"s_cbranch_scc1 LJ0_14_%=\n"
"s_waitcnt vmcnt(5)\n"
"LJ0_14_%=:\n"
"s_waitcnt lgkmcnt(0)\n"
"s_barrier\n"
"v_mfma_f32_16x16x32_bf16 v[16:19], v[128:131], v[96:99], v[16:19]\n"
"ds_read_b128 v[108:111], v194 offset:16896\n"
"ds_read_b128 v[112:115], v196 offset:16896\n"
"s_add_u32 m0, %[hb], 49152\n"
"v_mfma_f32_16x16x32_bf16 v[48:51], v[128:131], v[100:103], v[48:51]\n"
"buffer_load_dwordx4 v198, %[srdw], s62 offen lds\n"
"v_mfma_f32_16x16x32_bf16 v[80:83], v[128:131], v[104:107], v[80:83]\n"
"ds_read_b128 v[116:119], %[xb] offset:59904\n"
"v_mfma_f32_16x16x32_bf16 v[20:23], v[132:135], v[96:99], v[20:23]\n"
"ds_read_b64_tr_b16 v[160:161], %[wbe] offset:32768\n"
"v_mfma_f32_16x16x32_bf16 v[52:55], v[132:135], v[100:103], v[52:55]\n"
"ds_read_b64_tr_b16 v[162:163], v192 offset:33792\n"
"ds_read_b64_tr_b16 v[164:165], v192 offset:32768\n"
"v_mfma_f32_16x16x32_bf16 v[84:87], v[132:135], v[104:107], v[84:87]\n"
"buffer_load_dwordx4 v195, %[srdw], s62 offen offset:1024 lds\n"
"s_add_u32 s62, s62, 0x40000\n"
"v_mfma_f32_16x16x32_bf16 v[24:27], v[136:139], v[96:99], v[24:27]\n"
"ds_read_b64_tr_b16 v[166:167], %[wbe] offset:33792\n"
"v_mfma_f32_16x16x32_bf16 v[56:59], v[136:139], v[100:103], v[56:59]\n"
"ds_read_b64_tr_b16 v[168:169], %[wbe] offset:33024\n"
"v_mfma_f32_16x16x32_bf16 v[28:31], v[140:143], v[96:99], v[28:31]\n"
"ds_read_b64_tr_b16 v[170:171], v192 offset:34048\n"
"v_mfma_f32_16x16x32_bf16 v[60:63], v[140:143], v[100:103], v[60:63]\n"
"ds_read_b64_tr_b16 v[172:173], v192 offset:33024\n"
"ds_read_b64_tr_b16 v[174:175], %[wbe] offset:34048\n"
"v_mfma_f32_16x16x32_bf16 v[32:35], v[144:147], v[96:99], v[32:35]\n"
"ds_read_b64_tr_b16 v[176:177], %[wbe] offset:33280\n"
"v_mfma_f32_16x16x32_bf16 v[64:67], v[144:147], v[100:103], v[64:67]\n"
"ds_read_b64_tr_b16 v[178:179], v192 offset:34304\n"
"v_mfma_f32_16x16x32_bf16 v[36:39], v[148:151], v[96:99], v[36:39]\n"
"ds_read_b64_tr_b16 v[180:181], v192 offset:33280\n"
"v_mfma_f32_16x16x32_bf16 v[68:71], v[148:151], v[100:103], v[68:71]\n"
"ds_read_b64_tr_b16 v[182:183], %[wbe] offset:34304\n"
"ds_read_b64_tr_b16 v[184:185], %[wbe] offset:33536\n"
"v_mfma_f32_16x16x32_bf16 v[40:43], v[152:155], v[96:99], v[40:43]\n"
"ds_read_b64_tr_b16 v[186:187], v192 offset:34560\n"
"v_mfma_f32_16x16x32_bf16 v[72:75], v[152:155], v[100:103], v[72:75]\n"
"ds_read_b64_tr_b16 v[188:189], v192 offset:33536\n"
"v_mfma_f32_16x16x32_bf16 v[44:47], v[156:159], v[96:99], v[44:47]\n"
"ds_read_b64_tr_b16 v[190:191], %[wbe] offset:34560\n"
"v_mfma_f32_16x16x32_bf16 v[76:79], v[156:159], v[100:103], v[76:79]\n"
"s_waitcnt vmcnt(5)\n"
"s_bitcmp1_b32 s91, 15\n"
"s_cbranch_scc1 LJ0_15_%=\n"
"s_waitcnt vmcnt(2)\n"
"LJ0_15_%=:\n"
"s_waitcnt lgkmcnt(0)\n"
"s_barrier\n"
"v_mfma_f32_16x16x32_bf16 v[16:19], v[160:163], v[108:111], v[16:19]\n"
"ds_read_b128 v[96:99], v194 offset:0\n"
"ds_read_b128 v[100:103], v196 offset:0\n"
"v_mfma_f32_16x16x32_bf16 v[48:51], v[160:163], v[112:115], v[48:51]\n"
"ds_read_b128 v[104:107], %[xb] offset:57344\n"
"v_mfma_f32_16x16x32_bf16 v[80:83], v[160:163], v[116:119], v[80:83]\n"
"ds_read_b64_tr_b16 v[128:129], %[wbe] offset:40960\n"
"v_mfma_f32_16x16x32_bf16 v[20:23], v[164:167], v[108:111], v[20:23]\n"
"ds_read_b64_tr_b16 v[130:131], v192 offset:41984\n"
"v_mfma_f32_16x16x32_bf16 v[52:55], v[164:167], v[112:115], v[52:55]\n"
"ds_read_b64_tr_b16 v[132:133], v192 offset:40960\n"
"v_mfma_f32_16x16x32_bf16 v[84:87], v[164:167], v[116:119], v[84:87]\n"
"ds_read_b64_tr_b16 v[134:135], %[wbe] offset:41984\n"
"v_mfma_f32_16x16x32_bf16 v[24:27], v[168:171], v[108:111], v[24:27]\n"
"ds_read_b64_tr_b16 v[136:137], %[wbe] offset:41216\n"
"v_mfma_f32_16x16x32_bf16 v[56:59], v[168:171], v[112:115], v[56:59]\n"
"ds_read_b64_tr_b16 v[138:139], v192 offset:42240\n"
"v_mfma_f32_16x16x32_bf16 v[28:31], v[172:175], v[108:111], v[28:31]\n"
"ds_read_b64_tr_b16 v[140:141], v192 offset:41216\n"
"ds_read_b64_tr_b16 v[142:143], %[wbe] offset:42240\n"
"v_mfma_f32_16x16x32_bf16 v[60:63], v[172:175], v[112:115], v[60:63]\n"
"ds_read_b64_tr_b16 v[144:145], %[wbe] offset:41472\n"
"v_mfma_f32_16x16x32_bf16 v[32:35], v[176:179], v[108:111], v[32:35]\n"
"ds_read_b64_tr_b16 v[146:147], v192 offset:42496\n"
"v_mfma_f32_16x16x32_bf16 v[64:67], v[176:179], v[112:115], v[64:67]\n"
"ds_read_b64_tr_b16 v[148:149], v192 offset:41472\n"
"v_mfma_f32_16x16x32_bf16 v[36:39], v[180:183], v[108:111], v[36:39]\n"
"ds_read_b64_tr_b16 v[150:151], %[wbe] offset:42496\n"
"v_mfma_f32_16x16x32_bf16 v[68:71], v[180:183], v[112:115], v[68:71]\n"
"ds_read_b64_tr_b16 v[152:153], %[wbe] offset:41728\n"
"v_mfma_f32_16x16x32_bf16 v[40:43], v[184:187], v[108:111], v[40:43]\n"
"ds_read_b64_tr_b16 v[154:155], v192 offset:42752\n"
"v_mfma_f32_16x16x32_bf16 v[72:75], v[184:187], v[112:115], v[72:75]\n"
"ds_read_b64_tr_b16 v[156:157], v192 offset:41728\n"
"v_mfma_f32_16x16x32_bf16 v[44:47], v[188:191], v[108:111], v[44:47]\n"
"ds_read_b64_tr_b16 v[158:159], %[wbe] offset:42752\n"
"v_mfma_f32_16x16x32_bf16 v[76:79], v[188:191], v[112:115], v[76:79]\n"
"s_waitcnt vmcnt(0) lgkmcnt(0)\n"
"s_barrier\n"
"v_mfma_f32_16x16x32_bf16 v[16:19], v[128:131], v[96:99], v[16:19]\n"
"ds_read_b128 v[108:111], v194 offset:512\n"
"ds_read_b128 v[112:115], v196 offset:512\n"
"s_mov_b32 m0, s54\n"
"v_mfma_f32_16x16x32_bf16 v[48:51], v[128:131], v[100:103], v[48:51]\n"
"buffer_load_dwordx4 %[g0], %[srdv], s61 offen nt lds\n"
"v_mfma_f32_16x16x32_bf16 v[80:83], v[128:131], v[104:107], v[80:83]\n"
"ds_read_b128 v[116:119], %[xb] offset:57856\n"
"v_mfma_f32_16x16x32_bf16 v[20:23], v[132:135], v[96:99], v[20:23]\n"
"ds_read_b64_tr_b16 v[160:161], %[wbe] offset:49152\n"
"ds_read_b64_tr_b16 v[162:163], v192 offset:50176\n"
"v_mfma_f32_16x16x32_bf16 v[52:55], v[132:135], v[100:103], v[52:55]\n"
"ds_read_b64_tr_b16 v[164:165], v192 offset:49152\n"
"v_mfma_f32_16x16x32_bf16 v[84:87], v[132:135], v[104:107], v[84:87]\n"
"buffer_load_dwordx4 %[g1], %[srdv], s61 offen offset:1024 nt lds\n"
"v_mfma_f32_16x16x32_bf16 v[24:27], v[136:139], v[96:99], v[24:27]\n"
"ds_read_b64_tr_b16 v[166:167], %[wbe] offset:50176\n"
"v_mfma_f32_16x16x32_bf16 v[56:59], v[136:139], v[100:103], v[56:59]\n"
"ds_read_b64_tr_b16 v[168:169], %[wbe] offset:49408\n"
"ds_read_b64_tr_b16 v[170:171], v192 offset:50432\n"
"v_mfma_f32_16x16x32_bf16 v[28:31], v[140:143], v[96:99], v[28:31]\n"
"ds_read_b64_tr_b16 v[172:173], v192 offset:49408\n"
"v_mfma_f32_16x16x32_bf16 v[60:63], v[140:143], v[100:103], v[60:63]\n"
"buffer_load_dwordx4 %[g0], %[srdk], s61 offen offset:2048 nt lds\n"
"s_add_u32 s61, s61, %[kvs]\n"
"v_mfma_f32_16x16x32_bf16 v[32:35], v[144:147], v[96:99], v[32:35]\n"
"ds_read_b64_tr_b16 v[174:175], %[wbe] offset:50432\n"
"v_mfma_f32_16x16x32_bf16 v[64:67], v[144:147], v[100:103], v[64:67]\n"
"ds_read_b64_tr_b16 v[176:177], %[wbe] offset:49664\n"
"ds_read_b64_tr_b16 v[178:179], v192 offset:50688\n"
"v_mfma_f32_16x16x32_bf16 v[36:39], v[148:151], v[96:99], v[36:39]\n"
"ds_read_b64_tr_b16 v[180:181], v192 offset:49664\n"
"v_mfma_f32_16x16x32_bf16 v[68:71], v[148:151], v[100:103], v[68:71]\n"
"ds_read_b64_tr_b16 v[182:183], %[wbe] offset:50688\n"
"v_mfma_f32_16x16x32_bf16 v[40:43], v[152:155], v[96:99], v[40:43]\n"
"ds_read_b64_tr_b16 v[184:185], %[wbe] offset:49920\n"
"ds_read_b64_tr_b16 v[186:187], v192 offset:50944\n"
"v_mfma_f32_16x16x32_bf16 v[72:75], v[152:155], v[100:103], v[72:75]\n"
"ds_read_b64_tr_b16 v[188:189], v192 offset:49920\n"
"v_mfma_f32_16x16x32_bf16 v[44:47], v[156:159], v[96:99], v[44:47]\n"
"ds_read_b64_tr_b16 v[190:191], %[wbe] offset:50944\n"
"v_mfma_f32_16x16x32_bf16 v[76:79], v[156:159], v[100:103], v[76:79]\n"
"s_waitcnt lgkmcnt(0)\n"
"s_barrier\n"
"v_mfma_f32_16x16x32_bf16 v[16:19], v[160:163], v[108:111], v[16:19]\n"
"ds_read_b128 v[128:131], %[kb] offset:2048\n"
"s_mov_b32 m0, s55\n"
"v_mfma_f32_16x16x32_bf16 v[48:51], v[160:163], v[112:115], v[48:51]\n"
"buffer_load_dwordx4 %[g0], %[srdv], s61 offen nt lds\n"
"v_mfma_f32_16x16x32_bf16 v[80:83], v[160:163], v[116:119], v[80:83]\n"
"ds_read_b128 v[132:135], %[kb] offset:2304\n"
"v_mfma_f32_16x16x32_bf16 v[20:23], v[164:167], v[108:111], v[20:23]\n"
"v_mfma_f32_16x16x32_bf16 v[52:55], v[164:167], v[112:115], v[52:55]\n"
"ds_read_b128 v[136:139], %[kb] offset:2560\n"
"v_mfma_f32_16x16x32_bf16 v[84:87], v[164:167], v[116:119], v[84:87]\n"
"buffer_load_dwordx4 %[g1], %[srdv], s61 offen offset:1024 nt lds\n"
"v_mfma_f32_16x16x32_bf16 v[24:27], v[168:171], v[108:111], v[24:27]\n"
"v_mfma_f32_16x16x32_bf16 v[56:59], v[168:171], v[112:115], v[56:59]\n"
"ds_read_b128 v[140:143], %[kb] offset:2816\n"
"v_mfma_f32_16x16x32_bf16 v[28:31], v[172:175], v[108:111], v[28:31]\n"
"v_mfma_f32_16x16x32_bf16 v[60:63], v[172:175], v[112:115], v[60:63]\n"
"buffer_load_dwordx4 %[g0], %[srdk], s61 offen offset:2048 nt lds\n"
"s_add_u32 s61, s61, %[kvs]\n"
"v_mfma_f32_16x16x32_bf16 v[32:35], v[176:179], v[108:111], v[32:35]\n"
"ds_read_b128 v[144:147], %[kb] offset:10240\n"
"v_mfma_f32_16x16x32_bf16 v[64:67], v[176:179], v[112:115], v[64:67]\n"
"ds_read_b128 v[148:151], %[kb] offset:10496\n"
"v_mfma_f32_16x16x32_bf16 v[36:39], v[180:183], v[108:111], v[36:39]\n"
"v_mfma_f32_16x16x32_bf16 v[68:71], v[180:183], v[112:115], v[68:71]\n"
"ds_read_b128 v[152:155], %[kb] offset:10752\n"
"v_mfma_f32_16x16x32_bf16 v[40:43], v[184:187], v[108:111], v[40:43]\n"
"v_mfma_f32_16x16x32_bf16 v[72:75], v[184:187], v[112:115], v[72:75]\n"
"ds_read_b128 v[156:159], %[kb] offset:11008\n"
"v_mfma_f32_16x16x32_bf16 v[44:47], v[188:191], v[108:111], v[44:47]\n"
"v_mfma_f32_16x16x32_bf16 v[76:79], v[188:191], v[112:115], v[76:79]\n"
"ds_read_b64_tr_b16 v[160:161], %[vbe] offset:0\n"
"ds_read_b64_tr_b16 v[162:163], %[vbe] offset:8192\n"
"ds_read_b64_tr_b16 v[164:165], v243 offset:0\n"
"ds_read_b64_tr_b16 v[166:167], v243 offset:8192\n"
"ds_read_b64_tr_b16 v[168:169], %[vbe] offset:256\n"
"ds_read_b64_tr_b16 v[170:171], %[vbe] offset:8448\n"
"ds_read_b64_tr_b16 v[172:173], v243 offset:256\n"
"ds_read_b64_tr_b16 v[174:175], v243 offset:8448\n"
"ds_read_b64_tr_b16 v[176:177], %[vbe] offset:512\n"
"ds_read_b64_tr_b16 v[178:179], %[vbe] offset:8704\n"
"ds_read_b64_tr_b16 v[180:181], v243 offset:512\n"
"ds_read_b64_tr_b16 v[182:183], v243 offset:8704\n"
"ds_read_b64_tr_b16 v[184:185], %[vbe] offset:768\n"
"ds_read_b64_tr_b16 v[186:187], %[vbe] offset:8960\n"
"ds_read_b64_tr_b16 v[188:189], v243 offset:768\n"
"ds_read_b64_tr_b16 v[190:191], v243 offset:8960\n"
"v_pk_mul_f32 v[80:81], v[80:81], s[66:67]\n"
"v_pk_mul_f32 v[82:83], v[82:83], s[66:67]\n"
"v_pk_mul_f32 v[84:85], v[84:85], s[66:67]\n"
"v_pk_mul_f32 v[86:87], v[86:87], s[66:67]\n"
"v_cvt_pk_bf16_f32 v232, v80, v81\n"
"v_cvt_pk_bf16_f32 v233, v82, v83\n"
"v_cvt_pk_bf16_f32 v234, v84, v85\n"
"v_cvt_pk_bf16_f32 v235, v86, v87\n"
"s_nop 0\n"
"v_permlane32_swap_b32 v232, v234\n"
"v_permlane32_swap_b32 v233, v235\n"
"s_nop 0\n"
"v_permlane16_swap_b32 v232, v234\n"
"v_permlane16_swap_b32 v233, v235\n"
"ds_write_b128 v247, v[232:235] offset:0\n"
"v_pk_mul_f32 v[16:17], v[16:17], s[66:67]\n"
"v_pk_mul_f32 v[18:19], v[18:19], s[66:67]\n"
"v_pk_mul_f32 v[20:21], v[20:21], s[66:67]\n"
"v_pk_mul_f32 v[22:23], v[22:23], s[66:67]\n"
"v_cvt_pk_bf16_f32 v96, v16, v17\n"
"v_cvt_pk_bf16_f32 v97, v18, v19\n"
"v_cvt_pk_bf16_f32 v98, v20, v21\n"
"v_cvt_pk_bf16_f32 v99, v22, v23\n"
"v_pk_mul_f32 v[24:25], v[24:25], s[66:67]\n"
"v_pk_mul_f32 v[26:27], v[26:27], s[66:67]\n"
"v_pk_mul_f32 v[28:29], v[28:29], s[66:67]\n"
"v_pk_mul_f32 v[30:31], v[30:31], s[66:67]\n"
"v_cvt_pk_bf16_f32 v100, v24, v25\n"
"v_cvt_pk_bf16_f32 v101, v26, v27\n"
"v_cvt_pk_bf16_f32 v102, v28, v29\n"
"v_cvt_pk_bf16_f32 v103, v30, v31\n"
"v_pk_mul_f32 v[32:33], v[32:33], s[66:67]\n"
"v_pk_mul_f32 v[34:35], v[34:35], s[66:67]\n"
"v_pk_mul_f32 v[36:37], v[36:37], s[66:67]\n"
"v_pk_mul_f32 v[38:39], v[38:39], s[66:67]\n"
"v_cvt_pk_bf16_f32 v104, v32, v33\n"
"v_cvt_pk_bf16_f32 v105, v34, v35\n"
"v_cvt_pk_bf16_f32 v106, v36, v37\n"
"v_cvt_pk_bf16_f32 v107, v38, v39\n"
"v_pk_mul_f32 v[40:41], v[40:41], s[66:67]\n"
"v_pk_mul_f32 v[42:43], v[42:43], s[66:67]\n"
"v_pk_mul_f32 v[44:45], v[44:45], s[66:67]\n"
"v_pk_mul_f32 v[46:47], v[46:47], s[66:67]\n"
"v_cvt_pk_bf16_f32 v108, v40, v41\n"
"v_cvt_pk_bf16_f32 v109, v42, v43\n"
"v_cvt_pk_bf16_f32 v110, v44, v45\n"
"v_cvt_pk_bf16_f32 v111, v46, v47\n"
"v_pk_mul_f32 v[48:49], v[48:49], s[66:67]\n"
"v_pk_mul_f32 v[50:51], v[50:51], s[66:67]\n"
"v_pk_mul_f32 v[52:53], v[52:53], s[66:67]\n"
"v_pk_mul_f32 v[54:55], v[54:55], s[66:67]\n"
"v_cvt_pk_bf16_f32 v112, v48, v49\n"
"v_cvt_pk_bf16_f32 v113, v50, v51\n"
"v_cvt_pk_bf16_f32 v114, v52, v53\n"
"v_cvt_pk_bf16_f32 v115, v54, v55\n"
"v_pk_mul_f32 v[56:57], v[56:57], s[66:67]\n"
"v_pk_mul_f32 v[58:59], v[58:59], s[66:67]\n"
"v_pk_mul_f32 v[60:61], v[60:61], s[66:67]\n"
"v_pk_mul_f32 v[62:63], v[62:63], s[66:67]\n"
"v_cvt_pk_bf16_f32 v116, v56, v57\n"
"v_cvt_pk_bf16_f32 v117, v58, v59\n"
"v_cvt_pk_bf16_f32 v118, v60, v61\n"
"v_cvt_pk_bf16_f32 v119, v62, v63\n"
"v_pk_mul_f32 v[64:65], v[64:65], s[66:67]\n"
"v_pk_mul_f32 v[66:67], v[66:67], s[66:67]\n"
"v_pk_mul_f32 v[68:69], v[68:69], s[66:67]\n"
"v_pk_mul_f32 v[70:71], v[70:71], s[66:67]\n"
"v_cvt_pk_bf16_f32 v120, v64, v65\n"
"v_cvt_pk_bf16_f32 v121, v66, v67\n"
"v_cvt_pk_bf16_f32 v122, v68, v69\n"
"v_cvt_pk_bf16_f32 v123, v70, v71\n"
"v_pk_mul_f32 v[72:73], v[72:73], s[66:67]\n"
"v_pk_mul_f32 v[74:75], v[74:75], s[66:67]\n"
"v_pk_mul_f32 v[76:77], v[76:77], s[66:67]\n"
"v_pk_mul_f32 v[78:79], v[78:79], s[66:67]\n"
"v_cvt_pk_bf16_f32 v124, v72, v73\n"
"v_cvt_pk_bf16_f32 v125, v74, v75\n"
"v_cvt_pk_bf16_f32 v126, v76, v77\n"
"v_cvt_pk_bf16_f32 v127, v78, v79\n"
"v_permlane32_swap_b32 v96, v98\n"
"v_permlane32_swap_b32 v97, v99\n"
"v_permlane32_swap_b32 v100, v102\n"
"v_permlane32_swap_b32 v101, v103\n"
"v_permlane32_swap_b32 v104, v106\n"
"v_permlane32_swap_b32 v105, v107\n"
"v_permlane32_swap_b32 v108, v110\n"
"v_permlane32_swap_b32 v109, v111\n"
"v_permlane32_swap_b32 v112, v114\n"
"v_permlane32_swap_b32 v113, v115\n"
"v_permlane32_swap_b32 v116, v118\n"
"v_permlane32_swap_b32 v117, v119\n"
"v_permlane32_swap_b32 v120, v122\n"
"v_permlane32_swap_b32 v121, v123\n"
"v_permlane32_swap_b32 v124, v126\n"
"v_permlane32_swap_b32 v125, v127\n"
"v_permlane16_swap_b32 v96, v98\n"
"v_permlane16_swap_b32 v97, v99\n"
"v_permlane16_swap_b32 v100, v102\n"
"v_permlane16_swap_b32 v101, v103\n"
"v_permlane16_swap_b32 v104, v106\n"
"v_permlane16_swap_b32 v105, v107\n"
"v_permlane16_swap_b32 v108, v110\n"
"v_permlane16_swap_b32 v109, v111\n"
"v_permlane16_swap_b32 v112, v114\n"
"v_permlane16_swap_b32 v113, v115\n"
"v_permlane16_swap_b32 v116, v118\n"
"v_permlane16_swap_b32 v117, v119\n"
"v_permlane16_swap_b32 v120, v122\n"
"v_permlane16_swap_b32 v121, v123\n"
"v_permlane16_swap_b32 v124, v126\n"
"v_permlane16_swap_b32 v125, v127\n"
"s_waitcnt lgkmcnt(0)\n"
"s_barrier\n"
"ds_read_b128 v[80:83], v247 offset:0\n"
"ds_read_b128 v[84:87], v247 offset:1024\n"
"ds_read_b128 v[88:91], v247 offset:2048\n"
"ds_read_b128 v[92:95], v247 offset:3072\n"
"v_mfma_f32_16x16x32_bf16 v[192:195], v[128:131], v[96:99], 0\n"
"v_mov_b32 v16, 0\n"
"v_mov_b32 v17, 0\n"
"v_mov_b32 v18, 0\n"
"v_mfma_f32_16x16x32_bf16 v[192:195], v[132:135], v[100:103], v[192:195]\n"
"v_mov_b32 v19, 0\n"
"v_mov_b32 v20, 0\n"
"v_mov_b32 v21, 0\n"
"v_mfma_f32_16x16x32_bf16 v[192:195], v[136:139], v[104:107], v[192:195]\n"
"v_mov_b32 v22, 0\n"
"v_mov_b32 v23, 0\n"
"v_mov_b32 v24, 0\n"
"v_mfma_f32_16x16x32_bf16 v[192:195], v[140:143], v[108:111], v[192:195]\n"
"v_mov_b32 v25, 0\n"
"v_mov_b32 v26, 0\n"
"v_mov_b32 v27, 0\n"
"v_mfma_f32_16x16x32_bf16 v[196:199], v[128:131], v[112:115], 0\n"
"v_mov_b32 v28, 0\n"
"v_mov_b32 v29, 0\n"
"v_mov_b32 v30, 0\n"
"v_mfma_f32_16x16x32_bf16 v[196:199], v[132:135], v[116:119], v[196:199]\n"
"v_mov_b32 v31, 0\n"
"v_mov_b32 v32, 0\n"
"v_mov_b32 v33, 0\n"
"v_mfma_f32_16x16x32_bf16 v[196:199], v[136:139], v[120:123], v[196:199]\n"
"v_mov_b32 v34, 0\n"
"v_mov_b32 v35, 0\n"
"v_mov_b32 v36, 0\n"
"v_mfma_f32_16x16x32_bf16 v[196:199], v[140:143], v[124:127], v[196:199]\n"
"v_mov_b32 v37, 0\n"
"v_mov_b32 v38, 0\n"
"v_mov_b32 v39, 0\n"
"v_mfma_f32_16x16x32_bf16 v[200:203], v[144:147], v[96:99], 0\n"
"v_mov_b32 v40, 0\n"
"v_mov_b32 v41, 0\n"
"v_mov_b32 v42, 0\n"
"v_mfma_f32_16x16x32_bf16 v[200:203], v[148:151], v[100:103], v[200:203]\n"
"v_mov_b32 v43, 0\n"
"v_mov_b32 v44, 0\n"
"v_mov_b32 v45, 0\n"
"v_mfma_f32_16x16x32_bf16 v[200:203], v[152:155], v[104:107], v[200:203]\n"
"v_mov_b32 v46, 0\n"
"v_mov_b32 v47, 0\n"
"v_mov_b32 v48, 0\n"
"v_mfma_f32_16x16x32_bf16 v[200:203], v[156:159], v[108:111], v[200:203]\n"
"v_mov_b32 v49, 0\n"
"v_mov_b32 v50, 0\n"
"v_mov_b32 v51, 0\n"
"v_mfma_f32_16x16x32_bf16 v[204:207], v[144:147], v[112:115], 0\n"
"v_mov_b32 v52, 0\n"
"v_mov_b32 v53, 0\n"
"v_mov_b32 v54, 0\n"
"v_mfma_f32_16x16x32_bf16 v[204:207], v[148:151], v[116:119], v[204:207]\n"
"v_mov_b32 v55, 0\n"
"v_mov_b32 v56, 0\n"
"v_mov_b32 v57, 0\n"
"v_mfma_f32_16x16x32_bf16 v[204:207], v[152:155], v[120:123], v[204:207]\n"
"v_mov_b32 v58, 0\n"
"v_mov_b32 v59, 0\n"
"v_mov_b32 v60, 0\n"
"v_mfma_f32_16x16x32_bf16 v[204:207], v[156:159], v[124:127], v[204:207]\n"
"v_mov_b32 v61, 0\n"
"v_mov_b32 v62, 0\n"
"v_mov_b32 v63, 0\n"
"s_waitcnt lgkmcnt(3)\n"
"v_mfma_f32_16x16x32_bf16 v[236:239], v[144:147], v[80:83], 0\n"
"v_mov_b32 v64, 0\n"
"v_mov_b32 v65, 0\n"
"v_mov_b32 v66, 0\n"
"s_waitcnt lgkmcnt(2)\n"
"v_mfma_f32_16x16x32_bf16 v[236:239], v[148:151], v[84:87], v[236:239]\n"
"v_mov_b32 v67, 0\n"
"v_mov_b32 v68, 0\n"
"v_mov_b32 v69, 0\n"
"s_waitcnt lgkmcnt(1)\n"
"v_mfma_f32_16x16x32_bf16 v[236:239], v[152:155], v[88:91], v[236:239]\n"
"v_mov_b32 v70, 0\n"
"v_mov_b32 v71, 0\n"
"v_mov_b32 v72, 0\n"
"s_waitcnt lgkmcnt(0)\n"
"v_mfma_f32_16x16x32_bf16 v[236:239], v[156:159], v[92:95], v[236:239]\n"
"v_mov_b32 v73, 0\n"
"v_mov_b32 v74, 0\n"
"v_mov_b32 v75, 0\n"
"v_mfma_f32_16x16x32_bf16 v[208:211], v[128:131], v[80:83], 0\n"
"v_mov_b32 v76, 0\n"
"v_mov_b32 v77, 0\n"
"v_mov_b32 v78, 0\n"
"v_mfma_f32_16x16x32_bf16 v[208:211], v[132:135], v[84:87], v[208:211]\n"
"v_mov_b32 v79, 0\n"
"v_mov_b32 v240, 0\n"
"v_mov_b32 v241, 0\n"
"v_mfma_f32_16x16x32_bf16 v[208:211], v[136:139], v[88:91], v[208:211]\n"
"v_mov_b32 v242, 0\n"
"v_mfma_f32_16x16x32_bf16 v[208:211], v[140:143], v[92:95], v[208:211]\n"
"v_max3_f32 v249, v192, v193, v194\n"
"v_max3_f32 v249, v249, v195, v200\n"
"v_max3_f32 v249, v249, v201, v202\n"
"v_max_f32 v249, v249, v203\n"
"v_max3_f32 v251, v196, v197, v198\n"
"v_max3_f32 v251, v251, v199, v204\n"
"v_max3_f32 v251, v251, v205, v206\n"
"v_max_f32_e64 v251, v251, v207\n"
"v_max3_f32 v253, v208, v209, v210\n"
"v_max3_f32 v253, v253, v211, v236\n"
"v_max3_f32 v253, v253, v237, v238\n"
"v_max_f32 v253, v253, v239\n"
"v_mov_b32 v250, v249\n"
"v_mov_b32 v252, v251\n"
"v_mov_b32 v254, v253\n"
"v_permlane32_swap_b32 v249, v250\n"
"v_permlane32_swap_b32 v251, v252\n"
"v_permlane32_swap_b32 v253, v254\n"
"v_max_f32 v249, v249, v250\n"
"v_max_f32 v251, v251, v252\n"
"v_max_f32 v253, v253, v254\n"
"v_mov_b32 v250, v249\n"
"v_mov_b32 v252, v251\n"
"v_mov_b32 v254, v253\n"
"v_permlane16_swap_b32 v249, v250\n"
"v_permlane16_swap_b32 v251, v252\n"
"v_permlane16_swap_b32 v253, v254\n"
"v_max_f32 v249, v249, v250\n"
"v_max_f32 v251, v251, v252\n"
"v_max_f32 v253, v253, v254\n"
"v_sub_f32 v212, 0, v249\n"
"v_sub_f32 v213, 0, v249\n"
"v_sub_f32 v214, 0, v249\n"
"v_sub_f32 v215, 0, v249\n"
"v_sub_f32 v192, v192, v249\n"
"v_sub_f32 v193, v193, v249\n"
"v_sub_f32 v194, v194, v249\n"
"v_sub_f32 v195, v195, v249\n"
"v_sub_f32 v200, v200, v249\n"
"v_sub_f32 v201, v201, v249\n"
"v_sub_f32 v202, v202, v249\n"
"v_sub_f32 v203, v203, v249\n"
"v_sub_f32 v216, 0, v251\n"
"v_sub_f32 v217, 0, v251\n"
"v_sub_f32 v218, 0, v251\n"
"v_sub_f32 v219, 0, v251\n"
"v_sub_f32 v196, v196, v251\n"
"v_sub_f32 v197, v197, v251\n"
"v_sub_f32 v198, v198, v251\n"
"v_sub_f32 v199, v199, v251\n"
"v_sub_f32 v204, v204, v251\n"
"v_sub_f32 v205, v205, v251\n"
"v_sub_f32 v206, v206, v251\n"
"v_sub_f32 v207, v207, v251\n"
"v_sub_f32 v220, 0, v253\n"
"v_sub_f32 v221, 0, v253\n"
"v_sub_f32 v222, 0, v253\n"
"v_sub_f32 v223, 0, v253\n"
"v_sub_f32 v208, v208, v253\n"
"v_sub_f32 v209, v209, v253\n"
"v_sub_f32 v210, v210, v253\n"
"v_sub_f32_e64 v211, v211, v253\n"
"s_movk_i32 s68, 1152\n"
"s_lshr_b32 s69, %[nb256], 3\n"
"s_add_u32 s69, s69, 5632\n"
"v_and_b32 v254, 255, %[g0]\n"
"v_add_u32_e64 v254, %[kq64], v254\n"
"v_and_b32 v254, 255, v254\n"
"v_lshrrev_b32_e64 v255, 8, %[g0]\n"
"v_mul_u32_u24 v255, s68, v255\n"
"v_add_u32 v255, %[nb256], v255\n"
"v_add_u32 v249, v254, v255\n"
"v_xor_b32 v254, 32, v254\n"
"v_add_u32 v250, v254, v255\n"
"v_add_u32 v250, 3584, v250\n"
"v_lshrrev_b32_e64 v254, 5, %[l16]\n"
"v_mul_u32_u24 v254, s68, v254\n"
"v_and_b32 v255, 16, %[l16]\n"
"v_add_u32 v254, v254, v255\n"
"v_add_u32 v252, s69, v254\n"
"s_cmp_eq_u32 %[hb], 0\n"
"s_cbranch_scc1 LH0_%=\n"
"s_barrier\n"
"LH0_%=:\n"
"v_exp_f32 v208, v208\n"
"v_exp_f32 v209, v209\n"
"v_exp_f32 v210, v210\n"
"v_exp_f32 v211, v211\n"
"v_cvt_pk_bf16_f32 v232, v208, v209\n"
"v_cvt_pk_bf16_f32 v233, v210, v211\n"
"ds_write_b64 v247, v[232:233] offset:4096\n"
"v_exp_f32 v192, v192\n"
"v_exp_f32 v193, v193\n"
"v_exp_f32 v194, v194\n"
"v_exp_f32 v195, v195\n"
"v_cvt_pk_bf16_f32 v224, v192, v193\n"
"v_cvt_pk_bf16_f32 v225, v194, v195\n"
"v_exp_f32 v196, v196\n"
"v_exp_f32 v197, v197\n"
"v_exp_f32 v198, v198\n"
"v_exp_f32 v199, v199\n"
"v_cvt_pk_bf16_f32 v228, v196, v197\n"
"v_cvt_pk_bf16_f32 v229, v198, v199\n"
"v_exp_f32 v200, v200\n"
"v_exp_f32 v201, v201\n"
"v_exp_f32 v202, v202\n"
"v_exp_f32 v203, v203\n"
"v_cvt_pk_bf16_f32 v226, v200, v201\n"
"v_cvt_pk_bf16_f32 v227, v202, v203\n"
"v_exp_f32 v204, v204\n"
"v_exp_f32 v205, v205\n"
"v_exp_f32 v206, v206\n"
"v_exp_f32 v207, v207\n"
"v_cvt_pk_bf16_f32 v230, v204, v205\n"
"v_cvt_pk_bf16_f32 v231, v206, v207\n"
"s_waitcnt lgkmcnt(0)\n"
"s_cmp_lg_u32 %[isa], 0\n"
"s_cbranch_scc1 LA0_%=\n"
"s_mov_b32 m0, s52\n"
"s_nop 0\n"
"buffer_load_dwordx4 %[g0], %[srdv], s61 offen nt lds\n"
"buffer_load_dwordx4 %[g1], %[srdv], s61 offen offset:1024 nt lds\n"
"buffer_load_dwordx4 %[g0], %[srdk], s61 offen offset:2048 nt lds\n"
"s_add_u32 s61, s61, 0x2000\n"
"s_waitcnt vmcnt(9)\n"
".p2align 6\n"
"LB0_%=:\n"
"KB0 16384,s53\n"
"KB0 32768,s54\n"
"KB0 49152,s55\n"
"KB0 0,s52\n"
"s_branch LB0_%=\n"
".p2align 3\n"
"LQ0_%=:\n"
"s_barrier\n"
"s_setprio 1\n"
"v_mfma_f32_16x16x32_bf16 v[16:19], v[160:163], v[224:227], v[16:19]\n"
"ds_read_b128 v[128:131], %[kb] offset:2048\n"
"v_add_f32 v240, v240, v192\n"
"v_add_f32 v241, v241, v196\n"
"v_mfma_f32_16x16x32_bf16 v[48:51], v[160:163], v[228:231], v[48:51]\n"
"ds_read_b128 v[132:135], %[kb] offset:2304\n"
"v_add_f32 v242, v242, v208\n"
"v_add_f32 v240, v240, v193\n"
"v_mfma_f32_16x16x32_bf16 v[20:23], v[164:167], v[224:227], v[20:23]\n"
"ds_read_b128 v[136:139], %[kb] offset:2560\n"
"v_add_f32 v241, v241, v197\n"
"v_add_f32 v242, v242, v209\n"
"v_mfma_f32_16x16x32_bf16 v[52:55], v[164:167], v[228:231], v[52:55]\n"
"ds_read_b128 v[140:143], %[kb] offset:2816\n"
"v_add_f32 v240, v240, v194\n"
"v_add_f32 v241, v241, v198\n"
"v_mfma_f32_16x16x32_bf16 v[24:27], v[168:171], v[224:227], v[24:27]\n"
"ds_read_b128 v[144:147], %[kb] offset:10240\n"
"v_add_f32 v242, v242, v210\n"
"v_add_f32 v240, v240, v195\n"
"v_mfma_f32_16x16x32_bf16 v[56:59], v[168:171], v[228:231], v[56:59]\n"
"ds_read_b128 v[148:151], %[kb] offset:10496\n"
"v_add_f32 v241, v241, v199\n"
"v_add_f32 v242, v242, v211\n"
"v_mfma_f32_16x16x32_bf16 v[28:31], v[172:175], v[224:227], v[28:31]\n"
"ds_read_b128 v[152:155], %[kb] offset:10752\n"
"v_add_f32 v240, v240, v200\n"
"v_add_f32 v241, v241, v204\n"
"v_mfma_f32_16x16x32_bf16 v[60:63], v[172:175], v[228:231], v[60:63]\n"
"ds_read_b128 v[156:159], %[kb] offset:11008\n"
"v_add_f32 v240, v240, v201\n"
"v_add_f32 v241, v241, v205\n"
"v_mfma_f32_16x16x32_bf16 v[32:35], v[176:179], v[224:227], v[32:35]\n"
"ds_read_b64_tr_b16 v[160:161], %[vbe] offset:0\n"
"v_add_f32 v240, v240, v202\n"
"v_add_f32 v241, v241, v206\n"
"v_mfma_f32_16x16x32_bf16 v[64:67], v[176:179], v[228:231], v[64:67]\n"
"ds_read_b64_tr_b16 v[162:163], %[vbe] offset:8192\n"
"v_add_f32 v240, v240, v203\n"
"v_add_f32 v241, v241, v207\n"
"v_mfma_f32_16x16x32_bf16 v[36:39], v[180:183], v[224:227], v[36:39]\n"
"ds_read_b64_tr_b16 v[164:165], v243 offset:0\n"
"v_mfma_f32_16x16x32_bf16 v[68:71], v[180:183], v[228:231], v[68:71]\n"
"ds_read_b64_tr_b16 v[166:167], v243 offset:8192\n"
"v_mfma_f32_16x16x32_bf16 v[40:43], v[184:187], v[224:227], v[40:43]\n"
"ds_read_b64_tr_b16 v[168:169], %[vbe] offset:256\n"
"v_mfma_f32_16x16x32_bf16 v[72:75], v[184:187], v[228:231], v[72:75]\n"
"ds_read_b64_tr_b16 v[170:171], %[vbe] offset:8448\n"
"v_mfma_f32_16x16x32_bf16 v[44:47], v[188:191], v[224:227], v[44:47]\n"
"ds_read_b64_tr_b16 v[172:173], v243 offset:256\n"
"v_mfma_f32_16x16x32_bf16 v[76:79], v[188:191], v[228:231], v[76:79]\n"
"ds_read_b64_tr_b16 v[174:175], v243 offset:8448\n"
"s_waitcnt lgkmcnt(8)\n"
"v_mfma_f32_16x16x32_bf16 v[208:211], v[128:131], v[80:83], v[220:223]\n"
"ds_read_b64_tr_b16 v[176:177], %[vbe] offset:512\n"
"v_mfma_f32_16x16x32_bf16 v[208:211], v[132:135], v[84:87], v[208:211]\n"
"ds_read_b64_tr_b16 v[178:179], %[vbe] offset:8704\n"
"v_mfma_f32_16x16x32_bf16 v[208:211], v[136:139], v[88:91], v[208:211]\n"
"ds_read_b64_tr_b16 v[180:181], v243 offset:512\n"
"v_mfma_f32_16x16x32_bf16 v[208:211], v[140:143], v[92:95], v[208:211]\n"
"ds_read_b64_tr_b16 v[182:183], v243 offset:8704\n"
"v_mfma_f32_16x16x32_bf16 v[192:195], v[128:131], v[96:99], v[212:215]\n"
"ds_read_b64_tr_b16 v[184:185], %[vbe] offset:768\n"
"v_mfma_f32_16x16x32_bf16 v[192:195], v[132:135], v[100:103], v[192:195]\n"
"ds_read_b64_tr_b16 v[186:187], %[vbe] offset:8960\n"
"v_mfma_f32_16x16x32_bf16 v[192:195], v[136:139], v[104:107], v[192:195]\n"
"ds_read_b64_tr_b16 v[188:189], v243 offset:768\n"
"v_mfma_f32_16x16x32_bf16 v[192:195], v[140:143], v[108:111], v[192:195]\n"
"ds_read_b64_tr_b16 v[190:191], v243 offset:8960\n"
"v_mfma_f32_16x16x32_bf16 v[196:199], v[128:131], v[112:115], v[216:219]\n"
"v_mfma_f32_16x16x32_bf16 v[196:199], v[132:135], v[116:119], v[196:199]\n"
"v_mfma_f32_16x16x32_bf16 v[196:199], v[136:139], v[120:123], v[196:199]\n"
"v_mfma_f32_16x16x32_bf16 v[196:199], v[140:143], v[124:127], v[196:199]\n"
"v_mfma_f32_16x16x32_bf16 v[200:203], v[144:147], v[96:99], v[212:215]\n"
"v_mfma_f32_16x16x32_bf16 v[200:203], v[148:151], v[100:103], v[200:203]\n"
"v_mfma_f32_16x16x32_bf16 v[200:203], v[152:155], v[104:107], v[200:203]\n"
"v_mfma_f32_16x16x32_bf16 v[200:203], v[156:159], v[108:111], v[200:203]\n"
"v_mfma_f32_16x16x32_bf16 v[204:207], v[144:147], v[112:115], v[216:219]\n"
"v_mfma_f32_16x16x32_bf16 v[204:207], v[148:151], v[116:119], v[204:207]\n"
"v_mfma_f32_16x16x32_bf16 v[204:207], v[152:155], v[120:123], v[204:207]\n"
"v_mfma_f32_16x16x32_bf16 v[204:207], v[156:159], v[124:127], v[204:207]\n"
"s_waitcnt lgkmcnt(0)\n"
"s_barrier\n"
"s_setprio 0\n"
"s_getpc_b64 s[94:95]\n"
"LMb0_%=:\n"
"s_add_u32 s94, s94, (LZZ0_%=-16384-LMb0_%=)&0xffffffff\n"
"s_addc_u32 s95, s95, (LZZ0_%=-16384-LMb0_%=)>>32\n"
"v_lshlrev_b32 v236, 4, %[l16]\n"
"s_lshr_b32 s68, %[hb], 9\n"
"v_add_u32 v236, s68, v236\n"
"s_lshl_b32 exec_lo, 0x11111111, %[wrot]\n"
"s_mov_b32 exec_hi, exec_lo\n"
"global_load_dword v253, v236, s[94:95]\n"
"s_mov_b64 exec, -1\n"
"s_lshl_b32 s88, %[osoff], 2\n"
"s_mov_b32 m0, s52\n"
"s_nop 0\n"
"buffer_load_dwordx4 v249, s[76:79], s88 offen lds\n"
"buffer_load_dwordx4 v250, s[76:79], s88 offen offset:1024 lds\n"
"s_add_u32 m0, s90, 0\n"
"s_nop 0\n"
"buffer_load_dwordx4 v252, s[76:79], s88 offen lds\n"
"s_lshl_b32 s89, %[osoff], 2\n"
"s_add_u32 s89, s89, 34816\n"
"s_mov_b32 m0, s52\n"
"s_nop 0\n"
"buffer_load_dwordx4 v249, s[76:79], s89 offen offset:2048 lds\n"
"v_exp_f32 v208, v208\n"
"v_exp_f32 v209, v209\n"
"v_exp_f32 v210, v210\n"
"v_exp_f32 v211, v211\n"
"v_cvt_pk_bf16_f32 v232, v208, v209\n"
"v_cvt_pk_bf16_f32 v233, v210, v211\n"
"ds_write_b64 v247, v[232:233] offset:4096\n"
"v_exp_f32 v192, v192\n"
"v_exp_f32 v193, v193\n"
"v_exp_f32 v194, v194\n"
"v_exp_f32 v195, v195\n"
"v_cvt_pk_bf16_f32 v224, v192, v193\n"
"v_cvt_pk_bf16_f32 v225, v194, v195\n"
"v_exp_f32 v196, v196\n"
"v_exp_f32 v197, v197\n"
"v_exp_f32 v198, v198\n"
"v_exp_f32 v199, v199\n"
"v_cvt_pk_bf16_f32 v228, v196, v197\n"
"v_cvt_pk_bf16_f32 v229, v198, v199\n"
"v_exp_f32 v200, v200\n"
"v_exp_f32 v201, v201\n"
"v_exp_f32 v202, v202\n"
"v_exp_f32 v203, v203\n"
"v_cvt_pk_bf16_f32 v226, v200, v201\n"
"v_cvt_pk_bf16_f32 v227, v202, v203\n"
"v_exp_f32 v204, v204\n"
"v_exp_f32 v205, v205\n"
"v_exp_f32 v206, v206\n"
"v_exp_f32 v207, v207\n"
"v_cvt_pk_bf16_f32 v230, v204, v205\n"
"v_cvt_pk_bf16_f32 v231, v206, v207\n"
"s_waitcnt vmcnt(10) lgkmcnt(0)\n"
"s_barrier\n"
"s_setprio 1\n"
"v_mfma_f32_16x16x32_bf16 v[16:19], v[160:163], v[224:227], v[16:19]\n"
"ds_read_b128 v[128:131], %[kb] offset:18432\n"
"v_add_f32 v240, v240, v192\n"
"v_add_f32_e64 v241, v241, v196\n"
"v_mfma_f32_16x16x32_bf16 v[48:51], v[160:163], v[228:231], v[48:51]\n"
"ds_read_b128 v[132:135], %[kb] offset:18688\n"
"v_add_f32 v242, v242, v208\n"
"v_add_f32 v240, v240, v193\n"
"v_mfma_f32_16x16x32_bf16 v[20:23], v[164:167], v[224:227], v[20:23]\n"
"ds_read_b128 v[136:139], %[kb] offset:18944\n"
"v_add_f32 v241, v241, v197\n"
"v_add_f32 v242, v242, v209\n"
"v_mfma_f32_16x16x32_bf16 v[52:55], v[164:167], v[228:231], v[52:55]\n"
"ds_read_b128 v[140:143], %[kb] offset:19200\n"
"v_add_f32 v240, v240, v194\n"
"v_add_f32 v241, v241, v198\n"
"v_mfma_f32_16x16x32_bf16 v[24:27], v[168:171], v[224:227], v[24:27]\n"
"ds_read_b128 v[144:147], %[kb] offset:26624\n"
"v_add_f32 v242, v242, v210\n"
"v_add_f32 v240, v240, v195\n"
"v_mfma_f32_16x16x32_bf16 v[56:59], v[168:171], v[228:231], v[56:59]\n"
"ds_read_b128 v[148:151], %[kb] offset:26880\n"
"v_add_f32 v241, v241, v199\n"
"v_add_f32 v242, v242, v211\n"
"v_mfma_f32_16x16x32_bf16 v[28:31], v[172:175], v[224:227], v[28:31]\n"
"ds_read_b128 v[152:155], %[kb] offset:27136\n"
"v_add_f32 v240, v240, v200\n"
"v_add_f32 v241, v241, v204\n"
"v_mfma_f32_16x16x32_bf16 v[60:63], v[172:175], v[228:231], v[60:63]\n"
"ds_read_b128 v[156:159], %[kb] offset:27392\n"
"v_add_f32 v240, v240, v201\n"
"v_add_f32 v241, v241, v205\n"
"v_mfma_f32_16x16x32_bf16 v[32:35], v[176:179], v[224:227], v[32:35]\n"
"ds_read_b64_tr_b16 v[160:161], %[vbe] offset:16384\n"
"v_add_f32 v240, v240, v202\n"
"v_add_f32 v241, v241, v206\n"
"v_mfma_f32_16x16x32_bf16 v[64:67], v[176:179], v[228:231], v[64:67]\n"
"ds_read_b64_tr_b16 v[162:163], %[vbe] offset:24576\n"
"v_add_f32 v240, v240, v203\n"
"v_add_f32 v241, v241, v207\n"
"v_mfma_f32_16x16x32_bf16 v[36:39], v[180:183], v[224:227], v[36:39]\n"
"ds_read_b64_tr_b16 v[164:165], v243 offset:16384\n"
"v_mfma_f32_16x16x32_bf16 v[68:71], v[180:183], v[228:231], v[68:71]\n"
"ds_read_b64_tr_b16 v[166:167], v243 offset:24576\n"
"v_mfma_f32_16x16x32_bf16 v[40:43], v[184:187], v[224:227], v[40:43]\n"
"ds_read_b64_tr_b16 v[168:169], %[vbe] offset:16640\n"
"v_mfma_f32_16x16x32_bf16 v[72:75], v[184:187], v[228:231], v[72:75]\n"
"ds_read_b64_tr_b16 v[170:171], %[vbe] offset:24832\n"
"v_mfma_f32_16x16x32_bf16 v[44:47], v[188:191], v[224:227], v[44:47]\n"
"ds_read_b64_tr_b16 v[172:173], v243 offset:16640\n"
"v_mfma_f32_16x16x32_bf16 v[76:79], v[188:191], v[228:231], v[76:79]\n"
"ds_read_b64_tr_b16 v[174:175], v243 offset:24832\n"
"s_waitcnt lgkmcnt(8)\n"
"v_mfma_f32_16x16x32_bf16 v[208:211], v[128:131], v[80:83], v[220:223]\n"
"ds_read_b64_tr_b16 v[176:177], %[vbe] offset:16896\n"
"v_mfma_f32_16x16x32_bf16 v[208:211], v[132:135], v[84:87], v[208:211]\n"
"ds_read_b64_tr_b16 v[178:179], %[vbe] offset:25088\n"
"v_mfma_f32_16x16x32_bf16 v[208:211], v[136:139], v[88:91], v[208:211]\n"
"ds_read_b64_tr_b16 v[180:181], v243 offset:16896\n"
"v_mfma_f32_16x16x32_bf16 v[208:211], v[140:143], v[92:95], v[208:211]\n"
"ds_read_b64_tr_b16 v[182:183], v243 offset:25088\n"
"v_mfma_f32_16x16x32_bf16 v[192:195], v[128:131], v[96:99], v[212:215]\n"
"ds_read_b64_tr_b16 v[184:185], %[vbe] offset:17152\n"
"v_mfma_f32_16x16x32_bf16 v[192:195], v[132:135], v[100:103], v[192:195]\n"
"ds_read_b64_tr_b16 v[186:187], %[vbe] offset:25344\n"
"v_mfma_f32_16x16x32_bf16 v[192:195], v[136:139], v[104:107], v[192:195]\n"
"ds_read_b64_tr_b16 v[188:189], v243 offset:17152\n"
"v_mfma_f32_16x16x32_bf16 v[192:195], v[140:143], v[108:111], v[192:195]\n"
"ds_read_b64_tr_b16 v[190:191], v243 offset:25344\n"
"v_mfma_f32_16x16x32_bf16 v[196:199], v[128:131], v[112:115], v[216:219]\n"
"v_mfma_f32_16x16x32_bf16 v[196:199], v[132:135], v[116:119], v[196:199]\n"
"v_mfma_f32_16x16x32_bf16 v[196:199], v[136:139], v[120:123], v[196:199]\n"
"v_mfma_f32_16x16x32_bf16 v[196:199], v[140:143], v[124:127], v[196:199]\n"
"v_mfma_f32_16x16x32_bf16 v[200:203], v[144:147], v[96:99], v[212:215]\n"
"v_mfma_f32_16x16x32_bf16 v[200:203], v[148:151], v[100:103], v[200:203]\n"
"v_mfma_f32_16x16x32_bf16 v[200:203], v[152:155], v[104:107], v[200:203]\n"
"v_mfma_f32_16x16x32_bf16 v[200:203], v[156:159], v[108:111], v[200:203]\n"
"v_mfma_f32_16x16x32_bf16 v[204:207], v[144:147], v[112:115], v[216:219]\n"
"v_mfma_f32_16x16x32_bf16 v[204:207], v[148:151], v[116:119], v[204:207]\n"
"v_mfma_f32_16x16x32_bf16 v[204:207], v[152:155], v[120:123], v[204:207]\n"
"v_mfma_f32_16x16x32_bf16 v[204:207], v[156:159], v[124:127], v[204:207]\n"
"s_waitcnt lgkmcnt(0)\n"
"s_barrier\n"
"s_setprio 0\n"
"s_lshl_b32 s88, %[osoff], 2\n"
"s_add_u32 s88, s88, 73728\n"
"s_mov_b32 m0, s53\n"
"s_nop 0\n"
"buffer_load_dwordx4 v249, s[76:79], s88 offen lds\n"
"buffer_load_dwordx4 v250, s[76:79], s88 offen offset:1024 lds\n"
"s_lshl_b32 s89, %[osoff], 2\n"
"s_add_u32 s89, s89, 108544\n"
"buffer_load_dwordx4 v249, s[76:79], s89 offen offset:2048 lds\n"
"v_exp_f32 v208, v208\n"
"v_exp_f32 v209, v209\n"
"v_exp_f32 v210, v210\n"
"v_exp_f32 v211, v211\n"
"v_cvt_pk_bf16_f32 v232, v208, v209\n"
"v_cvt_pk_bf16_f32 v233, v210, v211\n"
"ds_write_b64 v247, v[232:233] offset:4096\n"
"v_exp_f32 v192, v192\n"
"v_exp_f32 v193, v193\n"
"v_exp_f32 v194, v194\n"
"v_exp_f32 v195, v195\n"
"v_cvt_pk_bf16_f32 v224, v192, v193\n"
"v_cvt_pk_bf16_f32 v225, v194, v195\n"
"v_exp_f32 v196, v196\n"
"v_exp_f32 v197, v197\n"
"v_exp_f32 v198, v198\n"
"v_exp_f32 v199, v199\n"
"v_cvt_pk_bf16_f32 v228, v196, v197\n"
"v_cvt_pk_bf16_f32 v229, v198, v199\n"
"v_exp_f32 v200, v200\n"
"v_exp_f32 v201, v201\n"
"v_exp_f32 v202, v202\n"
"v_exp_f32 v203, v203\n"
"v_cvt_pk_bf16_f32 v226, v200, v201\n"
"v_cvt_pk_bf16_f32 v227, v202, v203\n"
"v_exp_f32 v204, v204\n"
"v_exp_f32 v205, v205\n"
"v_exp_f32 v206, v206\n"
"v_exp_f32 v207, v207\n"
"v_cvt_pk_bf16_f32 v230, v204, v205\n"
"v_cvt_pk_bf16_f32 v231, v206, v207\n"
"s_waitcnt vmcnt(10) lgkmcnt(0)\n"
"s_barrier\n"
"s_setprio 1\n"
"v_mfma_f32_16x16x32_bf16 v[16:19], v[160:163], v[224:227], v[16:19]\n"
"ds_read_b128 v[128:131], %[kb] offset:34816\n"
"v_add_f32 v240, v240, v192\n"
"v_add_f32_e64 v241, v241, v196\n"
"v_mfma_f32_16x16x32_bf16 v[48:51], v[160:163], v[228:231], v[48:51]\n"
"ds_read_b128 v[132:135], %[kb] offset:35072\n"
"v_add_f32 v242, v242, v208\n"
"v_add_f32 v240, v240, v193\n"
"v_mfma_f32_16x16x32_bf16 v[20:23], v[164:167], v[224:227], v[20:23]\n"
"ds_read_b128 v[136:139], %[kb] offset:35328\n"
"v_add_f32 v241, v241, v197\n"
"v_add_f32 v242, v242, v209\n"
"v_mfma_f32_16x16x32_bf16 v[52:55], v[164:167], v[228:231], v[52:55]\n"
"ds_read_b128 v[140:143], %[kb] offset:35584\n"
"v_add_f32 v240, v240, v194\n"
"v_add_f32 v241, v241, v198\n"
"v_mfma_f32_16x16x32_bf16 v[24:27], v[168:171], v[224:227], v[24:27]\n"
"ds_read_b128 v[144:147], %[kb] offset:43008\n"
"v_add_f32 v242, v242, v210\n"
"v_add_f32 v240, v240, v195\n"
"v_mfma_f32_16x16x32_bf16 v[56:59], v[168:171], v[228:231], v[56:59]\n"
"ds_read_b128 v[148:151], %[kb] offset:43264\n"
"v_add_f32 v241, v241, v199\n"
"v_add_f32 v242, v242, v211\n"
"v_mfma_f32_16x16x32_bf16 v[28:31], v[172:175], v[224:227], v[28:31]\n"
"ds_read_b128 v[152:155], %[kb] offset:43520\n"
"v_add_f32 v240, v240, v200\n"
"v_add_f32 v241, v241, v204\n"
"v_mfma_f32_16x16x32_bf16 v[60:63], v[172:175], v[228:231], v[60:63]\n"
"ds_read_b128 v[156:159], %[kb] offset:43776\n"
"v_add_f32 v240, v240, v201\n"
"v_add_f32 v241, v241, v205\n"
"v_mfma_f32_16x16x32_bf16 v[32:35], v[176:179], v[224:227], v[32:35]\n"
"ds_read_b64_tr_b16 v[160:161], %[vbe] offset:32768\n"
"v_add_f32 v240, v240, v202\n"
"v_add_f32 v241, v241, v206\n"
"v_mfma_f32_16x16x32_bf16 v[64:67], v[176:179], v[228:231], v[64:67]\n"
"ds_read_b64_tr_b16 v[162:163], %[vbe] offset:40960\n"
"v_add_f32 v240, v240, v203\n"
"v_add_f32 v241, v241, v207\n"
"v_mfma_f32_16x16x32_bf16 v[36:39], v[180:183], v[224:227], v[36:39]\n"
"ds_read_b64_tr_b16 v[164:165], v243 offset:32768\n"
"v_mfma_f32_16x16x32_bf16 v[68:71], v[180:183], v[228:231], v[68:71]\n"
"ds_read_b64_tr_b16 v[166:167], v243 offset:40960\n"
"v_mfma_f32_16x16x32_bf16 v[40:43], v[184:187], v[224:227], v[40:43]\n"
"ds_read_b64_tr_b16 v[168:169], %[vbe] offset:33024\n"
"v_mfma_f32_16x16x32_bf16 v[72:75], v[184:187], v[228:231], v[72:75]\n"
"ds_read_b64_tr_b16 v[170:171], %[vbe] offset:41216\n"
"v_mfma_f32_16x16x32_bf16 v[44:47], v[188:191], v[224:227], v[44:47]\n"
"ds_read_b64_tr_b16 v[172:173], v243 offset:33024\n"
"v_mfma_f32_16x16x32_bf16 v[76:79], v[188:191], v[228:231], v[76:79]\n"
"ds_read_b64_tr_b16 v[174:175], v243 offset:41216\n"
"s_waitcnt lgkmcnt(8)\n"
"v_mfma_f32_16x16x32_bf16 v[208:211], v[128:131], v[80:83], v[220:223]\n"
"ds_read_b64_tr_b16 v[176:177], %[vbe] offset:33280\n"
"v_mfma_f32_16x16x32_bf16 v[208:211], v[132:135], v[84:87], v[208:211]\n"
"ds_read_b64_tr_b16 v[178:179], %[vbe] offset:41472\n"
"v_mfma_f32_16x16x32_bf16 v[208:211], v[136:139], v[88:91], v[208:211]\n"
"ds_read_b64_tr_b16 v[180:181], v243 offset:33280\n"
"v_mfma_f32_16x16x32_bf16 v[208:211], v[140:143], v[92:95], v[208:211]\n"
"ds_read_b64_tr_b16 v[182:183], v243 offset:41472\n"
"v_mfma_f32_16x16x32_bf16 v[192:195], v[128:131], v[96:99], v[212:215]\n"
"ds_read_b64_tr_b16 v[184:185], %[vbe] offset:33536\n"
"v_mfma_f32_16x16x32_bf16 v[192:195], v[132:135], v[100:103], v[192:195]\n"
"ds_read_b64_tr_b16 v[186:187], %[vbe] offset:41728\n"
"v_mfma_f32_16x16x32_bf16 v[192:195], v[136:139], v[104:107], v[192:195]\n"
"ds_read_b64_tr_b16 v[188:189], v243 offset:33536\n"
"v_mfma_f32_16x16x32_bf16 v[192:195], v[140:143], v[108:111], v[192:195]\n"
"ds_read_b64_tr_b16 v[190:191], v243 offset:41728\n"
"v_mfma_f32_16x16x32_bf16 v[196:199], v[128:131], v[112:115], v[216:219]\n"
"v_mfma_f32_16x16x32_bf16 v[196:199], v[132:135], v[116:119], v[196:199]\n"
"v_mfma_f32_16x16x32_bf16 v[196:199], v[136:139], v[120:123], v[196:199]\n"
"v_mfma_f32_16x16x32_bf16 v[196:199], v[140:143], v[124:127], v[196:199]\n"
"v_mfma_f32_16x16x32_bf16 v[200:203], v[144:147], v[96:99], v[212:215]\n"
"v_mfma_f32_16x16x32_bf16 v[200:203], v[148:151], v[100:103], v[200:203]\n"
"v_mfma_f32_16x16x32_bf16 v[200:203], v[152:155], v[104:107], v[200:203]\n"
"v_mfma_f32_16x16x32_bf16 v[200:203], v[156:159], v[108:111], v[200:203]\n"
"v_mfma_f32_16x16x32_bf16 v[204:207], v[144:147], v[112:115], v[216:219]\n"
"v_mfma_f32_16x16x32_bf16 v[204:207], v[148:151], v[116:119], v[204:207]\n"
"v_mfma_f32_16x16x32_bf16 v[204:207], v[152:155], v[120:123], v[204:207]\n"
"v_mfma_f32_16x16x32_bf16 v[204:207], v[156:159], v[124:127], v[204:207]\n"
"s_waitcnt lgkmcnt(0)\n"
"s_barrier\n"
"s_setprio 0\n"
"s_lshl_b32 s88, %[os1], 2\n"
"s_mov_b32 m0, s54\n"
"s_nop 0\n"
"buffer_load_dwordx4 v249, s[76:79], s88 offen lds\n"
"buffer_load_dwordx4 v250, s[76:79], s88 offen offset:1024 lds\n"
"s_add_u32 m0, s90, 4096\n"
"s_nop 0\n"
"buffer_load_dwordx4 v252, s[76:79], s88 offen lds\n"
"s_lshl_b32 s89, %[os1], 2\n"
"s_add_u32 s89, s89, 34816\n"
"s_mov_b32 m0, s54\n"
"s_nop 0\n"
"buffer_load_dwordx4 v249, s[76:79], s89 offen offset:2048 lds\n"
"v_exp_f32 v208, v208\n"
"v_exp_f32 v209, v209\n"
"v_exp_f32 v210, v210\n"
"v_exp_f32_e64 v211, v211\n"
"v_cvt_pk_bf16_f32 v232, v208, v209\n"
"v_cvt_pk_bf16_f32 v233, v210, v211\n"
"ds_write_b64 v247, v[232:233] offset:4096\n"
"v_exp_f32 v192, v192\n"
"v_exp_f32 v193, v193\n"
"v_exp_f32 v194, v194\n"
"v_exp_f32 v195, v195\n"
"v_cvt_pk_bf16_f32 v224, v192, v193\n"
"v_cvt_pk_bf16_f32 v225, v194, v195\n"
"v_exp_f32 v196, v196\n"
"v_exp_f32 v197, v197\n"
"v_exp_f32 v198, v198\n"
"v_exp_f32 v199, v199\n"
"v_cvt_pk_bf16_f32 v228, v196, v197\n"
"v_cvt_pk_bf16_f32 v229, v198, v199\n"
"v_exp_f32 v200, v200\n"
"v_exp_f32 v201, v201\n"
"v_exp_f32 v202, v202\n"
"v_exp_f32 v203, v203\n"
"v_cvt_pk_bf16_f32 v226, v200, v201\n"
"v_cvt_pk_bf16_f32 v227, v202, v203\n"
"v_exp_f32 v204, v204\n"
"v_exp_f32 v205, v205\n"
"v_exp_f32 v206, v206\n"
"v_exp_f32 v207, v207\n"
"v_cvt_pk_bf16_f32 v230, v204, v205\n"
"v_cvt_pk_bf16_f32 v231, v206, v207\n"
"s_waitcnt vmcnt(11) lgkmcnt(0)\n"
"s_barrier\n"
"s_setprio 1\n"
"v_mfma_f32_16x16x32_bf16 v[16:19], v[160:163], v[224:227], v[16:19]\n"
"ds_read_b128 v[128:131], %[kb] offset:51200\n"
"v_add_f32 v240, v240, v192\n"
"v_add_f32_e64 v241, v241, v196\n"
"v_mfma_f32_16x16x32_bf16 v[48:51], v[160:163], v[228:231], v[48:51]\n"
"ds_read_b128 v[132:135], %[kb] offset:51456\n"
"v_add_f32 v242, v242, v208\n"
"v_add_f32 v240, v240, v193\n"
"v_mfma_f32_16x16x32_bf16 v[20:23], v[164:167], v[224:227], v[20:23]\n"
"ds_read_b128 v[136:139], %[kb] offset:51712\n"
"v_add_f32 v241, v241, v197\n"
"v_add_f32 v242, v242, v209\n"
"v_mfma_f32_16x16x32_bf16 v[52:55], v[164:167], v[228:231], v[52:55]\n"
"ds_read_b128 v[140:143], %[kb] offset:51968\n"
"v_add_f32 v240, v240, v194\n"
"v_add_f32 v241, v241, v198\n"
"v_mfma_f32_16x16x32_bf16 v[24:27], v[168:171], v[224:227], v[24:27]\n"
"ds_read_b128 v[144:147], %[kb] offset:59392\n"
"v_add_f32 v242, v242, v210\n"
"v_add_f32 v240, v240, v195\n"
"v_mfma_f32_16x16x32_bf16 v[56:59], v[168:171], v[228:231], v[56:59]\n"
"ds_read_b128 v[148:151], %[kb] offset:59648\n"
"v_add_f32 v241, v241, v199\n"
"v_add_f32 v242, v242, v211\n"
"v_mfma_f32_16x16x32_bf16 v[28:31], v[172:175], v[224:227], v[28:31]\n"
"ds_read_b128 v[152:155], %[kb] offset:59904\n"
"v_add_f32 v240, v240, v200\n"
"v_add_f32 v241, v241, v204\n"
"v_mfma_f32_16x16x32_bf16 v[60:63], v[172:175], v[228:231], v[60:63]\n"
"ds_read_b128 v[156:159], %[kb] offset:60160\n"
"v_add_f32 v240, v240, v201\n"
"v_add_f32 v241, v241, v205\n"
"v_mfma_f32_16x16x32_bf16 v[32:35], v[176:179], v[224:227], v[32:35]\n"
"ds_read_b64_tr_b16 v[160:161], %[vbe] offset:49152\n"
"v_add_f32 v240, v240, v202\n"
"v_add_f32 v241, v241, v206\n"
"v_mfma_f32_16x16x32_bf16 v[64:67], v[176:179], v[228:231], v[64:67]\n"
"ds_read_b64_tr_b16 v[162:163], %[vbe] offset:57344\n"
"v_add_f32 v240, v240, v203\n"
"v_add_f32 v241, v241, v207\n"
"v_mfma_f32_16x16x32_bf16 v[36:39], v[180:183], v[224:227], v[36:39]\n"
"ds_read_b64_tr_b16 v[164:165], v243 offset:49152\n"
"v_mfma_f32_16x16x32_bf16 v[68:71], v[180:183], v[228:231], v[68:71]\n"
"ds_read_b64_tr_b16 v[166:167], v243 offset:57344\n"
"v_mfma_f32_16x16x32_bf16 v[40:43], v[184:187], v[224:227], v[40:43]\n"
"ds_read_b64_tr_b16 v[168:169], %[vbe] offset:49408\n"
"v_mfma_f32_16x16x32_bf16 v[72:75], v[184:187], v[228:231], v[72:75]\n"
"ds_read_b64_tr_b16 v[170:171], %[vbe] offset:57600\n"
"v_mfma_f32_16x16x32_bf16 v[44:47], v[188:191], v[224:227], v[44:47]\n"
"ds_read_b64_tr_b16 v[172:173], v243 offset:49408\n"
"v_mfma_f32_16x16x32_bf16 v[76:79], v[188:191], v[228:231], v[76:79]\n"
"ds_read_b64_tr_b16 v[174:175], v243 offset:57600\n"
"s_waitcnt lgkmcnt(8)\n"
"v_mfma_f32_16x16x32_bf16 v[208:211], v[128:131], v[80:83], v[220:223]\n"
"ds_read_b64_tr_b16 v[176:177], %[vbe] offset:49664\n"
"v_mfma_f32_16x16x32_bf16 v[208:211], v[132:135], v[84:87], v[208:211]\n"
"ds_read_b64_tr_b16 v[178:179], %[vbe] offset:57856\n"
"v_mfma_f32_16x16x32_bf16 v[208:211], v[136:139], v[88:91], v[208:211]\n"
"ds_read_b64_tr_b16 v[180:181], v243 offset:49664\n"
"v_mfma_f32_16x16x32_bf16 v[208:211], v[140:143], v[92:95], v[208:211]\n"
"ds_read_b64_tr_b16 v[182:183], v243 offset:57856\n"
"v_mfma_f32_16x16x32_bf16 v[192:195], v[128:131], v[96:99], v[212:215]\n"
"ds_read_b64_tr_b16 v[184:185], %[vbe] offset:49920\n"
"v_mfma_f32_16x16x32_bf16 v[192:195], v[132:135], v[100:103], v[192:195]\n"
"ds_read_b64_tr_b16 v[186:187], %[vbe] offset:58112\n"
"v_mfma_f32_16x16x32_bf16 v[192:195], v[136:139], v[104:107], v[192:195]\n"
"ds_read_b64_tr_b16 v[188:189], v243 offset:49920\n"
"v_mfma_f32_16x16x32_bf16 v[192:195], v[140:143], v[108:111], v[192:195]\n"
"ds_read_b64_tr_b16 v[190:191], v243 offset:58112\n"
"v_mfma_f32_16x16x32_bf16 v[196:199], v[128:131], v[112:115], v[216:219]\n"
"v_mfma_f32_16x16x32_bf16 v[196:199], v[132:135], v[116:119], v[196:199]\n"
"v_mfma_f32_16x16x32_bf16 v[196:199], v[136:139], v[120:123], v[196:199]\n"
"v_mfma_f32_16x16x32_bf16 v[196:199], v[140:143], v[124:127], v[196:199]\n"
"v_mfma_f32_16x16x32_bf16 v[200:203], v[144:147], v[96:99], v[212:215]\n"
"v_mfma_f32_16x16x32_bf16 v[200:203], v[148:151], v[100:103], v[200:203]\n"
"v_mfma_f32_16x16x32_bf16 v[200:203], v[152:155], v[104:107], v[200:203]\n"
"v_mfma_f32_16x16x32_bf16 v[200:203], v[156:159], v[108:111], v[200:203]\n"
"v_mfma_f32_16x16x32_bf16 v[204:207], v[144:147], v[112:115], v[216:219]\n"
"v_mfma_f32_16x16x32_bf16 v[204:207], v[148:151], v[116:119], v[204:207]\n"
"v_mfma_f32_16x16x32_bf16 v[204:207], v[152:155], v[120:123], v[204:207]\n"
"v_mfma_f32_16x16x32_bf16 v[204:207], v[156:159], v[124:127], v[204:207]\n"
"s_waitcnt lgkmcnt(0)\n"
"s_barrier\n"
"s_setprio 0\n"
"s_branch LT0_%=\n"
".p2align 3\n"
"LF0_%=:\n"
"s_add_u32 s52, %[hb], 0\n"
"s_add_u32 s53, %[hb], 16384\n"
"s_add_u32 s54, %[hb], 32768\n"
"s_add_u32 s55, %[hb], 49152\n"
"s_lshr_b32 s90, %[hb], 3\n"
"s_add_u32 s90, s90, 131072\n"
"s_movk_i32 s68, 1152\n"
"s_lshr_b32 s69, %[nb256], 3\n"
"s_add_u32 s69, s69, 5632\n"
"v_and_b32 v254, 255, %[g0]\n"
"v_add_u32 v254, %[kq64], v254\n"
"v_and_b32 v254, 255, v254\n"
"v_lshrrev_b32 v255, 8, %[g0]\n"
"v_mul_u32_u24 v255, s68, v255\n"
"v_add_u32 v255, %[nb256], v255\n"
"v_add_u32 v249, v254, v255\n"
"v_xor_b32 v254, 32, v254\n"
"v_add_u32 v250, v254, v255\n"
"v_add_u32 v250, 3584, v250\n"
"v_lshrrev_b32 v254, 5, %[l16]\n"
"v_mul_u32_u24 v254, s68, v254\n"
"v_and_b32 v255, 16, %[l16]\n"
"v_add_u32 v254, v254, v255\n"
"v_add_u32 v252, s69, v254\n"
"s_lshr_b32 s68, %[hb], 11\n"
"s_or_b32 s68, s68, %[hb]\n"
"v_and_b32 v244, 0xffff, %[vbe]\n"
"v_xor_b32 v244, s68, v244\n"
"v_xor_b32 v245, 32, v244\n"
"v_lshrrev_b32 v246, 1, %[l16]\n"
"v_add_u32 v246, s90, v246\n"
"v_add_u32 v228, 8192, %[l16]\n"
"buffer_load_dword v248, %[gout], s[80:83], 0 offen sc1\n"
"buffer_load_dword v251, %[gin], s[80:83], 0 offen sc1\n"
"s_add_u32 s68, s90, 16384\n"
"v_add_u32 v247, s68, %[l16]\n"
"s_lshl_b32 s88, %[osoff], 2\n"
"s_mov_b32 m0, s52\n"
"s_nop 0\n"
"buffer_load_dwordx4 v249, s[76:79], s88 offen lds\n"
"buffer_load_dwordx4 v250, s[76:79], s88 offen offset:1024 lds\n"
"s_add_u32 m0, s90, 0\n"
"s_nop 0\n"
"buffer_load_dwordx4 v252, s[76:79], s88 offen lds\n"
"s_lshl_b32 s89, %[osoff], 2\n"
"s_add_u32 s89, s89, 34816\n"
"s_mov_b32 m0, s52\n"
"s_nop 0\n"
"buffer_load_dwordx4 v249, s[76:79], s89 offen offset:2048 lds\n"
"s_lshl_b32 s88, %[osoff], 2\n"
"s_add_u32 s88, s88, 73728\n"
"s_mov_b32 m0, s53\n"
"s_nop 0\n"
"buffer_load_dwordx4 v249, s[76:79], s88 offen lds\n"
"buffer_load_dwordx4 v250, s[76:79], s88 offen offset:1024 lds\n"
"s_lshl_b32 s89, %[osoff], 2\n"
"s_add_u32 s89, s89, 108544\n"
"buffer_load_dwordx4 v249, s[76:79], s89 offen offset:2048 lds\n"
"s_lshl_b32 s88, %[os1], 2\n"
"s_mov_b32 m0, s54\n"
"s_nop 0\n"
"buffer_load_dwordx4 v249, s[76:79], s88 offen lds\n"
"buffer_load_dwordx4 v250, s[76:79], s88 offen offset:1024 lds\n"
"s_add_u32 m0, s90, 4096\n"
"s_nop 0\n"
"buffer_load_dwordx4 v252, s[76:79], s88 offen lds\n"
"s_lshl_b32 s89, %[os1], 2\n"
"s_add_u32 s89, s89, 34816\n"
"s_mov_b32 m0, s54\n"
"s_nop 0\n"
"buffer_load_dwordx4 v249, s[76:79], s89 offen offset:2048 lds\n"
"s_lshl_b32 s88, %[os1], 2\n"
"s_add_u32 s88, s88, 73728\n"
"s_mov_b32 m0, s55\n"
"s_nop 0\n"
"buffer_load_dwordx4 v249, s[76:79], s88 offen lds\n"
"buffer_load_dwordx4 v250, s[76:79], s88 offen offset:1024 lds\n"
"s_lshl_b32 s89, %[os1], 2\n"
"s_add_u32 s89, s89, 108544\n"
"buffer_load_dwordx4 v249, s[76:79], s89 offen offset:2048 lds\n"
"s_add_u32 s60, %[osoff], 0\n"
"buffer_load_dwordx4 v[144:147], %[l16], %[srdo], s60 offen offset:1024 sc1\n"
"buffer_load_dwordx4 v[128:131], %[l16], %[srdo], s60 offen sc1\n"
"buffer_load_dwordx4 v[160:163], v228, %[srdo], s60 offen sc1\n"
"s_add_u32 s61, %[osoff], 9216\n"
"buffer_load_dwordx4 v[148:151], %[l16], %[srdo], s61 offen offset:1024 sc1\n"
"buffer_load_dwordx4 v[132:135], %[l16], %[srdo], s61 offen sc1\n"
"buffer_load_dwordx4 v[164:167], v228, %[srdo], s61 offen sc1\n"
"s_add_u32 s62, %[osoff], 18432\n"
"buffer_load_dwordx4 v[152:155], %[l16], %[srdo], s62 offen offset:1024 sc1\n"
"buffer_load_dwordx4 v[136:139], %[l16], %[srdo], s62 offen sc1\n"
"buffer_load_dwordx4 v[168:171], v228, %[srdo], s62 offen sc1\n"
"s_add_u32 s63, %[osoff], 27648\n"
"buffer_load_dwordx4 v[156:159], %[l16], %[srdo], s63 offen offset:1024 sc1\n"
"buffer_load_dwordx4 v[140:143], %[l16], %[srdo], s63 offen sc1\n"
"buffer_load_dwordx4 v[172:175], v228, %[srdo], s63 offen sc1\n"
"v_mov_b32 v176, 0\n"
"v_mov_b32 v177, 0\n"
"v_mov_b32 v178, 0\n"
"v_mov_b32 v179, 0\n"
"v_mov_b32 v180, 0\n"
"v_mov_b32 v181, 0\n"
"v_mov_b32 v182, 0\n"
"v_mov_b32 v183, 0\n"
"s_waitcnt vmcnt(0)\n"
"s_barrier\n"
"ds_read_b64_tr_b16 v[100:101], v244 offset:0\n"
"ds_read_b64_tr_b16 v[102:103], v244 offset:8192\n"
"ds_read_b64_tr_b16 v[104:105], v245 offset:0\n"
"ds_read_b64_tr_b16 v[106:107], v245 offset:8192\n"
"ds_read_b64_tr_b16 v[108:109], v244 offset:256\n"
"ds_read_b64_tr_b16 v[110:111], v244 offset:8448\n"
"ds_read_b64_tr_b16 v[112:113], v245 offset:256\n"
"ds_read_b64_tr_b16 v[114:115], v245 offset:8448\n"
"ds_read_b64_tr_b16 v[116:117], v244 offset:512\n"
"ds_read_b64_tr_b16 v[118:119], v244 offset:8704\n"
"ds_read_b64_tr_b16 v[120:121], v245 offset:512\n"
"ds_read_b64_tr_b16 v[122:123], v245 offset:8704\n"
"ds_read_b64_tr_b16 v[124:125], v244 offset:768\n"
"ds_read_b64_tr_b16 v[126:127], v244 offset:8960\n"
"s_waitcnt lgkmcnt(0)\n"
"s_nop 7\n"
"s_branch LO0_%=\n"
".p2align 6\n"
"LA0_%=:\n"
"s_cmp_eq_u32 s61, 0x30000\n"
"s_cbranch_scc0 LNd0_%=\n"
"s_getpc_b64 s[94:95]\n"
"LMd0_%=:\n"
"s_add_u32 s94, s94, (LZZ0_%=-16384-LMd0_%=)&0xffffffff\n"
"s_addc_u32 s95, s95, (LZZ0_%=-16384-LMd0_%=)>>32\n"
"v_lshlrev_b32 v236, 4, %[l16]\n"
"s_lshr_b32 s68, %[hb], 9\n"
"v_add_u32 v236, s68, v236\n"
"s_lshl_b32 exec_lo, 0x11111111, %[wrot]\n"
"s_mov_b32 exec_hi, exec_lo\n"
"global_load_dword v253, v236, s[94:95]\n"
"s_mov_b64 exec, -1\n"
"LNd0_%=:\n"
"s_nop 0\n"
"KA0 0,s52,16384\n"
"KA0 16384,s53,32768\n"
"KA0 32768,s54,49152\n"
"KA0 49152,s55,0\n"
"s_cbranch_scc0 LA0_%=\n"
"s_nop 0\n"
"s_barrier\n"
"s_setprio 1\n"
"v_mfma_f32_16x16x32_bf16 v[16:19], v[160:163], v[224:227], v[16:19]\n"
"ds_read_b128 v[128:131], v244 offset:2048\n"
"v_add_f32 v240, v240, v192\n"
"v_add_f32 v241, v241, v196\n"
"v_mfma_f32_16x16x32_bf16 v[48:51], v[160:163], v[228:231], v[48:51]\n"
"ds_read_b128 v[132:135], v244 offset:2304\n"
"v_add_f32 v242, v242, v208\n"
"v_add_f32 v240, v240, v193\n"
"v_mfma_f32_16x16x32_bf16 v[20:23], v[164:167], v[224:227], v[20:23]\n"
"ds_read_b128 v[136:139], v244 offset:2560\n"
"v_add_f32 v241, v241, v197\n"
"v_add_f32 v242, v242, v209\n"
"v_mfma_f32_16x16x32_bf16 v[52:55], v[164:167], v[228:231], v[52:55]\n"
"ds_read_b128 v[140:143], v244 offset:2816\n"
"v_add_f32 v240, v240, v194\n"
"v_add_f32 v241, v241, v198\n"
"v_mfma_f32_16x16x32_bf16 v[24:27], v[168:171], v[224:227], v[24:27]\n"
"ds_read_b128 v[144:147], v244 offset:10240\n"
"v_add_f32 v242, v242, v210\n"
"v_add_f32 v240, v240, v195\n"
"v_mfma_f32_16x16x32_bf16 v[56:59], v[168:171], v[228:231], v[56:59]\n"
"ds_read_b128 v[148:151], v244 offset:10496\n"
"v_add_f32 v241, v241, v199\n"
"v_add_f32 v242, v242, v211\n"
"v_mfma_f32_16x16x32_bf16 v[28:31], v[172:175], v[224:227], v[28:31]\n"
"ds_read_b128 v[152:155], v244 offset:10752\n"
"v_add_f32 v240, v240, v200\n"
"v_add_f32 v241, v241, v204\n"
"v_mfma_f32_16x16x32_bf16 v[60:63], v[172:175], v[228:231], v[60:63]\n"
"ds_read_b128 v[156:159], v244 offset:11008\n"
"v_add_f32 v240, v240, v201\n"
"v_add_f32 v241, v241, v205\n"
"v_mfma_f32_16x16x32_bf16 v[32:35], v[176:179], v[224:227], v[32:35]\n"
"ds_read_b64_tr_b16 v[160:161], v245 offset:0\n"
"v_add_f32 v240, v240, v202\n"
"v_add_f32 v241, v241, v206\n"
"v_mfma_f32_16x16x32_bf16 v[64:67], v[176:179], v[228:231], v[64:67]\n"
"ds_read_b64_tr_b16 v[162:163], v245 offset:8192\n"
"v_add_f32 v240, v240, v203\n"
"v_add_f32 v241, v241, v207\n"
"v_mfma_f32_16x16x32_bf16 v[36:39], v[180:183], v[224:227], v[36:39]\n"
"ds_read_b64_tr_b16 v[164:165], v246 offset:0\n"
"v_mfma_f32_16x16x32_bf16 v[68:71], v[180:183], v[228:231], v[68:71]\n"
"ds_read_b64_tr_b16 v[166:167], v246 offset:8192\n"
"v_mfma_f32_16x16x32_bf16 v[40:43], v[184:187], v[224:227], v[40:43]\n"
"ds_read_b64_tr_b16 v[168:169], v245 offset:256\n"
"v_mfma_f32_16x16x32_bf16 v[72:75], v[184:187], v[228:231], v[72:75]\n"
"ds_read_b64_tr_b16 v[170:171], v245 offset:8448\n"
"v_mfma_f32_16x16x32_bf16 v[44:47], v[188:191], v[224:227], v[44:47]\n"
"ds_read_b64_tr_b16 v[172:173], v246 offset:256\n"
"v_mfma_f32_16x16x32_bf16 v[76:79], v[188:191], v[228:231], v[76:79]\n"
"ds_read_b64_tr_b16 v[174:175], v246 offset:8448\n"
"s_waitcnt lgkmcnt(8)\n"
"v_mfma_f32_16x16x32_bf16 v[208:211], v[128:131], v[80:83], v[220:223]\n"
"ds_read_b64_tr_b16 v[176:177], v245 offset:512\n"
"v_mfma_f32_16x16x32_bf16 v[208:211], v[132:135], v[84:87], v[208:211]\n"
"ds_read_b64_tr_b16 v[178:179], v245 offset:8704\n"
"v_mfma_f32_16x16x32_bf16 v[208:211], v[136:139], v[88:91], v[208:211]\n"
"ds_read_b64_tr_b16 v[180:181], v246 offset:512\n"
"v_mfma_f32_16x16x32_bf16 v[208:211], v[140:143], v[92:95], v[208:211]\n"
"ds_read_b64_tr_b16 v[182:183], v246 offset:8704\n"
"v_mfma_f32_16x16x32_bf16 v[192:195], v[128:131], v[96:99], v[212:215]\n"
"ds_read_b64_tr_b16 v[184:185], v245 offset:768\n"
"v_mfma_f32_16x16x32_bf16 v[192:195], v[132:135], v[100:103], v[192:195]\n"
"ds_read_b64_tr_b16 v[186:187], v245 offset:8960\n"
"v_mfma_f32_16x16x32_bf16 v[192:195], v[136:139], v[104:107], v[192:195]\n"
"ds_read_b64_tr_b16 v[188:189], v246 offset:768\n"
"v_mfma_f32_16x16x32_bf16 v[192:195], v[140:143], v[108:111], v[192:195]\n"
"ds_read_b64_tr_b16 v[190:191], v246 offset:8960\n"
"v_mfma_f32_16x16x32_bf16 v[196:199], v[128:131], v[112:115], v[216:219]\n"
"v_mfma_f32_16x16x32_bf16 v[196:199], v[132:135], v[116:119], v[196:199]\n"
"v_mfma_f32_16x16x32_bf16 v[196:199], v[136:139], v[120:123], v[196:199]\n"
"v_mfma_f32_16x16x32_bf16 v[196:199], v[140:143], v[124:127], v[196:199]\n"
"v_mfma_f32_16x16x32_bf16 v[200:203], v[144:147], v[96:99], v[212:215]\n"
"v_mfma_f32_16x16x32_bf16 v[200:203], v[148:151], v[100:103], v[200:203]\n"
"v_mfma_f32_16x16x32_bf16 v[200:203], v[152:155], v[104:107], v[200:203]\n"
"v_mfma_f32_16x16x32_bf16 v[200:203], v[156:159], v[108:111], v[200:203]\n"
"v_mfma_f32_16x16x32_bf16 v[204:207], v[144:147], v[112:115], v[216:219]\n"
"v_mfma_f32_16x16x32_bf16 v[204:207], v[148:151], v[116:119], v[204:207]\n"
"v_mfma_f32_16x16x32_bf16 v[204:207], v[152:155], v[120:123], v[204:207]\n"
"v_mfma_f32_16x16x32_bf16 v[204:207], v[156:159], v[124:127], v[204:207]\n"
"s_waitcnt lgkmcnt(0)\n"
"s_barrier\n"
"s_setprio 0\n"
"s_lshl_b32 s88, %[osoff], 2\n"
"s_mov_b32 m0, s52\n"
"s_nop 0\n"
"buffer_load_dwordx4 v249, s[76:79], s88 offen lds\n"
"buffer_load_dwordx4 v250, s[76:79], s88 offen offset:1024 lds\n"
"s_add_u32 m0, s90, 0\n"
"s_nop 0\n"
"buffer_load_dwordx4 v252, s[76:79], s88 offen lds\n"
"v_exp_f32 v208, v208\n"
"v_exp_f32 v209, v209\n"
"v_exp_f32 v210, v210\n"
"v_exp_f32_e64 v211, v211\n"
"v_cvt_pk_bf16_f32 v232, v208, v209\n"
"v_cvt_pk_bf16_f32 v233, v210, v211\n"
"ds_write_b64 v247, v[232:233] offset:4096\n"
"v_exp_f32 v192, v192\n"
"v_exp_f32 v193, v193\n"
"v_exp_f32 v194, v194\n"
"v_exp_f32 v195, v195\n"
"v_cvt_pk_bf16_f32 v224, v192, v193\n"
"v_cvt_pk_bf16_f32 v225, v194, v195\n"
"v_exp_f32 v196, v196\n"
"v_exp_f32 v197, v197\n"
"v_exp_f32 v198, v198\n"
"v_exp_f32 v199, v199\n"
"v_cvt_pk_bf16_f32 v228, v196, v197\n"
"v_cvt_pk_bf16_f32 v229, v198, v199\n"
"v_exp_f32 v200, v200\n"
"v_exp_f32 v201, v201\n"
"v_exp_f32 v202, v202\n"
"v_exp_f32 v203, v203\n"
"v_cvt_pk_bf16_f32 v226, v200, v201\n"
"v_cvt_pk_bf16_f32 v227, v202, v203\n"
"v_exp_f32 v204, v204\n"
"v_exp_f32 v205, v205\n"
"v_exp_f32 v206, v206\n"
"v_exp_f32 v207, v207\n"
"v_cvt_pk_bf16_f32 v230, v204, v205\n"
"v_cvt_pk_bf16_f32 v231, v206, v207\n"
"s_waitcnt vmcnt(9) lgkmcnt(0)\n"
"s_barrier\n"
"s_setprio 1\n"
"v_mfma_f32_16x16x32_bf16 v[16:19], v[160:163], v[224:227], v[16:19]\n"
"ds_read_b128 v[128:131], %[kb] offset:18432\n"
"v_add_f32 v240, v240, v192\n"
"v_add_f32_e64 v241, v241, v196\n"
"v_mfma_f32_16x16x32_bf16 v[48:51], v[160:163], v[228:231], v[48:51]\n"
"ds_read_b128 v[132:135], %[kb] offset:18688\n"
"v_add_f32 v242, v242, v208\n"
"v_add_f32 v240, v240, v193\n"
"v_mfma_f32_16x16x32_bf16 v[20:23], v[164:167], v[224:227], v[20:23]\n"
"ds_read_b128 v[136:139], %[kb] offset:18944\n"
"v_add_f32 v241, v241, v197\n"
"v_add_f32 v242, v242, v209\n"
"v_mfma_f32_16x16x32_bf16 v[52:55], v[164:167], v[228:231], v[52:55]\n"
"ds_read_b128 v[140:143], %[kb] offset:19200\n"
"v_add_f32 v240, v240, v194\n"
"v_add_f32 v241, v241, v198\n"
"v_mfma_f32_16x16x32_bf16 v[24:27], v[168:171], v[224:227], v[24:27]\n"
"ds_read_b128 v[144:147], %[kb] offset:26624\n"
"v_add_f32 v242, v242, v210\n"
"v_add_f32 v240, v240, v195\n"
"v_mfma_f32_16x16x32_bf16 v[56:59], v[168:171], v[228:231], v[56:59]\n"
"ds_read_b128 v[148:151], %[kb] offset:26880\n"
"v_add_f32 v241, v241, v199\n"
"v_add_f32 v242, v242, v211\n"
"v_mfma_f32_16x16x32_bf16 v[28:31], v[172:175], v[224:227], v[28:31]\n"
"ds_read_b128 v[152:155], %[kb] offset:27136\n"
"v_add_f32 v240, v240, v200\n"
"v_add_f32 v241, v241, v204\n"
"v_mfma_f32_16x16x32_bf16 v[60:63], v[172:175], v[228:231], v[60:63]\n"
"ds_read_b128 v[156:159], %[kb] offset:27392\n"
"v_add_f32 v240, v240, v201\n"
"v_add_f32 v241, v241, v205\n"
"v_mfma_f32_16x16x32_bf16 v[32:35], v[176:179], v[224:227], v[32:35]\n"
"ds_read_b64_tr_b16 v[160:161], %[vbe] offset:16384\n"
"v_add_f32 v240, v240, v202\n"
"v_add_f32 v241, v241, v206\n"
"v_mfma_f32_16x16x32_bf16 v[64:67], v[176:179], v[228:231], v[64:67]\n"
"ds_read_b64_tr_b16 v[162:163], %[vbe] offset:24576\n"
"v_add_f32 v240, v240, v203\n"
"v_add_f32 v241, v241, v207\n"
"v_mfma_f32_16x16x32_bf16 v[36:39], v[180:183], v[224:227], v[36:39]\n"
"ds_read_b64_tr_b16 v[164:165], v243 offset:16384\n"
"v_mfma_f32_16x16x32_bf16 v[68:71], v[180:183], v[228:231], v[68:71]\n"
"ds_read_b64_tr_b16 v[166:167], v243 offset:24576\n"
"v_mfma_f32_16x16x32_bf16 v[40:43], v[184:187], v[224:227], v[40:43]\n"
"ds_read_b64_tr_b16 v[168:169], %[vbe] offset:16640\n"
"v_mfma_f32_16x16x32_bf16 v[72:75], v[184:187], v[228:231], v[72:75]\n"
"ds_read_b64_tr_b16 v[170:171], %[vbe] offset:24832\n"
"v_mfma_f32_16x16x32_bf16 v[44:47], v[188:191], v[224:227], v[44:47]\n"
"ds_read_b64_tr_b16 v[172:173], v243 offset:16640\n"
"v_mfma_f32_16x16x32_bf16 v[76:79], v[188:191], v[228:231], v[76:79]\n"
"ds_read_b64_tr_b16 v[174:175], v243 offset:24832\n"
"s_waitcnt lgkmcnt(8)\n"
"v_mfma_f32_16x16x32_bf16 v[208:211], v[128:131], v[80:83], v[220:223]\n"
"ds_read_b64_tr_b16 v[176:177], %[vbe] offset:16896\n"
"v_mfma_f32_16x16x32_bf16 v[208:211], v[132:135], v[84:87], v[208:211]\n"
"ds_read_b64_tr_b16 v[178:179], %[vbe] offset:25088\n"
"v_mfma_f32_16x16x32_bf16 v[208:211], v[136:139], v[88:91], v[208:211]\n"
"ds_read_b64_tr_b16 v[180:181], v243 offset:16896\n"
"v_mfma_f32_16x16x32_bf16 v[208:211], v[140:143], v[92:95], v[208:211]\n"
"ds_read_b64_tr_b16 v[182:183], v243 offset:25088\n"
"v_mfma_f32_16x16x32_bf16 v[192:195], v[128:131], v[96:99], v[212:215]\n"
"ds_read_b64_tr_b16 v[184:185], %[vbe] offset:17152\n"
"v_mfma_f32_16x16x32_bf16 v[192:195], v[132:135], v[100:103], v[192:195]\n"
"ds_read_b64_tr_b16 v[186:187], %[vbe] offset:25344\n"
"v_mfma_f32_16x16x32_bf16 v[192:195], v[136:139], v[104:107], v[192:195]\n"
"ds_read_b64_tr_b16 v[188:189], v243 offset:17152\n"
"v_mfma_f32_16x16x32_bf16 v[192:195], v[140:143], v[108:111], v[192:195]\n"
"ds_read_b64_tr_b16 v[190:191], v243 offset:25344\n"
"v_mfma_f32_16x16x32_bf16 v[196:199], v[128:131], v[112:115], v[216:219]\n"
"v_mfma_f32_16x16x32_bf16 v[196:199], v[132:135], v[116:119], v[196:199]\n"
"v_mfma_f32_16x16x32_bf16 v[196:199], v[136:139], v[120:123], v[196:199]\n"
"v_mfma_f32_16x16x32_bf16 v[196:199], v[140:143], v[124:127], v[196:199]\n"
"v_mfma_f32_16x16x32_bf16 v[200:203], v[144:147], v[96:99], v[212:215]\n"
"v_mfma_f32_16x16x32_bf16 v[200:203], v[148:151], v[100:103], v[200:203]\n"
"v_mfma_f32_16x16x32_bf16 v[200:203], v[152:155], v[104:107], v[200:203]\n"
"v_mfma_f32_16x16x32_bf16 v[200:203], v[156:159], v[108:111], v[200:203]\n"
"v_mfma_f32_16x16x32_bf16 v[204:207], v[144:147], v[112:115], v[216:219]\n"
"v_mfma_f32_16x16x32_bf16 v[204:207], v[148:151], v[116:119], v[204:207]\n"
"v_mfma_f32_16x16x32_bf16 v[204:207], v[152:155], v[120:123], v[204:207]\n"
"v_mfma_f32_16x16x32_bf16 v[204:207], v[156:159], v[124:127], v[204:207]\n"
"s_waitcnt lgkmcnt(0)\n"
"s_barrier\n"
"s_setprio 0\n"
"s_lshl_b32 s89, %[osoff], 2\n"
"s_add_u32 s89, s89, 34816\n"
"s_mov_b32 m0, s52\n"
"s_nop 0\n"
"buffer_load_dwordx4 v249, s[76:79], s89 offen offset:2048 lds\n"
"v_exp_f32 v208, v208\n"
"v_exp_f32 v209, v209\n"
"v_exp_f32 v210, v210\n"
"v_exp_f32_e64 v211, v211\n"
"v_cvt_pk_bf16_f32 v232, v208, v209\n"
"v_cvt_pk_bf16_f32 v233, v210, v211\n"
"ds_write_b64 v247, v[232:233] offset:4096\n"
"v_exp_f32 v192, v192\n"
"v_exp_f32 v193, v193\n"
"v_exp_f32 v194, v194\n"
"v_exp_f32 v195, v195\n"
"v_cvt_pk_bf16_f32 v224, v192, v193\n"
"v_cvt_pk_bf16_f32 v225, v194, v195\n"
"v_exp_f32 v196, v196\n"
"v_exp_f32 v197, v197\n"
"v_exp_f32 v198, v198\n"
"v_exp_f32 v199, v199\n"
"v_cvt_pk_bf16_f32 v228, v196, v197\n"
"v_cvt_pk_bf16_f32 v229, v198, v199\n"
"v_exp_f32 v200, v200\n"
"v_exp_f32 v201, v201\n"
"v_exp_f32 v202, v202\n"
"v_exp_f32 v203, v203\n"
"v_cvt_pk_bf16_f32 v226, v200, v201\n"
"v_cvt_pk_bf16_f32 v227, v202, v203\n"
"v_exp_f32 v204, v204\n"
"v_exp_f32 v205, v205\n"
"v_exp_f32 v206, v206\n"
"v_exp_f32 v207, v207\n"
"v_cvt_pk_bf16_f32 v230, v204, v205\n"
"v_cvt_pk_bf16_f32 v231, v206, v207\n"
"s_waitcnt lgkmcnt(0)\n"
"s_barrier\n"
"s_setprio 1\n"
"v_mfma_f32_16x16x32_bf16 v[16:19], v[160:163], v[224:227], v[16:19]\n"
"ds_read_b128 v[128:131], v244 offset:18432\n"
"v_add_f32 v240, v240, v192\n"
"v_add_f32_e64 v241, v241, v196\n"
"v_mfma_f32_16x16x32_bf16 v[48:51], v[160:163], v[228:231], v[48:51]\n"
"ds_read_b128 v[132:135], v244 offset:18688\n"
"v_add_f32 v242, v242, v208\n"
"v_add_f32 v240, v240, v193\n"
"v_mfma_f32_16x16x32_bf16 v[20:23], v[164:167], v[224:227], v[20:23]\n"
"ds_read_b128 v[136:139], v244 offset:18944\n"
"v_add_f32 v241, v241, v197\n"
"v_add_f32 v242, v242, v209\n"
"v_mfma_f32_16x16x32_bf16 v[52:55], v[164:167], v[228:231], v[52:55]\n"
"ds_read_b128 v[140:143], v244 offset:19200\n"
"v_add_f32 v240, v240, v194\n"
"v_add_f32 v241, v241, v198\n"
"v_mfma_f32_16x16x32_bf16 v[24:27], v[168:171], v[224:227], v[24:27]\n"
"ds_read_b128 v[144:147], v244 offset:26624\n"
"v_add_f32 v242, v242, v210\n"
"v_add_f32 v240, v240, v195\n"
"v_mfma_f32_16x16x32_bf16 v[56:59], v[168:171], v[228:231], v[56:59]\n"
"ds_read_b128 v[148:151], v244 offset:26880\n"
"v_add_f32 v241, v241, v199\n"
"v_add_f32 v242, v242, v211\n"
"v_mfma_f32_16x16x32_bf16 v[28:31], v[172:175], v[224:227], v[28:31]\n"
"ds_read_b128 v[152:155], v244 offset:27136\n"
"v_add_f32 v240, v240, v200\n"
"v_add_f32 v241, v241, v204\n"
"v_mfma_f32_16x16x32_bf16 v[60:63], v[172:175], v[228:231], v[60:63]\n"
"ds_read_b128 v[156:159], v244 offset:27392\n"
"v_add_f32 v240, v240, v201\n"
"v_add_f32 v241, v241, v205\n"
"v_mfma_f32_16x16x32_bf16 v[32:35], v[176:179], v[224:227], v[32:35]\n"
"ds_read_b64_tr_b16 v[160:161], v245 offset:16384\n"
"v_add_f32 v240, v240, v202\n"
"v_add_f32 v241, v241, v206\n"
"v_mfma_f32_16x16x32_bf16 v[64:67], v[176:179], v[228:231], v[64:67]\n"
"ds_read_b64_tr_b16 v[162:163], v245 offset:24576\n"
"v_add_f32 v240, v240, v203\n"
"v_add_f32 v241, v241, v207\n"
"v_mfma_f32_16x16x32_bf16 v[36:39], v[180:183], v[224:227], v[36:39]\n"
"ds_read_b64_tr_b16 v[164:165], v246 offset:16384\n"
"v_mfma_f32_16x16x32_bf16 v[68:71], v[180:183], v[228:231], v[68:71]\n"
"ds_read_b64_tr_b16 v[166:167], v246 offset:24576\n"
"v_mfma_f32_16x16x32_bf16 v[40:43], v[184:187], v[224:227], v[40:43]\n"
"ds_read_b64_tr_b16 v[168:169], v245 offset:16640\n"
"v_mfma_f32_16x16x32_bf16 v[72:75], v[184:187], v[228:231], v[72:75]\n"
"ds_read_b64_tr_b16 v[170:171], v245 offset:24832\n"
"v_mfma_f32_16x16x32_bf16 v[44:47], v[188:191], v[224:227], v[44:47]\n"
"ds_read_b64_tr_b16 v[172:173], v246 offset:16640\n"
"v_mfma_f32_16x16x32_bf16 v[76:79], v[188:191], v[228:231], v[76:79]\n"
"ds_read_b64_tr_b16 v[174:175], v246 offset:24832\n"
"s_waitcnt lgkmcnt(8)\n"
"v_mfma_f32_16x16x32_bf16 v[208:211], v[128:131], v[80:83], v[220:223]\n"
"ds_read_b64_tr_b16 v[176:177], v245 offset:16896\n"
"v_mfma_f32_16x16x32_bf16 v[208:211], v[132:135], v[84:87], v[208:211]\n"
"ds_read_b64_tr_b16 v[178:179], v245 offset:25088\n"
"v_mfma_f32_16x16x32_bf16 v[208:211], v[136:139], v[88:91], v[208:211]\n"
"ds_read_b64_tr_b16 v[180:181], v246 offset:16896\n"
"v_mfma_f32_16x16x32_bf16 v[208:211], v[140:143], v[92:95], v[208:211]\n"
"ds_read_b64_tr_b16 v[182:183], v246 offset:25088\n"
"v_mfma_f32_16x16x32_bf16 v[192:195], v[128:131], v[96:99], v[212:215]\n"
"ds_read_b64_tr_b16 v[184:185], v245 offset:17152\n"
"v_mfma_f32_16x16x32_bf16 v[192:195], v[132:135], v[100:103], v[192:195]\n"
"ds_read_b64_tr_b16 v[186:187], v245 offset:25344\n"
"v_mfma_f32_16x16x32_bf16 v[192:195], v[136:139], v[104:107], v[192:195]\n"
"ds_read_b64_tr_b16 v[188:189], v246 offset:17152\n"
"v_mfma_f32_16x16x32_bf16 v[192:195], v[140:143], v[108:111], v[192:195]\n"
"ds_read_b64_tr_b16 v[190:191], v246 offset:25344\n"
"v_mfma_f32_16x16x32_bf16 v[196:199], v[128:131], v[112:115], v[216:219]\n"
"v_mfma_f32_16x16x32_bf16 v[196:199], v[132:135], v[116:119], v[196:199]\n"
"v_mfma_f32_16x16x32_bf16 v[196:199], v[136:139], v[120:123], v[196:199]\n"
"v_mfma_f32_16x16x32_bf16 v[196:199], v[140:143], v[124:127], v[196:199]\n"
"v_mfma_f32_16x16x32_bf16 v[200:203], v[144:147], v[96:99], v[212:215]\n"
"v_mfma_f32_16x16x32_bf16 v[200:203], v[148:151], v[100:103], v[200:203]\n"
"v_mfma_f32_16x16x32_bf16 v[200:203], v[152:155], v[104:107], v[200:203]\n"
"v_mfma_f32_16x16x32_bf16 v[200:203], v[156:159], v[108:111], v[200:203]\n"
"v_mfma_f32_16x16x32_bf16 v[204:207], v[144:147], v[112:115], v[216:219]\n"
"v_mfma_f32_16x16x32_bf16 v[204:207], v[148:151], v[116:119], v[204:207]\n"
"v_mfma_f32_16x16x32_bf16 v[204:207], v[152:155], v[120:123], v[204:207]\n"
"v_mfma_f32_16x16x32_bf16 v[204:207], v[156:159], v[124:127], v[204:207]\n"
"s_waitcnt lgkmcnt(0)\n"
"s_barrier\n"
"s_setprio 0\n"
"s_lshl_b32 s88, %[osoff], 2\n"
"s_add_u32 s88, s88, 73728\n"
"s_mov_b32 m0, s53\n"
"s_nop 0\n"
"buffer_load_dwordx4 v249, s[76:79], s88 offen lds\n"
"buffer_load_dwordx4 v250, s[76:79], s88 offen offset:1024 lds\n"
"v_exp_f32 v208, v208\n"
"v_exp_f32 v209, v209\n"
"v_exp_f32 v210, v210\n"
"v_exp_f32_e64 v211, v211\n"
"v_cvt_pk_bf16_f32 v232, v208, v209\n"
"v_cvt_pk_bf16_f32 v233, v210, v211\n"
"ds_write_b64 v247, v[232:233] offset:4096\n"
"v_exp_f32 v192, v192\n"
"v_exp_f32 v193, v193\n"
"v_exp_f32 v194, v194\n"
"v_exp_f32 v195, v195\n"
"v_cvt_pk_bf16_f32 v224, v192, v193\n"
"v_cvt_pk_bf16_f32 v225, v194, v195\n"
"v_exp_f32 v196, v196\n"
"v_exp_f32 v197, v197\n"
"v_exp_f32 v198, v198\n"
"v_exp_f32 v199, v199\n"
"v_cvt_pk_bf16_f32 v228, v196, v197\n"
"v_cvt_pk_bf16_f32 v229, v198, v199\n"
"v_exp_f32 v200, v200\n"
"v_exp_f32 v201, v201\n"
"v_exp_f32 v202, v202\n"
"v_exp_f32 v203, v203\n"
"v_cvt_pk_bf16_f32 v226, v200, v201\n"
"v_cvt_pk_bf16_f32 v227, v202, v203\n"
"v_exp_f32 v204, v204\n"
"v_exp_f32 v205, v205\n"
"v_exp_f32 v206, v206\n"
"v_exp_f32 v207, v207\n"
"v_cvt_pk_bf16_f32 v230, v204, v205\n"
"v_cvt_pk_bf16_f32 v231, v206, v207\n"
"s_waitcnt vmcnt(9) lgkmcnt(0)\n"
"s_barrier\n"
"s_setprio 1\n"
"v_mfma_f32_16x16x32_bf16 v[16:19], v[160:163], v[224:227], v[16:19]\n"
"ds_read_b128 v[128:131], %[kb] offset:34816\n"
"v_add_f32 v240, v240, v192\n"
"v_add_f32_e64 v241, v241, v196\n"
"v_mfma_f32_16x16x32_bf16 v[48:51], v[160:163], v[228:231], v[48:51]\n"
"ds_read_b128 v[132:135], %[kb] offset:35072\n"
"v_add_f32 v242, v242, v208\n"
"v_add_f32 v240, v240, v193\n"
"v_mfma_f32_16x16x32_bf16 v[20:23], v[164:167], v[224:227], v[20:23]\n"
"ds_read_b128 v[136:139], %[kb] offset:35328\n"
"v_add_f32 v241, v241, v197\n"
"v_add_f32 v242, v242, v209\n"
"v_mfma_f32_16x16x32_bf16 v[52:55], v[164:167], v[228:231], v[52:55]\n"
"ds_read_b128 v[140:143], %[kb] offset:35584\n"
"v_add_f32 v240, v240, v194\n"
"v_add_f32 v241, v241, v198\n"
"v_mfma_f32_16x16x32_bf16 v[24:27], v[168:171], v[224:227], v[24:27]\n"
"ds_read_b128 v[144:147], %[kb] offset:43008\n"
"v_add_f32 v242, v242, v210\n"
"v_add_f32 v240, v240, v195\n"
"v_mfma_f32_16x16x32_bf16 v[56:59], v[168:171], v[228:231], v[56:59]\n"
"ds_read_b128 v[148:151], %[kb] offset:43264\n"
"v_add_f32 v241, v241, v199\n"
"v_add_f32 v242, v242, v211\n"
"v_mfma_f32_16x16x32_bf16 v[28:31], v[172:175], v[224:227], v[28:31]\n"
"ds_read_b128 v[152:155], %[kb] offset:43520\n"
"v_add_f32 v240, v240, v200\n"
"v_add_f32 v241, v241, v204\n"
"v_mfma_f32_16x16x32_bf16 v[60:63], v[172:175], v[228:231], v[60:63]\n"
"ds_read_b128 v[156:159], %[kb] offset:43776\n"
"v_add_f32 v240, v240, v201\n"
"v_add_f32 v241, v241, v205\n"
"v_mfma_f32_16x16x32_bf16 v[32:35], v[176:179], v[224:227], v[32:35]\n"
"ds_read_b64_tr_b16 v[160:161], %[vbe] offset:32768\n"
"v_add_f32 v240, v240, v202\n"
"v_add_f32 v241, v241, v206\n"
"v_mfma_f32_16x16x32_bf16 v[64:67], v[176:179], v[228:231], v[64:67]\n"
"ds_read_b64_tr_b16 v[162:163], %[vbe] offset:40960\n"
"v_add_f32 v240, v240, v203\n"
"v_add_f32 v241, v241, v207\n"
"v_mfma_f32_16x16x32_bf16 v[36:39], v[180:183], v[224:227], v[36:39]\n"
"ds_read_b64_tr_b16 v[164:165], v243 offset:32768\n"
"v_mfma_f32_16x16x32_bf16 v[68:71], v[180:183], v[228:231], v[68:71]\n"
"ds_read_b64_tr_b16 v[166:167], v243 offset:40960\n"
"v_mfma_f32_16x16x32_bf16 v[40:43], v[184:187], v[224:227], v[40:43]\n"
"ds_read_b64_tr_b16 v[168:169], %[vbe] offset:33024\n"
"v_mfma_f32_16x16x32_bf16 v[72:75], v[184:187], v[228:231], v[72:75]\n"
"ds_read_b64_tr_b16 v[170:171], %[vbe] offset:41216\n"
"v_mfma_f32_16x16x32_bf16 v[44:47], v[188:191], v[224:227], v[44:47]\n"
"ds_read_b64_tr_b16 v[172:173], v243 offset:33024\n"
"v_mfma_f32_16x16x32_bf16 v[76:79], v[188:191], v[228:231], v[76:79]\n"
"ds_read_b64_tr_b16 v[174:175], v243 offset:41216\n"
"s_waitcnt lgkmcnt(8)\n"
"v_mfma_f32_16x16x32_bf16 v[208:211], v[128:131], v[80:83], v[220:223]\n"
"ds_read_b64_tr_b16 v[176:177], %[vbe] offset:33280\n"
"v_mfma_f32_16x16x32_bf16 v[208:211], v[132:135], v[84:87], v[208:211]\n"
"ds_read_b64_tr_b16 v[178:179], %[vbe] offset:41472\n"
"v_mfma_f32_16x16x32_bf16 v[208:211], v[136:139], v[88:91], v[208:211]\n"
"ds_read_b64_tr_b16 v[180:181], v243 offset:33280\n"
"v_mfma_f32_16x16x32_bf16 v[208:211], v[140:143], v[92:95], v[208:211]\n"
"ds_read_b64_tr_b16 v[182:183], v243 offset:41472\n"
"v_mfma_f32_16x16x32_bf16 v[192:195], v[128:131], v[96:99], v[212:215]\n"
"ds_read_b64_tr_b16 v[184:185], %[vbe] offset:33536\n"
"v_mfma_f32_16x16x32_bf16 v[192:195], v[132:135], v[100:103], v[192:195]\n"
"ds_read_b64_tr_b16 v[186:187], %[vbe] offset:41728\n"
"v_mfma_f32_16x16x32_bf16 v[192:195], v[136:139], v[104:107], v[192:195]\n"
"ds_read_b64_tr_b16 v[188:189], v243 offset:33536\n"
"v_mfma_f32_16x16x32_bf16 v[192:195], v[140:143], v[108:111], v[192:195]\n"
"ds_read_b64_tr_b16 v[190:191], v243 offset:41728\n"
"v_mfma_f32_16x16x32_bf16 v[196:199], v[128:131], v[112:115], v[216:219]\n"
"v_mfma_f32_16x16x32_bf16 v[196:199], v[132:135], v[116:119], v[196:199]\n"
"v_mfma_f32_16x16x32_bf16 v[196:199], v[136:139], v[120:123], v[196:199]\n"
"v_mfma_f32_16x16x32_bf16 v[196:199], v[140:143], v[124:127], v[196:199]\n"
"v_mfma_f32_16x16x32_bf16 v[200:203], v[144:147], v[96:99], v[212:215]\n"
"v_mfma_f32_16x16x32_bf16 v[200:203], v[148:151], v[100:103], v[200:203]\n"
"v_mfma_f32_16x16x32_bf16 v[200:203], v[152:155], v[104:107], v[200:203]\n"
"v_mfma_f32_16x16x32_bf16 v[200:203], v[156:159], v[108:111], v[200:203]\n"
"v_mfma_f32_16x16x32_bf16 v[204:207], v[144:147], v[112:115], v[216:219]\n"
"v_mfma_f32_16x16x32_bf16 v[204:207], v[148:151], v[116:119], v[204:207]\n"
"v_mfma_f32_16x16x32_bf16 v[204:207], v[152:155], v[120:123], v[204:207]\n"
"v_mfma_f32_16x16x32_bf16 v[204:207], v[156:159], v[124:127], v[204:207]\n"
"s_waitcnt lgkmcnt(0)\n"
"s_barrier\n"
"s_setprio 0\n"
"s_lshl_b32 s89, %[osoff], 2\n"
"s_add_u32 s89, s89, 108544\n"
"buffer_load_dwordx4 v249, s[76:79], s89 offen offset:2048 lds\n"
"v_exp_f32 v208, v208\n"
"v_exp_f32 v209, v209\n"
"v_exp_f32 v210, v210\n"
"v_exp_f32_e64 v211, v211\n"
"v_cvt_pk_bf16_f32 v232, v208, v209\n"
"v_cvt_pk_bf16_f32 v233, v210, v211\n"
"ds_write_b64 v247, v[232:233] offset:4096\n"
"v_exp_f32 v192, v192\n"
"v_exp_f32 v193, v193\n"
"v_exp_f32 v194, v194\n"
"v_exp_f32 v195, v195\n"
"v_cvt_pk_bf16_f32 v224, v192, v193\n"
"v_cvt_pk_bf16_f32 v225, v194, v195\n"
"v_exp_f32 v196, v196\n"
"v_exp_f32 v197, v197\n"
"v_exp_f32 v198, v198\n"
"v_exp_f32 v199, v199\n"
"v_cvt_pk_bf16_f32 v228, v196, v197\n"
"v_cvt_pk_bf16_f32 v229, v198, v199\n"
"v_exp_f32 v200, v200\n"
"v_exp_f32 v201, v201\n"
"v_exp_f32 v202, v202\n"
"v_exp_f32 v203, v203\n"
"v_cvt_pk_bf16_f32 v226, v200, v201\n"
"v_cvt_pk_bf16_f32 v227, v202, v203\n"
"v_exp_f32 v204, v204\n"
"v_exp_f32 v205, v205\n"
"v_exp_f32 v206, v206\n"
"v_exp_f32 v207, v207\n"
"v_cvt_pk_bf16_f32 v230, v204, v205\n"
"v_cvt_pk_bf16_f32 v231, v206, v207\n"
"s_waitcnt lgkmcnt(0)\n"
"s_barrier\n"
"s_setprio 1\n"
"v_mfma_f32_16x16x32_bf16 v[16:19], v[160:163], v[224:227], v[16:19]\n"
"ds_read_b128 v[128:131], v244 offset:34816\n"
"v_add_f32 v240, v240, v192\n"
"v_add_f32_e64 v241, v241, v196\n"
"v_mfma_f32_16x16x32_bf16 v[48:51], v[160:163], v[228:231], v[48:51]\n"
"ds_read_b128 v[132:135], v244 offset:35072\n"
"v_add_f32 v242, v242, v208\n"
"v_add_f32 v240, v240, v193\n"
"v_mfma_f32_16x16x32_bf16 v[20:23], v[164:167], v[224:227], v[20:23]\n"
"ds_read_b128 v[136:139], v244 offset:35328\n"
"v_add_f32 v241, v241, v197\n"
"v_add_f32 v242, v242, v209\n"
"v_mfma_f32_16x16x32_bf16 v[52:55], v[164:167], v[228:231], v[52:55]\n"
"ds_read_b128 v[140:143], v244 offset:35584\n"
"v_add_f32 v240, v240, v194\n"
"v_add_f32 v241, v241, v198\n"
"v_mfma_f32_16x16x32_bf16 v[24:27], v[168:171], v[224:227], v[24:27]\n"
"ds_read_b128 v[144:147], v244 offset:43008\n"
"v_add_f32 v242, v242, v210\n"
"v_add_f32 v240, v240, v195\n"
"v_mfma_f32_16x16x32_bf16 v[56:59], v[168:171], v[228:231], v[56:59]\n"
"ds_read_b128 v[148:151], v244 offset:43264\n"
"v_add_f32 v241, v241, v199\n"
"v_add_f32 v242, v242, v211\n"
"v_mfma_f32_16x16x32_bf16 v[28:31], v[172:175], v[224:227], v[28:31]\n"
"ds_read_b128 v[152:155], v244 offset:43520\n"
"v_add_f32 v240, v240, v200\n"
"v_add_f32 v241, v241, v204\n"
"v_mfma_f32_16x16x32_bf16 v[60:63], v[172:175], v[228:231], v[60:63]\n"
"ds_read_b128 v[156:159], v244 offset:43776\n"
"v_add_f32 v240, v240, v201\n"
"v_add_f32 v241, v241, v205\n"
"v_mfma_f32_16x16x32_bf16 v[32:35], v[176:179], v[224:227], v[32:35]\n"
"ds_read_b64_tr_b16 v[160:161], v245 offset:32768\n"
"v_add_f32 v240, v240, v202\n"
"v_add_f32 v241, v241, v206\n"
"v_mfma_f32_16x16x32_bf16 v[64:67], v[176:179], v[228:231], v[64:67]\n"
"ds_read_b64_tr_b16 v[162:163], v245 offset:40960\n"
"v_add_f32 v240, v240, v203\n"
"v_add_f32 v241, v241, v207\n"
"v_mfma_f32_16x16x32_bf16 v[36:39], v[180:183], v[224:227], v[36:39]\n"
"ds_read_b64_tr_b16 v[164:165], v246 offset:32768\n"
"v_mfma_f32_16x16x32_bf16 v[68:71], v[180:183], v[228:231], v[68:71]\n"
"ds_read_b64_tr_b16 v[166:167], v246 offset:40960\n"
"v_mfma_f32_16x16x32_bf16 v[40:43], v[184:187], v[224:227], v[40:43]\n"
"ds_read_b64_tr_b16 v[168:169], v245 offset:33024\n"
"v_mfma_f32_16x16x32_bf16 v[72:75], v[184:187], v[228:231], v[72:75]\n"
"ds_read_b64_tr_b16 v[170:171], v245 offset:41216\n"
"v_mfma_f32_16x16x32_bf16 v[44:47], v[188:191], v[224:227], v[44:47]\n"
"ds_read_b64_tr_b16 v[172:173], v246 offset:33024\n"
"v_mfma_f32_16x16x32_bf16 v[76:79], v[188:191], v[228:231], v[76:79]\n"
"ds_read_b64_tr_b16 v[174:175], v246 offset:41216\n"
"s_waitcnt lgkmcnt(8)\n"
"v_mfma_f32_16x16x32_bf16 v[208:211], v[128:131], v[80:83], v[220:223]\n"
"ds_read_b64_tr_b16 v[176:177], v245 offset:33280\n"
"v_mfma_f32_16x16x32_bf16 v[208:211], v[132:135], v[84:87], v[208:211]\n"
"ds_read_b64_tr_b16 v[178:179], v245 offset:41472\n"
"v_mfma_f32_16x16x32_bf16 v[208:211], v[136:139], v[88:91], v[208:211]\n"
"ds_read_b64_tr_b16 v[180:181], v246 offset:33280\n"
"v_mfma_f32_16x16x32_bf16 v[208:211], v[140:143], v[92:95], v[208:211]\n"
"ds_read_b64_tr_b16 v[182:183], v246 offset:41472\n"
"v_mfma_f32_16x16x32_bf16 v[192:195], v[128:131], v[96:99], v[212:215]\n"
"ds_read_b64_tr_b16 v[184:185], v245 offset:33536\n"
"v_mfma_f32_16x16x32_bf16 v[192:195], v[132:135], v[100:103], v[192:195]\n"
"ds_read_b64_tr_b16 v[186:187], v245 offset:41728\n"
"v_mfma_f32_16x16x32_bf16 v[192:195], v[136:139], v[104:107], v[192:195]\n"
"ds_read_b64_tr_b16 v[188:189], v246 offset:33536\n"
"v_mfma_f32_16x16x32_bf16 v[192:195], v[140:143], v[108:111], v[192:195]\n"
"ds_read_b64_tr_b16 v[190:191], v246 offset:41728\n"
"v_mfma_f32_16x16x32_bf16 v[196:199], v[128:131], v[112:115], v[216:219]\n"
"v_mfma_f32_16x16x32_bf16 v[196:199], v[132:135], v[116:119], v[196:199]\n"
"v_mfma_f32_16x16x32_bf16 v[196:199], v[136:139], v[120:123], v[196:199]\n"
"v_mfma_f32_16x16x32_bf16 v[196:199], v[140:143], v[124:127], v[196:199]\n"
"v_mfma_f32_16x16x32_bf16 v[200:203], v[144:147], v[96:99], v[212:215]\n"
"v_mfma_f32_16x16x32_bf16 v[200:203], v[148:151], v[100:103], v[200:203]\n"
"v_mfma_f32_16x16x32_bf16 v[200:203], v[152:155], v[104:107], v[200:203]\n"
"v_mfma_f32_16x16x32_bf16 v[200:203], v[156:159], v[108:111], v[200:203]\n"
"v_mfma_f32_16x16x32_bf16 v[204:207], v[144:147], v[112:115], v[216:219]\n"
"v_mfma_f32_16x16x32_bf16 v[204:207], v[148:151], v[116:119], v[204:207]\n"
"v_mfma_f32_16x16x32_bf16 v[204:207], v[152:155], v[120:123], v[204:207]\n"
"v_mfma_f32_16x16x32_bf16 v[204:207], v[156:159], v[124:127], v[204:207]\n"
"s_waitcnt lgkmcnt(0)\n"
"s_barrier\n"
"s_setprio 0\n"
"s_getpc_b64 s[94:95]\n"
"LMa0_%=:\n"
"s_add_u32 s94, s94, (LZZ0_%=-16384-LMa0_%=)&0xffffffff\n"
"s_addc_u32 s95, s95, (LZZ0_%=-16384-LMa0_%=)>>32\n"
"v_lshlrev_b32 v236, 4, %[l16]\n"
"s_lshr_b32 s68, %[hb], 9\n"
"v_add_u32 v236, s68, v236\n"
"s_lshl_b32 exec_lo, 0x11111111, %[wrot]\n"
"s_mov_b32 exec_hi, exec_lo\n"
"global_load_dword v253, v236, s[94:95]\n"
"s_mov_b64 exec, -1\n"
"s_lshl_b32 s88, %[os1], 2\n"
"s_mov_b32 m0, s54\n"
"s_nop 0\n"
"buffer_load_dwordx4 v249, s[76:79], s88 offen lds\n"
"buffer_load_dwordx4 v250, s[76:79], s88 offen offset:1024 lds\n"
"s_add_u32 m0, s90, 4096\n"
"s_nop 0\n"
"buffer_load_dwordx4 v252, s[76:79], s88 offen lds\n"
"v_exp_f32 v208, v208\n"
"v_exp_f32 v209, v209\n"
"v_exp_f32 v210, v210\n"
"v_exp_f32 v211, v211\n"
"v_cvt_pk_bf16_f32 v232, v208, v209\n"
"v_cvt_pk_bf16_f32 v233, v210, v211\n"
"ds_write_b64 v247, v[232:233] offset:4096\n"
"v_exp_f32 v192, v192\n"
"v_exp_f32 v193, v193\n"
"v_exp_f32 v194, v194\n"
"v_exp_f32 v195, v195\n"
"v_cvt_pk_bf16_f32 v224, v192, v193\n"
"v_cvt_pk_bf16_f32 v225, v194, v195\n"
"v_exp_f32 v196, v196\n"
"v_exp_f32 v197, v197\n"
"v_exp_f32 v198, v198\n"
"v_exp_f32 v199, v199\n"
"v_cvt_pk_bf16_f32 v228, v196, v197\n"
"v_cvt_pk_bf16_f32 v229, v198, v199\n"
"v_exp_f32 v200, v200\n"
"v_exp_f32 v201, v201\n"
"v_exp_f32 v202, v202\n"
"v_exp_f32 v203, v203\n"
"v_cvt_pk_bf16_f32 v226, v200, v201\n"
"v_cvt_pk_bf16_f32 v227, v202, v203\n"
"v_exp_f32 v204, v204\n"
"v_exp_f32 v205, v205\n"
"v_exp_f32 v206, v206\n"
"v_exp_f32 v207, v207\n"
"v_cvt_pk_bf16_f32 v230, v204, v205\n"
"v_cvt_pk_bf16_f32 v231, v206, v207\n"
"s_waitcnt vmcnt(10) lgkmcnt(0)\n"
"s_barrier\n"
"s_setprio 1\n"
"v_mfma_f32_16x16x32_bf16 v[16:19], v[160:163], v[224:227], v[16:19]\n"
"ds_read_b128 v[128:131], %[kb] offset:51200\n"
"v_add_f32 v240, v240, v192\n"
"v_add_f32_e64 v241, v241, v196\n"
"v_mfma_f32_16x16x32_bf16 v[48:51], v[160:163], v[228:231], v[48:51]\n"
"ds_read_b128 v[132:135], %[kb] offset:51456\n"
"v_add_f32 v242, v242, v208\n"
"v_add_f32 v240, v240, v193\n"
"v_mfma_f32_16x16x32_bf16 v[20:23], v[164:167], v[224:227], v[20:23]\n"
"ds_read_b128 v[136:139], %[kb] offset:51712\n"
"v_add_f32 v241, v241, v197\n"
"v_add_f32 v242, v242, v209\n"
"v_mfma_f32_16x16x32_bf16 v[52:55], v[164:167], v[228:231], v[52:55]\n"
"ds_read_b128 v[140:143], %[kb] offset:51968\n"
"v_add_f32 v240, v240, v194\n"
"v_add_f32 v241, v241, v198\n"
"v_mfma_f32_16x16x32_bf16 v[24:27], v[168:171], v[224:227], v[24:27]\n"
"ds_read_b128 v[144:147], %[kb] offset:59392\n"
"v_add_f32 v242, v242, v210\n"
"v_add_f32 v240, v240, v195\n"
"v_mfma_f32_16x16x32_bf16 v[56:59], v[168:171], v[228:231], v[56:59]\n"
"ds_read_b128 v[148:151], %[kb] offset:59648\n"
"v_add_f32 v241, v241, v199\n"
"v_add_f32 v242, v242, v211\n"
"v_mfma_f32_16x16x32_bf16 v[28:31], v[172:175], v[224:227], v[28:31]\n"
"ds_read_b128 v[152:155], %[kb] offset:59904\n"
"v_add_f32 v240, v240, v200\n"
"v_add_f32 v241, v241, v204\n"
"v_mfma_f32_16x16x32_bf16 v[60:63], v[172:175], v[228:231], v[60:63]\n"
"ds_read_b128 v[156:159], %[kb] offset:60160\n"
"v_add_f32 v240, v240, v201\n"
"v_add_f32 v241, v241, v205\n"
"v_mfma_f32_16x16x32_bf16 v[32:35], v[176:179], v[224:227], v[32:35]\n"
"ds_read_b64_tr_b16 v[160:161], %[vbe] offset:49152\n"
"v_add_f32 v240, v240, v202\n"
"v_add_f32 v241, v241, v206\n"
"v_mfma_f32_16x16x32_bf16 v[64:67], v[176:179], v[228:231], v[64:67]\n"
"ds_read_b64_tr_b16 v[162:163], %[vbe] offset:57344\n"
"v_add_f32 v240, v240, v203\n"
"v_add_f32 v241, v241, v207\n"
"v_mfma_f32_16x16x32_bf16 v[36:39], v[180:183], v[224:227], v[36:39]\n"
"ds_read_b64_tr_b16 v[164:165], v243 offset:49152\n"
"v_mfma_f32_16x16x32_bf16 v[68:71], v[180:183], v[228:231], v[68:71]\n"
"ds_read_b64_tr_b16 v[166:167], v243 offset:57344\n"
"v_mfma_f32_16x16x32_bf16 v[40:43], v[184:187], v[224:227], v[40:43]\n"
"ds_read_b64_tr_b16 v[168:169], %[vbe] offset:49408\n"
"v_mfma_f32_16x16x32_bf16 v[72:75], v[184:187], v[228:231], v[72:75]\n"
"ds_read_b64_tr_b16 v[170:171], %[vbe] offset:57600\n"
"v_mfma_f32_16x16x32_bf16 v[44:47], v[188:191], v[224:227], v[44:47]\n"
"ds_read_b64_tr_b16 v[172:173], v243 offset:49408\n"
"v_mfma_f32_16x16x32_bf16 v[76:79], v[188:191], v[228:231], v[76:79]\n"
"ds_read_b64_tr_b16 v[174:175], v243 offset:57600\n"
"s_waitcnt lgkmcnt(8)\n"
"v_mfma_f32_16x16x32_bf16 v[208:211], v[128:131], v[80:83], v[220:223]\n"
"ds_read_b64_tr_b16 v[176:177], %[vbe] offset:49664\n"
"v_mfma_f32_16x16x32_bf16 v[208:211], v[132:135], v[84:87], v[208:211]\n"
"ds_read_b64_tr_b16 v[178:179], %[vbe] offset:57856\n"
"v_mfma_f32_16x16x32_bf16 v[208:211], v[136:139], v[88:91], v[208:211]\n"
"ds_read_b64_tr_b16 v[180:181], v243 offset:49664\n"
"v_mfma_f32_16x16x32_bf16 v[208:211], v[140:143], v[92:95], v[208:211]\n"
"ds_read_b64_tr_b16 v[182:183], v243 offset:57856\n"
"v_mfma_f32_16x16x32_bf16 v[192:195], v[128:131], v[96:99], v[212:215]\n"
"ds_read_b64_tr_b16 v[184:185], %[vbe] offset:49920\n"
"v_mfma_f32_16x16x32_bf16 v[192:195], v[132:135], v[100:103], v[192:195]\n"
"ds_read_b64_tr_b16 v[186:187], %[vbe] offset:58112\n"
"v_mfma_f32_16x16x32_bf16 v[192:195], v[136:139], v[104:107], v[192:195]\n"
"ds_read_b64_tr_b16 v[188:189], v243 offset:49920\n"
"v_mfma_f32_16x16x32_bf16 v[192:195], v[140:143], v[108:111], v[192:195]\n"
"ds_read_b64_tr_b16 v[190:191], v243 offset:58112\n"
"v_mfma_f32_16x16x32_bf16 v[196:199], v[128:131], v[112:115], v[216:219]\n"
"v_mfma_f32_16x16x32_bf16 v[196:199], v[132:135], v[116:119], v[196:199]\n"
"v_mfma_f32_16x16x32_bf16 v[196:199], v[136:139], v[120:123], v[196:199]\n"
"v_mfma_f32_16x16x32_bf16 v[196:199], v[140:143], v[124:127], v[196:199]\n"
"v_mfma_f32_16x16x32_bf16 v[200:203], v[144:147], v[96:99], v[212:215]\n"
"v_mfma_f32_16x16x32_bf16 v[200:203], v[148:151], v[100:103], v[200:203]\n"
"v_mfma_f32_16x16x32_bf16 v[200:203], v[152:155], v[104:107], v[200:203]\n"
"v_mfma_f32_16x16x32_bf16 v[200:203], v[156:159], v[108:111], v[200:203]\n"
"v_mfma_f32_16x16x32_bf16 v[204:207], v[144:147], v[112:115], v[216:219]\n"
"v_mfma_f32_16x16x32_bf16 v[204:207], v[148:151], v[116:119], v[204:207]\n"
"v_mfma_f32_16x16x32_bf16 v[204:207], v[152:155], v[120:123], v[204:207]\n"
"v_mfma_f32_16x16x32_bf16 v[204:207], v[156:159], v[124:127], v[204:207]\n"
"s_waitcnt lgkmcnt(0)\n"
"s_barrier\n"
"s_setprio 0\n"
"s_lshl_b32 s89, %[os1], 2\n"
"s_add_u32 s89, s89, 34816\n"
"s_mov_b32 m0, s54\n"
"s_nop 0\n"
"buffer_load_dwordx4 v249, s[76:79], s89 offen offset:2048 lds\n"
"v_exp_f32 v208, v208\n"
"v_exp_f32 v209, v209\n"
"v_exp_f32 v210, v210\n"
"v_exp_f32_e64 v211, v211\n"
"v_cvt_pk_bf16_f32 v232, v208, v209\n"
"v_cvt_pk_bf16_f32 v233, v210, v211\n"
"ds_write_b64 v247, v[232:233] offset:4096\n"
"v_exp_f32 v192, v192\n"
"v_exp_f32 v193, v193\n"
"v_exp_f32 v194, v194\n"
"v_exp_f32 v195, v195\n"
"v_cvt_pk_bf16_f32 v224, v192, v193\n"
"v_cvt_pk_bf16_f32 v225, v194, v195\n"
"v_exp_f32 v196, v196\n"
"v_exp_f32 v197, v197\n"
"v_exp_f32 v198, v198\n"
"v_exp_f32 v199, v199\n"
"v_cvt_pk_bf16_f32 v228, v196, v197\n"
"v_cvt_pk_bf16_f32 v229, v198, v199\n"
"v_exp_f32 v200, v200\n"
"v_exp_f32 v201, v201\n"
"v_exp_f32 v202, v202\n"
"v_exp_f32 v203, v203\n"
"v_cvt_pk_bf16_f32 v226, v200, v201\n"
"v_cvt_pk_bf16_f32 v227, v202, v203\n"
"v_exp_f32 v204, v204\n"
"v_exp_f32 v205, v205\n"
"v_exp_f32 v206, v206\n"
"v_exp_f32 v207, v207\n"
"v_cvt_pk_bf16_f32 v230, v204, v205\n"
"v_cvt_pk_bf16_f32 v231, v206, v207\n"
"s_waitcnt lgkmcnt(0)\n"
"s_barrier\n"
"s_setprio 1\n"
"v_mfma_f32_16x16x32_bf16 v[16:19], v[160:163], v[224:227], v[16:19]\n"
"ds_read_b128 v[128:131], v244 offset:51200\n"
"v_add_f32 v240, v240, v192\n"
"v_add_f32_e64 v241, v241, v196\n"
"v_mfma_f32_16x16x32_bf16 v[48:51], v[160:163], v[228:231], v[48:51]\n"
"ds_read_b128 v[132:135], v244 offset:51456\n"
"v_add_f32 v242, v242, v208\n"
"v_add_f32 v240, v240, v193\n"
"v_mfma_f32_16x16x32_bf16 v[20:23], v[164:167], v[224:227], v[20:23]\n"
"ds_read_b128 v[136:139], v244 offset:51712\n"
"v_add_f32 v241, v241, v197\n"
"v_add_f32 v242, v242, v209\n"
"v_mfma_f32_16x16x32_bf16 v[52:55], v[164:167], v[228:231], v[52:55]\n"
"ds_read_b128 v[140:143], v244 offset:51968\n"
"v_add_f32 v240, v240, v194\n"
"v_add_f32 v241, v241, v198\n"
"v_mfma_f32_16x16x32_bf16 v[24:27], v[168:171], v[224:227], v[24:27]\n"
"ds_read_b128 v[144:147], v244 offset:59392\n"
"v_add_f32 v242, v242, v210\n"
"v_add_f32 v240, v240, v195\n"
"v_mfma_f32_16x16x32_bf16 v[56:59], v[168:171], v[228:231], v[56:59]\n"
"ds_read_b128 v[148:151], v244 offset:59648\n"
"v_add_f32 v241, v241, v199\n"
"v_add_f32 v242, v242, v211\n"
"v_mfma_f32_16x16x32_bf16 v[28:31], v[172:175], v[224:227], v[28:31]\n"
"ds_read_b128 v[152:155], v244 offset:59904\n"
"v_add_f32 v240, v240, v200\n"
"v_add_f32 v241, v241, v204\n"
"v_mfma_f32_16x16x32_bf16 v[60:63], v[172:175], v[228:231], v[60:63]\n"
"ds_read_b128 v[156:159], v244 offset:60160\n"
"v_add_f32 v240, v240, v201\n"
"v_add_f32 v241, v241, v205\n"
"v_mfma_f32_16x16x32_bf16 v[32:35], v[176:179], v[224:227], v[32:35]\n"
"ds_read_b64_tr_b16 v[160:161], v245 offset:49152\n"
"v_add_f32 v240, v240, v202\n"
"v_add_f32 v241, v241, v206\n"
"v_mfma_f32_16x16x32_bf16 v[64:67], v[176:179], v[228:231], v[64:67]\n"
"ds_read_b64_tr_b16 v[162:163], v245 offset:57344\n"
"v_add_f32 v240, v240, v203\n"
"v_add_f32 v241, v241, v207\n"
"v_mfma_f32_16x16x32_bf16 v[36:39], v[180:183], v[224:227], v[36:39]\n"
"ds_read_b64_tr_b16 v[164:165], v246 offset:49152\n"
"v_mfma_f32_16x16x32_bf16 v[68:71], v[180:183], v[228:231], v[68:71]\n"
"ds_read_b64_tr_b16 v[166:167], v246 offset:57344\n"
"v_mfma_f32_16x16x32_bf16 v[40:43], v[184:187], v[224:227], v[40:43]\n"
"ds_read_b64_tr_b16 v[168:169], v245 offset:49408\n"
"v_mfma_f32_16x16x32_bf16 v[72:75], v[184:187], v[228:231], v[72:75]\n"
"ds_read_b64_tr_b16 v[170:171], v245 offset:57600\n"
"v_mfma_f32_16x16x32_bf16 v[44:47], v[188:191], v[224:227], v[44:47]\n"
"ds_read_b64_tr_b16 v[172:173], v246 offset:49408\n"
"v_mfma_f32_16x16x32_bf16 v[76:79], v[188:191], v[228:231], v[76:79]\n"
"ds_read_b64_tr_b16 v[174:175], v246 offset:57600\n"
"s_waitcnt lgkmcnt(8)\n"
"v_mfma_f32_16x16x32_bf16 v[208:211], v[128:131], v[80:83], v[220:223]\n"
"ds_read_b64_tr_b16 v[176:177], v245 offset:49664\n"
"v_mfma_f32_16x16x32_bf16 v[208:211], v[132:135], v[84:87], v[208:211]\n"
"ds_read_b64_tr_b16 v[178:179], v245 offset:57856\n"
"v_mfma_f32_16x16x32_bf16 v[208:211], v[136:139], v[88:91], v[208:211]\n"
"ds_read_b64_tr_b16 v[180:181], v246 offset:49664\n"
"v_mfma_f32_16x16x32_bf16 v[208:211], v[140:143], v[92:95], v[208:211]\n"
"ds_read_b64_tr_b16 v[182:183], v246 offset:57856\n"
"v_mfma_f32_16x16x32_bf16 v[192:195], v[128:131], v[96:99], v[212:215]\n"
"ds_read_b64_tr_b16 v[184:185], v245 offset:49920\n"
"v_mfma_f32_16x16x32_bf16 v[192:195], v[132:135], v[100:103], v[192:195]\n"
"ds_read_b64_tr_b16 v[186:187], v245 offset:58112\n"
"v_mfma_f32_16x16x32_bf16 v[192:195], v[136:139], v[104:107], v[192:195]\n"
"ds_read_b64_tr_b16 v[188:189], v246 offset:49920\n"
"v_mfma_f32_16x16x32_bf16 v[192:195], v[140:143], v[108:111], v[192:195]\n"
"ds_read_b64_tr_b16 v[190:191], v246 offset:58112\n"
"v_mfma_f32_16x16x32_bf16 v[196:199], v[128:131], v[112:115], v[216:219]\n"
"v_mfma_f32_16x16x32_bf16 v[196:199], v[132:135], v[116:119], v[196:199]\n"
"v_mfma_f32_16x16x32_bf16 v[196:199], v[136:139], v[120:123], v[196:199]\n"
"v_mfma_f32_16x16x32_bf16 v[196:199], v[140:143], v[124:127], v[196:199]\n"
"v_mfma_f32_16x16x32_bf16 v[200:203], v[144:147], v[96:99], v[212:215]\n"
"v_mfma_f32_16x16x32_bf16 v[200:203], v[148:151], v[100:103], v[200:203]\n"
"v_mfma_f32_16x16x32_bf16 v[200:203], v[152:155], v[104:107], v[200:203]\n"
"v_mfma_f32_16x16x32_bf16 v[200:203], v[156:159], v[108:111], v[200:203]\n"
"v_mfma_f32_16x16x32_bf16 v[204:207], v[144:147], v[112:115], v[216:219]\n"
"v_mfma_f32_16x16x32_bf16 v[204:207], v[148:151], v[116:119], v[204:207]\n"
"v_mfma_f32_16x16x32_bf16 v[204:207], v[152:155], v[120:123], v[204:207]\n"
"v_mfma_f32_16x16x32_bf16 v[204:207], v[156:159], v[124:127], v[204:207]\n"
"s_waitcnt lgkmcnt(0)\n"
"s_barrier\n"
"s_setprio 0\n"
"LT0_%=:\n"
"s_lshl_b32 s88, %[os1], 2\n"
"s_add_u32 s88, s88, 73728\n"
"s_mov_b32 m0, s55\n"
"s_nop 0\n"
"buffer_load_dwordx4 v249, s[76:79], s88 offen lds\n"
"buffer_load_dwordx4 v250, s[76:79], s88 offen offset:1024 lds\n"
"s_lshl_b32 s89, %[os1], 2\n"
"s_add_u32 s89, s89, 108544\n"
"buffer_load_dwordx4 v249, s[76:79], s89 offen offset:2048 lds\n"
"buffer_load_dword v248, %[gout], s[80:83], 0 offen sc1\n"
"buffer_load_dword v251, %[gin], s[80:83], 0 offen sc1\n"
"v_exp_f32 v208, v208\n"
"v_exp_f32 v209, v209\n"
"v_exp_f32 v210, v210\n"
"v_exp_f32 v211, v211\n"
"v_cvt_pk_bf16_f32 v232, v208, v209\n"
"v_cvt_pk_bf16_f32 v233, v210, v211\n"
"ds_write_b64 v247, v[232:233] offset:4096\n"
"v_exp_f32 v192, v192\n"
"v_add_f32 v242, v242, v208\n"
"v_exp_f32 v193, v193\n"
"v_add_f32 v242, v242, v209\n"
"v_exp_f32 v194, v194\n"
"v_add_f32 v242, v242, v210\n"
"v_exp_f32 v195, v195\n"
"v_add_f32 v242, v242, v211\n"
"v_cvt_pk_bf16_f32 v224, v192, v193\n"
"v_cvt_pk_bf16_f32 v225, v194, v195\n"
"v_exp_f32 v196, v196\n"
"v_mov_b32 v98, v242\n"
"v_exp_f32 v197, v197\n"
"s_nop 0\n"
"v_permlane32_swap_b32 v242, v98\n"
"v_exp_f32 v198, v198\n"
"v_add_f32 v242, v242, v98\n"
"v_exp_f32 v199, v199\n"
"v_cvt_pk_bf16_f32 v228, v196, v197\n"
"v_cvt_pk_bf16_f32 v229, v198, v199\n"
"v_exp_f32 v200, v200\n"
"v_mov_b32 v98, v242\n"
"v_exp_f32 v201, v201\n"
"s_nop 0\n"
"v_permlane16_swap_b32 v242, v98\n"
"v_exp_f32 v202, v202\n"
"v_add_f32 v242, v242, v98\n"
"v_exp_f32 v203, v203\n"
"v_cvt_pk_bf16_f32 v226, v200, v201\n"
"v_cvt_pk_bf16_f32 v227, v202, v203\n"
"ds_write_b32 v247, v242 offset:5120\n"
"v_exp_f32 v204, v204\n"
"v_exp_f32 v205, v205\n"
"v_exp_f32 v206, v206\n"
"v_exp_f32 v207, v207\n"
"v_cvt_pk_bf16_f32 v230, v204, v205\n"
"v_cvt_pk_bf16_f32 v231, v206, v207\n"
"s_waitcnt vmcnt(9) lgkmcnt(0)\n"
"s_barrier\n"
"s_setprio 1\n"
"v_mfma_f32_16x16x32_bf16 v[16:19], v[160:163], v[224:227], v[16:19]\n"
"v_add_f32 v240, v240, v192\n"
"v_add_f32_e64 v241, v241, v196\n"
"v_mfma_f32_16x16x32_bf16 v[48:51], v[160:163], v[228:231], v[48:51]\n"
"v_add_f32 v240, v240, v193\n"
"v_add_f32 v241, v241, v197\n"
"v_mfma_f32_16x16x32_bf16 v[20:23], v[164:167], v[224:227], v[20:23]\n"
"v_add_f32 v240, v240, v194\n"
"v_add_f32 v241, v241, v198\n"
"v_mfma_f32_16x16x32_bf16 v[52:55], v[164:167], v[228:231], v[52:55]\n"
"v_add_f32 v240, v240, v195\n"
"v_add_f32 v241, v241, v199\n"
"v_mfma_f32_16x16x32_bf16 v[24:27], v[168:171], v[224:227], v[24:27]\n"
"v_add_f32 v240, v240, v200\n"
"v_add_f32 v241, v241, v204\n"
"v_mfma_f32_16x16x32_bf16 v[56:59], v[168:171], v[228:231], v[56:59]\n"
"v_add_f32 v240, v240, v201\n"
"v_add_f32 v241, v241, v205\n"
"v_mfma_f32_16x16x32_bf16 v[28:31], v[172:175], v[224:227], v[28:31]\n"
"v_add_f32 v240, v240, v202\n"
"v_add_f32 v241, v241, v206\n"
"v_mfma_f32_16x16x32_bf16 v[60:63], v[172:175], v[228:231], v[60:63]\n"
"v_add_f32 v240, v240, v203\n"
"v_add_f32 v241, v241, v207\n"
"v_mfma_f32_16x16x32_bf16 v[32:35], v[176:179], v[224:227], v[32:35]\n"
"v_mfma_f32_16x16x32_bf16 v[64:67], v[176:179], v[228:231], v[64:67]\n"
"v_mfma_f32_16x16x32_bf16 v[36:39], v[180:183], v[224:227], v[36:39]\n"
"v_mfma_f32_16x16x32_bf16 v[68:71], v[180:183], v[228:231], v[68:71]\n"
"v_mfma_f32_16x16x32_bf16 v[40:43], v[184:187], v[224:227], v[40:43]\n"
"v_mfma_f32_16x16x32_bf16 v[72:75], v[184:187], v[228:231], v[72:75]\n"
"v_mfma_f32_16x16x32_bf16 v[44:47], v[188:191], v[224:227], v[44:47]\n"
"v_mfma_f32_16x16x32_bf16 v[76:79], v[188:191], v[228:231], v[76:79]\n"
"s_setprio 0\n"
"s_cmp_lg_u32 %[hb], 0\n"
"s_cbranch_scc1 LG0_%=\n"
"s_barrier\n"
"LG0_%=:\n"
"v_mov_b32 v206, v240\n"
"v_mov_b32 v207, v241\n"
"s_nop 0\n"
"v_permlane32_swap_b32 v240, v206\n"
"v_permlane32_swap_b32 v241, v207\n"
"v_add_f32 v240, v240, v206\n"
"v_add_f32 v241, v241, v207\n"
"v_mov_b32 v206, v240\n"
"v_mov_b32 v207, v241\n"
"s_nop 0\n"
"v_permlane16_swap_b32 v240, v206\n"
"v_permlane16_swap_b32 v241, v207\n"
"v_add_f32 v240, v240, v206\n"
"v_add_f32 v241, v241, v207\n"
"v_rcp_f32 v192, v240\n"
"v_rcp_f32 v194, v241\n"
"s_lshr_b32 s68, %[hb], 11\n"
"s_or_b32 s68, s68, %[hb]\n"
"v_and_b32 v244, 0xffff, %[vbe]\n"
"v_xor_b32 v244, s68, v244\n"
"v_xor_b32 v245, 32, v244\n"
"v_lshrrev_b32 v246, 1, %[l16]\n"
"v_add_u32 v246, s90, v246\n"
"v_add_u32 v228, 8192, %[l16]\n"
"s_add_u32 s60, %[osoff], 0\n"
"s_add_u32 s61, %[osoff], 9216\n"
"s_add_u32 s62, %[osoff], 18432\n"
"s_add_u32 s63, %[osoff], 27648\n"
"v_pk_mul_f32 v[16:17], v[16:17], v[192:193] op_sel_hi:[1,0]\n"
"v_pk_mul_f32 v[18:19], v[18:19], v[192:193] op_sel_hi:[1,0]\n"
"v_pk_mul_f32 v[20:21], v[20:21], v[192:193] op_sel_hi:[1,0]\n"
"v_pk_mul_f32 v[22:23], v[22:23], v[192:193] op_sel_hi:[1,0]\n"
"v_cvt_pk_bf16_f32 v128, v16, v17\n"
"v_cvt_pk_bf16_f32 v129, v18, v19\n"
"v_cvt_pk_bf16_f32 v130, v20, v21\n"
"v_cvt_pk_bf16_f32 v131, v22, v23\n"
"buffer_store_dwordx4 v[128:131], %[l16], %[srdo], s60 offen sc1\n"
"v_pk_mul_f32 v[24:25], v[24:25], v[192:193] op_sel_hi:[1,0]\n"
"v_pk_mul_f32 v[26:27], v[26:27], v[192:193] op_sel_hi:[1,0]\n"
"v_pk_mul_f32 v[28:29], v[28:29], v[192:193] op_sel_hi:[1,0]\n"
"v_pk_mul_f32 v[30:31], v[30:31], v[192:193] op_sel_hi:[1,0]\n"
"v_cvt_pk_bf16_f32 v132, v24, v25\n"
"v_cvt_pk_bf16_f32 v133, v26, v27\n"
"v_cvt_pk_bf16_f32 v134, v28, v29\n"
"v_cvt_pk_bf16_f32 v135, v30, v31\n"
"buffer_store_dwordx4 v[132:135], %[l16], %[srdo], s61 offen sc1\n"
"v_pk_mul_f32 v[32:33], v[32:33], v[192:193] op_sel_hi:[1,0]\n"
"v_pk_mul_f32 v[34:35], v[34:35], v[192:193] op_sel_hi:[1,0]\n"
"v_pk_mul_f32 v[36:37], v[36:37], v[192:193] op_sel_hi:[1,0]\n"
"v_pk_mul_f32 v[38:39], v[38:39], v[192:193] op_sel_hi:[1,0]\n"
"v_cvt_pk_bf16_f32 v136, v32, v33\n"
"v_cvt_pk_bf16_f32 v137, v34, v35\n"
"v_cvt_pk_bf16_f32 v138, v36, v37\n"
"v_cvt_pk_bf16_f32 v139, v38, v39\n"
"buffer_store_dwordx4 v[136:139], %[l16], %[srdo], s62 offen sc1\n"
"v_pk_mul_f32 v[40:41], v[40:41], v[192:193] op_sel_hi:[1,0]\n"
"v_pk_mul_f32 v[42:43], v[42:43], v[192:193] op_sel_hi:[1,0]\n"
"v_pk_mul_f32 v[44:45], v[44:45], v[192:193] op_sel_hi:[1,0]\n"
"v_pk_mul_f32 v[46:47], v[46:47], v[192:193] op_sel_hi:[1,0]\n"
"v_cvt_pk_bf16_f32 v140, v40, v41\n"
"v_cvt_pk_bf16_f32 v141, v42, v43\n"
"v_cvt_pk_bf16_f32 v142, v44, v45\n"
"v_cvt_pk_bf16_f32 v143, v46, v47\n"
"buffer_store_dwordx4 v[140:143], %[l16], %[srdo], s63 offen sc1\n"
"v_pk_mul_f32 v[48:49], v[48:49], v[194:195] op_sel_hi:[1,0]\n"
"v_pk_mul_f32 v[50:51], v[50:51], v[194:195] op_sel_hi:[1,0]\n"
"v_pk_mul_f32 v[52:53], v[52:53], v[194:195] op_sel_hi:[1,0]\n"
"v_pk_mul_f32 v[54:55], v[54:55], v[194:195] op_sel_hi:[1,0]\n"
"v_cvt_pk_bf16_f32 v144, v48, v49\n"
"v_cvt_pk_bf16_f32 v145, v50, v51\n"
"v_cvt_pk_bf16_f32 v146, v52, v53\n"
"v_cvt_pk_bf16_f32 v147, v54, v55\n"
"buffer_store_dwordx4 v[144:147], %[l16], %[srdo], s60 offen offset:1024 sc1\n"
"v_pk_mul_f32 v[56:57], v[56:57], v[194:195] op_sel_hi:[1,0]\n"
"v_pk_mul_f32 v[58:59], v[58:59], v[194:195] op_sel_hi:[1,0]\n"
"v_pk_mul_f32 v[60:61], v[60:61], v[194:195] op_sel_hi:[1,0]\n"
"v_pk_mul_f32 v[62:63], v[62:63], v[194:195] op_sel_hi:[1,0]\n"
"v_cvt_pk_bf16_f32 v148, v56, v57\n"
"v_cvt_pk_bf16_f32 v149, v58, v59\n"
"v_cvt_pk_bf16_f32 v150, v60, v61\n"
"v_cvt_pk_bf16_f32 v151, v62, v63\n"
"buffer_store_dwordx4 v[148:151], %[l16], %[srdo], s61 offen offset:1024 sc1\n"
"v_pk_mul_f32 v[64:65], v[64:65], v[194:195] op_sel_hi:[1,0]\n"
"v_pk_mul_f32 v[66:67], v[66:67], v[194:195] op_sel_hi:[1,0]\n"
"v_pk_mul_f32 v[68:69], v[68:69], v[194:195] op_sel_hi:[1,0]\n"
"v_pk_mul_f32 v[70:71], v[70:71], v[194:195] op_sel_hi:[1,0]\n"
"v_cvt_pk_bf16_f32 v152, v64, v65\n"
"v_cvt_pk_bf16_f32 v153, v66, v67\n"
"v_cvt_pk_bf16_f32 v154, v68, v69\n"
"v_cvt_pk_bf16_f32 v155, v70, v71\n"
"buffer_store_dwordx4 v[152:155], %[l16], %[srdo], s62 offen offset:1024 sc1\n"
"v_pk_mul_f32 v[72:73], v[72:73], v[194:195] op_sel_hi:[1,0]\n"
"v_pk_mul_f32 v[74:75], v[74:75], v[194:195] op_sel_hi:[1,0]\n"
"v_pk_mul_f32 v[76:77], v[76:77], v[194:195] op_sel_hi:[1,0]\n"
"v_pk_mul_f32 v[78:79], v[78:79], v[194:195] op_sel_hi:[1,0]\n"
"v_cvt_pk_bf16_f32 v156, v72, v73\n"
"v_cvt_pk_bf16_f32 v157, v74, v75\n"
"v_cvt_pk_bf16_f32 v158, v76, v77\n"
"v_cvt_pk_bf16_f32 v159, v78, v79\n"
"buffer_store_dwordx4 v[156:159], %[l16], %[srdo], s63 offen offset:1024 sc1\n"
"v_max_f32 v202, v240, v241\n"
"v_add_f32 v200, v240, v241\n"
"v_max_f32 v202, v202, v242\n"
"v_add_f32 v201, v242, v242\n"
"v_pk_add_f32 v[200:201], v[200:201], v[16:17]\n"
"ds_read_b64_tr_b16 v[100:101], v244 offset:0\n"
"v_pk_add_f32 v[200:201], v[200:201], v[18:19]\n"
"v_pk_add_f32 v[200:201], v[200:201], v[20:21]\n"
"ds_read_b64_tr_b16 v[102:103], v244 offset:8192\n"
"v_pk_add_f32 v[200:201], v[200:201], v[22:23]\n"
"v_pk_add_f32 v[200:201], v[200:201], v[24:25]\n"
"ds_read_b64_tr_b16 v[104:105], v245 offset:0\n"
"v_pk_add_f32 v[200:201], v[200:201], v[26:27]\n"
"v_pk_add_f32 v[200:201], v[200:201], v[28:29]\n"
"ds_read_b64_tr_b16 v[106:107], v245 offset:8192\n"
"v_pk_add_f32 v[200:201], v[200:201], v[30:31]\n"
"v_pk_add_f32 v[200:201], v[200:201], v[32:33]\n"
"ds_read_b64_tr_b16 v[108:109], v244 offset:256\n"
"v_pk_add_f32 v[200:201], v[200:201], v[34:35]\n"
"v_pk_add_f32 v[200:201], v[200:201], v[36:37]\n"
"ds_read_b64_tr_b16 v[110:111], v244 offset:8448\n"
"v_pk_add_f32 v[200:201], v[200:201], v[38:39]\n"
"v_pk_add_f32 v[200:201], v[200:201], v[40:41]\n"
"ds_read_b64_tr_b16 v[112:113], v245 offset:256\n"
"v_pk_add_f32 v[200:201], v[200:201], v[42:43]\n"
"v_pk_add_f32 v[200:201], v[200:201], v[44:45]\n"
"ds_read_b64_tr_b16 v[114:115], v245 offset:8448\n"
"v_pk_add_f32 v[200:201], v[200:201], v[46:47]\n"
"v_pk_add_f32 v[200:201], v[200:201], v[48:49]\n"
"ds_read_b64_tr_b16 v[116:117], v244 offset:512\n"
"v_pk_add_f32 v[200:201], v[200:201], v[50:51]\n"
"v_pk_add_f32 v[200:201], v[200:201], v[52:53]\n"
"ds_read_b64_tr_b16 v[118:119], v244 offset:8704\n"
"v_pk_add_f32 v[200:201], v[200:201], v[54:55]\n"
"v_pk_add_f32 v[200:201], v[200:201], v[56:57]\n"
"ds_read_b64_tr_b16 v[120:121], v245 offset:512\n"
"v_pk_add_f32 v[200:201], v[200:201], v[58:59]\n"
"v_pk_add_f32 v[200:201], v[200:201], v[60:61]\n"
"ds_read_b64_tr_b16 v[122:123], v245 offset:8704\n"
"v_pk_add_f32 v[200:201], v[200:201], v[62:63]\n"
"v_pk_add_f32 v[200:201], v[200:201], v[64:65]\n"
"ds_read_b64_tr_b16 v[124:125], v244 offset:768\n"
"v_pk_add_f32 v[200:201], v[200:201], v[66:67]\n"
"v_pk_add_f32 v[200:201], v[200:201], v[68:69]\n"
"ds_read_b64_tr_b16 v[126:127], v244 offset:8960\n"
"v_pk_add_f32 v[200:201], v[200:201], v[70:71]\n"
"v_pk_add_f32 v[200:201], v[200:201], v[72:73]\n"
"v_pk_add_f32 v[200:201], v[200:201], v[74:75]\n"
"v_pk_add_f32 v[200:201], v[200:201], v[76:77]\n"
"v_pk_add_f32 v[200:201], v[200:201], v[78:79]\n"
"v_add_f32_e64 v200, v200, v201\n"
"v_mov_b32 v203, 0x207\n"
"v_mov_b32 v205, 0x71800000\n"
"v_xor_b32 v204, 8192, v247\n"
"v_cmp_class_f32 s[68:69], v200, v203\n"
"v_cmp_nlt_f32 vcc, v202, v205\n"
"s_nop 4\n"
"s_or_b64 vcc, vcc, s[68:69]\n"
"s_nop 1\n"
"v_cndmask_b32 v232, 0, 1, vcc\n"
"ds_write_b32 v247, v232 offset:6144\n"
"s_waitcnt lgkmcnt(0)\n"
"s_barrier\n"
"ds_read_b128 v[160:163], v247 offset:0\n"
"ds_read_b128 v[164:167], v247 offset:1024\n"
"ds_read_b128 v[168:171], v247 offset:2048\n"
"ds_read_b128 v[172:175], v247 offset:3072\n"
"ds_read_b128 v[176:179], v247 offset:6144\n"
"ds_read_b128 v[180:183], v204 offset:6144\n"
"LO0_%=:\n"
"v_mfma_f32_16x16x32_bf16 v[52:55], v[100:103], v[144:147], 0\n"
"ds_read_b64_tr_b16 v[224:225], v245 offset:768\n"
"v_mfma_f32_16x16x32_bf16 v[16:19], v[100:103], v[128:131], 0\n"
"ds_read_b64_tr_b16 v[226:227], v245 offset:8960\n"
"s_waitcnt lgkmcnt(7)\n"
"v_mfma_f32_16x16x32_bf16 v[88:91], v[100:103], v[160:163], 0\n"
"v_mfma_f32_16x16x32_bf16 v[56:59], v[104:107], v[144:147], 0\n"
"ds_read_b64_tr_b16 v[100:101], v246 offset:0\n"
"v_mfma_f32_16x16x32_bf16 v[20:23], v[104:107], v[128:131], 0\n"
"ds_read_b64_tr_b16 v[102:103], v246 offset:512\n"
"v_mfma_f32_16x16x32_bf16 v[92:95], v[104:107], v[160:163], 0\n"
"v_mfma_f32_16x16x32_bf16 v[60:63], v[108:111], v[144:147], 0\n"
"s_waitcnt lgkmcnt(5)\n"
"v_or3_b32 v176, v176, v177, v178\n"
"ds_read_b64_tr_b16 v[104:105], v244 offset:2048\n"
"v_mfma_f32_16x16x32_bf16 v[24:27], v[108:111], v[128:131], 0\n"
"s_waitcnt lgkmcnt(5)\n"
"v_or3_b32 v180, v180, v181, v182\n"
"ds_read_b64_tr_b16 v[106:107], v244 offset:10240\n"
"v_mfma_f32_16x16x32_bf16 v[64:67], v[112:115], v[144:147], 0\n"
"v_or3_b32 v176, v176, v179, v183\n"
"ds_read_b64_tr_b16 v[108:109], v245 offset:2048\n"
"v_mfma_f32_16x16x32_bf16 v[28:31], v[112:115], v[128:131], 0\n"
"v_or_b32 v176, v176, v180\n"
"ds_read_b64_tr_b16 v[110:111], v245 offset:10240\n"
"v_mfma_f32_16x16x32_bf16 v[68:71], v[116:119], v[144:147], 0\n"
"v_cmp_ne_u32 vcc, 0, v176\n"
"ds_read_b64_tr_b16 v[112:113], v244 offset:2304\n"
"v_mfma_f32_16x16x32_bf16 v[32:35], v[116:119], v[128:131], 0\n"
"ds_read_b64_tr_b16 v[114:115], v244 offset:10496\n"
"v_mfma_f32_16x16x32_bf16 v[72:75], v[120:123], v[144:147], 0\n"
"ds_read_b64_tr_b16 v[116:117], v245 offset:2304\n"
"v_mfma_f32_16x16x32_bf16 v[36:39], v[120:123], v[128:131], 0\n"
"ds_read_b64_tr_b16 v[118:119], v245 offset:10496\n"
"v_mfma_f32_16x16x32_bf16 v[76:79], v[124:127], v[144:147], 0\n"
"ds_read_b64_tr_b16 v[120:121], v244 offset:2560\n"
"v_mfma_f32_16x16x32_bf16 v[40:43], v[124:127], v[128:131], 0\n"
"ds_read_b64_tr_b16 v[122:123], v244 offset:10752\n"
"s_waitcnt lgkmcnt(12)\n"
"v_mfma_f32_16x16x32_bf16 v[80:83], v[224:227], v[144:147], 0\n"
"ds_read_b64_tr_b16 v[124:125], v245 offset:2560\n"
"v_mfma_f32_16x16x32_bf16 v[44:47], v[224:227], v[128:131], 0\n"
"ds_read_b64_tr_b16 v[126:127], v245 offset:10752\n"
"s_waitcnt lgkmcnt(12)\n"
"v_mfma_f32_16x16x32_bf16 v[84:87], v[100:103], v[144:147], 0\n"
"ds_read_b64_tr_b16 v[224:225], v244 offset:2816\n"
"v_mfma_f32_16x16x32_bf16 v[48:51], v[100:103], v[128:131], 0\n"
"ds_read_b64_tr_b16 v[226:227], v244 offset:11008\n"
"s_waitcnt lgkmcnt(12)\n"
"v_mfma_f32_16x16x32_bf16 v[52:55], v[104:107], v[148:151], v[52:55]\n"
"ds_read_b64_tr_b16 v[100:101], v245 offset:2816\n"
"v_mfma_f32_16x16x32_bf16 v[16:19], v[104:107], v[132:135], v[16:19]\n"
"ds_read_b64_tr_b16 v[102:103], v245 offset:11008\n"
"v_mfma_f32_16x16x32_bf16 v[88:91], v[104:107], v[164:167], v[88:91]\n"
"s_waitcnt lgkmcnt(12)\n"
"v_mfma_f32_16x16x32_bf16 v[56:59], v[108:111], v[148:151], v[56:59]\n"
"ds_read_b64_tr_b16 v[104:105], v246 offset:1024\n"
"v_mfma_f32_16x16x32_bf16 v[20:23], v[108:111], v[132:135], v[20:23]\n"
"ds_read_b64_tr_b16 v[106:107], v246 offset:1536\n"
"v_mfma_f32_16x16x32_bf16 v[92:95], v[108:111], v[164:167], v[92:95]\n"
"s_waitcnt lgkmcnt(12)\n"
"v_mfma_f32_16x16x32_bf16 v[60:63], v[112:115], v[148:151], v[60:63]\n"
"v_mfma_f32_16x16x32_bf16 v[24:27], v[112:115], v[132:135], v[24:27]\n"
"s_waitcnt lgkmcnt(10)\n"
"v_mfma_f32_16x16x32_bf16 v[64:67], v[116:119], v[148:151], v[64:67]\n"
"v_mfma_f32_16x16x32_bf16 v[28:31], v[116:119], v[132:135], v[28:31]\n"
"s_waitcnt lgkmcnt(8)\n"
"v_mfma_f32_16x16x32_bf16 v[68:71], v[120:123], v[148:151], v[68:71]\n"
"s_waitcnt vmcnt(0)\n"
"s_cmp_lg_u64 vcc, 0\n"
"s_cbranch_scc1 LBAD_%=\n"
"s_waitcnt lgkmcnt(0)\n"
"s_barrier\n"
"s_cmp_lg_u32 %[hb], 0\n"
"s_cbranch_scc1 LSa_%=\n"
"buffer_store_dword v251, %[gout], s[80:83], 0 offen offset:4 sc1\n"
"LSa_%=:\n"
"ds_read_b64_tr_b16 v[108:109], v244 offset:16384\n"
"ds_read_b64_tr_b16 v[112:113], v245 offset:16384\n"
"ds_read_b64_tr_b16 v[116:117], v244 offset:16640\n"
"v_mfma_f32_16x16x32_bf16 v[32:35], v[120:123], v[132:135], v[32:35]\n"
"s_lshl_b32 s88, %[os2], 2\n"
"s_mov_b32 m0, s52\n"
"s_nop 0\n"
"buffer_load_dwordx4 v249, s[76:79], s88 offen lds\n"
"ds_read_b64_tr_b16 v[110:111], v244 offset:24576\n"
"ds_read_b64_tr_b16 v[114:115], v245 offset:24576\n"
"ds_read_b64_tr_b16 v[118:119], v244 offset:24832\n"
"v_mfma_f32_16x16x32_bf16 v[72:75], v[124:127], v[148:151], v[72:75]\n"
"buffer_load_dwordx4 v250, s[76:79], s88 offen offset:1024 lds\n"
"ds_read_b64_tr_b16 v[120:121], v245 offset:16640\n"
"s_add_u32 m0, s90, 0\n"
"v_mfma_f32_16x16x32_bf16 v[36:39], v[124:127], v[132:135], v[36:39]\n"
"buffer_load_dwordx4 v252, s[76:79], s88 offen lds\n"
"ds_read_b64_tr_b16 v[122:123], v245 offset:24832\n"
"v_mfma_f32_16x16x32_bf16 v[76:79], v[224:227], v[148:151], v[76:79]\n"
"s_lshl_b32 s89, %[os2], 2\n"
"s_add_u32 s89, s89, 34816\n"
"s_mov_b32 m0, s52\n"
"s_nop 0\n"
"buffer_load_dwordx4 v249, s[76:79], s89 offen offset:2048 lds\n"
"ds_read_b64_tr_b16 v[124:125], v244 offset:16896\n"
"v_mfma_f32_16x16x32_bf16 v[40:43], v[224:227], v[132:135], v[40:43]\n"
"buffer_load_dwordx4 v250, s[76:79], s89 offen offset:3072 lds\n"
"ds_read_b64_tr_b16 v[126:127], v244 offset:25088\n"
"v_mfma_f32_16x16x32_bf16 v[80:83], v[100:103], v[148:151], v[80:83]\n"
"ds_read_b64_tr_b16 v[224:225], v245 offset:16896\n"
"v_mfma_f32_16x16x32_bf16 v[44:47], v[100:103], v[132:135], v[44:47]\n"
"ds_read_b64_tr_b16 v[226:227], v245 offset:25088\n"
"v_mfma_f32_16x16x32_bf16 v[84:87], v[104:107], v[148:151], v[84:87]\n"
"ds_read_b64_tr_b16 v[100:101], v244 offset:17152\n"
"v_mfma_f32_16x16x32_bf16 v[48:51], v[104:107], v[132:135], v[48:51]\n"
"ds_read_b64_tr_b16 v[102:103], v244 offset:25344\n"
"s_waitcnt lgkmcnt(10)\n"
"v_mfma_f32_16x16x32_bf16 v[52:55], v[108:111], v[152:155], v[52:55]\n"
"ds_read_b64_tr_b16 v[104:105], v245 offset:17152\n"
"v_mfma_f32_16x16x32_bf16 v[16:19], v[108:111], v[136:139], v[16:19]\n"
"ds_read_b64_tr_b16 v[106:107], v245 offset:25344\n"
"v_mfma_f32_16x16x32_bf16 v[88:91], v[108:111], v[168:171], v[88:91]\n"
"s_waitcnt lgkmcnt(11)\n"
"v_mfma_f32_16x16x32_bf16 v[56:59], v[112:115], v[152:155], v[56:59]\n"
"ds_read_b64_tr_b16 v[108:109], v246 offset:2048\n"
"v_mfma_f32_16x16x32_bf16 v[20:23], v[112:115], v[136:139], v[20:23]\n"
"ds_read_b64_tr_b16 v[110:111], v246 offset:2560\n"
"v_mfma_f32_16x16x32_bf16 v[92:95], v[112:115], v[168:171], v[92:95]\n"
"s_waitcnt lgkmcnt(12)\n"
"v_mfma_f32_16x16x32_bf16 v[60:63], v[116:119], v[152:155], v[60:63]\n"
"ds_read_b64_tr_b16 v[112:113], v244 offset:18432\n"
"v_mfma_f32_16x16x32_bf16 v[24:27], v[116:119], v[136:139], v[24:27]\n"
"ds_read_b64_tr_b16 v[114:115], v244 offset:26624\n"
"s_waitcnt lgkmcnt(12)\n"
"v_mfma_f32_16x16x32_bf16 v[64:67], v[120:123], v[152:155], v[64:67]\n"
"ds_read_b64_tr_b16 v[116:117], v245 offset:18432\n"
"v_mfma_f32_16x16x32_bf16 v[28:31], v[120:123], v[136:139], v[28:31]\n"
"ds_read_b64_tr_b16 v[118:119], v245 offset:26624\n"
"s_waitcnt lgkmcnt(12)\n"
"v_mfma_f32_16x16x32_bf16 v[68:71], v[124:127], v[152:155], v[68:71]\n"
"ds_read_b64_tr_b16 v[120:121], v244 offset:18688\n"
"v_mfma_f32_16x16x32_bf16 v[32:35], v[124:127], v[136:139], v[32:35]\n"
"ds_read_b64_tr_b16 v[122:123], v244 offset:26880\n"
"s_waitcnt lgkmcnt(12)\n"
"v_mfma_f32_16x16x32_bf16 v[72:75], v[224:227], v[152:155], v[72:75]\n"
"ds_read_b64_tr_b16 v[124:125], v245 offset:18688\n"
"v_mfma_f32_16x16x32_bf16 v[36:39], v[224:227], v[136:139], v[36:39]\n"
"ds_read_b64_tr_b16 v[126:127], v245 offset:26880\n"
"s_waitcnt lgkmcnt(12)\n"
"v_mfma_f32_16x16x32_bf16 v[76:79], v[100:103], v[152:155], v[76:79]\n"
"ds_read_b64_tr_b16 v[224:225], v244 offset:18944\n"
"v_mfma_f32_16x16x32_bf16 v[40:43], v[100:103], v[136:139], v[40:43]\n"
"ds_read_b64_tr_b16 v[226:227], v244 offset:27136\n"
"s_waitcnt lgkmcnt(12)\n"
"v_mfma_f32_16x16x32_bf16 v[80:83], v[104:107], v[152:155], v[80:83]\n"
"ds_read_b64_tr_b16 v[100:101], v245 offset:18944\n"
"v_mfma_f32_16x16x32_bf16 v[44:47], v[104:107], v[136:139], v[44:47]\n"
"ds_read_b64_tr_b16 v[102:103], v245 offset:27136\n"
"s_waitcnt lgkmcnt(12)\n"
"v_mfma_f32_16x16x32_bf16 v[84:87], v[108:111], v[152:155], v[84:87]\n"
"ds_read_b64_tr_b16 v[104:105], v244 offset:19200\n"
"v_mfma_f32_16x16x32_bf16 v[48:51], v[108:111], v[136:139], v[48:51]\n"
"ds_read_b64_tr_b16 v[106:107], v244 offset:27392\n"
"s_waitcnt lgkmcnt(12)\n"
"v_mfma_f32_16x16x32_bf16 v[52:55], v[112:115], v[156:159], v[52:55]\n"
"ds_read_b64_tr_b16 v[108:109], v245 offset:19200\n"
"v_mfma_f32_16x16x32_bf16 v[16:19], v[112:115], v[140:143], v[16:19]\n"
"ds_read_b64_tr_b16 v[110:111], v245 offset:27392\n"
"v_mfma_f32_16x16x32_bf16 v[88:91], v[112:115], v[172:175], v[88:91]\n"
"s_waitcnt lgkmcnt(12)\n"
"v_mfma_f32_16x16x32_bf16 v[56:59], v[116:119], v[156:159], v[56:59]\n"
"ds_read_b64_tr_b16 v[112:113], v246 offset:3072\n"
"v_mfma_f32_16x16x32_bf16 v[20:23], v[116:119], v[140:143], v[20:23]\n"
"ds_read_b64_tr_b16 v[114:115], v246 offset:3584\n"
"v_mfma_f32_16x16x32_bf16 v[92:95], v[116:119], v[172:175], v[92:95]\n"
"s_waitcnt lgkmcnt(12)\n"
"v_mfma_f32_16x16x32_bf16 v[60:63], v[120:123], v[156:159], v[60:63]\n"
"v_mfma_f32_16x16x32_bf16 v[24:27], v[120:123], v[140:143], v[24:27]\n"
"s_waitcnt lgkmcnt(10)\n"
"v_mfma_f32_16x16x32_bf16 v[64:67], v[124:127], v[156:159], v[64:67]\n"
"v_mfma_f32_16x16x32_bf16 v[28:31], v[124:127], v[140:143], v[28:31]\n"
"s_waitcnt lgkmcnt(8)\n"
"v_mfma_f32_16x16x32_bf16 v[68:71], v[224:227], v[156:159], v[68:71]\n"
"s_waitcnt lgkmcnt(0)\n"
"s_barrier\n"
"ds_read_b64_tr_b16 v[160:161], v244 offset:32768\n"
"ds_read_b64_tr_b16 v[164:165], v245 offset:32768\n"
"ds_read_b64_tr_b16 v[124:125], v244 offset:33024\n"
"v_mfma_f32_16x16x32_bf16 v[32:35], v[224:227], v[140:143], v[32:35]\n"
"s_cmp_lg_u32 %[hb], 0\n"
"s_cbranch_scc1 LIa_%=\n"
"buffer_load_dwordx2 v[254:255], %[gin], s[80:83], 0 offen sc1\n"
"LIa_%=:\n"
"ds_read_b64_tr_b16 v[162:163], v244 offset:40960\n"
"ds_read_b64_tr_b16 v[166:167], v245 offset:40960\n"
"ds_read_b64_tr_b16 v[126:127], v244 offset:41216\n"
"v_mfma_f32_16x16x32_bf16 v[72:75], v[100:103], v[156:159], v[72:75]\n"
"s_lshl_b32 s88, %[os2], 2\n"
"s_add_u32 s88, s88, 73728\n"
"s_mov_b32 m0, s53\n"
"s_nop 0\n"
"buffer_load_dwordx4 v249, s[76:79], s88 offen lds\n"
"ds_read_b64_tr_b16 v[224:225], v245 offset:33024\n"
"v_mfma_f32_16x16x32_bf16 v[36:39], v[100:103], v[140:143], v[36:39]\n"
"buffer_load_dwordx4 v250, s[76:79], s88 offen offset:1024 lds\n"
"ds_read_b64_tr_b16 v[226:227], v245 offset:41216\n"
"v_mfma_f32_16x16x32_bf16 v[76:79], v[104:107], v[156:159], v[76:79]\n"
"s_lshl_b32 s89, %[os2], 2\n"
"s_add_u32 s89, s89, 108544\n"
"buffer_load_dwordx4 v249, s[76:79], s89 offen offset:2048 lds\n"
"ds_read_b64_tr_b16 v[100:101], v244 offset:33280\n"
"v_mfma_f32_16x16x32_bf16 v[40:43], v[104:107], v[140:143], v[40:43]\n"
"buffer_load_dwordx4 v250, s[76:79], s89 offen offset:3072 lds\n"
"ds_read_b64_tr_b16 v[102:103], v244 offset:41472\n"
"v_mfma_f32_16x16x32_bf16 v[80:83], v[108:111], v[156:159], v[80:83]\n"
"ds_read_b64_tr_b16 v[104:105], v245 offset:33280\n"
"v_mfma_f32_16x16x32_bf16 v[44:47], v[108:111], v[140:143], v[44:47]\n"
"ds_read_b64_tr_b16 v[106:107], v245 offset:41472\n"
"v_mfma_f32_16x16x32_bf16 v[84:87], v[112:115], v[156:159], v[84:87]\n"
"ds_read_b64_tr_b16 v[108:109], v244 offset:33536\n"
"v_mfma_f32_16x16x32_bf16 v[48:51], v[112:115], v[140:143], v[48:51]\n"
"s_cmp_lg_u32 %[hb], 0\n"
"s_cbranch_scc1 LDa_%=\n"
"s_mov_b32 s69, 0x40000\n"
"s_waitcnt vmcnt(4)\n"
"s_branch LCa_%=\n"
"LPa_%=:\n"
"buffer_load_dwordx2 v[254:255], %[gin], s[80:83], 0 offen sc1\n"
"s_waitcnt vmcnt(0)\n"
"LCa_%=:\n"
"v_cmp_ne_u32 vcc, v254, v251\n"
"s_nop 4\n"
"s_cmp_eq_u64 vcc, 0\n"
"s_cbranch_scc1 LEa_%=\n"
"v_mov_b32 v251, v254\n"
"buffer_store_dword v251, %[gout], s[80:83], 0 offen offset:4 sc1\n"
"LEa_%=:\n"
"v_cmp_ne_u32 vcc, v255, v248\n"
"s_nop 4\n"
"s_and_b32 s68, vcc_lo, 14\n"
"s_cbranch_scc0 LDa_%=\n"
"s_sub_u32 s69, s69, 1\n"
"s_cbranch_scc0 LPa_%=\n"
"LDa_%=:\n"
"s_barrier\n"
"s_add_u32 s60, %[os1], 0\n"
"buffer_load_dwordx4 v[192:195], %[l16], %[srdo], s60 offen offset:1024 sc1\n"
"buffer_load_dwordx4 v[176:179], %[l16], %[srdo], s60 offen sc1\n"
"s_add_u32 s64, %[os1], 8192\n"
"buffer_load_dwordx4 v[208:211], %[l16], %[srdo], s64 offen sc1\n"
"s_add_u32 s61, %[os1], 9216\n"
"buffer_load_dwordx4 v[196:199], %[l16], %[srdo], s61 offen offset:1024 sc1\n"
"buffer_load_dwordx4 v[180:183], %[l16], %[srdo], s61 offen sc1\n"
"s_add_u32 s62, %[os1], 18432\n"
"buffer_load_dwordx4 v[200:203], %[l16], %[srdo], s62 offen offset:1024 sc1\n"
"buffer_load_dwordx4 v[184:187], %[l16], %[srdo], s62 offen sc1\n"
"s_add_u32 s63, %[os1], 27648\n"
"buffer_load_dwordx4 v[204:207], %[l16], %[srdo], s63 offen offset:1024 sc1\n"
"buffer_load_dwordx4 v[188:191], %[l16], %[srdo], s63 offen sc1\n"
"s_add_u32 s64, %[os2], 8192\n"
"buffer_load_dwordx4 v[240:243], %[l16], %[srdo], s64 offen sc1\n"
"ds_read_b64_tr_b16 v[110:111], v244 offset:41728\n"
"s_waitcnt vmcnt(9) lgkmcnt(10)\n"
"v_mfma_f32_16x16x32_bf16 v[52:55], v[160:163], v[192:195], v[52:55]\n"
"ds_read_b64_tr_b16 v[112:113], v245 offset:33536\n"
"s_waitcnt vmcnt(8)\n"
"v_mfma_f32_16x16x32_bf16 v[16:19], v[160:163], v[176:179], v[16:19]\n"
"s_add_u32 s60, %[os2], 0\n"
"buffer_load_dwordx4 v[144:147], %[l16], %[srdo], s60 offen offset:1024 sc1\n"
"ds_read_b64_tr_b16 v[114:115], v245 offset:41728\n"
"s_waitcnt lgkmcnt(11)\n"
"v_mfma_f32_16x16x32_bf16 v[56:59], v[164:167], v[192:195], v[56:59]\n"
"ds_read_b64_tr_b16 v[116:117], v246 offset:4096\n"
"v_mfma_f32_16x16x32_bf16 v[20:23], v[164:167], v[176:179], v[20:23]\n"
"buffer_load_dwordx4 v[128:131], %[l16], %[srdo], s60 offen sc1\n"
"ds_read_b64_tr_b16 v[118:119], v246 offset:4608\n"
"s_waitcnt lgkmcnt(12)\n"
"v_mfma_f32_16x16x32_bf16 v[60:63], v[124:127], v[192:195], v[60:63]\n"
"ds_read_b64_tr_b16 v[168:169], v244 offset:34816\n"
"v_mfma_f32_16x16x32_bf16 v[24:27], v[124:127], v[176:179], v[24:27]\n"
"ds_read_b64_tr_b16 v[170:171], v244 offset:43008\n"
"s_waitcnt lgkmcnt(12)\n"
"v_mfma_f32_16x16x32_bf16 v[64:67], v[224:227], v[192:195], v[64:67]\n"
"ds_read_b64_tr_b16 v[172:173], v245 offset:34816\n"
"v_mfma_f32_16x16x32_bf16 v[28:31], v[224:227], v[176:179], v[28:31]\n"
"s_add_u32 s61, %[os2], 9216\n"
"buffer_load_dwordx4 v[148:151], %[l16], %[srdo], s61 offen offset:1024 sc1\n"
"ds_read_b64_tr_b16 v[174:175], v245 offset:43008\n"
"s_waitcnt lgkmcnt(12)\n"
"v_mfma_f32_16x16x32_bf16 v[68:71], v[100:103], v[192:195], v[68:71]\n"
"ds_read_b64_tr_b16 v[224:225], v244 offset:35072\n"
"v_mfma_f32_16x16x32_bf16 v[32:35], v[100:103], v[176:179], v[32:35]\n"
"buffer_load_dwordx4 v[132:135], %[l16], %[srdo], s61 offen sc1\n"
"ds_read_b64_tr_b16 v[226:227], v244 offset:43264\n"
"s_waitcnt lgkmcnt(12)\n"
"v_mfma_f32_16x16x32_bf16 v[72:75], v[104:107], v[192:195], v[72:75]\n"
"ds_read_b64_tr_b16 v[100:101], v245 offset:35072\n"
"v_mfma_f32_16x16x32_bf16 v[36:39], v[104:107], v[176:179], v[36:39]\n"
"ds_read_b64_tr_b16 v[102:103], v245 offset:43264\n"
"s_waitcnt lgkmcnt(12)\n"
"v_mfma_f32_16x16x32_bf16 v[76:79], v[108:111], v[192:195], v[76:79]\n"
"ds_read_b64_tr_b16 v[104:105], v244 offset:35328\n"
"v_mfma_f32_16x16x32_bf16 v[40:43], v[108:111], v[176:179], v[40:43]\n"
"s_add_u32 s62, %[os2], 18432\n"
"buffer_load_dwordx4 v[152:155], %[l16], %[srdo], s62 offen offset:1024 sc1\n"
"ds_read_b64_tr_b16 v[106:107], v244 offset:43520\n"
"s_waitcnt lgkmcnt(12)\n"
"v_mfma_f32_16x16x32_bf16 v[80:83], v[112:115], v[192:195], v[80:83]\n"
"ds_read_b64_tr_b16 v[108:109], v245 offset:35328\n"
"v_mfma_f32_16x16x32_bf16 v[44:47], v[112:115], v[176:179], v[44:47]\n"
"buffer_load_dwordx4 v[136:139], %[l16], %[srdo], s62 offen sc1\n"
"ds_read_b64_tr_b16 v[110:111], v245 offset:43520\n"
"s_waitcnt lgkmcnt(12)\n"
"v_mfma_f32_16x16x32_bf16 v[84:87], v[116:119], v[192:195], v[84:87]\n"
"ds_read_b64_tr_b16 v[112:113], v244 offset:35584\n"
"v_mfma_f32_16x16x32_bf16 v[48:51], v[116:119], v[176:179], v[48:51]\n"
"ds_read_b64_tr_b16 v[114:115], v244 offset:43776\n"
"s_waitcnt vmcnt(12) lgkmcnt(12)\n"
"v_mfma_f32_16x16x32_bf16 v[52:55], v[168:171], v[196:199], v[52:55]\n"
"ds_read_b64_tr_b16 v[116:117], v245 offset:35584\n"
"s_waitcnt vmcnt(11)\n"
"v_mfma_f32_16x16x32_bf16 v[16:19], v[168:171], v[180:183], v[16:19]\n"
"s_add_u32 s63, %[os2], 27648\n"
"buffer_load_dwordx4 v[156:159], %[l16], %[srdo], s63 offen offset:1024 sc1\n"
"ds_read_b64_tr_b16 v[118:119], v245 offset:43776\n"
"s_waitcnt lgkmcnt(12)\n"
"v_mfma_f32_16x16x32_bf16 v[56:59], v[172:175], v[196:199], v[56:59]\n"
"ds_read_b64_tr_b16 v[120:121], v246 offset:5120\n"
"v_mfma_f32_16x16x32_bf16 v[20:23], v[172:175], v[180:183], v[20:23]\n"
"buffer_load_dwordx4 v[140:143], %[l16], %[srdo], s63 offen sc1\n"
"ds_read_b64_tr_b16 v[122:123], v246 offset:5632\n"
"s_waitcnt lgkmcnt(12)\n"
"v_mfma_f32_16x16x32_bf16 v[60:63], v[224:227], v[196:199], v[60:63]\n"
"v_mfma_f32_16x16x32_bf16 v[24:27], v[224:227], v[180:183], v[24:27]\n"
"ds_write_b128 v247, v[208:211] offset:0\n"
"s_waitcnt lgkmcnt(11)\n"
"v_mfma_f32_16x16x32_bf16 v[64:67], v[100:103], v[196:199], v[64:67]\n"
"v_mfma_f32_16x16x32_bf16 v[28:31], v[100:103], v[180:183], v[28:31]\n"
"s_add_u32 s60, %[os3], 0\n"
"buffer_load_dwordx4 v[192:195], %[l16], %[srdo], s60 offen offset:1024 sc1\n"
"s_waitcnt lgkmcnt(9)\n"
"v_mfma_f32_16x16x32_bf16 v[68:71], v[104:107], v[196:199], v[68:71]\n"
"s_waitcnt lgkmcnt(0)\n"
"s_barrier\n"
"ds_read_b64_tr_b16 v[124:125], v244 offset:49152\n"
"ds_read_b64_tr_b16 v[224:225], v245 offset:49152\n"
"ds_read_b64_tr_b16 v[100:101], v244 offset:49408\n"
"v_mfma_f32_16x16x32_bf16 v[32:35], v[104:107], v[180:183], v[32:35]\n"
"ds_read_b128 v[212:215], v247 offset:1024\n"
"s_lshl_b32 s88, %[os3], 2\n"
"s_mov_b32 m0, s54\n"
"s_nop 0\n"
"buffer_load_dwordx4 v249, s[76:79], s88 offen lds\n"
"buffer_load_dwordx4 v[176:179], %[l16], %[srdo], s60 offen sc1\n"
"ds_read_b64_tr_b16 v[126:127], v244 offset:57344\n"
"ds_read_b64_tr_b16 v[226:227], v245 offset:57344\n"
"ds_read_b64_tr_b16 v[102:103], v244 offset:57600\n"
"v_mfma_f32_16x16x32_bf16 v[72:75], v[108:111], v[196:199], v[72:75]\n"
"ds_read_b128 v[216:219], v247 offset:2048\n"
"buffer_load_dwordx4 v250, s[76:79], s88 offen offset:1024 lds\n"
"ds_read_b64_tr_b16 v[104:105], v245 offset:49408\n"
"v_mfma_f32_16x16x32_bf16 v[36:39], v[108:111], v[180:183], v[36:39]\n"
"s_add_u32 m0, s90, 4096\n"
"ds_read_b128 v[220:223], v247 offset:3072\n"
"buffer_load_dwordx4 v252, s[76:79], s88 offen lds\n"
"ds_read_b64_tr_b16 v[106:107], v245 offset:57600\n"
"v_mfma_f32_16x16x32_bf16 v[76:79], v[112:115], v[196:199], v[76:79]\n"
"s_lshl_b32 s89, %[os3], 2\n"
"s_add_u32 s89, s89, 34816\n"
"s_mov_b32 m0, s54\n"
"s_nop 0\n"
"buffer_load_dwordx4 v249, s[76:79], s89 offen offset:2048 lds\n"
"ds_read_b64_tr_b16 v[108:109], v244 offset:49664\n"
"v_mfma_f32_16x16x32_bf16 v[40:43], v[112:115], v[180:183], v[40:43]\n"
"buffer_load_dwordx4 v250, s[76:79], s89 offen offset:3072 lds\n"
"ds_read_b64_tr_b16 v[110:111], v244 offset:57856\n"
"v_mfma_f32_16x16x32_bf16 v[80:83], v[116:119], v[196:199], v[80:83]\n"
"ds_read_b64_tr_b16 v[112:113], v245 offset:49664\n"
"v_mfma_f32_16x16x32_bf16 v[44:47], v[116:119], v[180:183], v[44:47]\n"
"ds_read_b64_tr_b16 v[114:115], v245 offset:57856\n"
"v_mfma_f32_16x16x32_bf16 v[84:87], v[120:123], v[196:199], v[84:87]\n"
"ds_read_b64_tr_b16 v[116:117], v244 offset:49920\n"
"v_mfma_f32_16x16x32_bf16 v[48:51], v[120:123], v[180:183], v[48:51]\n"
"ds_read_b64_tr_b16 v[118:119], v244 offset:58112\n"
"v_mfma_f32_16x16x32_bf16 v[88:91], v[160:163], v[208:211], v[88:91]\n"
"s_waitcnt vmcnt(19) lgkmcnt(12)\n"
"v_mfma_f32_16x16x32_bf16 v[52:55], v[124:127], v[200:203], v[52:55]\n"
"ds_read_b64_tr_b16 v[120:121], v245 offset:49920\n"
"s_waitcnt vmcnt(18)\n"
"v_mfma_f32_16x16x32_bf16 v[16:19], v[124:127], v[184:187], v[16:19]\n"
"s_add_u32 s61, %[os3], 9216\n"
"buffer_load_dwordx4 v[196:199], %[l16], %[srdo], s61 offen offset:1024 sc1\n"
"ds_read_b64_tr_b16 v[122:123], v245 offset:58112\n"
"s_waitcnt lgkmcnt(11)\n"
"v_mfma_f32_16x16x32_bf16 v[88:91], v[124:127], v[216:219], v[88:91]\n"
"buffer_load_dwordx4 v[180:183], %[l16], %[srdo], s61 offen sc1\n"
"v_mfma_f32_16x16x32_bf16 v[92:95], v[164:167], v[208:211], v[92:95]\n"
"v_mfma_f32_16x16x32_bf16 v[56:59], v[224:227], v[200:203], v[56:59]\n"
"ds_read_b64_tr_b16 v[124:125], v246 offset:6144\n"
"v_mfma_f32_16x16x32_bf16 v[20:23], v[224:227], v[184:187], v[20:23]\n"
"ds_read_b64_tr_b16 v[126:127], v246 offset:6656\n"
"v_mfma_f32_16x16x32_bf16 v[92:95], v[224:227], v[216:219], v[92:95]\n"
"v_mfma_f32_16x16x32_bf16 v[88:91], v[168:171], v[212:215], v[88:91]\n"
"v_mfma_f32_16x16x32_bf16 v[60:63], v[100:103], v[200:203], v[60:63]\n"
"ds_read_b64_tr_b16 v[224:225], v244 offset:51200\n"
"v_mfma_f32_16x16x32_bf16 v[24:27], v[100:103], v[184:187], v[24:27]\n"
"ds_read_b64_tr_b16 v[226:227], v244 offset:59392\n"
"v_mfma_f32_16x16x32_bf16 v[92:95], v[172:175], v[212:215], v[92:95]\n"
"s_waitcnt lgkmcnt(12)\n"
"v_mfma_f32_16x16x32_bf16 v[64:67], v[104:107], v[200:203], v[64:67]\n"
"ds_read_b64_tr_b16 v[100:101], v245 offset:51200\n"
"v_mfma_f32_16x16x32_bf16 v[28:31], v[104:107], v[184:187], v[28:31]\n"
"ds_read_b64_tr_b16 v[102:103], v245 offset:59392\n"
"s_waitcnt lgkmcnt(12)\n"
"v_mfma_f32_16x16x32_bf16 v[68:71], v[108:111], v[200:203], v[68:71]\n"
"ds_read_b64_tr_b16 v[104:105], v244 offset:51456\n"
"v_mfma_f32_16x16x32_bf16 v[32:35], v[108:111], v[184:187], v[32:35]\n"
"ds_read_b64_tr_b16 v[106:107], v244 offset:59648\n"
"s_waitcnt lgkmcnt(12)\n"
"v_mfma_f32_16x16x32_bf16 v[72:75], v[112:115], v[200:203], v[72:75]\n"
"ds_read_b64_tr_b16 v[108:109], v245 offset:51456\n"
"v_mfma_f32_16x16x32_bf16 v[36:39], v[112:115], v[184:187], v[36:39]\n"
"ds_read_b64_tr_b16 v[110:111], v245 offset:59648\n"
"s_waitcnt lgkmcnt(12)\n"
"v_mfma_f32_16x16x32_bf16 v[76:79], v[116:119], v[200:203], v[76:79]\n"
"ds_read_b64_tr_b16 v[112:113], v244 offset:51712\n"
"v_mfma_f32_16x16x32_bf16 v[40:43], v[116:119], v[184:187], v[40:43]\n"
"ds_read_b64_tr_b16 v[114:115], v244 offset:59904\n"
"s_waitcnt lgkmcnt(12)\n"
"v_mfma_f32_16x16x32_bf16 v[80:83], v[120:123], v[200:203], v[80:83]\n"
"ds_read_b64_tr_b16 v[116:117], v245 offset:51712\n"
"v_mfma_f32_16x16x32_bf16 v[44:47], v[120:123], v[184:187], v[44:47]\n"
"ds_read_b64_tr_b16 v[118:119], v245 offset:59904\n"
"s_waitcnt lgkmcnt(12)\n"
"v_mfma_f32_16x16x32_bf16 v[84:87], v[124:127], v[200:203], v[84:87]\n"
"ds_read_b64_tr_b16 v[120:121], v244 offset:51968\n"
"v_mfma_f32_16x16x32_bf16 v[48:51], v[124:127], v[184:187], v[48:51]\n"
"ds_read_b64_tr_b16 v[122:123], v244 offset:60160\n"
"s_waitcnt vmcnt(19) lgkmcnt(12)\n"
"v_mfma_f32_16x16x32_bf16 v[52:55], v[224:227], v[204:207], v[52:55]\n"
"ds_read_b64_tr_b16 v[124:125], v245 offset:51968\n"
"s_waitcnt vmcnt(18)\n"
"v_mfma_f32_16x16x32_bf16 v[16:19], v[224:227], v[188:191], v[16:19]\n"
"s_add_u32 s62, %[os3], 18432\n"
"buffer_load_dwordx4 v[200:203], %[l16], %[srdo], s62 offen offset:1024 sc1\n"
"ds_read_b64_tr_b16 v[126:127], v245 offset:60160\n"
"v_mfma_f32_16x16x32_bf16 v[88:91], v[224:227], v[220:223], v[88:91]\n"
"buffer_load_dwordx4 v[184:187], %[l16], %[srdo], s62 offen sc1\n"
"s_waitcnt lgkmcnt(12)\n"
"v_mfma_f32_16x16x32_bf16 v[56:59], v[100:103], v[204:207], v[56:59]\n"
"ds_read_b64_tr_b16 v[224:225], v246 offset:7168\n"
"v_mfma_f32_16x16x32_bf16 v[20:23], v[100:103], v[188:191], v[20:23]\n"
"ds_read_b64_tr_b16 v[226:227], v246 offset:7680\n"
"v_mfma_f32_16x16x32_bf16 v[92:95], v[100:103], v[220:223], v[92:95]\n"
"s_waitcnt lgkmcnt(12)\n"
"v_mfma_f32_16x16x32_bf16 v[60:63], v[104:107], v[204:207], v[60:63]\n"
"v_mfma_f32_16x16x32_bf16 v[24:27], v[104:107], v[188:191], v[24:27]\n"
"s_waitcnt vmcnt(19)\n"
"ds_write_b128 v247, v[240:243] offset:4096\n"
"s_waitcnt lgkmcnt(11)\n"
"v_mfma_f32_16x16x32_bf16 v[64:67], v[108:111], v[204:207], v[64:67]\n"
"v_mfma_f32_16x16x32_bf16 v[28:31], v[108:111], v[188:191], v[28:31]\n"
"s_waitcnt lgkmcnt(9)\n"
"v_mfma_f32_16x16x32_bf16 v[68:71], v[112:115], v[204:207], v[68:71]\n"
"s_waitcnt lgkmcnt(0)\n"
"s_barrier\n"
"s_add_u32 s64, %[os3], 8192\n"
"buffer_load_dwordx4 v[240:243], %[l16], %[srdo], s64 offen sc1\n"
"ds_read_b64_tr_b16 v[100:101], v244 offset:0\n"
"ds_read_b64_tr_b16 v[104:105], v245 offset:0\n"
"ds_read_b64_tr_b16 v[108:109], v244 offset:256\n"
"v_mfma_f32_16x16x32_bf16 v[32:35], v[112:115], v[188:191], v[32:35]\n"
"ds_read_b128 v[160:163], v247 offset:4096\n"
"s_lshl_b32 s88, %[os3], 2\n"
"s_add_u32 s88, s88, 73728\n"
"s_mov_b32 m0, s55\n"
"s_nop 0\n"
"buffer_load_dwordx4 v249, s[76:79], s88 offen lds\n"
"ds_read_b64_tr_b16 v[102:103], v244 offset:8192\n"
"ds_read_b64_tr_b16 v[106:107], v245 offset:8192\n"
"ds_read_b64_tr_b16 v[110:111], v244 offset:8448\n"
"v_mfma_f32_16x16x32_bf16 v[72:75], v[116:119], v[204:207], v[72:75]\n"
"ds_read_b128 v[164:167], v247 offset:5120\n"
"buffer_load_dwordx4 v250, s[76:79], s88 offen offset:1024 lds\n"
"ds_read_b64_tr_b16 v[112:113], v245 offset:256\n"
"v_mfma_f32_16x16x32_bf16 v[36:39], v[116:119], v[188:191], v[36:39]\n"
"ds_read_b128 v[168:171], v247 offset:6144\n"
"s_lshl_b32 s89, %[os3], 2\n"
"s_add_u32 s89, s89, 108544\n"
"buffer_load_dwordx4 v249, s[76:79], s89 offen offset:2048 lds\n"
"ds_read_b64_tr_b16 v[114:115], v245 offset:8448\n"
"v_mfma_f32_16x16x32_bf16 v[76:79], v[120:123], v[204:207], v[76:79]\n"
"ds_read_b128 v[172:175], v247 offset:7168\n"
"buffer_load_dwordx4 v250, s[76:79], s89 offen offset:3072 lds\n"
"ds_read_b64_tr_b16 v[116:117], v244 offset:512\n"
"v_mfma_f32_16x16x32_bf16 v[40:43], v[120:123], v[188:191], v[40:43]\n"
"ds_read_b64_tr_b16 v[118:119], v244 offset:8704\n"
"v_mfma_f32_16x16x32_bf16 v[80:83], v[124:127], v[204:207], v[80:83]\n"
"ds_read_b64_tr_b16 v[120:121], v245 offset:512\n"
"v_mfma_f32_16x16x32_bf16 v[44:47], v[124:127], v[188:191], v[44:47]\n"
"ds_read_b64_tr_b16 v[122:123], v245 offset:8704\n"
"v_mfma_f32_16x16x32_bf16 v[84:87], v[224:227], v[204:207], v[84:87]\n"
"ds_read_b64_tr_b16 v[124:125], v244 offset:768\n"
"v_mfma_f32_16x16x32_bf16 v[48:51], v[224:227], v[188:191], v[48:51]\n"
"ds_read_b64_tr_b16 v[126:127], v244 offset:8960\n"
"s_waitcnt vmcnt(23) lgkmcnt(13)\n"
"v_mfma_f32_16x16x32_bf16 v[52:55], v[100:103], v[144:147], v[52:55]\n"
"ds_read_b64_tr_b16 v[224:225], v245 offset:768\n"
"s_waitcnt vmcnt(22)\n"
"v_mfma_f32_16x16x32_bf16 v[16:19], v[100:103], v[128:131], v[16:19]\n"
"s_add_u32 s63, %[os3], 27648\n"
"buffer_load_dwordx4 v[204:207], %[l16], %[srdo], s63 offen offset:1024 sc1\n"
"ds_read_b64_tr_b16 v[226:227], v245 offset:8960\n"
"v_mfma_f32_16x16x32_bf16 v[88:91], v[100:103], v[160:163], v[88:91]\n"
"buffer_load_dwordx4 v[188:191], %[l16], %[srdo], s63 offen sc1\n"
"s_waitcnt lgkmcnt(14)\n"
"v_mfma_f32_16x16x32_bf16 v[56:59], v[104:107], v[144:147], v[56:59]\n"
"ds_read_b64_tr_b16 v[100:101], v246 offset:0\n"
"v_mfma_f32_16x16x32_bf16 v[20:23], v[104:107], v[128:131], v[20:23]\n"
"ds_read_b64_tr_b16 v[102:103], v246 offset:512\n"
"v_mfma_f32_16x16x32_bf16 v[92:95], v[104:107], v[160:163], v[92:95]\n"
"s_waitcnt lgkmcnt(15)\n"
"v_mfma_f32_16x16x32_bf16 v[60:63], v[108:111], v[144:147], v[60:63]\n"
"ds_read_b64_tr_b16 v[104:105], v244 offset:2048\n"
"v_mfma_f32_16x16x32_bf16 v[24:27], v[108:111], v[128:131], v[24:27]\n"
"ds_read_b64_tr_b16 v[106:107], v244 offset:10240\n"
"s_waitcnt lgkmcnt(13)\n"
"v_mfma_f32_16x16x32_bf16 v[64:67], v[112:115], v[144:147], v[64:67]\n"
"ds_read_b64_tr_b16 v[108:109], v245 offset:2048\n"
"v_mfma_f32_16x16x32_bf16 v[28:31], v[112:115], v[128:131], v[28:31]\n"
"ds_read_b64_tr_b16 v[110:111], v245 offset:10240\n"
"s_waitcnt lgkmcnt(12)\n"
"v_mfma_f32_16x16x32_bf16 v[68:71], v[116:119], v[144:147], v[68:71]\n"
"ds_read_b64_tr_b16 v[112:113], v244 offset:2304\n"
"v_mfma_f32_16x16x32_bf16 v[32:35], v[116:119], v[128:131], v[32:35]\n"
"ds_read_b64_tr_b16 v[114:115], v244 offset:10496\n"
"s_waitcnt lgkmcnt(12)\n"
"v_mfma_f32_16x16x32_bf16 v[72:75], v[120:123], v[144:147], v[72:75]\n"
"ds_read_b64_tr_b16 v[116:117], v245 offset:2304\n"
"v_mfma_f32_16x16x32_bf16 v[36:39], v[120:123], v[128:131], v[36:39]\n"
"ds_read_b64_tr_b16 v[118:119], v245 offset:10496\n"
"s_waitcnt lgkmcnt(12)\n"
"v_mfma_f32_16x16x32_bf16 v[76:79], v[124:127], v[144:147], v[76:79]\n"
"ds_read_b64_tr_b16 v[120:121], v244 offset:2560\n"
"v_mfma_f32_16x16x32_bf16 v[40:43], v[124:127], v[128:131], v[40:43]\n"
"ds_read_b64_tr_b16 v[122:123], v244 offset:10752\n"
"s_waitcnt lgkmcnt(12)\n"
"v_mfma_f32_16x16x32_bf16 v[80:83], v[224:227], v[144:147], v[80:83]\n"
"ds_read_b64_tr_b16 v[124:125], v245 offset:2560\n"
"v_mfma_f32_16x16x32_bf16 v[44:47], v[224:227], v[128:131], v[44:47]\n"
"ds_read_b64_tr_b16 v[126:127], v245 offset:10752\n"
"s_waitcnt lgkmcnt(12)\n"
"v_mfma_f32_16x16x32_bf16 v[84:87], v[100:103], v[144:147], v[84:87]\n"
"ds_read_b64_tr_b16 v[224:225], v244 offset:2816\n"
"v_mfma_f32_16x16x32_bf16 v[48:51], v[100:103], v[128:131], v[48:51]\n"
"ds_read_b64_tr_b16 v[226:227], v244 offset:11008\n"
"s_waitcnt vmcnt(23) lgkmcnt(12)\n"
"v_mfma_f32_16x16x32_bf16 v[52:55], v[104:107], v[148:151], v[52:55]\n"
"ds_read_b64_tr_b16 v[100:101], v245 offset:2816\n"
"s_waitcnt vmcnt(22)\n"
"v_mfma_f32_16x16x32_bf16 v[16:19], v[104:107], v[132:135], v[16:19]\n"
"ds_read_b64_tr_b16 v[102:103], v245 offset:11008\n"
"v_mfma_f32_16x16x32_bf16 v[88:91], v[104:107], v[164:167], v[88:91]\n"
"s_waitcnt lgkmcnt(12)\n"
"v_mfma_f32_16x16x32_bf16 v[56:59], v[108:111], v[148:151], v[56:59]\n"
"ds_read_b64_tr_b16 v[104:105], v246 offset:1024\n"
"v_mfma_f32_16x16x32_bf16 v[20:23], v[108:111], v[132:135], v[20:23]\n"
"ds_read_b64_tr_b16 v[106:107], v246 offset:1536\n"
"v_mfma_f32_16x16x32_bf16 v[92:95], v[108:111], v[164:167], v[92:95]\n"
"s_waitcnt lgkmcnt(12)\n"
"v_mfma_f32_16x16x32_bf16 v[60:63], v[112:115], v[148:151], v[60:63]\n"
"v_mfma_f32_16x16x32_bf16 v[24:27], v[112:115], v[132:135], v[24:27]\n"
"s_waitcnt lgkmcnt(10)\n"
"v_mfma_f32_16x16x32_bf16 v[64:67], v[116:119], v[148:151], v[64:67]\n"
"v_mfma_f32_16x16x32_bf16 v[28:31], v[116:119], v[132:135], v[28:31]\n"
"s_waitcnt lgkmcnt(8)\n"
"v_mfma_f32_16x16x32_bf16 v[68:71], v[120:123], v[148:151], v[68:71]\n"
"s_barrier\n"
"ds_read_b64_tr_b16 v[108:109], v244 offset:16384\n"
"ds_read_b64_tr_b16 v[112:113], v245 offset:16384\n"
"ds_read_b64_tr_b16 v[116:117], v244 offset:16640\n"
"v_mfma_f32_16x16x32_bf16 v[32:35], v[120:123], v[132:135], v[32:35]\n"
"ds_read_b64_tr_b16 v[110:111], v244 offset:24576\n"
"ds_read_b64_tr_b16 v[114:115], v245 offset:24576\n"
"ds_read_b64_tr_b16 v[118:119], v244 offset:24832\n"
"s_waitcnt lgkmcnt(12)\n"
"v_mfma_f32_16x16x32_bf16 v[72:75], v[124:127], v[148:151], v[72:75]\n"
"ds_read_b64_tr_b16 v[120:121], v245 offset:16640\n"
"v_mfma_f32_16x16x32_bf16 v[36:39], v[124:127], v[132:135], v[36:39]\n"
"ds_read_b64_tr_b16 v[122:123], v245 offset:24832\n"
"s_waitcnt lgkmcnt(12)\n"
"v_mfma_f32_16x16x32_bf16 v[76:79], v[224:227], v[148:151], v[76:79]\n"
"ds_read_b64_tr_b16 v[124:125], v244 offset:16896\n"
"v_mfma_f32_16x16x32_bf16 v[40:43], v[224:227], v[132:135], v[40:43]\n"
"ds_read_b64_tr_b16 v[126:127], v244 offset:25088\n"
"s_waitcnt lgkmcnt(12)\n"
"v_mfma_f32_16x16x32_bf16 v[80:83], v[100:103], v[148:151], v[80:83]\n"
"ds_read_b64_tr_b16 v[224:225], v245 offset:16896\n"
"v_mfma_f32_16x16x32_bf16 v[44:47], v[100:103], v[132:135], v[44:47]\n"
"ds_read_b64_tr_b16 v[226:227], v245 offset:25088\n"
"s_waitcnt lgkmcnt(12)\n"
"v_mfma_f32_16x16x32_bf16 v[84:87], v[104:107], v[148:151], v[84:87]\n"
"ds_read_b64_tr_b16 v[100:101], v244 offset:17152\n"
"v_mfma_f32_16x16x32_bf16 v[48:51], v[104:107], v[132:135], v[48:51]\n"
"ds_read_b64_tr_b16 v[102:103], v244 offset:25344\n"
"s_waitcnt vmcnt(21) lgkmcnt(10)\n"
"v_mfma_f32_16x16x32_bf16 v[52:55], v[108:111], v[152:155], v[52:55]\n"
"ds_read_b64_tr_b16 v[104:105], v245 offset:17152\n"
"s_waitcnt vmcnt(20)\n"
"v_mfma_f32_16x16x32_bf16 v[16:19], v[108:111], v[136:139], v[16:19]\n"
"ds_read_b64_tr_b16 v[106:107], v245 offset:25344\n"
"v_mfma_f32_16x16x32_bf16 v[88:91], v[108:111], v[168:171], v[88:91]\n"
"s_waitcnt lgkmcnt(11)\n"
"v_mfma_f32_16x16x32_bf16 v[56:59], v[112:115], v[152:155], v[56:59]\n"
"ds_read_b64_tr_b16 v[108:109], v246 offset:2048\n"
"v_mfma_f32_16x16x32_bf16 v[20:23], v[112:115], v[136:139], v[20:23]\n"
"ds_read_b64_tr_b16 v[110:111], v246 offset:2560\n"
"v_mfma_f32_16x16x32_bf16 v[92:95], v[112:115], v[168:171], v[92:95]\n"
"s_waitcnt lgkmcnt(12)\n"
"v_mfma_f32_16x16x32_bf16 v[60:63], v[116:119], v[152:155], v[60:63]\n"
"ds_read_b64_tr_b16 v[112:113], v244 offset:18432\n"
"v_mfma_f32_16x16x32_bf16 v[24:27], v[116:119], v[136:139], v[24:27]\n"
"ds_read_b64_tr_b16 v[114:115], v244 offset:26624\n"
"s_waitcnt lgkmcnt(12)\n"
"v_mfma_f32_16x16x32_bf16 v[64:67], v[120:123], v[152:155], v[64:67]\n"
"ds_read_b64_tr_b16 v[116:117], v245 offset:18432\n"
"v_mfma_f32_16x16x32_bf16 v[28:31], v[120:123], v[136:139], v[28:31]\n"
"ds_read_b64_tr_b16 v[118:119], v245 offset:26624\n"
"s_waitcnt lgkmcnt(12)\n"
"v_mfma_f32_16x16x32_bf16 v[68:71], v[124:127], v[152:155], v[68:71]\n"
"ds_read_b64_tr_b16 v[120:121], v244 offset:18688\n"
"v_mfma_f32_16x16x32_bf16 v[32:35], v[124:127], v[136:139], v[32:35]\n"
"ds_read_b64_tr_b16 v[122:123], v244 offset:26880\n"
"s_waitcnt lgkmcnt(12)\n"
"v_mfma_f32_16x16x32_bf16 v[72:75], v[224:227], v[152:155], v[72:75]\n"
"ds_read_b64_tr_b16 v[124:125], v245 offset:18688\n"
"v_mfma_f32_16x16x32_bf16 v[36:39], v[224:227], v[136:139], v[36:39]\n"
"ds_read_b64_tr_b16 v[126:127], v245 offset:26880\n"
"s_waitcnt lgkmcnt(12)\n"
"v_mfma_f32_16x16x32_bf16 v[76:79], v[100:103], v[152:155], v[76:79]\n"
"ds_read_b64_tr_b16 v[224:225], v244 offset:18944\n"
"v_mfma_f32_16x16x32_bf16 v[40:43], v[100:103], v[136:139], v[40:43]\n"
"ds_read_b64_tr_b16 v[226:227], v244 offset:27136\n"
"s_waitcnt lgkmcnt(12)\n"
"v_mfma_f32_16x16x32_bf16 v[80:83], v[104:107], v[152:155], v[80:83]\n"
"ds_read_b64_tr_b16 v[100:101], v245 offset:18944\n"
"v_mfma_f32_16x16x32_bf16 v[44:47], v[104:107], v[136:139], v[44:47]\n"
"ds_read_b64_tr_b16 v[102:103], v245 offset:27136\n"
"s_waitcnt lgkmcnt(12)\n"
"v_mfma_f32_16x16x32_bf16 v[84:87], v[108:111], v[152:155], v[84:87]\n"
"ds_read_b64_tr_b16 v[104:105], v244 offset:19200\n"
"v_mfma_f32_16x16x32_bf16 v[48:51], v[108:111], v[136:139], v[48:51]\n"
"ds_read_b64_tr_b16 v[106:107], v244 offset:27392\n"
"s_waitcnt vmcnt(19) lgkmcnt(12)\n"
"v_mfma_f32_16x16x32_bf16 v[52:55], v[112:115], v[156:159], v[52:55]\n"
"ds_read_b64_tr_b16 v[108:109], v245 offset:19200\n"
"s_waitcnt vmcnt(18)\n"
"v_mfma_f32_16x16x32_bf16 v[16:19], v[112:115], v[140:143], v[16:19]\n"
"ds_read_b64_tr_b16 v[110:111], v245 offset:27392\n"
"v_mfma_f32_16x16x32_bf16 v[88:91], v[112:115], v[172:175], v[88:91]\n"
"s_waitcnt lgkmcnt(12)\n"
"v_mfma_f32_16x16x32_bf16 v[56:59], v[116:119], v[156:159], v[56:59]\n"
"ds_read_b64_tr_b16 v[112:113], v246 offset:3072\n"
"v_mfma_f32_16x16x32_bf16 v[20:23], v[116:119], v[140:143], v[20:23]\n"
"ds_read_b64_tr_b16 v[114:115], v246 offset:3584\n"
"v_mfma_f32_16x16x32_bf16 v[92:95], v[116:119], v[172:175], v[92:95]\n"
"s_waitcnt lgkmcnt(12)\n"
"v_mfma_f32_16x16x32_bf16 v[60:63], v[120:123], v[156:159], v[60:63]\n"
"v_mfma_f32_16x16x32_bf16 v[24:27], v[120:123], v[140:143], v[24:27]\n"
"s_waitcnt vmcnt(6)\n"
"ds_write_b128 v247, v[240:243] offset:0\n"
"s_waitcnt lgkmcnt(11)\n"
"v_mfma_f32_16x16x32_bf16 v[64:67], v[124:127], v[156:159], v[64:67]\n"
"v_mfma_f32_16x16x32_bf16 v[28:31], v[124:127], v[140:143], v[28:31]\n"
"s_waitcnt lgkmcnt(9)\n"
"v_mfma_f32_16x16x32_bf16 v[68:71], v[224:227], v[156:159], v[68:71]\n"
"s_waitcnt lgkmcnt(0)\n"
"s_barrier\n"
"ds_read_b64_tr_b16 v[116:117], v244 offset:33024\n"
"ds_read_b64_tr_b16 v[120:121], v245 offset:33024\n"
"ds_read_b64_tr_b16 v[124:125], v244 offset:33280\n"
"v_mfma_f32_16x16x32_bf16 v[32:35], v[224:227], v[140:143], v[32:35]\n"
"ds_read_b128 v[208:211], v247 offset:0\n"
"ds_read_b64_tr_b16 v[118:119], v244 offset:41216\n"
"ds_read_b64_tr_b16 v[122:123], v245 offset:41216\n"
"ds_read_b64_tr_b16 v[126:127], v244 offset:41472\n"
"v_mfma_f32_16x16x32_bf16 v[72:75], v[100:103], v[156:159], v[72:75]\n"
"ds_read_b128 v[212:215], v247 offset:1024\n"
"ds_read_b64_tr_b16 v[224:225], v245 offset:33280\n"
"v_mfma_f32_16x16x32_bf16 v[36:39], v[100:103], v[140:143], v[36:39]\n"
"ds_read_b128 v[216:219], v247 offset:2048\n"
"ds_read_b64_tr_b16 v[226:227], v245 offset:41472\n"
"v_mfma_f32_16x16x32_bf16 v[76:79], v[104:107], v[156:159], v[76:79]\n"
"ds_read_b128 v[220:223], v247 offset:3072\n"
"ds_read_b64_tr_b16 v[100:101], v244 offset:33536\n"
"v_mfma_f32_16x16x32_bf16 v[40:43], v[104:107], v[140:143], v[40:43]\n"
"ds_read_b64_tr_b16 v[102:103], v244 offset:41728\n"
"v_mfma_f32_16x16x32_bf16 v[80:83], v[108:111], v[156:159], v[80:83]\n"
"ds_read_b64_tr_b16 v[104:105], v245 offset:33536\n"
"v_mfma_f32_16x16x32_bf16 v[44:47], v[108:111], v[140:143], v[44:47]\n"
"ds_read_b64_tr_b16 v[106:107], v245 offset:41728\n"
"v_mfma_f32_16x16x32_bf16 v[84:87], v[112:115], v[156:159], v[84:87]\n"
"ds_read_b64_tr_b16 v[108:109], v246 offset:4096\n"
"v_mfma_f32_16x16x32_bf16 v[48:51], v[112:115], v[140:143], v[48:51]\n"
"ds_read_b64_tr_b16 v[110:111], v246 offset:4608\n"
"s_waitcnt lgkmcnt(13)\n"
"v_mfma_f32_16x16x32_bf16 v[60:63], v[116:119], v[192:195], v[60:63]\n"
"ds_read_b64_tr_b16 v[112:113], v244 offset:35072\n"
"v_mfma_f32_16x16x32_bf16 v[24:27], v[116:119], v[176:179], v[24:27]\n"
"ds_read_b64_tr_b16 v[114:115], v244 offset:43264\n"
"s_waitcnt lgkmcnt(14)\n"
"v_mfma_f32_16x16x32_bf16 v[64:67], v[120:123], v[192:195], v[64:67]\n"
"ds_read_b64_tr_b16 v[116:117], v245 offset:35072\n"
"s_add_u32 s68, %[hb], 0\n"
"v_add_u32 v229, s68, %[l16]\n"
"v_xor_b32 v230, 65536, v229\n"
"v_mfma_f32_16x16x32_bf16 v[28:31], v[120:123], v[176:179], v[28:31]\n"
"ds_read_b64_tr_b16 v[118:119], v245 offset:43264\n"
"v_xor_b32 v234, 8192, v247\n"
"s_lshr_b32 s64, %[kq64], 6\n"
"s_lshr_b32 s65, %[hb], 6\n"
"s_waitcnt lgkmcnt(15)\n"
"v_mfma_f32_16x16x32_bf16 v[68:71], v[124:127], v[192:195], v[68:71]\n"
"ds_read_b64_tr_b16 v[120:121], v244 offset:35328\n"
"s_add_u32 s60, s64, 1\n"
"s_and_b32 s60, s60, 3\n"
"s_lshl_b32 s60, s60, 16\n"
"v_mfma_f32_16x16x32_bf16 v[32:35], v[124:127], v[176:179], v[32:35]\n"
"ds_read_b64_tr_b16 v[122:123], v244 offset:43520\n"
"s_add_u32 s60, s60, %[pbase]\n"
"s_add_u32 s60, s60, 0\n"
"s_add_u32 s61, s64, 2\n"
"s_waitcnt lgkmcnt(13)\n"
"v_mfma_f32_16x16x32_bf16 v[72:75], v[224:227], v[192:195], v[72:75]\n"
"ds_read_b64_tr_b16 v[124:125], v245 offset:35328\n"
"s_and_b32 s61, s61, 3\n"
"s_lshl_b32 s61, s61, 16\n"
"s_add_u32 s61, s61, %[pbase]\n"
"v_mfma_f32_16x16x32_bf16 v[36:39], v[224:227], v[176:179], v[36:39]\n"
"ds_read_b64_tr_b16 v[126:127], v245 offset:43520\n"
"s_add_u32 s61, s61, 21504\n"
"s_add_u32 s62, s64, 3\n"
"s_and_b32 s62, s62, 3\n"
"s_waitcnt lgkmcnt(12)\n"
"v_mfma_f32_16x16x32_bf16 v[76:79], v[100:103], v[192:195], v[76:79]\n"
"ds_read_b64_tr_b16 v[224:225], v244 offset:35584\n"
"s_lshl_b32 s62, s62, 16\n"
"s_add_u32 s62, s62, %[pbase]\n"
"s_add_u32 s62, s62, 43008\n"
"v_mfma_f32_16x16x32_bf16 v[40:43], v[100:103], v[176:179], v[40:43]\n"
"ds_read_b64_tr_b16 v[226:227], v244 offset:43776\n"
"s_lshl_b32 s63, s64, 16\n"
"s_add_u32 s63, s63, %[pbase]\n"
"s_lshl_b32 s66, s65, 1\n"
"s_waitcnt lgkmcnt(12)\n"
"v_mfma_f32_16x16x32_bf16 v[80:83], v[104:107], v[192:195], v[80:83]\n"
"ds_read_b64_tr_b16 v[100:101], v245 offset:35584\n"
"s_add_u32 s66, s66, 0\n"
"v_add_u32 v232, s66, %[l16]\n"
"s_add_u32 s66, s65, 16384\n"
"v_mfma_f32_16x16x32_bf16 v[44:47], v[104:107], v[176:179], v[44:47]\n"
"ds_read_b64_tr_b16 v[102:103], v245 offset:43776\n"
"v_add_u32 v228, s66, %[l16]\n"
"s_sub_u32 s67, 3, s64\n"
"s_and_b32 s67, s67, 3\n"
"s_waitcnt lgkmcnt(12)\n"
"v_mfma_f32_16x16x32_bf16 v[84:87], v[108:111], v[192:195], v[84:87]\n"
"ds_read_b64_tr_b16 v[104:105], v246 offset:5120\n"
"s_mul_i32 s67, s67, 21504\n"
"s_add_u32 s67, s67, %[pbase]\n"
"s_add_u32 s67, s67, 0\n"
"v_mfma_f32_16x16x32_bf16 v[48:51], v[108:111], v[176:179], v[48:51]\n"
"ds_read_b64_tr_b16 v[106:107], v246 offset:5632\n"
"v_lshrrev_b32 v231, 4, %[l16]\n"
"v_and_b32_e64 v231, 15, v231\n"
"s_movk_i32 s68, 2304\n"
"s_waitcnt lgkmcnt(12)\n"
"v_mfma_f32_16x16x32_bf16 v[60:63], v[112:115], v[196:199], v[60:63]\n"
"v_mul_u32_u24 v231, s68, v231\n"
"v_lshrrev_b32_e64 v233, 8, %[l16]\n"
"v_lshl_add_u32 v231, v233, 3, v231\n"
"v_mfma_f32_16x16x32_bf16 v[24:27], v[112:115], v[180:183], v[24:27]\n"
"s_waitcnt lgkmcnt(10)\n"
"v_mfma_f32_16x16x32_bf16 v[64:67], v[116:119], v[196:199], v[64:67]\n"
"v_mfma_f32_16x16x32_bf16 v[28:31], v[116:119], v[180:183], v[28:31]\n"
"s_waitcnt lgkmcnt(8)\n"
"v_mfma_f32_16x16x32_bf16 v[68:71], v[120:123], v[196:199], v[68:71]\n"
"s_waitcnt vmcnt(2) lgkmcnt(0)\n"
"s_barrier\n"
"ds_read_b64_tr_b16 v[108:109], v244 offset:49408\n"
"ds_read_b64_tr_b16 v[112:113], v245 offset:49408\n"
"ds_read_b64_tr_b16 v[116:117], v244 offset:51456\n"
"v_mfma_f32_16x16x32_bf16 v[32:35], v[120:123], v[180:183], v[32:35]\n"
"ds_read_b64_tr_b16 v[110:111], v244 offset:57600\n"
"ds_read_b64_tr_b16 v[114:115], v245 offset:57600\n"
"ds_read_b64_tr_b16 v[118:119], v244 offset:59648\n"
"v_mfma_f32_16x16x32_bf16 v[72:75], v[124:127], v[196:199], v[72:75]\n"
"ds_read_b64_tr_b16 v[120:121], v245 offset:51456\n"
"v_mfma_f32_16x16x32_bf16 v[36:39], v[124:127], v[180:183], v[36:39]\n"
"ds_read_b64_tr_b16 v[122:123], v245 offset:59648\n"
"v_mfma_f32_16x16x32_bf16 v[76:79], v[224:227], v[196:199], v[76:79]\n"
"ds_read_b64_tr_b16 v[124:125], v244 offset:49664\n"
"v_mfma_f32_16x16x32_bf16 v[40:43], v[224:227], v[180:183], v[40:43]\n"
"ds_read_b64_tr_b16 v[126:127], v244 offset:57856\n"
"v_mfma_f32_16x16x32_bf16 v[80:83], v[100:103], v[196:199], v[80:83]\n"
"ds_read_b64_tr_b16 v[224:225], v245 offset:49664\n"
"v_mfma_f32_16x16x32_bf16 v[44:47], v[100:103], v[180:183], v[44:47]\n"
"ds_read_b64_tr_b16 v[226:227], v245 offset:57856\n"
"v_mfma_f32_16x16x32_bf16 v[84:87], v[104:107], v[196:199], v[84:87]\n"
"ds_read_b64_tr_b16 v[100:101], v244 offset:51712\n"
"v_mfma_f32_16x16x32_bf16 v[48:51], v[104:107], v[180:183], v[48:51]\n"
"ds_read_b64_tr_b16 v[102:103], v244 offset:59904\n"
"s_waitcnt lgkmcnt(10)\n"
"v_mfma_f32_16x16x32_bf16 v[60:63], v[108:111], v[200:203], v[60:63]\n"
"ds_read_b64_tr_b16 v[104:105], v245 offset:51712\n"
"v_mfma_f32_16x16x32_bf16 v[24:27], v[108:111], v[184:187], v[24:27]\n"
"ds_read_b64_tr_b16 v[106:107], v245 offset:59904\n"
"s_waitcnt lgkmcnt(11)\n"
"v_mfma_f32_16x16x32_bf16 v[64:67], v[112:115], v[200:203], v[64:67]\n"
"ds_read_b64_tr_b16 v[108:109], v244 offset:49920\n"
"v_mfma_f32_16x16x32_bf16 v[28:31], v[112:115], v[184:187], v[28:31]\n"
"ds_read_b64_tr_b16 v[110:111], v244 offset:58112\n"
"s_waitcnt vmcnt(1) lgkmcnt(12)\n"
"v_mfma_f32_16x16x32_bf16 v[60:63], v[116:119], v[204:207], v[60:63]\n"
"ds_read_b64_tr_b16 v[112:113], v245 offset:49920\n"
"s_waitcnt vmcnt(0)\n"
"v_mfma_f32_16x16x32_bf16 v[24:27], v[116:119], v[188:191], v[24:27]\n"
"ds_read_b64_tr_b16 v[114:115], v245 offset:58112\n"
"s_waitcnt lgkmcnt(12)\n"
"v_mfma_f32_16x16x32_bf16 v[64:67], v[120:123], v[204:207], v[64:67]\n"
"ds_read_b64_tr_b16 v[116:117], v246 offset:6144\n"
"v_mfma_f32_16x16x32_bf16 v[28:31], v[120:123], v[188:191], v[28:31]\n"
"ds_read_b64_tr_b16 v[118:119], v246 offset:6656\n"
"s_waitcnt lgkmcnt(12)\n"
"v_mfma_f32_16x16x32_bf16 v[68:71], v[124:127], v[200:203], v[68:71]\n"
"ds_read_b64_tr_b16 v[120:121], v244 offset:51968\n"
"ds_write_b128 v229, v[60:63] offset:0\n"
"v_mfma_f32_16x16x32_bf16 v[32:35], v[124:127], v[184:187], v[32:35]\n"
"ds_read_b64_tr_b16 v[122:123], v244 offset:60160\n"
"s_waitcnt lgkmcnt(13)\n"
"v_mfma_f32_16x16x32_bf16 v[72:75], v[224:227], v[200:203], v[72:75]\n"
"ds_read_b64_tr_b16 v[124:125], v245 offset:51968\n"
"ds_write_b128 v229, v[64:67] offset:1024\n"
"v_mfma_f32_16x16x32_bf16 v[36:39], v[224:227], v[184:187], v[36:39]\n"
"ds_read_b64_tr_b16 v[126:127], v245 offset:60160\n"
"s_waitcnt lgkmcnt(14)\n"
"v_mfma_f32_16x16x32_bf16 v[68:71], v[100:103], v[204:207], v[68:71]\n"
"ds_read_b64_tr_b16 v[224:225], v246 offset:7168\n"
"v_mfma_f32_16x16x32_bf16 v[32:35], v[100:103], v[188:191], v[32:35]\n"
"ds_read_b64_tr_b16 v[226:227], v246 offset:7680\n"
"s_waitcnt lgkmcnt(14)\n"
"v_mfma_f32_16x16x32_bf16 v[72:75], v[104:107], v[204:207], v[72:75]\n"
"ds_read_b64_tr_b16 v[100:101], v244 offset:32768\n"
"v_mfma_f32_16x16x32_bf16 v[36:39], v[104:107], v[188:191], v[36:39]\n"
"ds_read_b64_tr_b16 v[102:103], v244 offset:40960\n"
"s_waitcnt lgkmcnt(14)\n"
"v_mfma_f32_16x16x32_bf16 v[76:79], v[108:111], v[200:203], v[76:79]\n"
"ds_read_b64_tr_b16 v[104:105], v245 offset:32768\n"
"ds_write_b128 v229, v[68:71] offset:2048\n"
"v_mfma_f32_16x16x32_bf16 v[40:43], v[108:111], v[184:187], v[40:43]\n"
"ds_read_b64_tr_b16 v[106:107], v245 offset:40960\n"
"s_waitcnt lgkmcnt(15)\n"
"v_mfma_f32_16x16x32_bf16 v[80:83], v[112:115], v[200:203], v[80:83]\n"
"ds_read_b64_tr_b16 v[108:109], v244 offset:34816\n"
"ds_write_b128 v229, v[72:75] offset:3072\n"
"v_mfma_f32_16x16x32_bf16 v[44:47], v[112:115], v[184:187], v[44:47]\n"
"ds_read_b64_tr_b16 v[110:111], v244 offset:43008\n"
"s_waitcnt lgkmcnt(15)\n"
"v_mfma_f32_16x16x32_bf16 v[84:87], v[116:119], v[200:203], v[84:87]\n"
"ds_read_b64_tr_b16 v[112:113], v245 offset:34816\n"
"v_mfma_f32_16x16x32_bf16 v[48:51], v[116:119], v[184:187], v[48:51]\n"
"ds_read_b64_tr_b16 v[114:115], v245 offset:43008\n"
"s_waitcnt lgkmcnt(15)\n"
"v_mfma_f32_16x16x32_bf16 v[76:79], v[120:123], v[204:207], v[76:79]\n"
"ds_read_b64_tr_b16 v[116:117], v244 offset:49152\n"
"s_waitcnt lgkmcnt(4)\n"
"s_barrier\n"
"ds_read_b128 v[128:131], v230 offset:1024\n"
"ds_read_b128 v[132:135], v230 offset:0\n"
"v_mfma_f32_16x16x32_bf16 v[40:43], v[120:123], v[188:191], v[40:43]\n"
"ds_read_b64_tr_b16 v[118:119], v244 offset:57344\n"
"ds_read_b128 v[136:139], v230 offset:3072\n"
"ds_read_b128 v[140:143], v230 offset:2048\n"
"v_mfma_f32_16x16x32_bf16 v[80:83], v[124:127], v[204:207], v[80:83]\n"
"ds_read_b64_tr_b16 v[120:121], v245 offset:49152\n"
"v_mfma_f32_16x16x32_bf16 v[44:47], v[124:127], v[188:191], v[44:47]\n"
"ds_read_b64_tr_b16 v[122:123], v245 offset:57344\n"
"s_waitcnt lgkmcnt(6)\n"
"v_pk_add_f32 v[24:25], v[24:25], v[128:129]\n"
"v_pk_add_f32 v[26:27], v[26:27], v[130:131]\n"
"buffer_store_dwordx4 v[24:27], v232, s[80:83], s60 offen sc1\n"
"v_mfma_f32_16x16x32_bf16 v[84:87], v[224:227], v[204:207], v[84:87]\n"
"ds_read_b64_tr_b16 v[124:125], v244 offset:51200\n"
"s_waitcnt lgkmcnt(6)\n"
"v_pk_add_f32 v[28:29], v[28:29], v[132:133]\n"
"v_pk_add_f32 v[30:31], v[30:31], v[134:135]\n"
"buffer_store_dwordx4 v[28:31], v232, s[80:83], s60 offen offset:1024 sc1\n"
"ds_write_b128 v229, v[76:79] offset:4096\n"
"v_mfma_f32_16x16x32_bf16 v[48:51], v[224:227], v[188:191], v[48:51]\n"
"ds_read_b64_tr_b16 v[126:127], v244 offset:59392\n"
"s_waitcnt lgkmcnt(6)\n"
"v_pk_add_f32 v[32:33], v[32:33], v[136:137]\n"
"v_pk_add_f32 v[34:35], v[34:35], v[138:139]\n"
"buffer_store_dwordx4 v[32:35], v232, s[80:83], s61 offen sc1\n"
"v_mfma_f32_16x16x32_bf16 v[52:55], v[100:103], v[192:195], v[52:55]\n"
"ds_read_b64_tr_b16 v[224:225], v245 offset:51200\n"
"s_waitcnt lgkmcnt(6)\n"
"v_pk_add_f32 v[36:37], v[36:37], v[140:141]\n"
"v_pk_add_f32 v[38:39], v[38:39], v[142:143]\n"
"buffer_store_dwordx4 v[36:39], v232, s[80:83], s61 offen offset:1024 sc1\n"
"ds_write_b128 v229, v[80:83] offset:5120\n"
"v_mfma_f32_16x16x32_bf16 v[16:19], v[100:103], v[176:179], v[16:19]\n"
"ds_read_b64_tr_b16 v[226:227], v245 offset:59392\n"
"v_mfma_f32_16x16x32_bf16 v[88:91], v[100:103], v[208:211], v[88:91]\n"
"ds_write_b128 v229, v[84:87] offset:6144\n"
"v_mfma_f32_16x16x32_bf16 v[56:59], v[104:107], v[192:195], v[56:59]\n"
"v_mfma_f32_16x16x32_bf16 v[20:23], v[104:107], v[176:179], v[20:23]\n"
"v_mfma_f32_16x16x32_bf16 v[92:95], v[104:107], v[208:211], v[92:95]\n"
"v_mfma_f32_16x16x32_bf16 v[52:55], v[108:111], v[196:199], v[52:55]\n"
"v_mfma_f32_16x16x32_bf16 v[16:19], v[108:111], v[180:183], v[16:19]\n"
"v_mfma_f32_16x16x32_bf16 v[88:91], v[108:111], v[212:215], v[88:91]\n"
"v_mfma_f32_16x16x32_bf16 v[56:59], v[112:115], v[196:199], v[56:59]\n"
"s_waitcnt lgkmcnt(0)\n"
"s_barrier\n"
"ds_read_b128 v[144:147], v230 offset:5120\n"
"ds_read_b128 v[148:151], v230 offset:4096\n"
"v_mfma_f32_16x16x32_bf16 v[20:23], v[112:115], v[180:183], v[20:23]\n"
"ds_read_b128 v[152:155], v230 offset:6144\n"
"v_mfma_f32_16x16x32_bf16 v[92:95], v[112:115], v[212:215], v[92:95]\n"
"v_mfma_f32_16x16x32_bf16 v[52:55], v[116:119], v[200:203], v[52:55]\n"
"s_waitcnt lgkmcnt(2)\n"
"v_pk_add_f32 v[40:41], v[40:41], v[144:145]\n"
"v_pk_add_f32 v[42:43], v[42:43], v[146:147]\n"
"buffer_store_dwordx4 v[40:43], v232, s[80:83], s62 offen sc1\n"
"v_mfma_f32_16x16x32_bf16 v[16:19], v[116:119], v[184:187], v[16:19]\n"
"s_waitcnt lgkmcnt(1)\n"
"v_pk_add_f32 v[44:45], v[44:45], v[148:149]\n"
"v_pk_add_f32 v[46:47], v[46:47], v[150:151]\n"
"buffer_store_dwordx4 v[44:47], v232, s[80:83], s62 offen offset:1024 sc1\n"
"v_mfma_f32_16x16x32_bf16 v[88:91], v[116:119], v[216:219], v[88:91]\n"
"s_waitcnt lgkmcnt(0)\n"
"v_pk_add_f32 v[48:49], v[48:49], v[152:153]\n"
"v_pk_add_f32 v[50:51], v[50:51], v[154:155]\n"
"s_cmp_eq_u32 s64, 0\n"
"s_cbranch_scc1 LX0_%=\n"
"buffer_store_dwordx4 v[48:51], v228, s[80:83], s67 offen sc1\n"
"LX0_%=:\n"
"v_mfma_f32_16x16x32_bf16 v[56:59], v[120:123], v[200:203], v[56:59]\n"
"v_mfma_f32_16x16x32_bf16 v[20:23], v[120:123], v[184:187], v[20:23]\n"
"v_mfma_f32_16x16x32_bf16 v[92:95], v[120:123], v[216:219], v[92:95]\n"
"v_mfma_f32_16x16x32_bf16 v[52:55], v[124:127], v[204:207], v[52:55]\n"
"v_mfma_f32_16x16x32_bf16 v[16:19], v[124:127], v[188:191], v[16:19]\n"
"v_mfma_f32_16x16x32_bf16 v[88:91], v[124:127], v[220:223], v[88:91]\n"
"v_mfma_f32_16x16x32_bf16 v[56:59], v[224:227], v[204:207], v[56:59]\n"
"v_mfma_f32_16x16x32_bf16 v[20:23], v[224:227], v[188:191], v[20:23]\n"
"v_mfma_f32_16x16x32_bf16 v[92:95], v[224:227], v[220:223], v[92:95]\n"
"s_nop 2\n"
"ds_write_b128 v229, v[52:55] offset:0\n"
"s_nop 1\n"
"ds_write_b128 v229, v[56:59] offset:1024\n"
"s_nop 0\n"
"ds_write_b128 v229, v[92:95] offset:7168\n"
"s_mul_i32 s60, s65, 36\n"
"s_add_u32 s60, s60, 0\n"
"s_add_u32 s61, %[nb256], %[kq64]\n"
"s_lshr_b32 s62, %[nb256], 3\n"
"s_add_u32 s62, s62, 1024\n"
"s_lshr_b32 s65, s65, 5\n"
"s_waitcnt vmcnt(0) lgkmcnt(0)\n"
"s_barrier\n"
"s_cmp_eq_u32 %[hb], 0\n"
"s_cbranch_scc1 LSz_%=\n"
"buffer_store_dword v251, %[gout], s[80:83], 0 offen offset:4 sc1\n"
"LSz_%=:\n"
"s_cmp_eq_u32 %[hb], 0\n"
"s_cbranch_scc1 LIz0_%=\n"
"buffer_load_dwordx2 v[208:209], %[gin], s[80:83], 0 offen sc1\n"
"LIz0_%=:\n"
"ds_read_b128 v[192:195], v230 offset:1024\n"
"ds_read_b128 v[196:199], v230 offset:0\n"
"ds_read_b128 v[200:203], v230 offset:7168\n"
"s_waitcnt lgkmcnt(2)\n"
"v_pk_add_f32 v[16:17], v[16:17], v[192:193]\n"
"v_pk_add_f32 v[18:19], v[18:19], v[194:195]\n"
"s_waitcnt lgkmcnt(1)\n"
"v_pk_add_f32 v[20:21], v[20:21], v[196:197]\n"
"v_pk_add_f32 v[22:23], v[22:23], v[198:199]\n"
"s_waitcnt lgkmcnt(0)\n"
"v_pk_add_f32 v[88:89], v[88:89], v[200:201]\n"
"v_pk_add_f32 v[90:91], v[90:91], v[202:203]\n"
"s_cmp_eq_u32 %[hb], 0\n"
"s_cbranch_scc1 LIz1_%=\n"
"s_sleep 3\n"
"buffer_load_dwordx2 v[210:211], %[gin], s[80:83], 0 offen sc1\n"
"LIz1_%=:\n"
"s_cmp_eq_u32 %[hb], 0\n"
"s_cbranch_scc1 LIz2_%=\n"
"s_sleep 6\n"
"buffer_load_dwordx2 v[212:213], %[gin], s[80:83], 0 offen sc1\n"
"LIz2_%=:\n"
"s_cmp_eq_u32 %[hb], 0\n"
"s_cbranch_scc1 LIz3_%=\n"
"s_sleep 6\n"
"buffer_load_dwordx2 v[214:215], %[gin], s[80:83], 0 offen sc1\n"
"LIz3_%=:\n"
"s_cmp_eq_u32 %[hb], 0\n"
"s_cbranch_scc1 LDz_%=\n"
"s_mov_b32 s69, 0x40000\n"
"LPz_%=:\n"
"s_waitcnt vmcnt(3)\n"
"v_cmp_ne_u32 vcc, v208, v251\n"
"v_cmp_ne_u32 s[94:95], v209, v248\n"
"s_nop 4\n"
"s_cmp_eq_u64 vcc, 0\n"
"s_cbranch_scc1 LEz0_%=\n"
"v_mov_b32 v251, v208\n"
"buffer_store_dword v251, %[gout], s[80:83], 0 offen offset:4 sc1\n"
"LEz0_%=:\n"
"s_and_b32 s68, s94, 14\n"
"s_cbranch_scc0 LDz_%=\n"
"buffer_load_dwordx2 v[208:209], %[gin], s[80:83], 0 offen sc1\n"
"s_waitcnt vmcnt(3)\n"
"v_cmp_ne_u32 vcc, v210, v251\n"
"v_cmp_ne_u32 s[94:95], v211, v248\n"
"s_nop 4\n"
"s_cmp_eq_u64 vcc, 0\n"
"s_cbranch_scc1 LEz1_%=\n"
"v_mov_b32 v251, v210\n"
"buffer_store_dword v251, %[gout], s[80:83], 0 offen offset:4 sc1\n"
"LEz1_%=:\n"
"s_and_b32 s68, s94, 14\n"
"s_cbranch_scc0 LDz_%=\n"
"buffer_load_dwordx2 v[210:211], %[gin], s[80:83], 0 offen sc1\n"
"s_waitcnt vmcnt(3)\n"
"v_cmp_ne_u32 vcc, v212, v251\n"
"v_cmp_ne_u32 s[94:95], v213, v248\n"
"s_nop 4\n"
"s_cmp_eq_u64 vcc, 0\n"
"s_cbranch_scc1 LEz2_%=\n"
"v_mov_b32 v251, v212\n"
"buffer_store_dword v251, %[gout], s[80:83], 0 offen offset:4 sc1\n"
"LEz2_%=:\n"
"s_and_b32 s68, s94, 14\n"
"s_cbranch_scc0 LDz_%=\n"
"buffer_load_dwordx2 v[212:213], %[gin], s[80:83], 0 offen sc1\n"
"s_waitcnt vmcnt(3)\n"
"v_cmp_ne_u32 vcc, v214, v251\n"
"v_cmp_ne_u32 s[94:95], v215, v248\n"
"s_nop 4\n"
"s_cmp_eq_u64 vcc, 0\n"
"s_cbranch_scc1 LEz3_%=\n"
"v_mov_b32 v251, v214\n"
"buffer_store_dword v251, %[gout], s[80:83], 0 offen offset:4 sc1\n"
"LEz3_%=:\n"
"s_and_b32 s68, s94, 14\n"
"s_cbranch_scc0 LDz_%=\n"
"buffer_load_dwordx2 v[214:215], %[gin], s[80:83], 0 offen sc1\n"
"s_sub_u32 s69, s69, 1\n"
"s_cbranch_scc0 LPz_%=\n"
"LDz_%=:\n"
"s_barrier\n"
"s_add_u32 s68, s63, 0\n"
"buffer_load_dwordx4 v[128:131], v232, s[80:83], s68 offen sc1\n"
"s_add_u32 s68, s63, 21504\n"
"buffer_load_dwordx4 v[132:135], v232, s[80:83], s68 offen sc1\n"
"s_add_u32 s68, s63, 43008\n"
"buffer_load_dwordx4 v[136:139], v232, s[80:83], s68 offen sc1\n"
"s_add_u32 s68, s63, 0\n"
"buffer_load_dwordx4 v[140:143], v232, s[80:83], s68 offen offset:1024 sc1\n"
"s_add_u32 s68, s63, 21504\n"
"buffer_load_dwordx4 v[144:147], v232, s[80:83], s68 offen offset:1024 sc1\n"
"s_add_u32 s68, s63, 43008\n"
"buffer_load_dwordx4 v[148:151], v232, s[80:83], s68 offen offset:1024 sc1\n"
"s_add_u32 s68, s63, 0\n"
"buffer_load_dwordx4 v[152:155], v228, s[80:83], s68 offen offset:2048 sc1\n"
"s_add_u32 s68, s63, 21504\n"
"buffer_load_dwordx4 v[156:159], v228, s[80:83], s68 offen offset:2048 sc1\n"
"s_add_u32 s68, s63, 43008\n"
"buffer_load_dwordx4 v[160:163], v228, s[80:83], s68 offen offset:2048 sc1\n"
"s_cmp_lg_u32 s64, 0\n"
"s_cbranch_scc1 LU0_%=\n"
"s_add_u32 s68, s63, 0\n"
"buffer_load_dwordx4 v[164:167], v228, s[80:83], s68 offen sc1\n"
"s_add_u32 s68, s63, 21504\n"
"buffer_load_dwordx4 v[168:171], v228, s[80:83], s68 offen sc1\n"
"s_add_u32 s68, s63, 43008\n"
"buffer_load_dwordx4 v[172:175], v228, s[80:83], s68 offen sc1\n"
"LU0_%=:\n"
"s_waitcnt vmcnt(0)\n"
"v_pk_add_f32 v[128:129], v[128:129], v[132:133]\n"
"v_pk_add_f32 v[130:131], v[130:131], v[134:135]\n"
"v_pk_add_f32 v[16:17], v[16:17], v[136:137]\n"
"v_pk_add_f32 v[18:19], v[18:19], v[138:139]\n"
"v_pk_add_f32 v[16:17], v[16:17], v[128:129]\n"
"v_pk_add_f32 v[18:19], v[18:19], v[130:131]\n"
"v_cvt_pk_bf16_f32 v16, v16, v17\n"
"v_cvt_pk_bf16_f32 v17, v18, v19\n"
"s_add_u32 s68, s60, s61\n"
"s_add_u32 s68, s68, s65\n"
"buffer_store_dwordx2 v[16:17], v231, s[84:87], s68 offen\n"
"v_pk_add_f32 v[140:141], v[140:141], v[144:145]\n"
"v_pk_add_f32 v[142:143], v[142:143], v[146:147]\n"
"v_pk_add_f32 v[20:21], v[20:21], v[148:149]\n"
"v_pk_add_f32 v[22:23], v[22:23], v[150:151]\n"
"v_pk_add_f32 v[20:21], v[20:21], v[140:141]\n"
"v_pk_add_f32 v[22:23], v[22:23], v[142:143]\n"
"v_cvt_pk_bf16_f32 v20, v20, v21\n"
"v_cvt_pk_bf16_f32 v21, v22, v23\n"
"s_xor_b32 s68, s65, 32\n"
"s_add_u32 s68, s68, s60\n"
"s_add_u32 s68, s68, s61\n"
"buffer_store_dwordx2 v[20:21], v231, s[84:87], s68 offen\n"
"v_pk_add_f32 v[152:153], v[152:153], v[156:157]\n"
"v_pk_add_f32 v[154:155], v[154:155], v[158:159]\n"
"v_pk_add_f32 v[88:89], v[88:89], v[160:161]\n"
"v_pk_add_f32 v[90:91], v[90:91], v[162:163]\n"
"v_pk_add_f32 v[88:89], v[88:89], v[152:153]\n"
"v_pk_add_f32 v[90:91], v[90:91], v[154:155]\n"
"v_cvt_pk_bf16_f32 v88, v88, v89\n"
"v_cvt_pk_bf16_f32 v89, v90, v91\n"
"s_add_u32 s68, s61, 294912\n"
"s_add_u32 s68, s68, s65\n"
"buffer_store_dwordx2 v[88:89], v231, s[84:87], s68 offen\n"
"s_cmp_lg_u32 s64, 0\n"
"s_cbranch_scc1 LW0_%=\n"
"v_pk_add_f32 v[164:165], v[164:165], v[168:169]\n"
"v_pk_add_f32 v[166:167], v[166:167], v[170:171]\n"
"v_pk_add_f32 v[48:49], v[48:49], v[172:173]\n"
"v_pk_add_f32 v[50:51], v[50:51], v[174:175]\n"
"v_pk_add_f32 v[48:49], v[48:49], v[164:165]\n"
"v_pk_add_f32 v[50:51], v[50:51], v[166:167]\n"
"v_cvt_pk_bf16_f32 v48, v48, v49\n"
"v_cvt_pk_bf16_f32 v49, v50, v51\n"
"s_add_u32 s68, s60, s62\n"
"buffer_store_dwordx2 v[48:49], v231, s[84:87], s68 offen\n"
"LW0_%=:\n"
"s_branch LEND_%=\n"
"LZZ0_%=:\n"
".macro KB1 p0,p1\n"
"s_barrier\n"
"s_cmp_lt_u32 s61, 0x36000\n"
"s_cbranch_scc0 7f\n"
"s_sleep 3\n"
"7:\n"
"s_sleep 1\n"
"s_setprio 1\n"
"v_mfma_f32_16x16x32_bf16 v[16:19], v[160:163], v[224:227], v[16:19]\n"
"ds_read_b128 v[128:131], %[kb] offset:\\p0+2048\n"
"v_add_f32 v240, v240, v192\n"
"v_add_f32_e64 v241, v241, v196\n"
"v_mfma_f32_16x16x32_bf16 v[48:51], v[160:163], v[228:231], v[48:51]\n"
"ds_read_b128 v[132:135], %[kb] offset:\\p0+2304\n"
"v_add_f32 v242, v242, v208\n"
"v_add_f32 v240, v240, v193\n"
"v_mfma_f32_16x16x32_bf16 v[20:23], v[164:167], v[224:227], v[20:23]\n"
"ds_read_b128 v[136:139], %[kb] offset:\\p0+2560\n"
"v_add_f32 v241, v241, v197\n"
"v_add_f32 v242, v242, v209\n"
"v_mfma_f32_16x16x32_bf16 v[52:55], v[164:167], v[228:231], v[52:55]\n"
"ds_read_b128 v[140:143], %[kb] offset:\\p0+2816\n"
"v_add_f32 v240, v240, v194\n"
"v_add_f32 v241, v241, v198\n"
"v_mfma_f32_16x16x32_bf16 v[24:27], v[168:171], v[224:227], v[24:27]\n"
"ds_read_b128 v[144:147], %[kb] offset:\\p0+10240\n"
"v_add_f32 v242, v242, v210\n"
"v_add_f32 v240, v240, v195\n"
"v_mfma_f32_16x16x32_bf16 v[56:59], v[168:171], v[228:231], v[56:59]\n"
"ds_read_b128 v[148:151], %[kb] offset:\\p0+10496\n"
"v_add_f32 v241, v241, v199\n"
"v_add_f32 v242, v242, v211\n"
"v_mfma_f32_16x16x32_bf16 v[28:31], v[172:175], v[224:227], v[28:31]\n"
"ds_read_b128 v[152:155], %[kb] offset:\\p0+10752\n"
"v_add_f32 v240, v240, v200\n"
"v_add_f32 v241, v241, v204\n"
"v_mfma_f32_16x16x32_bf16 v[60:63], v[172:175], v[228:231], v[60:63]\n"
"ds_read_b128 v[156:159], %[kb] offset:\\p0+11008\n"
"v_add_f32 v240, v240, v201\n"
"v_add_f32 v241, v241, v205\n"
"v_mfma_f32_16x16x32_bf16 v[32:35], v[176:179], v[224:227], v[32:35]\n"
"ds_read_b64_tr_b16 v[160:161], %[vbe] offset:\\p0+0\n"
"v_add_f32 v240, v240, v202\n"
"v_add_f32 v241, v241, v206\n"
"v_mfma_f32_16x16x32_bf16 v[64:67], v[176:179], v[228:231], v[64:67]\n"
"ds_read_b64_tr_b16 v[162:163], %[vbe] offset:\\p0+8192\n"
"v_add_f32 v240, v240, v203\n"
"v_add_f32 v241, v241, v207\n"
"v_mfma_f32_16x16x32_bf16 v[36:39], v[180:183], v[224:227], v[36:39]\n"
"ds_read_b64_tr_b16 v[164:165], v243 offset:\\p0+0\n"
"v_mfma_f32_16x16x32_bf16 v[68:71], v[180:183], v[228:231], v[68:71]\n"
"ds_read_b64_tr_b16 v[166:167], v243 offset:\\p0+8192\n"
"v_mfma_f32_16x16x32_bf16 v[40:43], v[184:187], v[224:227], v[40:43]\n"
"ds_read_b64_tr_b16 v[168:169], %[vbe] offset:\\p0+256\n"
"v_mfma_f32_16x16x32_bf16 v[72:75], v[184:187], v[228:231], v[72:75]\n"
"ds_read_b64_tr_b16 v[170:171], %[vbe] offset:\\p0+8448\n"
"v_mfma_f32_16x16x32_bf16 v[44:47], v[188:191], v[224:227], v[44:47]\n"
"ds_read_b64_tr_b16 v[172:173], v243 offset:\\p0+256\n"
"v_mfma_f32_16x16x32_bf16 v[76:79], v[188:191], v[228:231], v[76:79]\n"
"ds_read_b64_tr_b16 v[174:175], v243 offset:\\p0+8448\n"
"s_waitcnt lgkmcnt(8)\n"
"v_mfma_f32_16x16x32_bf16 v[208:211], v[144:147], v[80:83], v[220:223]\n"
"ds_read_b64_tr_b16 v[176:177], %[vbe] offset:\\p0+512\n"
"v_mfma_f32_16x16x32_bf16 v[208:211], v[148:151], v[84:87], v[208:211]\n"
"ds_read_b64_tr_b16 v[178:179], %[vbe] offset:\\p0+8704\n"
"v_mfma_f32_16x16x32_bf16 v[208:211], v[152:155], v[88:91], v[208:211]\n"
"ds_read_b64_tr_b16 v[180:181], v243 offset:\\p0+512\n"
"v_mfma_f32_16x16x32_bf16 v[208:211], v[156:159], v[92:95], v[208:211]\n"
"ds_read_b64_tr_b16 v[182:183], v243 offset:\\p0+8704\n"
"v_mfma_f32_16x16x32_bf16 v[192:195], v[128:131], v[96:99], v[212:215]\n"
"ds_read_b64_tr_b16 v[184:185], %[vbe] offset:\\p0+768\n"
"v_mfma_f32_16x16x32_bf16 v[192:195], v[132:135], v[100:103], v[192:195]\n"
"ds_read_b64_tr_b16 v[186:187], %[vbe] offset:\\p0+8960\n"
"v_mfma_f32_16x16x32_bf16 v[192:195], v[136:139], v[104:107], v[192:195]\n"
"ds_read_b64_tr_b16 v[188:189], v243 offset:\\p0+768\n"
"v_mfma_f32_16x16x32_bf16 v[192:195], v[140:143], v[108:111], v[192:195]\n"
"ds_read_b64_tr_b16 v[190:191], v243 offset:\\p0+8960\n"
"v_mfma_f32_16x16x32_bf16 v[196:199], v[128:131], v[112:115], v[216:219]\n"
"v_mfma_f32_16x16x32_bf16 v[196:199], v[132:135], v[116:119], v[196:199]\n"
"v_mfma_f32_16x16x32_bf16 v[196:199], v[136:139], v[120:123], v[196:199]\n"
"v_mfma_f32_16x16x32_bf16 v[196:199], v[140:143], v[124:127], v[196:199]\n"
"v_mfma_f32_16x16x32_bf16 v[200:203], v[144:147], v[96:99], v[212:215]\n"
"v_mfma_f32_16x16x32_bf16 v[200:203], v[148:151], v[100:103], v[200:203]\n"
"v_mfma_f32_16x16x32_bf16 v[200:203], v[152:155], v[104:107], v[200:203]\n"
"v_mfma_f32_16x16x32_bf16 v[200:203], v[156:159], v[108:111], v[200:203]\n"
"v_mfma_f32_16x16x32_bf16 v[204:207], v[144:147], v[112:115], v[216:219]\n"
"v_mfma_f32_16x16x32_bf16 v[204:207], v[148:151], v[116:119], v[204:207]\n"
"v_mfma_f32_16x16x32_bf16 v[204:207], v[152:155], v[120:123], v[204:207]\n"
"v_mfma_f32_16x16x32_bf16 v[204:207], v[156:159], v[124:127], v[204:207]\n"
"s_waitcnt lgkmcnt(0)\n"
"s_barrier\n"
"s_mov_b32 m0, \\p1\n"
"s_setprio 0\n"
"buffer_load_dwordx4 %[g0], %[srdv], s61 offen nt lds\n"
"buffer_load_dwordx4 %[g1], %[srdv], s61 offen offset:1024 nt lds\n"
"buffer_load_dwordx4 %[g0], %[srdk], s61 offen offset:2048 nt lds\n"
"s_add_u32 s61, s61, 0x2000\n"
"s_cmp_eq_u32 s61, 0x40000\n"
"v_exp_f32 v208, v208\n"
"v_exp_f32 v209, v209\n"
"v_exp_f32 v210, v210\n"
"v_exp_f32_e64 v211, v211\n"
"v_cvt_pk_bf16_f32 v232, v208, v209\n"
"v_cvt_pk_bf16_f32 v233, v210, v211\n"
"ds_write_b64 v247, v[232:233] offset:4104\n"
"v_exp_f32 v192, v192\n"
"v_exp_f32 v193, v193\n"
"v_exp_f32 v194, v194\n"
"v_exp_f32 v195, v195\n"
"v_cvt_pk_bf16_f32 v224, v192, v193\n"
"v_cvt_pk_bf16_f32 v225, v194, v195\n"
"v_exp_f32 v196, v196\n"
"v_exp_f32 v197, v197\n"
"v_exp_f32 v198, v198\n"
"v_exp_f32 v199, v199\n"
"v_cvt_pk_bf16_f32 v228, v196, v197\n"
"v_cvt_pk_bf16_f32 v229, v198, v199\n"
"v_exp_f32 v200, v200\n"
"v_exp_f32 v201, v201\n"
"v_exp_f32 v202, v202\n"
"v_exp_f32 v203, v203\n"
"v_cvt_pk_bf16_f32 v226, v200, v201\n"
"v_cvt_pk_bf16_f32 v227, v202, v203\n"
"v_exp_f32 v204, v204\n"
"v_exp_f32 v205, v205\n"
"v_exp_f32 v206, v206\n"
"v_exp_f32 v207, v207\n"
"v_cvt_pk_bf16_f32 v230, v204, v205\n"
"v_cvt_pk_bf16_f32 v231, v206, v207\n"
"s_waitcnt vmcnt(9) lgkmcnt(0)\n"
"s_cbranch_scc1 LQ1_%=\n"
".endm\n"
".macro KA1 p0,p1,p2\n"
"s_barrier\n"
"s_sleep 3\n"
"s_setprio 1\n"
"v_mfma_f32_16x16x32_bf16 v[16:19], v[160:163], v[224:227], v[16:19]\n"
"ds_read_b128 v[128:131], v244 offset:\\p0+2048\n"
"v_add_f32 v240, v240, v192\n"
"v_add_f32_e64 v241, v241, v196\n"
"v_mfma_f32_16x16x32_bf16 v[48:51], v[160:163], v[228:231], v[48:51]\n"
"ds_read_b128 v[132:135], v244 offset:\\p0+2304\n"
"v_add_f32 v242, v242, v208\n"
"v_add_f32 v240, v240, v193\n"
"v_mfma_f32_16x16x32_bf16 v[20:23], v[164:167], v[224:227], v[20:23]\n"
"ds_read_b128 v[136:139], v244 offset:\\p0+2560\n"
"v_add_f32 v241, v241, v197\n"
"v_add_f32 v242, v242, v209\n"
"v_mfma_f32_16x16x32_bf16 v[52:55], v[164:167], v[228:231], v[52:55]\n"
"ds_read_b128 v[140:143], v244 offset:\\p0+2816\n"
"v_add_f32 v240, v240, v194\n"
"v_add_f32 v241, v241, v198\n"
"v_mfma_f32_16x16x32_bf16 v[24:27], v[168:171], v[224:227], v[24:27]\n"
"ds_read_b128 v[144:147], v244 offset:\\p0+10240\n"
"v_add_f32 v242, v242, v210\n"
"v_add_f32 v240, v240, v195\n"
"v_mfma_f32_16x16x32_bf16 v[56:59], v[168:171], v[228:231], v[56:59]\n"
"ds_read_b128 v[148:151], v244 offset:\\p0+10496\n"
"v_add_f32 v241, v241, v199\n"
"v_add_f32 v242, v242, v211\n"
"v_mfma_f32_16x16x32_bf16 v[28:31], v[172:175], v[224:227], v[28:31]\n"
"ds_read_b128 v[152:155], v244 offset:\\p0+10752\n"
"v_add_f32 v240, v240, v200\n"
"v_add_f32 v241, v241, v204\n"
"v_mfma_f32_16x16x32_bf16 v[60:63], v[172:175], v[228:231], v[60:63]\n"
"ds_read_b128 v[156:159], v244 offset:\\p0+11008\n"
"v_add_f32 v240, v240, v201\n"
"v_add_f32 v241, v241, v205\n"
"v_mfma_f32_16x16x32_bf16 v[32:35], v[176:179], v[224:227], v[32:35]\n"
"ds_read_b64_tr_b16 v[160:161], v245 offset:\\p0+0\n"
"v_add_f32 v240, v240, v202\n"
"v_add_f32 v241, v241, v206\n"
"v_mfma_f32_16x16x32_bf16 v[64:67], v[176:179], v[228:231], v[64:67]\n"
"ds_read_b64_tr_b16 v[162:163], v245 offset:\\p0+8192\n"
"v_add_f32 v240, v240, v203\n"
"v_add_f32 v241, v241, v207\n"
"v_mfma_f32_16x16x32_bf16 v[36:39], v[180:183], v[224:227], v[36:39]\n"
"ds_read_b64_tr_b16 v[164:165], v246 offset:\\p0+0\n"
"v_mfma_f32_16x16x32_bf16 v[68:71], v[180:183], v[228:231], v[68:71]\n"
"ds_read_b64_tr_b16 v[166:167], v246 offset:\\p0+8192\n"
"v_mfma_f32_16x16x32_bf16 v[40:43], v[184:187], v[224:227], v[40:43]\n"
"ds_read_b64_tr_b16 v[168:169], v245 offset:\\p0+256\n"
"v_mfma_f32_16x16x32_bf16 v[72:75], v[184:187], v[228:231], v[72:75]\n"
"ds_read_b64_tr_b16 v[170:171], v245 offset:\\p0+8448\n"
"v_mfma_f32_16x16x32_bf16 v[44:47], v[188:191], v[224:227], v[44:47]\n"
"ds_read_b64_tr_b16 v[172:173], v246 offset:\\p0+256\n"
"v_mfma_f32_16x16x32_bf16 v[76:79], v[188:191], v[228:231], v[76:79]\n"
"ds_read_b64_tr_b16 v[174:175], v246 offset:\\p0+8448\n"
"s_waitcnt lgkmcnt(8)\n"
"v_mfma_f32_16x16x32_bf16 v[208:211], v[144:147], v[80:83], v[220:223]\n"
"ds_read_b64_tr_b16 v[176:177], v245 offset:\\p0+512\n"
"v_mfma_f32_16x16x32_bf16 v[208:211], v[148:151], v[84:87], v[208:211]\n"
"ds_read_b64_tr_b16 v[178:179], v245 offset:\\p0+8704\n"
"v_mfma_f32_16x16x32_bf16 v[208:211], v[152:155], v[88:91], v[208:211]\n"
"ds_read_b64_tr_b16 v[180:181], v246 offset:\\p0+512\n"
"v_mfma_f32_16x16x32_bf16 v[208:211], v[156:159], v[92:95], v[208:211]\n"
"ds_read_b64_tr_b16 v[182:183], v246 offset:\\p0+8704\n"
"v_mfma_f32_16x16x32_bf16 v[192:195], v[128:131], v[96:99], v[212:215]\n"
"ds_read_b64_tr_b16 v[184:185], v245 offset:\\p0+768\n"
"v_mfma_f32_16x16x32_bf16 v[192:195], v[132:135], v[100:103], v[192:195]\n"
"ds_read_b64_tr_b16 v[186:187], v245 offset:\\p0+8960\n"
"v_mfma_f32_16x16x32_bf16 v[192:195], v[136:139], v[104:107], v[192:195]\n"
"ds_read_b64_tr_b16 v[188:189], v246 offset:\\p0+768\n"
"v_mfma_f32_16x16x32_bf16 v[192:195], v[140:143], v[108:111], v[192:195]\n"
"ds_read_b64_tr_b16 v[190:191], v246 offset:\\p0+8960\n"
"v_mfma_f32_16x16x32_bf16 v[196:199], v[128:131], v[112:115], v[216:219]\n"
"v_mfma_f32_16x16x32_bf16 v[196:199], v[132:135], v[116:119], v[196:199]\n"
"v_mfma_f32_16x16x32_bf16 v[196:199], v[136:139], v[120:123], v[196:199]\n"
"v_mfma_f32_16x16x32_bf16 v[196:199], v[140:143], v[124:127], v[196:199]\n"
"v_mfma_f32_16x16x32_bf16 v[200:203], v[144:147], v[96:99], v[212:215]\n"
"v_mfma_f32_16x16x32_bf16 v[200:203], v[148:151], v[100:103], v[200:203]\n"
"v_mfma_f32_16x16x32_bf16 v[200:203], v[152:155], v[104:107], v[200:203]\n"
"v_mfma_f32_16x16x32_bf16 v[200:203], v[156:159], v[108:111], v[200:203]\n"
"v_mfma_f32_16x16x32_bf16 v[204:207], v[144:147], v[112:115], v[216:219]\n"
"v_mfma_f32_16x16x32_bf16 v[204:207], v[148:151], v[116:119], v[204:207]\n"
"v_mfma_f32_16x16x32_bf16 v[204:207], v[152:155], v[120:123], v[204:207]\n"
"v_mfma_f32_16x16x32_bf16 v[204:207], v[156:159], v[124:127], v[204:207]\n"
"s_waitcnt lgkmcnt(0)\n"
"s_barrier\n"
"s_mov_b32 m0, \\p1\n"
"s_setprio 0\n"
"buffer_load_dwordx4 %[g0], %[srdv], s61 offen nt lds\n"
"buffer_load_dwordx4 %[g1], %[srdv], s61 offen offset:1024 nt lds\n"
"v_exp_f32 v208, v208\n"
"v_exp_f32 v209, v209\n"
"v_exp_f32 v210, v210\n"
"v_exp_f32_e64 v211, v211\n"
"v_cvt_pk_bf16_f32 v232, v208, v209\n"
"v_cvt_pk_bf16_f32 v233, v210, v211\n"
"ds_write_b64 v247, v[232:233] offset:4104\n"
"v_exp_f32 v192, v192\n"
"v_exp_f32 v193, v193\n"
"v_exp_f32 v194, v194\n"
"v_exp_f32 v195, v195\n"
"v_cvt_pk_bf16_f32 v224, v192, v193\n"
"v_cvt_pk_bf16_f32 v225, v194, v195\n"
"v_exp_f32 v196, v196\n"
"v_exp_f32 v197, v197\n"
"v_exp_f32 v198, v198\n"
"v_exp_f32 v199, v199\n"
"v_cvt_pk_bf16_f32 v228, v196, v197\n"
"v_cvt_pk_bf16_f32 v229, v198, v199\n"
"v_exp_f32 v200, v200\n"
"v_exp_f32 v201, v201\n"
"v_exp_f32 v202, v202\n"
"v_exp_f32 v203, v203\n"
"v_cvt_pk_bf16_f32 v226, v200, v201\n"
"v_cvt_pk_bf16_f32 v227, v202, v203\n"
"v_exp_f32 v204, v204\n"
"v_exp_f32 v205, v205\n"
"v_exp_f32 v206, v206\n"
"v_exp_f32 v207, v207\n"
"v_cvt_pk_bf16_f32 v230, v204, v205\n"
"v_cvt_pk_bf16_f32 v231, v206, v207\n"
"s_waitcnt vmcnt(8) lgkmcnt(0)\n"
"s_barrier\n"
"s_sleep 3\n"
"s_setprio 1\n"
"v_mfma_f32_16x16x32_bf16 v[16:19], v[160:163], v[224:227], v[16:19]\n"
"ds_read_b128 v[128:131], %[kb] offset:\\p2+2048\n"
"v_add_f32 v240, v240, v192\n"
"v_add_f32 v241, v241, v196\n"
"v_mfma_f32_16x16x32_bf16 v[48:51], v[160:163], v[228:231], v[48:51]\n"
"ds_read_b128 v[132:135], %[kb] offset:\\p2+2304\n"
"v_add_f32 v242, v242, v208\n"
"v_add_f32 v240, v240, v193\n"
"v_mfma_f32_16x16x32_bf16 v[20:23], v[164:167], v[224:227], v[20:23]\n"
"ds_read_b128 v[136:139], %[kb] offset:\\p2+2560\n"
"v_add_f32 v241, v241, v197\n"
"v_add_f32 v242, v242, v209\n"
"v_mfma_f32_16x16x32_bf16 v[52:55], v[164:167], v[228:231], v[52:55]\n"
"ds_read_b128 v[140:143], %[kb] offset:\\p2+2816\n"
"v_add_f32 v240, v240, v194\n"
"v_add_f32 v241, v241, v198\n"
"v_mfma_f32_16x16x32_bf16 v[24:27], v[168:171], v[224:227], v[24:27]\n"
"ds_read_b128 v[144:147], %[kb] offset:\\p2+10240\n"
"v_add_f32 v242, v242, v210\n"
"v_add_f32 v240, v240, v195\n"
"v_mfma_f32_16x16x32_bf16 v[56:59], v[168:171], v[228:231], v[56:59]\n"
"ds_read_b128 v[148:151], %[kb] offset:\\p2+10496\n"
"v_add_f32 v241, v241, v199\n"
"v_add_f32 v242, v242, v211\n"
"v_mfma_f32_16x16x32_bf16 v[28:31], v[172:175], v[224:227], v[28:31]\n"
"ds_read_b128 v[152:155], %[kb] offset:\\p2+10752\n"
"v_add_f32 v240, v240, v200\n"
"v_add_f32 v241, v241, v204\n"
"v_mfma_f32_16x16x32_bf16 v[60:63], v[172:175], v[228:231], v[60:63]\n"
"ds_read_b128 v[156:159], %[kb] offset:\\p2+11008\n"
"v_add_f32 v240, v240, v201\n"
"v_add_f32 v241, v241, v205\n"
"v_mfma_f32_16x16x32_bf16 v[32:35], v[176:179], v[224:227], v[32:35]\n"
"ds_read_b64_tr_b16 v[160:161], %[vbe] offset:\\p2+0\n"
"v_add_f32 v240, v240, v202\n"
"v_add_f32 v241, v241, v206\n"
"v_mfma_f32_16x16x32_bf16 v[64:67], v[176:179], v[228:231], v[64:67]\n"
"ds_read_b64_tr_b16 v[162:163], %[vbe] offset:\\p2+8192\n"
"v_add_f32 v240, v240, v203\n"
"v_add_f32 v241, v241, v207\n"
"v_mfma_f32_16x16x32_bf16 v[36:39], v[180:183], v[224:227], v[36:39]\n"
"ds_read_b64_tr_b16 v[164:165], v243 offset:\\p2+0\n"
"v_mfma_f32_16x16x32_bf16 v[68:71], v[180:183], v[228:231], v[68:71]\n"
"ds_read_b64_tr_b16 v[166:167], v243 offset:\\p2+8192\n"
"v_mfma_f32_16x16x32_bf16 v[40:43], v[184:187], v[224:227], v[40:43]\n"
"ds_read_b64_tr_b16 v[168:169], %[vbe] offset:\\p2+256\n"
"v_mfma_f32_16x16x32_bf16 v[72:75], v[184:187], v[228:231], v[72:75]\n"
"ds_read_b64_tr_b16 v[170:171], %[vbe] offset:\\p2+8448\n"
"v_mfma_f32_16x16x32_bf16 v[44:47], v[188:191], v[224:227], v[44:47]\n"
"ds_read_b64_tr_b16 v[172:173], v243 offset:\\p2+256\n"
"v_mfma_f32_16x16x32_bf16 v[76:79], v[188:191], v[228:231], v[76:79]\n"
"ds_read_b64_tr_b16 v[174:175], v243 offset:\\p2+8448\n"
"s_waitcnt lgkmcnt(8)\n"
"v_mfma_f32_16x16x32_bf16 v[208:211], v[144:147], v[80:83], v[220:223]\n"
"ds_read_b64_tr_b16 v[176:177], %[vbe] offset:\\p2+512\n"
"v_mfma_f32_16x16x32_bf16 v[208:211], v[148:151], v[84:87], v[208:211]\n"
"ds_read_b64_tr_b16 v[178:179], %[vbe] offset:\\p2+8704\n"
"v_mfma_f32_16x16x32_bf16 v[208:211], v[152:155], v[88:91], v[208:211]\n"
"ds_read_b64_tr_b16 v[180:181], v243 offset:\\p2+512\n"
"v_mfma_f32_16x16x32_bf16 v[208:211], v[156:159], v[92:95], v[208:211]\n"
"ds_read_b64_tr_b16 v[182:183], v243 offset:\\p2+8704\n"
"v_mfma_f32_16x16x32_bf16 v[192:195], v[128:131], v[96:99], v[212:215]\n"
"ds_read_b64_tr_b16 v[184:185], %[vbe] offset:\\p2+768\n"
"v_mfma_f32_16x16x32_bf16 v[192:195], v[132:135], v[100:103], v[192:195]\n"
"ds_read_b64_tr_b16 v[186:187], %[vbe] offset:\\p2+8960\n"
"v_mfma_f32_16x16x32_bf16 v[192:195], v[136:139], v[104:107], v[192:195]\n"
"ds_read_b64_tr_b16 v[188:189], v243 offset:\\p2+768\n"
"v_mfma_f32_16x16x32_bf16 v[192:195], v[140:143], v[108:111], v[192:195]\n"
"ds_read_b64_tr_b16 v[190:191], v243 offset:\\p2+8960\n"
"v_mfma_f32_16x16x32_bf16 v[196:199], v[128:131], v[112:115], v[216:219]\n"
"v_mfma_f32_16x16x32_bf16 v[196:199], v[132:135], v[116:119], v[196:199]\n"
"v_mfma_f32_16x16x32_bf16 v[196:199], v[136:139], v[120:123], v[196:199]\n"
"v_mfma_f32_16x16x32_bf16 v[196:199], v[140:143], v[124:127], v[196:199]\n"
"v_mfma_f32_16x16x32_bf16 v[200:203], v[144:147], v[96:99], v[212:215]\n"
"v_mfma_f32_16x16x32_bf16 v[200:203], v[148:151], v[100:103], v[200:203]\n"
"v_mfma_f32_16x16x32_bf16 v[200:203], v[152:155], v[104:107], v[200:203]\n"
"v_mfma_f32_16x16x32_bf16 v[200:203], v[156:159], v[108:111], v[200:203]\n"
"v_mfma_f32_16x16x32_bf16 v[204:207], v[144:147], v[112:115], v[216:219]\n"
"v_mfma_f32_16x16x32_bf16 v[204:207], v[148:151], v[116:119], v[204:207]\n"
"v_mfma_f32_16x16x32_bf16 v[204:207], v[152:155], v[120:123], v[204:207]\n"
"v_mfma_f32_16x16x32_bf16 v[204:207], v[156:159], v[124:127], v[204:207]\n"
"s_waitcnt lgkmcnt(0)\n"
"s_barrier\n"
"s_mov_b32 m0, \\p1\n"
"s_setprio 0\n"
"buffer_load_dwordx4 %[g0], %[srdk], s61 offen offset:2048 nt lds\n"
"s_add_u32 s61, s61, 0x4000\n"
"s_cmp_eq_u32 s61, 0x40000\n"
"v_exp_f32 v208, v208\n"
"v_exp_f32 v209, v209\n"
"v_exp_f32 v210, v210\n"
"v_exp_f32_e64 v211, v211\n"
"v_cvt_pk_bf16_f32 v232, v208, v209\n"
"v_cvt_pk_bf16_f32 v233, v210, v211\n"
"ds_write_b64 v247, v[232:233] offset:4104\n"
"v_exp_f32 v192, v192\n"
"v_exp_f32 v193, v193\n"
"v_exp_f32 v194, v194\n"
"v_exp_f32 v195, v195\n"
"v_cvt_pk_bf16_f32 v224, v192, v193\n"
"v_cvt_pk_bf16_f32 v225, v194, v195\n"
"v_exp_f32 v196, v196\n"
"v_exp_f32 v197, v197\n"
"v_exp_f32 v198, v198\n"
"v_exp_f32 v199, v199\n"
"v_cvt_pk_bf16_f32 v228, v196, v197\n"
"v_cvt_pk_bf16_f32 v229, v198, v199\n"
"v_exp_f32 v200, v200\n"
"v_exp_f32 v201, v201\n"
"v_exp_f32 v202, v202\n"
"v_exp_f32 v203, v203\n"
"v_cvt_pk_bf16_f32 v226, v200, v201\n"
"v_cvt_pk_bf16_f32 v227, v202, v203\n"
"v_exp_f32 v204, v204\n"
"v_exp_f32 v205, v205\n"
"v_exp_f32 v206, v206\n"
"v_exp_f32 v207, v207\n"
"v_cvt_pk_bf16_f32 v230, v204, v205\n"
"v_cvt_pk_bf16_f32 v231, v206, v207\n"
"s_waitcnt lgkmcnt(0)\n"
"s_nop 0\n"
".endm\n"
".p2align 3\n"
"LR1_%=:\n"
"s_mov_b32 s62, 0\n"
"s_mov_b32 s63, 0\n"
"s_mov_b32 s61, 0\n"
"s_lshr_b32 s60, %[hb], 5\n"
"s_lshr_b32 s64, %[hb], 6\n"
"v_lshrrev_b32 v198, 8, %[g0]\n"
"v_mul_u32_u24 v198, 7936, v198\n"
"v_add_u32 v198, %[g0], v198\n"
"v_xor_b32 v195, 32, v198\n"
"v_add_u32 v195, 31744, v195\n"
"v_xor_b32_e64 v193, 48, %[xoff]\n"
"s_add_u32 m0, %[hb], 34816\n"
"v_add_u32 v193, 8192, v193\n"
"s_add_u32 s62, s62, 0x40000\n"
"s_add_u32 m0, s60, 135168\n"
"s_nop 0\n"
"s_add_u32 s63, s63, 0x80\n"
"s_lshr_b32 s68, %[hb], 9\n"
"s_mul_i32 s68, s68, 324\n"
"v_mul_u32_u24 v199, 72, %[l16]\n"
"v_add_u32_e64 v199, s68, v199\n"
"v_subrev_u32 v200, 576, %[l16]\n"
"v_not_b32_e64 v200, v200\n"
"v_and_b32 v200, 0x40000000, v200\n"
"v_or_b32 v199, v200, v199\n"
"s_cmp_eq_u32 %[wrot], 1\n"
"s_cselect_b32 s68, 0, 0x40000000\n"
"s_mov_b32 s69, 128\n"
"v_or_b32 v200, s68, v199\n"
"s_cmp_eq_u32 %[wrot], 2\n"
"s_cselect_b32 s68, 0, 0x40000000\n"
"s_mov_b32 s69, 83072\n"
"v_or_b32 v200, s68, v199\n"
"s_cmp_eq_u32 %[wrot], 3\n"
"s_cselect_b32 s68, 0, 0x40000000\n"
"s_mov_b32 s69, 256\n"
"v_or_b32 v200, s68, v199\n"
"s_cmp_eq_u32 %[wrot], 0\n"
"s_cselect_b32 s68, 0, 0x40000000\n"
"s_mov_b32 s69, 83200\n"
"v_or_b32 v200, s68, v199\n"
"s_mul_i32 s91, %[xrot], 6\n"
"s_add_u32 m0, %[hb], 43008\n"
"s_nop 0\n"
"s_add_u32 s62, s62, 0x40000\n"
"s_add_u32 s52, %[hb], 4096\n"
"s_add_u32 s53, %[hb], 20480\n"
"s_add_u32 s54, %[hb], 36864\n"
"s_add_u32 s55, %[hb], 53248\n"
"s_lshr_b32 s90, %[hb], 3\n"
"s_add_u32 s90, s90, 131072\n"
"v_xor_b32 v192, 32, %[wbe]\n"
"v_xor_b32 v243, 32, %[vbe]\n"
"v_add_u32 v194, 135168, %[xb]\n"
"v_add_u32_e64 v194, s60, v194\n"
"v_xor_b32 v196, 2048, v194\n"
"v_add_u32 v244, %[d1], %[kb]\n"
"v_add_u32 v245, %[d1], %[vbe]\n"
"v_add_u32 v246, %[d1], v243\n"
"s_lshr_b32 s65, %[hb], 3\n"
"s_add_u32 s65, s65, 147456\n"
"v_add_u32_e64 v247, s65, %[l16]\n"
"s_mov_b32 s66, 0x3e0293ee\n"
"s_mov_b32 s67, s66\n"
"s_waitcnt vmcnt(6)\n"
"s_barrier\n"
"ds_read_b128 v[96:99], v194 offset:0\n"
"ds_read_b128 v[100:103], v196 offset:0\n"
"ds_read_b128 v[104:107], %[xb] offset:57344\n"
"ds_read_b64_tr_b16 v[128:129], %[wbe] offset:32768\n"
"ds_read_b64_tr_b16 v[130:131], v192 offset:33792\n"
"ds_read_b64_tr_b16 v[132:133], v192 offset:32768\n"
"ds_read_b64_tr_b16 v[134:135], %[wbe] offset:33792\n"
"ds_read_b64_tr_b16 v[136:137], %[wbe] offset:33024\n"
"ds_read_b64_tr_b16 v[138:139], v192 offset:34048\n"
"ds_read_b64_tr_b16 v[140:141], v192 offset:33024\n"
"ds_read_b64_tr_b16 v[142:143], %[wbe] offset:34048\n"
"ds_read_b64_tr_b16 v[144:145], %[wbe] offset:33280\n"
"ds_read_b64_tr_b16 v[146:147], v192 offset:34304\n"
"ds_read_b64_tr_b16 v[148:149], v192 offset:33280\n"
"ds_read_b64_tr_b16 v[150:151], %[wbe] offset:34304\n"
"ds_read_b64_tr_b16 v[152:153], %[wbe] offset:33536\n"
"ds_read_b64_tr_b16 v[154:155], v192 offset:34560\n"
"ds_read_b64_tr_b16 v[156:157], v192 offset:33536\n"
"s_add_u32 m0, s60, 151552\n"
"ds_read_b64_tr_b16 v[158:159], %[wbe] offset:34560\n"
"buffer_load_dwordx4 %[xoff], %[srdx], s63 offen lds\n"
"buffer_load_dwordx4 v193, %[srdx], s63 offen offset:1024 lds\n"
"s_add_u32 m0, s64, 59392\n"
"s_nop 0\n"
"buffer_load_dwordx4 %[xo9], %[srdx], s63 offen lds\n"
"s_add_u32 s63, s63, 0x80\n"
"s_add_u32 m0, %[hb], 51200\n"
"s_nop 0\n"
"buffer_load_dwordx4 v198, %[srdw], s62 offen lds\n"
"buffer_load_dwordx4 v195, %[srdw], s62 offen offset:1024 lds\n"
"s_add_u32 s62, s62, 0x40000\n"
"s_cmp_eq_u32 %[wrot], 1\n"
"s_cselect_b32 s68, 0, 0x40000000\n"
"s_mov_b32 s69, 384\n"
"v_or_b32_e64 v200, s68, v199\n"
"buffer_load_dword v201, v200, %[srdx], s69 offen\n"
"s_cmp_eq_u32 %[wrot], 2\n"
"s_cselect_b32 s68, 0, 0x40000000\n"
"s_mov_b32 s69, 83328\n"
"v_or_b32 v200, s68, v199\n"
"buffer_load_dword v201, v200, %[srdx], s69 offen\n"
"s_waitcnt vmcnt(7) lgkmcnt(0)\n"
"s_barrier\n"
"v_mfma_f32_16x16x32_bf16 v[16:19], v[128:131], v[96:99], 0\n"
"ds_read_b128 v[108:111], v194 offset:512\n"
"ds_read_b128 v[112:115], v196 offset:512\n"
"v_mfma_f32_16x16x32_bf16 v[48:51], v[128:131], v[100:103], 0\n"
"ds_read_b128 v[116:119], %[xb] offset:57856\n"
"s_add_u32 m0, %[hb], 34816\n"
"v_mfma_f32_16x16x32_bf16 v[20:23], v[132:135], v[96:99], 0\n"
"buffer_load_dwordx4 v198, %[srdw], s62 offen lds\n"
"v_mfma_f32_16x16x32_bf16 v[52:55], v[132:135], v[100:103], 0\n"
"ds_read_b64_tr_b16 v[160:161], %[wbe] offset:40960\n"
"ds_read_b64_tr_b16 v[162:163], v192 offset:41984\n"
"v_mfma_f32_16x16x32_bf16 v[24:27], v[136:139], v[96:99], 0\n"
"ds_read_b64_tr_b16 v[164:165], v192 offset:40960\n"
"v_mfma_f32_16x16x32_bf16 v[56:59], v[136:139], v[100:103], 0\n"
"ds_read_b64_tr_b16 v[166:167], %[wbe] offset:41984\n"
"v_mfma_f32_16x16x32_bf16 v[80:83], v[136:139], v[104:107], 0\n"
"buffer_load_dwordx4 v195, %[srdw], s62 offen offset:1024 lds\n"
"s_add_u32 s62, s62, 0x40000\n"
"v_mfma_f32_16x16x32_bf16 v[28:31], v[140:143], v[96:99], 0\n"
"ds_read_b64_tr_b16 v[168:169], %[wbe] offset:41216\n"
"ds_read_b64_tr_b16 v[170:171], v192 offset:42240\n"
"v_mfma_f32_16x16x32_bf16 v[60:63], v[140:143], v[100:103], 0\n"
"ds_read_b64_tr_b16 v[172:173], v192 offset:41216\n"
"v_mfma_f32_16x16x32_bf16 v[84:87], v[140:143], v[104:107], 0\n"
"ds_read_b64_tr_b16 v[174:175], %[wbe] offset:42240\n"
"v_mfma_f32_16x16x32_bf16 v[32:35], v[144:147], v[96:99], 0\n"
"s_cmp_eq_u32 %[wrot], 3\n"
"s_cselect_b32 s68, 0, 0x40000000\n"
"s_mov_b32 s69, 512\n"
"v_or_b32 v200, s68, v199\n"
"buffer_load_dword v201, v200, %[srdx], s69 offen\n"
"v_mfma_f32_16x16x32_bf16 v[64:67], v[144:147], v[100:103], 0\n"
"ds_read_b64_tr_b16 v[176:177], %[wbe] offset:41472\n"
"ds_read_b64_tr_b16 v[178:179], v192 offset:42496\n"
"v_mfma_f32_16x16x32_bf16 v[36:39], v[148:151], v[96:99], 0\n"
"ds_read_b64_tr_b16 v[180:181], v192 offset:41472\n"
"v_mfma_f32_16x16x32_bf16 v[68:71], v[148:151], v[100:103], 0\n"
"ds_read_b64_tr_b16 v[182:183], %[wbe] offset:42496\n"
"v_mfma_f32_16x16x32_bf16 v[40:43], v[152:155], v[96:99], 0\n"
"ds_read_b64_tr_b16 v[184:185], %[wbe] offset:41728\n"
"ds_read_b64_tr_b16 v[186:187], v192 offset:42752\n"
"v_mfma_f32_16x16x32_bf16 v[72:75], v[152:155], v[100:103], 0\n"
"ds_read_b64_tr_b16 v[188:189], v192 offset:41728\n"
"v_mfma_f32_16x16x32_bf16 v[44:47], v[156:159], v[96:99], 0\n"
"ds_read_b64_tr_b16 v[190:191], %[wbe] offset:42752\n"
"v_mfma_f32_16x16x32_bf16 v[76:79], v[156:159], v[100:103], 0\n"
"s_waitcnt vmcnt(5) lgkmcnt(0)\n"
"s_barrier\n"
"v_mfma_f32_16x16x32_bf16 v[16:19], v[160:163], v[108:111], v[16:19]\n"
"ds_read_b128 v[96:99], v194 offset:16384\n"
"ds_read_b128 v[100:103], v196 offset:16384\n"
"s_add_u32 m0, s60, 135168\n"
"v_mfma_f32_16x16x32_bf16 v[48:51], v[160:163], v[112:115], v[48:51]\n"
"buffer_load_dwordx4 %[xoff], %[srdx], s63 offen lds\n"
"v_mfma_f32_16x16x32_bf16 v[20:23], v[164:167], v[108:111], v[20:23]\n"
"ds_read_b128 v[104:107], %[xb] offset:59392\n"
"ds_read_b64_tr_b16 v[128:129], %[wbe] offset:49152\n"
"v_mfma_f32_16x16x32_bf16 v[52:55], v[164:167], v[112:115], v[52:55]\n"
"ds_read_b64_tr_b16 v[130:131], v192 offset:50176\n"
"v_mfma_f32_16x16x32_bf16 v[24:27], v[168:171], v[108:111], v[24:27]\n"
"ds_read_b64_tr_b16 v[132:133], v192 offset:49152\n"
"ds_read_b64_tr_b16 v[134:135], %[wbe] offset:50176\n"
"v_mfma_f32_16x16x32_bf16 v[56:59], v[168:171], v[112:115], v[56:59]\n"
"buffer_load_dwordx4 v193, %[srdx], s63 offen offset:1024 lds\n"
"s_add_u32 s63, s63, 0x80\n"
"v_mfma_f32_16x16x32_bf16 v[80:83], v[168:171], v[116:119], v[80:83]\n"
"ds_read_b64_tr_b16 v[136:137], %[wbe] offset:49408\n"
"v_mfma_f32_16x16x32_bf16 v[28:31], v[172:175], v[108:111], v[28:31]\n"
"ds_read_b64_tr_b16 v[138:139], v192 offset:50432\n"
"ds_read_b64_tr_b16 v[140:141], v192 offset:49408\n"
"s_add_u32 m0, %[hb], 43008\n"
"v_mfma_f32_16x16x32_bf16 v[60:63], v[172:175], v[112:115], v[60:63]\n"
"buffer_load_dwordx4 v198, %[srdw], s62 offen lds\n"
"v_mfma_f32_16x16x32_bf16 v[84:87], v[172:175], v[116:119], v[84:87]\n"
"ds_read_b64_tr_b16 v[142:143], %[wbe] offset:50432\n"
"ds_read_b64_tr_b16 v[144:145], %[wbe] offset:49664\n"
"v_mfma_f32_16x16x32_bf16 v[32:35], v[176:179], v[108:111], v[32:35]\n"
"ds_read_b64_tr_b16 v[146:147], v192 offset:50688\n"
"v_mfma_f32_16x16x32_bf16 v[64:67], v[176:179], v[112:115], v[64:67]\n"
"buffer_load_dwordx4 v195, %[srdw], s62 offen offset:1024 lds\n"
"s_add_u32 s62, s62, 0x40000\n"
"v_mfma_f32_16x16x32_bf16 v[36:39], v[180:183], v[108:111], v[36:39]\n"
"ds_read_b64_tr_b16 v[148:149], v192 offset:49664\n"
"ds_read_b64_tr_b16 v[150:151], %[wbe] offset:50688\n"
"v_mfma_f32_16x16x32_bf16 v[68:71], v[180:183], v[112:115], v[68:71]\n"
"ds_read_b64_tr_b16 v[152:153], %[wbe] offset:49920\n"
"v_mfma_f32_16x16x32_bf16 v[40:43], v[184:187], v[108:111], v[40:43]\n"
"s_cmp_eq_u32 %[wrot], 0\n"
"s_cselect_b32 s68, 0, 0x40000000\n"
"s_mov_b32 s69, 83456\n"
"v_or_b32 v200, s68, v199\n"
"buffer_load_dword v201, v200, %[srdx], s69 offen\n"
"v_mfma_f32_16x16x32_bf16 v[72:75], v[184:187], v[112:115], v[72:75]\n"
"ds_read_b64_tr_b16 v[154:155], v192 offset:50944\n"
"ds_read_b64_tr_b16 v[156:157], v192 offset:49920\n"
"v_mfma_f32_16x16x32_bf16 v[44:47], v[188:191], v[108:111], v[44:47]\n"
"ds_read_b64_tr_b16 v[158:159], %[wbe] offset:50944\n"
"v_mfma_f32_16x16x32_bf16 v[76:79], v[188:191], v[112:115], v[76:79]\n"
"s_waitcnt vmcnt(6) lgkmcnt(0)\n"
"s_barrier\n"
"v_mfma_f32_16x16x32_bf16 v[16:19], v[128:131], v[96:99], v[16:19]\n"
"ds_read_b128 v[108:111], v194 offset:16896\n"
"ds_read_b128 v[112:115], v196 offset:16896\n"
"v_mfma_f32_16x16x32_bf16 v[48:51], v[128:131], v[100:103], v[48:51]\n"
"ds_read_b128 v[116:119], %[xb] offset:59904\n"
"s_add_u32 m0, %[hb], 51200\n"
"v_mfma_f32_16x16x32_bf16 v[20:23], v[132:135], v[96:99], v[20:23]\n"
"buffer_load_dwordx4 v198, %[srdw], s62 offen lds\n"
"v_mfma_f32_16x16x32_bf16 v[52:55], v[132:135], v[100:103], v[52:55]\n"
"ds_read_b64_tr_b16 v[160:161], %[wbe] offset:32768\n"
"ds_read_b64_tr_b16 v[162:163], v192 offset:33792\n"
"v_mfma_f32_16x16x32_bf16 v[24:27], v[136:139], v[96:99], v[24:27]\n"
"ds_read_b64_tr_b16 v[164:165], v192 offset:32768\n"
"v_mfma_f32_16x16x32_bf16 v[56:59], v[136:139], v[100:103], v[56:59]\n"
"ds_read_b64_tr_b16 v[166:167], %[wbe] offset:33792\n"
"v_mfma_f32_16x16x32_bf16 v[80:83], v[136:139], v[104:107], v[80:83]\n"
"buffer_load_dwordx4 v195, %[srdw], s62 offen offset:1024 lds\n"
"s_add_u32 s62, s62, 0x40000\n"
"v_mfma_f32_16x16x32_bf16 v[28:31], v[140:143], v[96:99], v[28:31]\n"
"ds_read_b64_tr_b16 v[168:169], %[wbe] offset:33024\n"
"ds_read_b64_tr_b16 v[170:171], v192 offset:34048\n"
"v_mfma_f32_16x16x32_bf16 v[60:63], v[140:143], v[100:103], v[60:63]\n"
"ds_read_b64_tr_b16 v[172:173], v192 offset:33024\n"
"v_mfma_f32_16x16x32_bf16 v[84:87], v[140:143], v[104:107], v[84:87]\n"
"ds_read_b64_tr_b16 v[174:175], %[wbe] offset:34048\n"
"v_mfma_f32_16x16x32_bf16 v[32:35], v[144:147], v[96:99], v[32:35]\n"
"s_cmp_eq_u32 %[wrot], 1\n"
"s_cselect_b32 s68, 0, 0x40000000\n"
"s_mov_b32 s69, 640\n"
"v_or_b32 v200, s68, v199\n"
"buffer_load_dword v201, v200, %[srdx], s69 offen\n"
"v_mfma_f32_16x16x32_bf16 v[64:67], v[144:147], v[100:103], v[64:67]\n"
"ds_read_b64_tr_b16 v[176:177], %[wbe] offset:33280\n"
"ds_read_b64_tr_b16 v[178:179], v192 offset:34304\n"
"v_mfma_f32_16x16x32_bf16 v[36:39], v[148:151], v[96:99], v[36:39]\n"
"ds_read_b64_tr_b16 v[180:181], v192 offset:33280\n"
"v_mfma_f32_16x16x32_bf16 v[68:71], v[148:151], v[100:103], v[68:71]\n"
"ds_read_b64_tr_b16 v[182:183], %[wbe] offset:34304\n"
"v_mfma_f32_16x16x32_bf16 v[40:43], v[152:155], v[96:99], v[40:43]\n"
"ds_read_b64_tr_b16 v[184:185], %[wbe] offset:33536\n"
"ds_read_b64_tr_b16 v[186:187], v192 offset:34560\n"
"v_mfma_f32_16x16x32_bf16 v[72:75], v[152:155], v[100:103], v[72:75]\n"
"ds_read_b64_tr_b16 v[188:189], v192 offset:33536\n"
"v_mfma_f32_16x16x32_bf16 v[44:47], v[156:159], v[96:99], v[44:47]\n"
"ds_read_b64_tr_b16 v[190:191], %[wbe] offset:34560\n"
"v_mfma_f32_16x16x32_bf16 v[76:79], v[156:159], v[100:103], v[76:79]\n"
"s_waitcnt vmcnt(4) lgkmcnt(0)\n"
"s_barrier\n"
"v_mfma_f32_16x16x32_bf16 v[16:19], v[160:163], v[108:111], v[16:19]\n"
"ds_read_b128 v[96:99], v194 offset:0\n"
"ds_read_b128 v[100:103], v196 offset:0\n"
"s_add_u32 m0, s60, 151552\n"
"v_mfma_f32_16x16x32_bf16 v[48:51], v[160:163], v[112:115], v[48:51]\n"
"buffer_load_dwordx4 %[xoff], %[srdx], s63 offen lds\n"
"v_mfma_f32_16x16x32_bf16 v[20:23], v[164:167], v[108:111], v[20:23]\n"
"ds_read_b128 v[104:107], %[xb] offset:57344\n"
"ds_read_b64_tr_b16 v[128:129], %[wbe] offset:40960\n"
"v_mfma_f32_16x16x32_bf16 v[52:55], v[164:167], v[112:115], v[52:55]\n"
"ds_read_b64_tr_b16 v[130:131], v192 offset:41984\n"
"v_mfma_f32_16x16x32_bf16 v[24:27], v[168:171], v[108:111], v[24:27]\n"
"ds_read_b64_tr_b16 v[132:133], v192 offset:40960\n"
"ds_read_b64_tr_b16 v[134:135], %[wbe] offset:41984\n"
"v_mfma_f32_16x16x32_bf16 v[56:59], v[168:171], v[112:115], v[56:59]\n"
"buffer_load_dwordx4 v193, %[srdx], s63 offen offset:1024 lds\n"
"s_add_u32 s63, s63, 0x80\n"
"v_mfma_f32_16x16x32_bf16 v[80:83], v[168:171], v[116:119], v[80:83]\n"
"ds_read_b64_tr_b16 v[136:137], %[wbe] offset:41216\n"
"v_mfma_f32_16x16x32_bf16 v[28:31], v[172:175], v[108:111], v[28:31]\n"
"ds_read_b64_tr_b16 v[138:139], v192 offset:42240\n"
"ds_read_b64_tr_b16 v[140:141], v192 offset:41216\n"
"s_add_u32 m0, %[hb], 34816\n"
"v_mfma_f32_16x16x32_bf16 v[60:63], v[172:175], v[112:115], v[60:63]\n"
"buffer_load_dwordx4 v198, %[srdw], s62 offen lds\n"
"v_mfma_f32_16x16x32_bf16 v[84:87], v[172:175], v[116:119], v[84:87]\n"
"ds_read_b64_tr_b16 v[142:143], %[wbe] offset:42240\n"
"ds_read_b64_tr_b16 v[144:145], %[wbe] offset:41472\n"
"v_mfma_f32_16x16x32_bf16 v[32:35], v[176:179], v[108:111], v[32:35]\n"
"ds_read_b64_tr_b16 v[146:147], v192 offset:42496\n"
"v_mfma_f32_16x16x32_bf16 v[64:67], v[176:179], v[112:115], v[64:67]\n"
"buffer_load_dwordx4 v195, %[srdw], s62 offen offset:1024 lds\n"
"s_add_u32 s62, s62, 0x40000\n"
"v_mfma_f32_16x16x32_bf16 v[36:39], v[180:183], v[108:111], v[36:39]\n"
"ds_read_b64_tr_b16 v[148:149], v192 offset:41472\n"
"ds_read_b64_tr_b16 v[150:151], %[wbe] offset:42496\n"
"v_mfma_f32_16x16x32_bf16 v[68:71], v[180:183], v[112:115], v[68:71]\n"
"ds_read_b64_tr_b16 v[152:153], %[wbe] offset:41728\n"
"v_mfma_f32_16x16x32_bf16 v[40:43], v[184:187], v[108:111], v[40:43]\n"
"s_cmp_eq_u32 %[wrot], 2\n"
"s_cselect_b32 s68, 0, 0x40000000\n"
"s_mov_b32 s69, 83584\n"
"v_or_b32 v200, s68, v199\n"
"buffer_load_dword v201, v200, %[srdx], s69 offen\n"
"v_mfma_f32_16x16x32_bf16 v[72:75], v[184:187], v[112:115], v[72:75]\n"
"ds_read_b64_tr_b16 v[154:155], v192 offset:42752\n"
"ds_read_b64_tr_b16 v[156:157], v192 offset:41728\n"
"v_mfma_f32_16x16x32_bf16 v[44:47], v[188:191], v[108:111], v[44:47]\n"
"ds_read_b64_tr_b16 v[158:159], %[wbe] offset:42752\n"
"v_mfma_f32_16x16x32_bf16 v[76:79], v[188:191], v[112:115], v[76:79]\n"
"s_waitcnt vmcnt(6) lgkmcnt(0)\n"
"s_barrier\n"
"v_mfma_f32_16x16x32_bf16 v[16:19], v[128:131], v[96:99], v[16:19]\n"
"ds_read_b128 v[108:111], v194 offset:512\n"
"ds_read_b128 v[112:115], v196 offset:512\n"
"v_mfma_f32_16x16x32_bf16 v[48:51], v[128:131], v[100:103], v[48:51]\n"
"ds_read_b128 v[116:119], %[xb] offset:57856\n"
"s_add_u32 m0, %[hb], 43008\n"
"v_mfma_f32_16x16x32_bf16 v[20:23], v[132:135], v[96:99], v[20:23]\n"
"buffer_load_dwordx4 v198, %[srdw], s62 offen lds\n"
"v_mfma_f32_16x16x32_bf16 v[52:55], v[132:135], v[100:103], v[52:55]\n"
"ds_read_b64_tr_b16 v[160:161], %[wbe] offset:49152\n"
"ds_read_b64_tr_b16 v[162:163], v192 offset:50176\n"
"v_mfma_f32_16x16x32_bf16 v[24:27], v[136:139], v[96:99], v[24:27]\n"
"ds_read_b64_tr_b16 v[164:165], v192 offset:49152\n"
"v_mfma_f32_16x16x32_bf16 v[56:59], v[136:139], v[100:103], v[56:59]\n"
"ds_read_b64_tr_b16 v[166:167], %[wbe] offset:50176\n"
"ds_read_b64_tr_b16 v[168:169], %[wbe] offset:49408\n"
"v_mfma_f32_16x16x32_bf16 v[80:83], v[136:139], v[104:107], v[80:83]\n"
"buffer_load_dwordx4 v195, %[srdw], s62 offen offset:1024 lds\n"
"s_add_u32 s62, s62, 0x40000\n"
"v_mfma_f32_16x16x32_bf16 v[28:31], v[140:143], v[96:99], v[28:31]\n"
"ds_read_b64_tr_b16 v[170:171], v192 offset:50432\n"
"v_mfma_f32_16x16x32_bf16 v[60:63], v[140:143], v[100:103], v[60:63]\n"
"ds_read_b64_tr_b16 v[172:173], v192 offset:49408\n"
"ds_read_b64_tr_b16 v[174:175], %[wbe] offset:50432\n"
"v_mfma_f32_16x16x32_bf16 v[84:87], v[140:143], v[104:107], v[84:87]\n"
"ds_read_b64_tr_b16 v[176:177], %[wbe] offset:49664\n"
"v_mfma_f32_16x16x32_bf16 v[32:35], v[144:147], v[96:99], v[32:35]\n"
"s_cmp_eq_u32 %[wrot], 3\n"
"s_cselect_b32 s68, 0, 0x40000000\n"
"s_mov_b32 s69, 768\n"
"v_or_b32 v200, s68, v199\n"
"buffer_load_dword v201, v200, %[srdx], s69 offen\n"
"v_mfma_f32_16x16x32_bf16 v[64:67], v[144:147], v[100:103], v[64:67]\n"
"ds_read_b64_tr_b16 v[178:179], v192 offset:50688\n"
"ds_read_b64_tr_b16 v[180:181], v192 offset:49664\n"
"v_mfma_f32_16x16x32_bf16 v[36:39], v[148:151], v[96:99], v[36:39]\n"
"ds_read_b64_tr_b16 v[182:183], %[wbe] offset:50688\n"
"v_mfma_f32_16x16x32_bf16 v[68:71], v[148:151], v[100:103], v[68:71]\n"
"ds_read_b64_tr_b16 v[184:185], %[wbe] offset:49920\n"
"ds_read_b64_tr_b16 v[186:187], v192 offset:50944\n"
"v_mfma_f32_16x16x32_bf16 v[40:43], v[152:155], v[96:99], v[40:43]\n"
"s_bitcmp0_b32 %[xrot], 4\n"
"s_cbranch_scc1 LK1_4_%=\n"
"s_mov_b32 m0, s52\n"
"s_nop 0\n"
"buffer_load_dwordx4 %[g0], %[srdv], s61 offen nt lds\n"
"buffer_load_dwordx4 %[g1], %[srdv], s61 offen offset:1024 nt lds\n"
"buffer_load_dwordx4 %[g0], %[srdk], s61 offen offset:2048 nt lds\n"
"s_add_u32 s61, s61, %[kvs]\n"
"LK1_4_%=:\n"
"v_mfma_f32_16x16x32_bf16 v[72:75], v[152:155], v[100:103], v[72:75]\n"
"ds_read_b64_tr_b16 v[188:189], v192 offset:49920\n"
"v_mfma_f32_16x16x32_bf16 v[44:47], v[156:159], v[96:99], v[44:47]\n"
"ds_read_b64_tr_b16 v[190:191], %[wbe] offset:50944\n"
"v_mfma_f32_16x16x32_bf16 v[76:79], v[156:159], v[100:103], v[76:79]\n"
"s_waitcnt vmcnt(7)\n"
"s_bitcmp1_b32 s91, 5\n"
"s_cbranch_scc1 LJ1_5_%=\n"
"s_waitcnt vmcnt(4)\n"
"LJ1_5_%=:\n"
"s_waitcnt lgkmcnt(0)\n"
"s_barrier\n"
"v_mfma_f32_16x16x32_bf16 v[16:19], v[160:163], v[108:111], v[16:19]\n"
"ds_read_b128 v[96:99], v194 offset:16384\n"
"ds_read_b128 v[100:103], v196 offset:16384\n"
"s_add_u32 m0, s60, 135168\n"
"v_mfma_f32_16x16x32_bf16 v[48:51], v[160:163], v[112:115], v[48:51]\n"
"buffer_load_dwordx4 %[xoff], %[srdx], s63 offen lds\n"
"v_mfma_f32_16x16x32_bf16 v[20:23], v[164:167], v[108:111], v[20:23]\n"
"ds_read_b128 v[104:107], %[xb] offset:59392\n"
"ds_read_b64_tr_b16 v[128:129], %[wbe] offset:32768\n"
"v_mfma_f32_16x16x32_bf16 v[52:55], v[164:167], v[112:115], v[52:55]\n"
"ds_read_b64_tr_b16 v[130:131], v192 offset:33792\n"
"ds_read_b64_tr_b16 v[132:133], v192 offset:32768\n"
"v_mfma_f32_16x16x32_bf16 v[24:27], v[168:171], v[108:111], v[24:27]\n"
"buffer_load_dwordx4 v193, %[srdx], s63 offen offset:1024 lds\n"
"s_add_u32 s63, s63, 0x80\n"
"v_mfma_f32_16x16x32_bf16 v[56:59], v[168:171], v[112:115], v[56:59]\n"
"ds_read_b64_tr_b16 v[134:135], %[wbe] offset:33792\n"
"v_mfma_f32_16x16x32_bf16 v[80:83], v[168:171], v[116:119], v[80:83]\n"
"ds_read_b64_tr_b16 v[136:137], %[wbe] offset:33024\n"
"ds_read_b64_tr_b16 v[138:139], v192 offset:34048\n"
"s_add_u32 m0, %[hb], 51200\n"
"v_mfma_f32_16x16x32_bf16 v[28:31], v[172:175], v[108:111], v[28:31]\n"
"buffer_load_dwordx4 v198, %[srdw], s62 offen lds\n"
"v_mfma_f32_16x16x32_bf16 v[60:63], v[172:175], v[112:115], v[60:63]\n"
"ds_read_b64_tr_b16 v[140:141], v192 offset:33024\n"
"ds_read_b64_tr_b16 v[142:143], %[wbe] offset:34048\n"
"v_mfma_f32_16x16x32_bf16 v[84:87], v[172:175], v[116:119], v[84:87]\n"
"buffer_load_dwordx4 v195, %[srdw], s62 offen offset:1024 lds\n"
"s_add_u32 s62, s62, 0x40000\n"
"v_mfma_f32_16x16x32_bf16 v[32:35], v[176:179], v[108:111], v[32:35]\n"
"ds_read_b64_tr_b16 v[144:145], %[wbe] offset:33280\n"
"ds_read_b64_tr_b16 v[146:147], v192 offset:34304\n"
"v_mfma_f32_16x16x32_bf16 v[64:67], v[176:179], v[112:115], v[64:67]\n"
"ds_read_b64_tr_b16 v[148:149], v192 offset:33280\n"
"v_mfma_f32_16x16x32_bf16 v[36:39], v[180:183], v[108:111], v[36:39]\n"
"s_cmp_eq_u32 %[wrot], 0\n"
"s_cselect_b32 s68, 0, 0x40000000\n"
"s_mov_b32 s69, 83712\n"
"v_or_b32_e64 v200, s68, v199\n"
"buffer_load_dword v201, v200, %[srdx], s69 offen\n"
"v_mfma_f32_16x16x32_bf16 v[68:71], v[180:183], v[112:115], v[68:71]\n"
"ds_read_b64_tr_b16 v[150:151], %[wbe] offset:34304\n"
"ds_read_b64_tr_b16 v[152:153], %[wbe] offset:33536\n"
"v_mfma_f32_16x16x32_bf16 v[40:43], v[184:187], v[108:111], v[40:43]\n"
"ds_read_b64_tr_b16 v[154:155], v192 offset:34560\n"
"ds_read_b64_tr_b16 v[156:157], v192 offset:33536\n"
"v_mfma_f32_16x16x32_bf16 v[72:75], v[184:187], v[112:115], v[72:75]\n"
"s_bitcmp0_b32 %[xrot], 5\n"
"s_cbranch_scc1 LK1_5_%=\n"
"s_mov_b32 m0, s52\n"
"s_nop 0\n"
"buffer_load_dwordx4 %[g0], %[srdv], s61 offen nt lds\n"
"buffer_load_dwordx4 %[g1], %[srdv], s61 offen offset:1024 nt lds\n"
"buffer_load_dwordx4 %[g0], %[srdk], s61 offen offset:2048 nt lds\n"
"s_add_u32 s61, s61, %[kvs]\n"
"LK1_5_%=:\n"
"v_mfma_f32_16x16x32_bf16 v[44:47], v[188:191], v[108:111], v[44:47]\n"
"ds_read_b64_tr_b16 v[158:159], %[wbe] offset:34560\n"
"v_mfma_f32_16x16x32_bf16 v[76:79], v[188:191], v[112:115], v[76:79]\n"
"s_waitcnt vmcnt(9)\n"
"s_bitcmp1_b32 s91, 6\n"
"s_cbranch_scc1 LJ1_6_%=\n"
"s_waitcnt vmcnt(6)\n"
"LJ1_6_%=:\n"
"s_waitcnt lgkmcnt(0)\n"
"s_barrier\n"
"v_mfma_f32_16x16x32_bf16 v[16:19], v[128:131], v[96:99], v[16:19]\n"
"ds_read_b128 v[108:111], v194 offset:16896\n"
"ds_read_b128 v[112:115], v196 offset:16896\n"
"v_mfma_f32_16x16x32_bf16 v[48:51], v[128:131], v[100:103], v[48:51]\n"
"ds_read_b128 v[116:119], %[xb] offset:59904\n"
"s_add_u32 m0, %[hb], 34816\n"
"v_mfma_f32_16x16x32_bf16 v[20:23], v[132:135], v[96:99], v[20:23]\n"
"buffer_load_dwordx4 v198, %[srdw], s62 offen lds\n"
"v_mfma_f32_16x16x32_bf16 v[52:55], v[132:135], v[100:103], v[52:55]\n"
"ds_read_b64_tr_b16 v[160:161], %[wbe] offset:40960\n"
"ds_read_b64_tr_b16 v[162:163], v192 offset:41984\n"
"v_mfma_f32_16x16x32_bf16 v[24:27], v[136:139], v[96:99], v[24:27]\n"
"ds_read_b64_tr_b16 v[164:165], v192 offset:40960\n"
"v_mfma_f32_16x16x32_bf16 v[56:59], v[136:139], v[100:103], v[56:59]\n"
"ds_read_b64_tr_b16 v[166:167], %[wbe] offset:41984\n"
"ds_read_b64_tr_b16 v[168:169], %[wbe] offset:41216\n"
"v_mfma_f32_16x16x32_bf16 v[80:83], v[136:139], v[104:107], v[80:83]\n"
"buffer_load_dwordx4 v195, %[srdw], s62 offen offset:1024 lds\n"
"s_add_u32 s62, s62, 0x40000\n"
"v_mfma_f32_16x16x32_bf16 v[28:31], v[140:143], v[96:99], v[28:31]\n"
"ds_read_b64_tr_b16 v[170:171], v192 offset:42240\n"
"v_mfma_f32_16x16x32_bf16 v[60:63], v[140:143], v[100:103], v[60:63]\n"
"ds_read_b64_tr_b16 v[172:173], v192 offset:41216\n"
"ds_read_b64_tr_b16 v[174:175], %[wbe] offset:42240\n"
"v_mfma_f32_16x16x32_bf16 v[84:87], v[140:143], v[104:107], v[84:87]\n"
"ds_read_b64_tr_b16 v[176:177], %[wbe] offset:41472\n"
"v_mfma_f32_16x16x32_bf16 v[32:35], v[144:147], v[96:99], v[32:35]\n"
"s_cmp_eq_u32 %[wrot], 1\n"
"s_cselect_b32 s68, 0, 0x40000000\n"
"s_mov_b32 s69, 896\n"
"v_or_b32_e64 v200, s68, v199\n"
"buffer_load_dword v201, v200, %[srdx], s69 offen\n"
"v_mfma_f32_16x16x32_bf16 v[64:67], v[144:147], v[100:103], v[64:67]\n"
"ds_read_b64_tr_b16 v[178:179], v192 offset:42496\n"
"ds_read_b64_tr_b16 v[180:181], v192 offset:41472\n"
"v_mfma_f32_16x16x32_bf16 v[36:39], v[148:151], v[96:99], v[36:39]\n"
"ds_read_b64_tr_b16 v[182:183], %[wbe] offset:42496\n"
"v_mfma_f32_16x16x32_bf16 v[68:71], v[148:151], v[100:103], v[68:71]\n"
"ds_read_b64_tr_b16 v[184:185], %[wbe] offset:41728\n"
"ds_read_b64_tr_b16 v[186:187], v192 offset:42752\n"
"v_mfma_f32_16x16x32_bf16 v[40:43], v[152:155], v[96:99], v[40:43]\n"
"s_bitcmp0_b32 %[xrot], 6\n"
"s_cbranch_scc1 LK1_6_%=\n"
"s_mov_b32 m0, s52\n"
"s_nop 0\n"
"buffer_load_dwordx4 %[g0], %[srdv], s61 offen nt lds\n"
"buffer_load_dwordx4 %[g1], %[srdv], s61 offen offset:1024 nt lds\n"
"buffer_load_dwordx4 %[g0], %[srdk], s61 offen offset:2048 nt lds\n"
"s_add_u32 s61, s61, %[kvs]\n"
"LK1_6_%=:\n"
"v_mfma_f32_16x16x32_bf16 v[72:75], v[152:155], v[100:103], v[72:75]\n"
"ds_read_b64_tr_b16 v[188:189], v192 offset:41728\n"
"v_mfma_f32_16x16x32_bf16 v[44:47], v[156:159], v[96:99], v[44:47]\n"
"ds_read_b64_tr_b16 v[190:191], %[wbe] offset:42752\n"
"v_mfma_f32_16x16x32_bf16 v[76:79], v[156:159], v[100:103], v[76:79]\n"
"s_waitcnt vmcnt(7)\n"
"s_bitcmp1_b32 s91, 7\n"
"s_cbranch_scc1 LJ1_7_%=\n"
"s_waitcnt vmcnt(4)\n"
"LJ1_7_%=:\n"
"s_waitcnt lgkmcnt(0)\n"
"s_barrier\n"
"v_mfma_f32_16x16x32_bf16 v[16:19], v[160:163], v[108:111], v[16:19]\n"
"ds_read_b128 v[96:99], v194 offset:0\n"
"ds_read_b128 v[100:103], v196 offset:0\n"
"s_add_u32 m0, s60, 151552\n"
"v_mfma_f32_16x16x32_bf16 v[48:51], v[160:163], v[112:115], v[48:51]\n"
"buffer_load_dwordx4 %[xoff], %[srdx], s63 offen lds\n"
"v_mfma_f32_16x16x32_bf16 v[20:23], v[164:167], v[108:111], v[20:23]\n"
"ds_read_b128 v[104:107], %[xb] offset:57344\n"
"ds_read_b64_tr_b16 v[128:129], %[wbe] offset:49152\n"
"v_mfma_f32_16x16x32_bf16 v[52:55], v[164:167], v[112:115], v[52:55]\n"
"buffer_load_dwordx4 v193, %[srdx], s63 offen offset:1024 lds\n"
"v_mfma_f32_16x16x32_bf16 v[24:27], v[168:171], v[108:111], v[24:27]\n"
"ds_read_b64_tr_b16 v[130:131], v192 offset:50176\n"
"ds_read_b64_tr_b16 v[132:133], v192 offset:49152\n"
"v_mfma_f32_16x16x32_bf16 v[56:59], v[168:171], v[112:115], v[56:59]\n"
"ds_read_b64_tr_b16 v[134:135], %[wbe] offset:50176\n"
"ds_read_b64_tr_b16 v[136:137], %[wbe] offset:49408\n"
"s_add_u32 m0, s64, 59392\n"
"v_mfma_f32_16x16x32_bf16 v[80:83], v[168:171], v[116:119], v[80:83]\n"
"buffer_load_dwordx4 %[xo9], %[srdx], s63 offen lds\n"
"s_add_u32 s63, s63, 0x80\n"
"v_mfma_f32_16x16x32_bf16 v[28:31], v[172:175], v[108:111], v[28:31]\n"
"ds_read_b64_tr_b16 v[138:139], v192 offset:50432\n"
"ds_read_b64_tr_b16 v[140:141], v192 offset:49408\n"
"s_add_u32 m0, %[hb], 43008\n"
"v_mfma_f32_16x16x32_bf16 v[60:63], v[172:175], v[112:115], v[60:63]\n"
"buffer_load_dwordx4 v198, %[srdw], s62 offen lds\n"
"v_mfma_f32_16x16x32_bf16 v[84:87], v[172:175], v[116:119], v[84:87]\n"
"ds_read_b64_tr_b16 v[142:143], %[wbe] offset:50432\n"
"ds_read_b64_tr_b16 v[144:145], %[wbe] offset:49664\n"
"v_mfma_f32_16x16x32_bf16 v[32:35], v[176:179], v[108:111], v[32:35]\n"
"buffer_load_dwordx4 v195, %[srdw], s62 offen offset:1024 lds\n"
"s_add_u32 s62, s62, 0x40000\n"
"v_mfma_f32_16x16x32_bf16 v[64:67], v[176:179], v[112:115], v[64:67]\n"
"ds_read_b64_tr_b16 v[146:147], v192 offset:50688\n"
"ds_read_b64_tr_b16 v[148:149], v192 offset:49664\n"
"v_mfma_f32_16x16x32_bf16 v[36:39], v[180:183], v[108:111], v[36:39]\n"
"ds_read_b64_tr_b16 v[150:151], %[wbe] offset:50688\n"
"ds_read_b64_tr_b16 v[152:153], %[wbe] offset:49920\n"
"v_mfma_f32_16x16x32_bf16 v[68:71], v[180:183], v[112:115], v[68:71]\n"
"s_cmp_eq_u32 %[wrot], 2\n"
"s_cselect_b32 s68, 0, 0x40000000\n"
"s_mov_b32 s69, 83840\n"
"v_or_b32_e64 v200, s68, v199\n"
"buffer_load_dword v201, v200, %[srdx], s69 offen\n"
"v_mfma_f32_16x16x32_bf16 v[40:43], v[184:187], v[108:111], v[40:43]\n"
"ds_read_b64_tr_b16 v[154:155], v192 offset:50944\n"
"ds_read_b64_tr_b16 v[156:157], v192 offset:49920\n"
"v_mfma_f32_16x16x32_bf16 v[72:75], v[184:187], v[112:115], v[72:75]\n"
"s_bitcmp0_b32 %[xrot], 7\n"
"s_cbranch_scc1 LK1_7_%=\n"
"s_mov_b32 m0, s52\n"
"s_nop 0\n"
"buffer_load_dwordx4 %[g0], %[srdv], s61 offen nt lds\n"
"buffer_load_dwordx4 %[g1], %[srdv], s61 offen offset:1024 nt lds\n"
"buffer_load_dwordx4 %[g0], %[srdk], s61 offen offset:2048 nt lds\n"
"s_add_u32 s61, s61, %[kvs]\n"
"LK1_7_%=:\n"
"v_mfma_f32_16x16x32_bf16 v[44:47], v[188:191], v[108:111], v[44:47]\n"
"ds_read_b64_tr_b16 v[158:159], %[wbe] offset:50944\n"
"v_mfma_f32_16x16x32_bf16 v[76:79], v[188:191], v[112:115], v[76:79]\n"
"s_waitcnt vmcnt(10)\n"
"s_bitcmp1_b32 s91, 8\n"
"s_cbranch_scc1 LJ1_8_%=\n"
"s_waitcnt vmcnt(7)\n"
"LJ1_8_%=:\n"
"s_waitcnt lgkmcnt(0)\n"
"s_barrier\n"
"v_mfma_f32_16x16x32_bf16 v[16:19], v[128:131], v[96:99], v[16:19]\n"
"ds_read_b128 v[108:111], v194 offset:512\n"
"ds_read_b128 v[112:115], v196 offset:512\n"
"v_mfma_f32_16x16x32_bf16 v[48:51], v[128:131], v[100:103], v[48:51]\n"
"ds_read_b128 v[116:119], %[xb] offset:57856\n"
"s_add_u32 m0, %[hb], 51200\n"
"v_mfma_f32_16x16x32_bf16 v[20:23], v[132:135], v[96:99], v[20:23]\n"
"buffer_load_dwordx4 v198, %[srdw], s62 offen lds\n"
"v_mfma_f32_16x16x32_bf16 v[52:55], v[132:135], v[100:103], v[52:55]\n"
"ds_read_b64_tr_b16 v[160:161], %[wbe] offset:32768\n"
"ds_read_b64_tr_b16 v[162:163], v192 offset:33792\n"
"v_mfma_f32_16x16x32_bf16 v[24:27], v[136:139], v[96:99], v[24:27]\n"
"ds_read_b64_tr_b16 v[164:165], v192 offset:32768\n"
"v_mfma_f32_16x16x32_bf16 v[56:59], v[136:139], v[100:103], v[56:59]\n"
"ds_read_b64_tr_b16 v[166:167], %[wbe] offset:33792\n"
"v_mfma_f32_16x16x32_bf16 v[80:83], v[136:139], v[104:107], v[80:83]\n"
"buffer_load_dwordx4 v195, %[srdw], s62 offen offset:1024 lds\n"
"s_add_u32 s62, s62, 0x40000\n"
"v_mfma_f32_16x16x32_bf16 v[28:31], v[140:143], v[96:99], v[28:31]\n"
"ds_read_b64_tr_b16 v[168:169], %[wbe] offset:33024\n"
"ds_read_b64_tr_b16 v[170:171], v192 offset:34048\n"
"v_mfma_f32_16x16x32_bf16 v[60:63], v[140:143], v[100:103], v[60:63]\n"
"ds_read_b64_tr_b16 v[172:173], v192 offset:33024\n"
"v_mfma_f32_16x16x32_bf16 v[84:87], v[140:143], v[104:107], v[84:87]\n"
"ds_read_b64_tr_b16 v[174:175], %[wbe] offset:34048\n"
"v_mfma_f32_16x16x32_bf16 v[32:35], v[144:147], v[96:99], v[32:35]\n"
"s_cmp_eq_u32 %[wrot], 3\n"
"s_cselect_b32 s68, 0, 0x40000000\n"
"s_mov_b32 s69, 1024\n"
"v_or_b32_e64 v200, s68, v199\n"
"buffer_load_dword v201, v200, %[srdx], s69 offen\n"
"v_mfma_f32_16x16x32_bf16 v[64:67], v[144:147], v[100:103], v[64:67]\n"
"ds_read_b64_tr_b16 v[176:177], %[wbe] offset:33280\n"
"ds_read_b64_tr_b16 v[178:179], v192 offset:34304\n"
"v_mfma_f32_16x16x32_bf16 v[36:39], v[148:151], v[96:99], v[36:39]\n"
"ds_read_b64_tr_b16 v[180:181], v192 offset:33280\n"
"v_mfma_f32_16x16x32_bf16 v[68:71], v[148:151], v[100:103], v[68:71]\n"
"ds_read_b64_tr_b16 v[182:183], %[wbe] offset:34304\n"
"v_mfma_f32_16x16x32_bf16 v[40:43], v[152:155], v[96:99], v[40:43]\n"
"ds_read_b64_tr_b16 v[184:185], %[wbe] offset:33536\n"
"ds_read_b64_tr_b16 v[186:187], v192 offset:34560\n"
"v_mfma_f32_16x16x32_bf16 v[72:75], v[152:155], v[100:103], v[72:75]\n"
"ds_read_b64_tr_b16 v[188:189], v192 offset:33536\n"
"v_mfma_f32_16x16x32_bf16 v[44:47], v[156:159], v[96:99], v[44:47]\n"
"ds_read_b64_tr_b16 v[190:191], %[wbe] offset:34560\n"
"v_mfma_f32_16x16x32_bf16 v[76:79], v[156:159], v[100:103], v[76:79]\n"
"s_waitcnt vmcnt(7)\n"
"s_bitcmp1_b32 s91, 9\n"
"s_cbranch_scc1 LJ1_9_%=\n"
"s_waitcnt vmcnt(4)\n"
"LJ1_9_%=:\n"
"s_waitcnt lgkmcnt(0)\n"
"s_barrier\n"
"v_mfma_f32_16x16x32_bf16 v[16:19], v[160:163], v[108:111], v[16:19]\n"
"ds_read_b128 v[96:99], v194 offset:16384\n"
"ds_read_b128 v[100:103], v196 offset:16384\n"
"s_add_u32 m0, s60, 135168\n"
"v_mfma_f32_16x16x32_bf16 v[48:51], v[160:163], v[112:115], v[48:51]\n"
"buffer_load_dwordx4 %[xoff], %[srdx], s63 offen lds\n"
"v_mfma_f32_16x16x32_bf16 v[20:23], v[164:167], v[108:111], v[20:23]\n"
"ds_read_b128 v[104:107], %[xb] offset:59392\n"
"ds_read_b64_tr_b16 v[128:129], %[wbe] offset:40960\n"
"v_mfma_f32_16x16x32_bf16 v[52:55], v[164:167], v[112:115], v[52:55]\n"
"ds_read_b64_tr_b16 v[130:131], v192 offset:41984\n"
"v_mfma_f32_16x16x32_bf16 v[24:27], v[168:171], v[108:111], v[24:27]\n"
"ds_read_b64_tr_b16 v[132:133], v192 offset:40960\n"
"ds_read_b64_tr_b16 v[134:135], %[wbe] offset:41984\n"
"v_mfma_f32_16x16x32_bf16 v[56:59], v[168:171], v[112:115], v[56:59]\n"
"buffer_load_dwordx4 v193, %[srdx], s63 offen offset:1024 lds\n"
"s_add_u32 s63, s63, 0x80\n"
"v_mfma_f32_16x16x32_bf16 v[80:83], v[168:171], v[116:119], v[80:83]\n"
"ds_read_b64_tr_b16 v[136:137], %[wbe] offset:41216\n"
"v_mfma_f32_16x16x32_bf16 v[28:31], v[172:175], v[108:111], v[28:31]\n"
"ds_read_b64_tr_b16 v[138:139], v192 offset:42240\n"
"ds_read_b64_tr_b16 v[140:141], v192 offset:41216\n"
"s_add_u32 m0, %[hb], 34816\n"
"v_mfma_f32_16x16x32_bf16 v[60:63], v[172:175], v[112:115], v[60:63]\n"
"buffer_load_dwordx4 v198, %[srdw], s62 offen lds\n"
"v_mfma_f32_16x16x32_bf16 v[84:87], v[172:175], v[116:119], v[84:87]\n"
"ds_read_b64_tr_b16 v[142:143], %[wbe] offset:42240\n"
"ds_read_b64_tr_b16 v[144:145], %[wbe] offset:41472\n"
"v_mfma_f32_16x16x32_bf16 v[32:35], v[176:179], v[108:111], v[32:35]\n"
"ds_read_b64_tr_b16 v[146:147], v192 offset:42496\n"
"v_mfma_f32_16x16x32_bf16 v[64:67], v[176:179], v[112:115], v[64:67]\n"
"buffer_load_dwordx4 v195, %[srdw], s62 offen offset:1024 lds\n"
"s_add_u32 s62, s62, 0x40000\n"
"v_mfma_f32_16x16x32_bf16 v[36:39], v[180:183], v[108:111], v[36:39]\n"
"ds_read_b64_tr_b16 v[148:149], v192 offset:41472\n"
"ds_read_b64_tr_b16 v[150:151], %[wbe] offset:42496\n"
"v_mfma_f32_16x16x32_bf16 v[68:71], v[180:183], v[112:115], v[68:71]\n"
"ds_read_b64_tr_b16 v[152:153], %[wbe] offset:41728\n"
"v_mfma_f32_16x16x32_bf16 v[40:43], v[184:187], v[108:111], v[40:43]\n"
"s_cmp_eq_u32 %[wrot], 0\n"
"s_cselect_b32 s68, 0, 0x40000000\n"
"s_mov_b32 s69, 83968\n"
"v_or_b32 v200, s68, v199\n"
"buffer_load_dword v201, v200, %[srdx], s69 offen\n"
"v_mfma_f32_16x16x32_bf16 v[72:75], v[184:187], v[112:115], v[72:75]\n"
"ds_read_b64_tr_b16 v[154:155], v192 offset:42752\n"
"ds_read_b64_tr_b16 v[156:157], v192 offset:41728\n"
"v_mfma_f32_16x16x32_bf16 v[44:47], v[188:191], v[108:111], v[44:47]\n"
"ds_read_b64_tr_b16 v[158:159], %[wbe] offset:42752\n"
"v_mfma_f32_16x16x32_bf16 v[76:79], v[188:191], v[112:115], v[76:79]\n"
"s_waitcnt vmcnt(6) lgkmcnt(0)\n"
"s_barrier\n"
"v_mfma_f32_16x16x32_bf16 v[16:19], v[128:131], v[96:99], v[16:19]\n"
"ds_read_b128 v[108:111], v194 offset:16896\n"
"ds_read_b128 v[112:115], v196 offset:16896\n"
"v_mfma_f32_16x16x32_bf16 v[48:51], v[128:131], v[100:103], v[48:51]\n"
"ds_read_b128 v[116:119], %[xb] offset:59904\n"
"s_add_u32 m0, %[hb], 43008\n"
"v_mfma_f32_16x16x32_bf16 v[20:23], v[132:135], v[96:99], v[20:23]\n"
"buffer_load_dwordx4 v198, %[srdw], s62 offen lds\n"
"v_mfma_f32_16x16x32_bf16 v[52:55], v[132:135], v[100:103], v[52:55]\n"
"ds_read_b64_tr_b16 v[160:161], %[wbe] offset:49152\n"
"ds_read_b64_tr_b16 v[162:163], v192 offset:50176\n"
"v_mfma_f32_16x16x32_bf16 v[24:27], v[136:139], v[96:99], v[24:27]\n"
"ds_read_b64_tr_b16 v[164:165], v192 offset:49152\n"
"v_mfma_f32_16x16x32_bf16 v[56:59], v[136:139], v[100:103], v[56:59]\n"
"ds_read_b64_tr_b16 v[166:167], %[wbe] offset:50176\n"
"v_mfma_f32_16x16x32_bf16 v[80:83], v[136:139], v[104:107], v[80:83]\n"
"buffer_load_dwordx4 v195, %[srdw], s62 offen offset:1024 lds\n"
"s_add_u32 s62, s62, 0x40000\n"
"v_mfma_f32_16x16x32_bf16 v[28:31], v[140:143], v[96:99], v[28:31]\n"
"ds_read_b64_tr_b16 v[168:169], %[wbe] offset:49408\n"
"ds_read_b64_tr_b16 v[170:171], v192 offset:50432\n"
"v_mfma_f32_16x16x32_bf16 v[60:63], v[140:143], v[100:103], v[60:63]\n"
"ds_read_b64_tr_b16 v[172:173], v192 offset:49408\n"
"v_mfma_f32_16x16x32_bf16 v[84:87], v[140:143], v[104:107], v[84:87]\n"
"ds_read_b64_tr_b16 v[174:175], %[wbe] offset:50432\n"
"v_mfma_f32_16x16x32_bf16 v[32:35], v[144:147], v[96:99], v[32:35]\n"
"s_bitcmp0_b32 %[xrot], 10\n"
"s_cbranch_scc1 LK1_10_%=\n"
"s_mov_b32 m0, s53\n"
"s_nop 0\n"
"buffer_load_dwordx4 %[g0], %[srdv], s61 offen nt lds\n"
"buffer_load_dwordx4 %[g1], %[srdv], s61 offen offset:1024 nt lds\n"
"buffer_load_dwordx4 %[g0], %[srdk], s61 offen offset:2048 nt lds\n"
"s_add_u32 s61, s61, %[kvs]\n"
"LK1_10_%=:\n"
"v_mfma_f32_16x16x32_bf16 v[64:67], v[144:147], v[100:103], v[64:67]\n"
"ds_read_b64_tr_b16 v[176:177], %[wbe] offset:49664\n"
"ds_read_b64_tr_b16 v[178:179], v192 offset:50688\n"
"v_mfma_f32_16x16x32_bf16 v[36:39], v[148:151], v[96:99], v[36:39]\n"
"ds_read_b64_tr_b16 v[180:181], v192 offset:49664\n"
"v_mfma_f32_16x16x32_bf16 v[68:71], v[148:151], v[100:103], v[68:71]\n"
"ds_read_b64_tr_b16 v[182:183], %[wbe] offset:50688\n"
"v_mfma_f32_16x16x32_bf16 v[40:43], v[152:155], v[96:99], v[40:43]\n"
"ds_read_b64_tr_b16 v[184:185], %[wbe] offset:49920\n"
"ds_read_b64_tr_b16 v[186:187], v192 offset:50944\n"
"v_mfma_f32_16x16x32_bf16 v[72:75], v[152:155], v[100:103], v[72:75]\n"
"ds_read_b64_tr_b16 v[188:189], v192 offset:49920\n"
"v_mfma_f32_16x16x32_bf16 v[44:47], v[156:159], v[96:99], v[44:47]\n"
"ds_read_b64_tr_b16 v[190:191], %[wbe] offset:50944\n"
"v_mfma_f32_16x16x32_bf16 v[76:79], v[156:159], v[100:103], v[76:79]\n"
"s_waitcnt vmcnt(6)\n"
"s_bitcmp1_b32 s91, 11\n"
"s_cbranch_scc1 LJ1_11_%=\n"
"s_waitcnt vmcnt(3)\n"
"LJ1_11_%=:\n"
"s_waitcnt lgkmcnt(0)\n"
"s_barrier\n"
"v_mfma_f32_16x16x32_bf16 v[16:19], v[160:163], v[108:111], v[16:19]\n"
"ds_read_b128 v[96:99], v194 offset:0\n"
"ds_read_b128 v[100:103], v196 offset:0\n"
"s_add_u32 m0, s60, 151552\n"
"v_mfma_f32_16x16x32_bf16 v[48:51], v[160:163], v[112:115], v[48:51]\n"
"buffer_load_dwordx4 %[xoff], %[srdx], s63 offen lds\n"
"v_mfma_f32_16x16x32_bf16 v[20:23], v[164:167], v[108:111], v[20:23]\n"
"ds_read_b128 v[104:107], %[xb] offset:57344\n"
"ds_read_b64_tr_b16 v[128:129], %[wbe] offset:32768\n"
"v_mfma_f32_16x16x32_bf16 v[52:55], v[164:167], v[112:115], v[52:55]\n"
"ds_read_b64_tr_b16 v[130:131], v192 offset:33792\n"
"v_mfma_f32_16x16x32_bf16 v[24:27], v[168:171], v[108:111], v[24:27]\n"
"ds_read_b64_tr_b16 v[132:133], v192 offset:32768\n"
"ds_read_b64_tr_b16 v[134:135], %[wbe] offset:33792\n"
"v_mfma_f32_16x16x32_bf16 v[56:59], v[168:171], v[112:115], v[56:59]\n"
"buffer_load_dwordx4 v193, %[srdx], s63 offen offset:1024 lds\n"
"s_add_u32 s63, s63, 0x80\n"
"v_mfma_f32_16x16x32_bf16 v[80:83], v[168:171], v[116:119], v[80:83]\n"
"ds_read_b64_tr_b16 v[136:137], %[wbe] offset:33024\n"
"v_mfma_f32_16x16x32_bf16 v[28:31], v[172:175], v[108:111], v[28:31]\n"
"ds_read_b64_tr_b16 v[138:139], v192 offset:34048\n"
"ds_read_b64_tr_b16 v[140:141], v192 offset:33024\n"
"s_add_u32 m0, %[hb], 51200\n"
"v_mfma_f32_16x16x32_bf16 v[60:63], v[172:175], v[112:115], v[60:63]\n"
"buffer_load_dwordx4 v198, %[srdw], s62 offen lds\n"
"v_mfma_f32_16x16x32_bf16 v[84:87], v[172:175], v[116:119], v[84:87]\n"
"ds_read_b64_tr_b16 v[142:143], %[wbe] offset:34048\n"
"ds_read_b64_tr_b16 v[144:145], %[wbe] offset:33280\n"
"v_mfma_f32_16x16x32_bf16 v[32:35], v[176:179], v[108:111], v[32:35]\n"
"ds_read_b64_tr_b16 v[146:147], v192 offset:34304\n"
"v_mfma_f32_16x16x32_bf16 v[64:67], v[176:179], v[112:115], v[64:67]\n"
"buffer_load_dwordx4 v195, %[srdw], s62 offen offset:1024 lds\n"
"s_add_u32 s62, s62, 0x40000\n"
"v_mfma_f32_16x16x32_bf16 v[36:39], v[180:183], v[108:111], v[36:39]\n"
"ds_read_b64_tr_b16 v[148:149], v192 offset:33280\n"
"ds_read_b64_tr_b16 v[150:151], %[wbe] offset:34304\n"
"v_mfma_f32_16x16x32_bf16 v[68:71], v[180:183], v[112:115], v[68:71]\n"
"ds_read_b64_tr_b16 v[152:153], %[wbe] offset:33536\n"
"v_mfma_f32_16x16x32_bf16 v[40:43], v[184:187], v[108:111], v[40:43]\n"
"s_bitcmp0_b32 %[xrot], 11\n"
"s_cbranch_scc1 LK1_11_%=\n"
"s_mov_b32 m0, s53\n"
"s_nop 0\n"
"buffer_load_dwordx4 %[g0], %[srdv], s61 offen nt lds\n"
"buffer_load_dwordx4 %[g1], %[srdv], s61 offen offset:1024 nt lds\n"
"buffer_load_dwordx4 %[g0], %[srdk], s61 offen offset:2048 nt lds\n"
"s_add_u32 s61, s61, %[kvs]\n"
"LK1_11_%=:\n"
"v_mfma_f32_16x16x32_bf16 v[72:75], v[184:187], v[112:115], v[72:75]\n"
"ds_read_b64_tr_b16 v[154:155], v192 offset:34560\n"
"ds_read_b64_tr_b16 v[156:157], v192 offset:33536\n"
"v_mfma_f32_16x16x32_bf16 v[44:47], v[188:191], v[108:111], v[44:47]\n"
"ds_read_b64_tr_b16 v[158:159], %[wbe] offset:34560\n"
"v_mfma_f32_16x16x32_bf16 v[76:79], v[188:191], v[112:115], v[76:79]\n"
"s_waitcnt vmcnt(7)\n"
"s_bitcmp1_b32 s91, 12\n"
"s_cbranch_scc1 LJ1_12_%=\n"
"s_waitcnt vmcnt(4)\n"
"LJ1_12_%=:\n"
"s_waitcnt lgkmcnt(0)\n"
"s_barrier\n"
"v_mfma_f32_16x16x32_bf16 v[16:19], v[128:131], v[96:99], v[16:19]\n"
"ds_read_b128 v[108:111], v194 offset:512\n"
"ds_read_b128 v[112:115], v196 offset:512\n"
"v_mfma_f32_16x16x32_bf16 v[48:51], v[128:131], v[100:103], v[48:51]\n"
"ds_read_b128 v[116:119], %[xb] offset:57856\n"
"s_add_u32 m0, %[hb], 34816\n"
"v_mfma_f32_16x16x32_bf16 v[20:23], v[132:135], v[96:99], v[20:23]\n"
"buffer_load_dwordx4 v198, %[srdw], s62 offen lds\n"
"v_mfma_f32_16x16x32_bf16 v[52:55], v[132:135], v[100:103], v[52:55]\n"
"ds_read_b64_tr_b16 v[160:161], %[wbe] offset:40960\n"
"ds_read_b64_tr_b16 v[162:163], v192 offset:41984\n"
"v_mfma_f32_16x16x32_bf16 v[24:27], v[136:139], v[96:99], v[24:27]\n"
"ds_read_b64_tr_b16 v[164:165], v192 offset:40960\n"
"v_mfma_f32_16x16x32_bf16 v[56:59], v[136:139], v[100:103], v[56:59]\n"
"ds_read_b64_tr_b16 v[166:167], %[wbe] offset:41984\n"
"v_mfma_f32_16x16x32_bf16 v[80:83], v[136:139], v[104:107], v[80:83]\n"
"buffer_load_dwordx4 v195, %[srdw], s62 offen offset:1024 lds\n"
"s_add_u32 s62, s62, 0x40000\n"
"v_mfma_f32_16x16x32_bf16 v[28:31], v[140:143], v[96:99], v[28:31]\n"
"ds_read_b64_tr_b16 v[168:169], %[wbe] offset:41216\n"
"ds_read_b64_tr_b16 v[170:171], v192 offset:42240\n"
"v_mfma_f32_16x16x32_bf16 v[60:63], v[140:143], v[100:103], v[60:63]\n"
"ds_read_b64_tr_b16 v[172:173], v192 offset:41216\n"
"v_mfma_f32_16x16x32_bf16 v[84:87], v[140:143], v[104:107], v[84:87]\n"
"ds_read_b64_tr_b16 v[174:175], %[wbe] offset:42240\n"
"v_mfma_f32_16x16x32_bf16 v[32:35], v[144:147], v[96:99], v[32:35]\n"
"s_bitcmp0_b32 %[xrot], 12\n"
"s_cbranch_scc1 LK1_12_%=\n"
"s_mov_b32 m0, s53\n"
"s_nop 0\n"
"buffer_load_dwordx4 %[g0], %[srdv], s61 offen nt lds\n"
"buffer_load_dwordx4 %[g1], %[srdv], s61 offen offset:1024 nt lds\n"
"buffer_load_dwordx4 %[g0], %[srdk], s61 offen offset:2048 nt lds\n"
"s_add_u32 s61, s61, %[kvs]\n"
"LK1_12_%=:\n"
"v_mfma_f32_16x16x32_bf16 v[64:67], v[144:147], v[100:103], v[64:67]\n"
"ds_read_b64_tr_b16 v[176:177], %[wbe] offset:41472\n"
"ds_read_b64_tr_b16 v[178:179], v192 offset:42496\n"
"v_mfma_f32_16x16x32_bf16 v[36:39], v[148:151], v[96:99], v[36:39]\n"
"ds_read_b64_tr_b16 v[180:181], v192 offset:41472\n"
"v_mfma_f32_16x16x32_bf16 v[68:71], v[148:151], v[100:103], v[68:71]\n"
"ds_read_b64_tr_b16 v[182:183], %[wbe] offset:42496\n"
"v_mfma_f32_16x16x32_bf16 v[40:43], v[152:155], v[96:99], v[40:43]\n"
"ds_read_b64_tr_b16 v[184:185], %[wbe] offset:41728\n"
"ds_read_b64_tr_b16 v[186:187], v192 offset:42752\n"
"v_mfma_f32_16x16x32_bf16 v[72:75], v[152:155], v[100:103], v[72:75]\n"
"ds_read_b64_tr_b16 v[188:189], v192 offset:41728\n"
"v_mfma_f32_16x16x32_bf16 v[44:47], v[156:159], v[96:99], v[44:47]\n"
"ds_read_b64_tr_b16 v[190:191], %[wbe] offset:42752\n"
"v_mfma_f32_16x16x32_bf16 v[76:79], v[156:159], v[100:103], v[76:79]\n"
"s_waitcnt vmcnt(5)\n"
"s_bitcmp1_b32 s91, 13\n"
"s_cbranch_scc1 LJ1_13_%=\n"
"s_waitcnt vmcnt(2)\n"
"LJ1_13_%=:\n"
"s_waitcnt lgkmcnt(0)\n"
"s_barrier\n"
"v_mfma_f32_16x16x32_bf16 v[16:19], v[160:163], v[108:111], v[16:19]\n"
"ds_read_b128 v[96:99], v194 offset:16384\n"
"ds_read_b128 v[100:103], v196 offset:16384\n"
"s_add_u32 m0, s60, 135168\n"
"v_mfma_f32_16x16x32_bf16 v[48:51], v[160:163], v[112:115], v[48:51]\n"
"buffer_load_dwordx4 %[xoff], %[srdx], s63 offen lds\n"
"v_mfma_f32_16x16x32_bf16 v[20:23], v[164:167], v[108:111], v[20:23]\n"
"ds_read_b128 v[104:107], %[xb] offset:59392\n"
"ds_read_b64_tr_b16 v[128:129], %[wbe] offset:49152\n"
"v_mfma_f32_16x16x32_bf16 v[52:55], v[164:167], v[112:115], v[52:55]\n"
"ds_read_b64_tr_b16 v[130:131], v192 offset:50176\n"
"v_mfma_f32_16x16x32_bf16 v[24:27], v[168:171], v[108:111], v[24:27]\n"
"ds_read_b64_tr_b16 v[132:133], v192 offset:49152\n"
"ds_read_b64_tr_b16 v[134:135], %[wbe] offset:50176\n"
"v_mfma_f32_16x16x32_bf16 v[56:59], v[168:171], v[112:115], v[56:59]\n"
"buffer_load_dwordx4 v193, %[srdx], s63 offen offset:1024 lds\n"
"s_add_u32 s63, s63, 0x80\n"
"v_mfma_f32_16x16x32_bf16 v[80:83], v[168:171], v[116:119], v[80:83]\n"
"ds_read_b64_tr_b16 v[136:137], %[wbe] offset:49408\n"
"v_mfma_f32_16x16x32_bf16 v[28:31], v[172:175], v[108:111], v[28:31]\n"
"ds_read_b64_tr_b16 v[138:139], v192 offset:50432\n"
"ds_read_b64_tr_b16 v[140:141], v192 offset:49408\n"
"s_add_u32 m0, %[hb], 43008\n"
"v_mfma_f32_16x16x32_bf16 v[60:63], v[172:175], v[112:115], v[60:63]\n"
"buffer_load_dwordx4 v198, %[srdw], s62 offen lds\n"
"v_mfma_f32_16x16x32_bf16 v[84:87], v[172:175], v[116:119], v[84:87]\n"
"ds_read_b64_tr_b16 v[142:143], %[wbe] offset:50432\n"
"ds_read_b64_tr_b16 v[144:145], %[wbe] offset:49664\n"
"v_mfma_f32_16x16x32_bf16 v[32:35], v[176:179], v[108:111], v[32:35]\n"
"ds_read_b64_tr_b16 v[146:147], v192 offset:50688\n"
"v_mfma_f32_16x16x32_bf16 v[64:67], v[176:179], v[112:115], v[64:67]\n"
"buffer_load_dwordx4 v195, %[srdw], s62 offen offset:1024 lds\n"
"s_add_u32 s62, s62, 0x40000\n"
"v_mfma_f32_16x16x32_bf16 v[36:39], v[180:183], v[108:111], v[36:39]\n"
"ds_read_b64_tr_b16 v[148:149], v192 offset:49664\n"
"ds_read_b64_tr_b16 v[150:151], %[wbe] offset:50688\n"
"v_mfma_f32_16x16x32_bf16 v[68:71], v[180:183], v[112:115], v[68:71]\n"
"ds_read_b64_tr_b16 v[152:153], %[wbe] offset:49920\n"
"v_mfma_f32_16x16x32_bf16 v[40:43], v[184:187], v[108:111], v[40:43]\n"
"s_bitcmp0_b32 %[xrot], 13\n"
"s_cbranch_scc1 LK1_13_%=\n"
"s_mov_b32 m0, s53\n"
"s_nop 0\n"
"buffer_load_dwordx4 %[g0], %[srdv], s61 offen nt lds\n"
"buffer_load_dwordx4 %[g1], %[srdv], s61 offen offset:1024 nt lds\n"
"buffer_load_dwordx4 %[g0], %[srdk], s61 offen offset:2048 nt lds\n"
"s_add_u32 s61, s61, %[kvs]\n"
"LK1_13_%=:\n"
"v_mfma_f32_16x16x32_bf16 v[72:75], v[184:187], v[112:115], v[72:75]\n"
"ds_read_b64_tr_b16 v[154:155], v192 offset:50944\n"
"ds_read_b64_tr_b16 v[156:157], v192 offset:49920\n"
"v_mfma_f32_16x16x32_bf16 v[44:47], v[188:191], v[108:111], v[44:47]\n"
"ds_read_b64_tr_b16 v[158:159], %[wbe] offset:50944\n"
"v_mfma_f32_16x16x32_bf16 v[76:79], v[188:191], v[112:115], v[76:79]\n"
"s_waitcnt vmcnt(7)\n"
"s_bitcmp1_b32 s91, 14\n"
"s_cbranch_scc1 LJ1_14_%=\n"
"s_waitcnt vmcnt(4)\n"
"LJ1_14_%=:\n"
"s_waitcnt lgkmcnt(0)\n"
"s_barrier\n"
"v_mfma_f32_16x16x32_bf16 v[16:19], v[128:131], v[96:99], v[16:19]\n"
"ds_read_b128 v[108:111], v194 offset:16896\n"
"ds_read_b128 v[112:115], v196 offset:16896\n"
"v_mfma_f32_16x16x32_bf16 v[48:51], v[128:131], v[100:103], v[48:51]\n"
"ds_read_b128 v[116:119], %[xb] offset:59904\n"
"s_add_u32 m0, %[hb], 51200\n"
"v_mfma_f32_16x16x32_bf16 v[20:23], v[132:135], v[96:99], v[20:23]\n"
"buffer_load_dwordx4 v198, %[srdw], s62 offen lds\n"
"v_mfma_f32_16x16x32_bf16 v[52:55], v[132:135], v[100:103], v[52:55]\n"
"ds_read_b64_tr_b16 v[160:161], %[wbe] offset:32768\n"
"v_mfma_f32_16x16x32_bf16 v[24:27], v[136:139], v[96:99], v[24:27]\n"
"ds_read_b64_tr_b16 v[162:163], v192 offset:33792\n"
"ds_read_b64_tr_b16 v[164:165], v192 offset:32768\n"
"v_mfma_f32_16x16x32_bf16 v[56:59], v[136:139], v[100:103], v[56:59]\n"
"ds_read_b64_tr_b16 v[166:167], %[wbe] offset:33792\n"
"v_mfma_f32_16x16x32_bf16 v[80:83], v[136:139], v[104:107], v[80:83]\n"
"buffer_load_dwordx4 v195, %[srdw], s62 offen offset:1024 lds\n"
"s_add_u32 s62, s62, 0x40000\n"
"v_mfma_f32_16x16x32_bf16 v[28:31], v[140:143], v[96:99], v[28:31]\n"
"ds_read_b64_tr_b16 v[168:169], %[wbe] offset:33024\n"
"v_mfma_f32_16x16x32_bf16 v[60:63], v[140:143], v[100:103], v[60:63]\n"
"ds_read_b64_tr_b16 v[170:171], v192 offset:34048\n"
"v_mfma_f32_16x16x32_bf16 v[84:87], v[140:143], v[104:107], v[84:87]\n"
"ds_read_b64_tr_b16 v[172:173], v192 offset:33024\n"
"ds_read_b64_tr_b16 v[174:175], %[wbe] offset:34048\n"
"v_mfma_f32_16x16x32_bf16 v[32:35], v[144:147], v[96:99], v[32:35]\n"
"ds_read_b64_tr_b16 v[176:177], %[wbe] offset:33280\n"
"v_mfma_f32_16x16x32_bf16 v[64:67], v[144:147], v[100:103], v[64:67]\n"
"ds_read_b64_tr_b16 v[178:179], v192 offset:34304\n"
"v_mfma_f32_16x16x32_bf16 v[36:39], v[148:151], v[96:99], v[36:39]\n"
"ds_read_b64_tr_b16 v[180:181], v192 offset:33280\n"
"v_mfma_f32_16x16x32_bf16 v[68:71], v[148:151], v[100:103], v[68:71]\n"
"ds_read_b64_tr_b16 v[182:183], %[wbe] offset:34304\n"
"ds_read_b64_tr_b16 v[184:185], %[wbe] offset:33536\n"
"v_mfma_f32_16x16x32_bf16 v[40:43], v[152:155], v[96:99], v[40:43]\n"
"ds_read_b64_tr_b16 v[186:187], v192 offset:34560\n"
"v_mfma_f32_16x16x32_bf16 v[72:75], v[152:155], v[100:103], v[72:75]\n"
"ds_read_b64_tr_b16 v[188:189], v192 offset:33536\n"
"v_mfma_f32_16x16x32_bf16 v[44:47], v[156:159], v[96:99], v[44:47]\n"
"ds_read_b64_tr_b16 v[190:191], %[wbe] offset:34560\n"
"v_mfma_f32_16x16x32_bf16 v[76:79], v[156:159], v[100:103], v[76:79]\n"
"s_waitcnt vmcnt(5)\n"
"s_bitcmp1_b32 s91, 15\n"
"s_cbranch_scc1 LJ1_15_%=\n"
"s_waitcnt vmcnt(2)\n"
"LJ1_15_%=:\n"
"s_waitcnt lgkmcnt(0)\n"
"s_barrier\n"
"v_mfma_f32_16x16x32_bf16 v[16:19], v[160:163], v[108:111], v[16:19]\n"
"ds_read_b128 v[96:99], v194 offset:0\n"
"ds_read_b128 v[100:103], v196 offset:0\n"
"v_mfma_f32_16x16x32_bf16 v[48:51], v[160:163], v[112:115], v[48:51]\n"
"ds_read_b128 v[104:107], %[xb] offset:57344\n"
"v_mfma_f32_16x16x32_bf16 v[20:23], v[164:167], v[108:111], v[20:23]\n"
"ds_read_b64_tr_b16 v[128:129], %[wbe] offset:40960\n"
"v_mfma_f32_16x16x32_bf16 v[52:55], v[164:167], v[112:115], v[52:55]\n"
"ds_read_b64_tr_b16 v[130:131], v192 offset:41984\n"
"v_mfma_f32_16x16x32_bf16 v[24:27], v[168:171], v[108:111], v[24:27]\n"
"ds_read_b64_tr_b16 v[132:133], v192 offset:40960\n"
"v_mfma_f32_16x16x32_bf16 v[56:59], v[168:171], v[112:115], v[56:59]\n"
"ds_read_b64_tr_b16 v[134:135], %[wbe] offset:41984\n"
"v_mfma_f32_16x16x32_bf16 v[80:83], v[168:171], v[116:119], v[80:83]\n"
"ds_read_b64_tr_b16 v[136:137], %[wbe] offset:41216\n"
"v_mfma_f32_16x16x32_bf16 v[28:31], v[172:175], v[108:111], v[28:31]\n"
"ds_read_b64_tr_b16 v[138:139], v192 offset:42240\n"
"v_mfma_f32_16x16x32_bf16 v[60:63], v[172:175], v[112:115], v[60:63]\n"
"ds_read_b64_tr_b16 v[140:141], v192 offset:41216\n"
"ds_read_b64_tr_b16 v[142:143], %[wbe] offset:42240\n"
"v_mfma_f32_16x16x32_bf16 v[84:87], v[172:175], v[116:119], v[84:87]\n"
"ds_read_b64_tr_b16 v[144:145], %[wbe] offset:41472\n"
"v_mfma_f32_16x16x32_bf16 v[32:35], v[176:179], v[108:111], v[32:35]\n"
"ds_read_b64_tr_b16 v[146:147], v192 offset:42496\n"
"v_mfma_f32_16x16x32_bf16 v[64:67], v[176:179], v[112:115], v[64:67]\n"
"ds_read_b64_tr_b16 v[148:149], v192 offset:41472\n"
"v_mfma_f32_16x16x32_bf16 v[36:39], v[180:183], v[108:111], v[36:39]\n"
"ds_read_b64_tr_b16 v[150:151], %[wbe] offset:42496\n"
"v_mfma_f32_16x16x32_bf16 v[68:71], v[180:183], v[112:115], v[68:71]\n"
"ds_read_b64_tr_b16 v[152:153], %[wbe] offset:41728\n"
"v_mfma_f32_16x16x32_bf16 v[40:43], v[184:187], v[108:111], v[40:43]\n"
"ds_read_b64_tr_b16 v[154:155], v192 offset:42752\n"
"v_mfma_f32_16x16x32_bf16 v[72:75], v[184:187], v[112:115], v[72:75]\n"
"ds_read_b64_tr_b16 v[156:157], v192 offset:41728\n"
"v_mfma_f32_16x16x32_bf16 v[44:47], v[188:191], v[108:111], v[44:47]\n"
"ds_read_b64_tr_b16 v[158:159], %[wbe] offset:42752\n"
"v_mfma_f32_16x16x32_bf16 v[76:79], v[188:191], v[112:115], v[76:79]\n"
"s_waitcnt vmcnt(0) lgkmcnt(0)\n"
"s_barrier\n"
"v_mfma_f32_16x16x32_bf16 v[16:19], v[128:131], v[96:99], v[16:19]\n"
"ds_read_b128 v[108:111], v194 offset:512\n"
"ds_read_b128 v[112:115], v196 offset:512\n"
"v_mfma_f32_16x16x32_bf16 v[48:51], v[128:131], v[100:103], v[48:51]\n"
"ds_read_b128 v[116:119], %[xb] offset:57856\n"
"s_mov_b32 m0, s54\n"
"v_mfma_f32_16x16x32_bf16 v[20:23], v[132:135], v[96:99], v[20:23]\n"
"buffer_load_dwordx4 %[g0], %[srdv], s61 offen nt lds\n"
"v_mfma_f32_16x16x32_bf16 v[52:55], v[132:135], v[100:103], v[52:55]\n"
"ds_read_b64_tr_b16 v[160:161], %[wbe] offset:49152\n"
"ds_read_b64_tr_b16 v[162:163], v192 offset:50176\n"
"v_mfma_f32_16x16x32_bf16 v[24:27], v[136:139], v[96:99], v[24:27]\n"
"ds_read_b64_tr_b16 v[164:165], v192 offset:49152\n"
"v_mfma_f32_16x16x32_bf16 v[56:59], v[136:139], v[100:103], v[56:59]\n"
"ds_read_b64_tr_b16 v[166:167], %[wbe] offset:50176\n"
"v_mfma_f32_16x16x32_bf16 v[80:83], v[136:139], v[104:107], v[80:83]\n"
"buffer_load_dwordx4 %[g1], %[srdv], s61 offen offset:1024 nt lds\n"
"v_mfma_f32_16x16x32_bf16 v[28:31], v[140:143], v[96:99], v[28:31]\n"
"ds_read_b64_tr_b16 v[168:169], %[wbe] offset:49408\n"
"ds_read_b64_tr_b16 v[170:171], v192 offset:50432\n"
"v_mfma_f32_16x16x32_bf16 v[60:63], v[140:143], v[100:103], v[60:63]\n"
"ds_read_b64_tr_b16 v[172:173], v192 offset:49408\n"
"v_mfma_f32_16x16x32_bf16 v[84:87], v[140:143], v[104:107], v[84:87]\n"
"ds_read_b64_tr_b16 v[174:175], %[wbe] offset:50432\n"
"v_mfma_f32_16x16x32_bf16 v[32:35], v[144:147], v[96:99], v[32:35]\n"
"buffer_load_dwordx4 %[g0], %[srdk], s61 offen offset:2048 nt lds\n"
"s_add_u32 s61, s61, %[kvs]\n"
"v_mfma_f32_16x16x32_bf16 v[64:67], v[144:147], v[100:103], v[64:67]\n"
"ds_read_b64_tr_b16 v[176:177], %[wbe] offset:49664\n"
"ds_read_b64_tr_b16 v[178:179], v192 offset:50688\n"
"v_mfma_f32_16x16x32_bf16 v[36:39], v[148:151], v[96:99], v[36:39]\n"
"ds_read_b64_tr_b16 v[180:181], v192 offset:49664\n"
"v_mfma_f32_16x16x32_bf16 v[68:71], v[148:151], v[100:103], v[68:71]\n"
"ds_read_b64_tr_b16 v[182:183], %[wbe] offset:50688\n"
"v_mfma_f32_16x16x32_bf16 v[40:43], v[152:155], v[96:99], v[40:43]\n"
"ds_read_b64_tr_b16 v[184:185], %[wbe] offset:49920\n"
"ds_read_b64_tr_b16 v[186:187], v192 offset:50944\n"
"v_mfma_f32_16x16x32_bf16 v[72:75], v[152:155], v[100:103], v[72:75]\n"
"ds_read_b64_tr_b16 v[188:189], v192 offset:49920\n"
"v_mfma_f32_16x16x32_bf16 v[44:47], v[156:159], v[96:99], v[44:47]\n"
"ds_read_b64_tr_b16 v[190:191], %[wbe] offset:50944\n"
"v_mfma_f32_16x16x32_bf16 v[76:79], v[156:159], v[100:103], v[76:79]\n"
"s_waitcnt lgkmcnt(0)\n"
"s_barrier\n"
"v_mfma_f32_16x16x32_bf16 v[16:19], v[160:163], v[108:111], v[16:19]\n"
"ds_read_b128 v[128:131], %[kb] offset:2048\n"
"v_mfma_f32_16x16x32_bf16 v[48:51], v[160:163], v[112:115], v[48:51]\n"
"ds_read_b128 v[132:135], %[kb] offset:2304\n"
"s_mov_b32 m0, s55\n"
"v_mfma_f32_16x16x32_bf16 v[20:23], v[164:167], v[108:111], v[20:23]\n"
"buffer_load_dwordx4 %[g0], %[srdv], s61 offen nt lds\n"
"v_mfma_f32_16x16x32_bf16 v[52:55], v[164:167], v[112:115], v[52:55]\n"
"v_mfma_f32_16x16x32_bf16 v[24:27], v[168:171], v[108:111], v[24:27]\n"
"ds_read_b128 v[136:139], %[kb] offset:2560\n"
"v_mfma_f32_16x16x32_bf16 v[56:59], v[168:171], v[112:115], v[56:59]\n"
"v_mfma_f32_16x16x32_bf16 v[80:83], v[168:171], v[116:119], v[80:83]\n"
"buffer_load_dwordx4 %[g1], %[srdv], s61 offen offset:1024 nt lds\n"
"v_mfma_f32_16x16x32_bf16 v[28:31], v[172:175], v[108:111], v[28:31]\n"
"ds_read_b128 v[140:143], %[kb] offset:2816\n"
"v_mfma_f32_16x16x32_bf16 v[60:63], v[172:175], v[112:115], v[60:63]\n"
"v_mfma_f32_16x16x32_bf16 v[84:87], v[172:175], v[116:119], v[84:87]\n"
"ds_read_b128 v[144:147], %[kb] offset:10240\n"
"v_mfma_f32_16x16x32_bf16 v[32:35], v[176:179], v[108:111], v[32:35]\n"
"buffer_load_dwordx4 %[g0], %[srdk], s61 offen offset:2048 nt lds\n"
"s_add_u32 s61, s61, %[kvs]\n"
"v_mfma_f32_16x16x32_bf16 v[64:67], v[176:179], v[112:115], v[64:67]\n"
"ds_read_b128 v[148:151], %[kb] offset:10496\n"
"v_mfma_f32_16x16x32_bf16 v[36:39], v[180:183], v[108:111], v[36:39]\n"
"v_mfma_f32_16x16x32_bf16 v[68:71], v[180:183], v[112:115], v[68:71]\n"
"ds_read_b128 v[152:155], %[kb] offset:10752\n"
"v_mfma_f32_16x16x32_bf16 v[40:43], v[184:187], v[108:111], v[40:43]\n"
"v_mfma_f32_16x16x32_bf16 v[72:75], v[184:187], v[112:115], v[72:75]\n"
"ds_read_b128 v[156:159], %[kb] offset:11008\n"
"v_mfma_f32_16x16x32_bf16 v[44:47], v[188:191], v[108:111], v[44:47]\n"
"v_mfma_f32_16x16x32_bf16 v[76:79], v[188:191], v[112:115], v[76:79]\n"
"ds_read_b64_tr_b16 v[160:161], %[vbe] offset:0\n"
"ds_read_b64_tr_b16 v[162:163], %[vbe] offset:8192\n"
"ds_read_b64_tr_b16 v[164:165], v243 offset:0\n"
"ds_read_b64_tr_b16 v[166:167], v243 offset:8192\n"
"ds_read_b64_tr_b16 v[168:169], %[vbe] offset:256\n"
"ds_read_b64_tr_b16 v[170:171], %[vbe] offset:8448\n"
"ds_read_b64_tr_b16 v[172:173], v243 offset:256\n"
"ds_read_b64_tr_b16 v[174:175], v243 offset:8448\n"
"ds_read_b64_tr_b16 v[176:177], %[vbe] offset:512\n"
"ds_read_b64_tr_b16 v[178:179], %[vbe] offset:8704\n"
"ds_read_b64_tr_b16 v[180:181], v243 offset:512\n"
"ds_read_b64_tr_b16 v[182:183], v243 offset:8704\n"
"ds_read_b64_tr_b16 v[184:185], %[vbe] offset:768\n"
"ds_read_b64_tr_b16 v[186:187], %[vbe] offset:8960\n"
"ds_read_b64_tr_b16 v[188:189], v243 offset:768\n"
"ds_read_b64_tr_b16 v[190:191], v243 offset:8960\n"
"v_pk_mul_f32 v[80:81], v[80:81], s[66:67]\n"
"v_pk_mul_f32 v[82:83], v[82:83], s[66:67]\n"
"v_pk_mul_f32 v[84:85], v[84:85], s[66:67]\n"
"v_pk_mul_f32 v[86:87], v[86:87], s[66:67]\n"
"v_cvt_pk_bf16_f32 v232, v80, v81\n"
"v_cvt_pk_bf16_f32 v233, v82, v83\n"
"v_cvt_pk_bf16_f32 v234, v84, v85\n"
"v_cvt_pk_bf16_f32 v235, v86, v87\n"
"s_nop 0\n"
"v_permlane32_swap_b32 v232, v234\n"
"v_permlane32_swap_b32 v233, v235\n"
"s_nop 0\n"
"v_permlane16_swap_b32 v232, v234\n"
"v_permlane16_swap_b32 v233, v235\n"
"ds_write_b128 v247, v[232:235] offset:1024\n"
"v_pk_mul_f32 v[16:17], v[16:17], s[66:67]\n"
"v_pk_mul_f32 v[18:19], v[18:19], s[66:67]\n"
"v_pk_mul_f32 v[20:21], v[20:21], s[66:67]\n"
"v_pk_mul_f32 v[22:23], v[22:23], s[66:67]\n"
"v_cvt_pk_bf16_f32 v96, v16, v17\n"
"v_cvt_pk_bf16_f32 v97, v18, v19\n"
"v_cvt_pk_bf16_f32 v98, v20, v21\n"
"v_cvt_pk_bf16_f32 v99, v22, v23\n"
"v_pk_mul_f32 v[24:25], v[24:25], s[66:67]\n"
"v_pk_mul_f32 v[26:27], v[26:27], s[66:67]\n"
"v_pk_mul_f32 v[28:29], v[28:29], s[66:67]\n"
"v_pk_mul_f32 v[30:31], v[30:31], s[66:67]\n"
"v_cvt_pk_bf16_f32 v100, v24, v25\n"
"v_cvt_pk_bf16_f32 v101, v26, v27\n"
"v_cvt_pk_bf16_f32 v102, v28, v29\n"
"v_cvt_pk_bf16_f32 v103, v30, v31\n"
"v_pk_mul_f32 v[32:33], v[32:33], s[66:67]\n"
"v_pk_mul_f32 v[34:35], v[34:35], s[66:67]\n"
"v_pk_mul_f32 v[36:37], v[36:37], s[66:67]\n"
"v_pk_mul_f32 v[38:39], v[38:39], s[66:67]\n"
"v_cvt_pk_bf16_f32 v104, v32, v33\n"
"v_cvt_pk_bf16_f32 v105, v34, v35\n"
"v_cvt_pk_bf16_f32 v106, v36, v37\n"
"v_cvt_pk_bf16_f32 v107, v38, v39\n"
"v_pk_mul_f32 v[40:41], v[40:41], s[66:67]\n"
"v_pk_mul_f32 v[42:43], v[42:43], s[66:67]\n"
"v_pk_mul_f32 v[44:45], v[44:45], s[66:67]\n"
"v_pk_mul_f32 v[46:47], v[46:47], s[66:67]\n"
"v_cvt_pk_bf16_f32 v108, v40, v41\n"
"v_cvt_pk_bf16_f32 v109, v42, v43\n"
"v_cvt_pk_bf16_f32 v110, v44, v45\n"
"v_cvt_pk_bf16_f32 v111, v46, v47\n"
"v_pk_mul_f32 v[48:49], v[48:49], s[66:67]\n"
"v_pk_mul_f32 v[50:51], v[50:51], s[66:67]\n"
"v_pk_mul_f32 v[52:53], v[52:53], s[66:67]\n"
"v_pk_mul_f32 v[54:55], v[54:55], s[66:67]\n"
"v_cvt_pk_bf16_f32 v112, v48, v49\n"
"v_cvt_pk_bf16_f32 v113, v50, v51\n"
"v_cvt_pk_bf16_f32 v114, v52, v53\n"
"v_cvt_pk_bf16_f32 v115, v54, v55\n"
"v_pk_mul_f32 v[56:57], v[56:57], s[66:67]\n"
"v_pk_mul_f32 v[58:59], v[58:59], s[66:67]\n"
"v_pk_mul_f32 v[60:61], v[60:61], s[66:67]\n"
"v_pk_mul_f32 v[62:63], v[62:63], s[66:67]\n"
"v_cvt_pk_bf16_f32 v116, v56, v57\n"
"v_cvt_pk_bf16_f32 v117, v58, v59\n"
"v_cvt_pk_bf16_f32 v118, v60, v61\n"
"v_cvt_pk_bf16_f32 v119, v62, v63\n"
"v_pk_mul_f32 v[64:65], v[64:65], s[66:67]\n"
"v_pk_mul_f32 v[66:67], v[66:67], s[66:67]\n"
"v_pk_mul_f32 v[68:69], v[68:69], s[66:67]\n"
"v_pk_mul_f32 v[70:71], v[70:71], s[66:67]\n"
"v_cvt_pk_bf16_f32 v120, v64, v65\n"
"v_cvt_pk_bf16_f32 v121, v66, v67\n"
"v_cvt_pk_bf16_f32 v122, v68, v69\n"
"v_cvt_pk_bf16_f32 v123, v70, v71\n"
"v_pk_mul_f32 v[72:73], v[72:73], s[66:67]\n"
"v_pk_mul_f32 v[74:75], v[74:75], s[66:67]\n"
"v_pk_mul_f32 v[76:77], v[76:77], s[66:67]\n"
"v_pk_mul_f32 v[78:79], v[78:79], s[66:67]\n"
"v_cvt_pk_bf16_f32 v124, v72, v73\n"
"v_cvt_pk_bf16_f32 v125, v74, v75\n"
"v_cvt_pk_bf16_f32 v126, v76, v77\n"
"v_cvt_pk_bf16_f32 v127, v78, v79\n"
"v_permlane32_swap_b32 v96, v98\n"
"v_permlane32_swap_b32 v97, v99\n"
"v_permlane32_swap_b32 v100, v102\n"
"v_permlane32_swap_b32 v101, v103\n"
"v_permlane32_swap_b32 v104, v106\n"
"v_permlane32_swap_b32 v105, v107\n"
"v_permlane32_swap_b32 v108, v110\n"
"v_permlane32_swap_b32 v109, v111\n"
"v_permlane32_swap_b32 v112, v114\n"
"v_permlane32_swap_b32 v113, v115\n"
"v_permlane32_swap_b32 v116, v118\n"
"v_permlane32_swap_b32 v117, v119\n"
"v_permlane32_swap_b32 v120, v122\n"
"v_permlane32_swap_b32 v121, v123\n"
"v_permlane32_swap_b32 v124, v126\n"
"v_permlane32_swap_b32 v125, v127\n"
"v_permlane16_swap_b32 v96, v98\n"
"v_permlane16_swap_b32 v97, v99\n"
"v_permlane16_swap_b32 v100, v102\n"
"v_permlane16_swap_b32 v101, v103\n"
"v_permlane16_swap_b32 v104, v106\n"
"v_permlane16_swap_b32 v105, v107\n"
"v_permlane16_swap_b32 v108, v110\n"
"v_permlane16_swap_b32 v109, v111\n"
"v_permlane16_swap_b32 v112, v114\n"
"v_permlane16_swap_b32 v113, v115\n"
"v_permlane16_swap_b32 v116, v118\n"
"v_permlane16_swap_b32 v117, v119\n"
"v_permlane16_swap_b32 v120, v122\n"
"v_permlane16_swap_b32 v121, v123\n"
"v_permlane16_swap_b32 v124, v126\n"
"v_permlane16_swap_b32 v125, v127\n"
"s_waitcnt lgkmcnt(0)\n"
"s_barrier\n"
"ds_read_b128 v[80:83], v247 offset:0\n"
"ds_read_b128 v[84:87], v247 offset:1024\n"
"ds_read_b128 v[88:91], v247 offset:2048\n"
"ds_read_b128 v[92:95], v247 offset:3072\n"
"v_mfma_f32_16x16x32_bf16 v[192:195], v[128:131], v[96:99], 0\n"
"v_mov_b32 v16, 0\n"
"v_mov_b32 v17, 0\n"
"v_mov_b32 v18, 0\n"
"v_mfma_f32_16x16x32_bf16 v[192:195], v[132:135], v[100:103], v[192:195]\n"
"v_mov_b32 v19, 0\n"
"v_mov_b32 v20, 0\n"
"v_mov_b32 v21, 0\n"
"v_mfma_f32_16x16x32_bf16 v[192:195], v[136:139], v[104:107], v[192:195]\n"
"v_mov_b32 v22, 0\n"
"v_mov_b32 v23, 0\n"
"v_mov_b32 v24, 0\n"
"v_mfma_f32_16x16x32_bf16 v[192:195], v[140:143], v[108:111], v[192:195]\n"
"v_mov_b32 v25, 0\n"
"v_mov_b32 v26, 0\n"
"v_mov_b32 v27, 0\n"
"v_mfma_f32_16x16x32_bf16 v[196:199], v[128:131], v[112:115], 0\n"
"v_mov_b32 v28, 0\n"
"v_mov_b32 v29, 0\n"
"v_mov_b32 v30, 0\n"
"v_mfma_f32_16x16x32_bf16 v[196:199], v[132:135], v[116:119], v[196:199]\n"
"v_mov_b32 v31, 0\n"
"v_mov_b32 v32, 0\n"
"v_mov_b32 v33, 0\n"
"v_mfma_f32_16x16x32_bf16 v[196:199], v[136:139], v[120:123], v[196:199]\n"
"v_mov_b32 v34, 0\n"
"v_mov_b32 v35, 0\n"
"v_mov_b32 v36, 0\n"
"v_mfma_f32_16x16x32_bf16 v[196:199], v[140:143], v[124:127], v[196:199]\n"
"v_mov_b32 v37, 0\n"
"v_mov_b32 v38, 0\n"
"v_mov_b32 v39, 0\n"
"v_mfma_f32_16x16x32_bf16 v[200:203], v[144:147], v[96:99], 0\n"
"v_mov_b32 v40, 0\n"
"v_mov_b32 v41, 0\n"
"v_mov_b32 v42, 0\n"
"v_mfma_f32_16x16x32_bf16 v[200:203], v[148:151], v[100:103], v[200:203]\n"
"v_mov_b32 v43, 0\n"
"v_mov_b32 v44, 0\n"
"v_mov_b32 v45, 0\n"
"v_mfma_f32_16x16x32_bf16 v[200:203], v[152:155], v[104:107], v[200:203]\n"
"v_mov_b32 v46, 0\n"
"v_mov_b32 v47, 0\n"
"v_mov_b32 v48, 0\n"
"v_mfma_f32_16x16x32_bf16 v[200:203], v[156:159], v[108:111], v[200:203]\n"
"v_mov_b32 v49, 0\n"
"v_mov_b32 v50, 0\n"
"v_mov_b32 v51, 0\n"
"v_mfma_f32_16x16x32_bf16 v[204:207], v[144:147], v[112:115], 0\n"
"v_mov_b32 v52, 0\n"
"v_mov_b32 v53, 0\n"
"v_mov_b32 v54, 0\n"
"v_mfma_f32_16x16x32_bf16 v[204:207], v[148:151], v[116:119], v[204:207]\n"
"v_mov_b32 v55, 0\n"
"v_mov_b32 v56, 0\n"
"v_mov_b32 v57, 0\n"
"v_mfma_f32_16x16x32_bf16 v[204:207], v[152:155], v[120:123], v[204:207]\n"
"v_mov_b32 v58, 0\n"
"v_mov_b32 v59, 0\n"
"v_mov_b32 v60, 0\n"
"v_mfma_f32_16x16x32_bf16 v[204:207], v[156:159], v[124:127], v[204:207]\n"
"v_mov_b32 v61, 0\n"
"v_mov_b32 v62, 0\n"
"v_mov_b32 v63, 0\n"
"s_waitcnt lgkmcnt(3)\n"
"v_mfma_f32_16x16x32_bf16 v[236:239], v[128:131], v[80:83], 0\n"
"v_mov_b32 v64, 0\n"
"v_mov_b32 v65, 0\n"
"v_mov_b32 v66, 0\n"
"s_waitcnt lgkmcnt(2)\n"
"v_mfma_f32_16x16x32_bf16 v[236:239], v[132:135], v[84:87], v[236:239]\n"
"v_mov_b32 v67, 0\n"
"v_mov_b32 v68, 0\n"
"v_mov_b32 v69, 0\n"
"s_waitcnt lgkmcnt(1)\n"
"v_mfma_f32_16x16x32_bf16 v[236:239], v[136:139], v[88:91], v[236:239]\n"
"v_mov_b32 v70, 0\n"
"v_mov_b32 v71, 0\n"
"v_mov_b32 v72, 0\n"
"s_waitcnt lgkmcnt(0)\n"
"v_mfma_f32_16x16x32_bf16 v[236:239], v[140:143], v[92:95], v[236:239]\n"
"v_mov_b32 v73, 0\n"
"v_mov_b32 v74, 0\n"
"v_mov_b32 v75, 0\n"
"v_mfma_f32_16x16x32_bf16 v[208:211], v[144:147], v[80:83], 0\n"
"v_mov_b32 v76, 0\n"
"v_mov_b32 v77, 0\n"
"v_mov_b32 v78, 0\n"
"v_mfma_f32_16x16x32_bf16 v[208:211], v[148:151], v[84:87], v[208:211]\n"
"v_mov_b32 v79, 0\n"
"v_mov_b32 v240, 0\n"
"v_mov_b32 v241, 0\n"
"v_mfma_f32_16x16x32_bf16 v[208:211], v[152:155], v[88:91], v[208:211]\n"
"v_mov_b32 v242, 0\n"
"v_mfma_f32_16x16x32_bf16 v[208:211], v[156:159], v[92:95], v[208:211]\n"
"v_max3_f32 v249, v192, v193, v194\n"
"v_max3_f32 v249, v249, v195, v200\n"
"v_max3_f32 v249, v249, v201, v202\n"
"v_max_f32 v249, v249, v203\n"
"v_max3_f32 v251, v196, v197, v198\n"
"v_max3_f32 v251, v251, v199, v204\n"
"v_max3_f32 v251, v251, v205, v206\n"
"v_max_f32_e64 v251, v251, v207\n"
"v_max3_f32 v253, v208, v209, v210\n"
"v_max3_f32 v253, v253, v211, v236\n"
"v_max3_f32 v253, v253, v237, v238\n"
"v_max_f32 v253, v253, v239\n"
"v_mov_b32 v250, v249\n"
"v_mov_b32 v252, v251\n"
"v_mov_b32 v254, v253\n"
"v_permlane32_swap_b32 v249, v250\n"
"v_permlane32_swap_b32 v251, v252\n"
"v_permlane32_swap_b32 v253, v254\n"
"v_max_f32 v249, v249, v250\n"
"v_max_f32 v251, v251, v252\n"
"v_max_f32 v253, v253, v254\n"
"v_mov_b32 v250, v249\n"
"v_mov_b32 v252, v251\n"
"v_mov_b32 v254, v253\n"
"v_permlane16_swap_b32 v249, v250\n"
"v_permlane16_swap_b32 v251, v252\n"
"v_permlane16_swap_b32 v253, v254\n"
"v_max_f32 v249, v249, v250\n"
"v_max_f32 v251, v251, v252\n"
"v_max_f32 v253, v253, v254\n"
"v_sub_f32 v212, 0, v249\n"
"v_sub_f32 v213, 0, v249\n"
"v_sub_f32 v214, 0, v249\n"
"v_sub_f32 v215, 0, v249\n"
"v_sub_f32 v192, v192, v249\n"
"v_sub_f32 v193, v193, v249\n"
"v_sub_f32 v194, v194, v249\n"
"v_sub_f32 v195, v195, v249\n"
"v_sub_f32 v200, v200, v249\n"
"v_sub_f32 v201, v201, v249\n"
"v_sub_f32 v202, v202, v249\n"
"v_sub_f32 v203, v203, v249\n"
"v_sub_f32 v216, 0, v251\n"
"v_sub_f32 v217, 0, v251\n"
"v_sub_f32 v218, 0, v251\n"
"v_sub_f32 v219, 0, v251\n"
"v_sub_f32 v196, v196, v251\n"
"v_sub_f32 v197, v197, v251\n"
"v_sub_f32 v198, v198, v251\n"
"v_sub_f32 v199, v199, v251\n"
"v_sub_f32 v204, v204, v251\n"
"v_sub_f32 v205, v205, v251\n"
"v_sub_f32 v206, v206, v251\n"
"v_sub_f32 v207, v207, v251\n"
"v_sub_f32 v220, 0, v253\n"
"v_sub_f32 v221, 0, v253\n"
"v_sub_f32 v222, 0, v253\n"
"v_sub_f32 v223, 0, v253\n"
"v_sub_f32 v208, v208, v253\n"
"v_sub_f32 v209, v209, v253\n"
"v_sub_f32 v210, v210, v253\n"
"v_sub_f32_e64 v211, v211, v253\n"
"s_movk_i32 s68, 1152\n"
"s_lshr_b32 s69, %[nb256], 3\n"
"s_add_u32 s69, s69, 5632\n"
"v_and_b32 v254, 255, %[g0]\n"
"v_add_u32_e64 v254, %[kq64], v254\n"
"v_and_b32 v254, 255, v254\n"
"v_lshrrev_b32_e64 v255, 8, %[g0]\n"
"v_mul_u32_u24 v255, s68, v255\n"
"v_add_u32 v255, %[nb256], v255\n"
"v_add_u32 v249, v254, v255\n"
"v_xor_b32 v254, 32, v254\n"
"v_add_u32 v250, v254, v255\n"
"v_add_u32 v250, 3584, v250\n"
"v_lshrrev_b32_e64 v254, 5, %[l16]\n"
"v_mul_u32_u24 v254, s68, v254\n"
"v_and_b32 v255, 16, %[l16]\n"
"v_add_u32 v254, v254, v255\n"
"v_add_u32 v252, s69, v254\n"
"s_cmp_eq_u32 %[hb], 0\n"
"s_cbranch_scc1 LH1_%=\n"
"s_barrier\n"
"LH1_%=:\n"
"v_exp_f32 v208, v208\n"
"v_exp_f32 v209, v209\n"
"v_exp_f32 v210, v210\n"
"v_exp_f32 v211, v211\n"
"v_cvt_pk_bf16_f32 v232, v208, v209\n"
"v_cvt_pk_bf16_f32 v233, v210, v211\n"
"ds_write_b64 v247, v[232:233] offset:4104\n"
"v_exp_f32 v192, v192\n"
"v_exp_f32 v193, v193\n"
"v_exp_f32 v194, v194\n"
"v_exp_f32 v195, v195\n"
"v_cvt_pk_bf16_f32 v224, v192, v193\n"
"v_cvt_pk_bf16_f32 v225, v194, v195\n"
"v_exp_f32 v196, v196\n"
"v_exp_f32 v197, v197\n"
"v_exp_f32 v198, v198\n"
"v_exp_f32 v199, v199\n"
"v_cvt_pk_bf16_f32 v228, v196, v197\n"
"v_cvt_pk_bf16_f32 v229, v198, v199\n"
"v_exp_f32 v200, v200\n"
"v_exp_f32 v201, v201\n"
"v_exp_f32 v202, v202\n"
"v_exp_f32 v203, v203\n"
"v_cvt_pk_bf16_f32 v226, v200, v201\n"
"v_cvt_pk_bf16_f32 v227, v202, v203\n"
"v_exp_f32 v204, v204\n"
"v_exp_f32 v205, v205\n"
"v_exp_f32 v206, v206\n"
"v_exp_f32 v207, v207\n"
"v_cvt_pk_bf16_f32 v230, v204, v205\n"
"v_cvt_pk_bf16_f32 v231, v206, v207\n"
"s_waitcnt lgkmcnt(0)\n"
"s_cmp_lg_u32 %[isa], 0\n"
"s_cbranch_scc1 LA1_%=\n"
"s_mov_b32 m0, s52\n"
"s_nop 0\n"
"buffer_load_dwordx4 %[g0], %[srdv], s61 offen nt lds\n"
"buffer_load_dwordx4 %[g1], %[srdv], s61 offen offset:1024 nt lds\n"
"buffer_load_dwordx4 %[g0], %[srdk], s61 offen offset:2048 nt lds\n"
"s_add_u32 s61, s61, 0x2000\n"
"s_waitcnt vmcnt(9)\n"
".p2align 6\n"
"LB1_%=:\n"
"KB1 16384,s53\n"
"KB1 32768,s54\n"
"KB1 49152,s55\n"
"KB1 0,s52\n"
"s_branch LB1_%=\n"
".p2align 3\n"
"LQ1_%=:\n"
"s_barrier\n"
"s_setprio 1\n"
"v_mfma_f32_16x16x32_bf16 v[16:19], v[160:163], v[224:227], v[16:19]\n"
"ds_read_b128 v[128:131], %[kb] offset:2048\n"
"v_add_f32 v240, v240, v192\n"
"v_add_f32 v241, v241, v196\n"
"v_mfma_f32_16x16x32_bf16 v[48:51], v[160:163], v[228:231], v[48:51]\n"
"ds_read_b128 v[132:135], %[kb] offset:2304\n"
"v_add_f32 v242, v242, v208\n"
"v_add_f32 v240, v240, v193\n"
"v_mfma_f32_16x16x32_bf16 v[20:23], v[164:167], v[224:227], v[20:23]\n"
"ds_read_b128 v[136:139], %[kb] offset:2560\n"
"v_add_f32 v241, v241, v197\n"
"v_add_f32 v242, v242, v209\n"
"v_mfma_f32_16x16x32_bf16 v[52:55], v[164:167], v[228:231], v[52:55]\n"
"ds_read_b128 v[140:143], %[kb] offset:2816\n"
"v_add_f32 v240, v240, v194\n"
"v_add_f32 v241, v241, v198\n"
"v_mfma_f32_16x16x32_bf16 v[24:27], v[168:171], v[224:227], v[24:27]\n"
"ds_read_b128 v[144:147], %[kb] offset:10240\n"
"v_add_f32 v242, v242, v210\n"
"v_add_f32 v240, v240, v195\n"
"v_mfma_f32_16x16x32_bf16 v[56:59], v[168:171], v[228:231], v[56:59]\n"
"ds_read_b128 v[148:151], %[kb] offset:10496\n"
"v_add_f32 v241, v241, v199\n"
"v_add_f32 v242, v242, v211\n"
"v_mfma_f32_16x16x32_bf16 v[28:31], v[172:175], v[224:227], v[28:31]\n"
"ds_read_b128 v[152:155], %[kb] offset:10752\n"
"v_add_f32 v240, v240, v200\n"
"v_add_f32 v241, v241, v204\n"
"v_mfma_f32_16x16x32_bf16 v[60:63], v[172:175], v[228:231], v[60:63]\n"
"ds_read_b128 v[156:159], %[kb] offset:11008\n"
"v_add_f32 v240, v240, v201\n"
"v_add_f32 v241, v241, v205\n"
"v_mfma_f32_16x16x32_bf16 v[32:35], v[176:179], v[224:227], v[32:35]\n"
"ds_read_b64_tr_b16 v[160:161], %[vbe] offset:0\n"
"v_add_f32 v240, v240, v202\n"
"v_add_f32 v241, v241, v206\n"
"v_mfma_f32_16x16x32_bf16 v[64:67], v[176:179], v[228:231], v[64:67]\n"
"ds_read_b64_tr_b16 v[162:163], %[vbe] offset:8192\n"
"v_add_f32 v240, v240, v203\n"
"v_add_f32 v241, v241, v207\n"
"v_mfma_f32_16x16x32_bf16 v[36:39], v[180:183], v[224:227], v[36:39]\n"
"ds_read_b64_tr_b16 v[164:165], v243 offset:0\n"
"v_mfma_f32_16x16x32_bf16 v[68:71], v[180:183], v[228:231], v[68:71]\n"
"ds_read_b64_tr_b16 v[166:167], v243 offset:8192\n"
"v_mfma_f32_16x16x32_bf16 v[40:43], v[184:187], v[224:227], v[40:43]\n"
"ds_read_b64_tr_b16 v[168:169], %[vbe] offset:256\n"
"v_mfma_f32_16x16x32_bf16 v[72:75], v[184:187], v[228:231], v[72:75]\n"
"ds_read_b64_tr_b16 v[170:171], %[vbe] offset:8448\n"
"v_mfma_f32_16x16x32_bf16 v[44:47], v[188:191], v[224:227], v[44:47]\n"
"ds_read_b64_tr_b16 v[172:173], v243 offset:256\n"
"v_mfma_f32_16x16x32_bf16 v[76:79], v[188:191], v[228:231], v[76:79]\n"
"ds_read_b64_tr_b16 v[174:175], v243 offset:8448\n"
"s_waitcnt lgkmcnt(8)\n"
"v_mfma_f32_16x16x32_bf16 v[208:211], v[144:147], v[80:83], v[220:223]\n"
"ds_read_b64_tr_b16 v[176:177], %[vbe] offset:512\n"
"v_mfma_f32_16x16x32_bf16 v[208:211], v[148:151], v[84:87], v[208:211]\n"
"ds_read_b64_tr_b16 v[178:179], %[vbe] offset:8704\n"
"v_mfma_f32_16x16x32_bf16 v[208:211], v[152:155], v[88:91], v[208:211]\n"
"ds_read_b64_tr_b16 v[180:181], v243 offset:512\n"
"v_mfma_f32_16x16x32_bf16 v[208:211], v[156:159], v[92:95], v[208:211]\n"
"ds_read_b64_tr_b16 v[182:183], v243 offset:8704\n"
"v_mfma_f32_16x16x32_bf16 v[192:195], v[128:131], v[96:99], v[212:215]\n"
"ds_read_b64_tr_b16 v[184:185], %[vbe] offset:768\n"
"v_mfma_f32_16x16x32_bf16 v[192:195], v[132:135], v[100:103], v[192:195]\n"
"ds_read_b64_tr_b16 v[186:187], %[vbe] offset:8960\n"
"v_mfma_f32_16x16x32_bf16 v[192:195], v[136:139], v[104:107], v[192:195]\n"
"ds_read_b64_tr_b16 v[188:189], v243 offset:768\n"
"v_mfma_f32_16x16x32_bf16 v[192:195], v[140:143], v[108:111], v[192:195]\n"
"ds_read_b64_tr_b16 v[190:191], v243 offset:8960\n"
"v_mfma_f32_16x16x32_bf16 v[196:199], v[128:131], v[112:115], v[216:219]\n"
"v_mfma_f32_16x16x32_bf16 v[196:199], v[132:135], v[116:119], v[196:199]\n"
"v_mfma_f32_16x16x32_bf16 v[196:199], v[136:139], v[120:123], v[196:199]\n"
"v_mfma_f32_16x16x32_bf16 v[196:199], v[140:143], v[124:127], v[196:199]\n"
"v_mfma_f32_16x16x32_bf16 v[200:203], v[144:147], v[96:99], v[212:215]\n"
"v_mfma_f32_16x16x32_bf16 v[200:203], v[148:151], v[100:103], v[200:203]\n"
"v_mfma_f32_16x16x32_bf16 v[200:203], v[152:155], v[104:107], v[200:203]\n"
"v_mfma_f32_16x16x32_bf16 v[200:203], v[156:159], v[108:111], v[200:203]\n"
"v_mfma_f32_16x16x32_bf16 v[204:207], v[144:147], v[112:115], v[216:219]\n"
"v_mfma_f32_16x16x32_bf16 v[204:207], v[148:151], v[116:119], v[204:207]\n"
"v_mfma_f32_16x16x32_bf16 v[204:207], v[152:155], v[120:123], v[204:207]\n"
"v_mfma_f32_16x16x32_bf16 v[204:207], v[156:159], v[124:127], v[204:207]\n"
"s_waitcnt lgkmcnt(0)\n"
"s_barrier\n"
"s_setprio 0\n"
"s_getpc_b64 s[94:95]\n"
"LMb1_%=:\n"
"s_add_u32 s94, s94, (LZZ1_%=-16384-LMb1_%=)&0xffffffff\n"
"s_addc_u32 s95, s95, (LZZ1_%=-16384-LMb1_%=)>>32\n"
"v_lshlrev_b32 v236, 4, %[l16]\n"
"s_lshr_b32 s68, %[hb], 9\n"
"v_add_u32 v236, s68, v236\n"
"s_lshl_b32 exec_lo, 0x11111111, %[wrot]\n"
"s_mov_b32 exec_hi, exec_lo\n"
"global_load_dword v253, v236, s[94:95]\n"
"s_mov_b64 exec, -1\n"
"s_lshl_b32 s88, %[osoff], 2\n"
"s_mov_b32 m0, s52\n"
"s_nop 0\n"
"buffer_load_dwordx4 v249, s[76:79], s88 offen lds\n"
"buffer_load_dwordx4 v250, s[76:79], s88 offen offset:1024 lds\n"
"s_lshl_b32 s89, %[osoff], 2\n"
"s_add_u32 s89, s89, 34816\n"
"buffer_load_dwordx4 v249, s[76:79], s89 offen offset:2048 lds\n"
"s_add_u32 m0, s90, -1024\n"
"s_nop 0\n"
"buffer_load_dwordx4 v252, s[76:79], s89 offen offset:2048 lds\n"
"v_exp_f32 v208, v208\n"
"v_exp_f32 v209, v209\n"
"v_exp_f32 v210, v210\n"
"v_exp_f32_e64 v211, v211\n"
"v_cvt_pk_bf16_f32 v232, v208, v209\n"
"v_cvt_pk_bf16_f32 v233, v210, v211\n"
"ds_write_b64 v247, v[232:233] offset:4104\n"
"v_exp_f32 v192, v192\n"
"v_exp_f32 v193, v193\n"
"v_exp_f32 v194, v194\n"
"v_exp_f32 v195, v195\n"
"v_cvt_pk_bf16_f32 v224, v192, v193\n"
"v_cvt_pk_bf16_f32 v225, v194, v195\n"
"v_exp_f32 v196, v196\n"
"v_exp_f32 v197, v197\n"
"v_exp_f32 v198, v198\n"
"v_exp_f32 v199, v199\n"
"v_cvt_pk_bf16_f32 v228, v196, v197\n"
"v_cvt_pk_bf16_f32 v229, v198, v199\n"
"v_exp_f32 v200, v200\n"
"v_exp_f32 v201, v201\n"
"v_exp_f32 v202, v202\n"
"v_exp_f32 v203, v203\n"
"v_cvt_pk_bf16_f32 v226, v200, v201\n"
"v_cvt_pk_bf16_f32 v227, v202, v203\n"
"v_exp_f32 v204, v204\n"
"v_exp_f32 v205, v205\n"
"v_exp_f32 v206, v206\n"
"v_exp_f32 v207, v207\n"
"v_cvt_pk_bf16_f32 v230, v204, v205\n"
"v_cvt_pk_bf16_f32 v231, v206, v207\n"
"s_waitcnt vmcnt(10) lgkmcnt(0)\n"
"s_barrier\n"
"s_setprio 1\n"
"v_mfma_f32_16x16x32_bf16 v[16:19], v[160:163], v[224:227], v[16:19]\n"
"ds_read_b128 v[128:131], %[kb] offset:18432\n"
"v_add_f32 v240, v240, v192\n"
"v_add_f32_e64 v241, v241, v196\n"
"v_mfma_f32_16x16x32_bf16 v[48:51], v[160:163], v[228:231], v[48:51]\n"
"ds_read_b128 v[132:135], %[kb] offset:18688\n"
"v_add_f32 v242, v242, v208\n"
"v_add_f32 v240, v240, v193\n"
"v_mfma_f32_16x16x32_bf16 v[20:23], v[164:167], v[224:227], v[20:23]\n"
"ds_read_b128 v[136:139], %[kb] offset:18944\n"
"v_add_f32 v241, v241, v197\n"
"v_add_f32 v242, v242, v209\n"
"v_mfma_f32_16x16x32_bf16 v[52:55], v[164:167], v[228:231], v[52:55]\n"
"ds_read_b128 v[140:143], %[kb] offset:19200\n"
"v_add_f32 v240, v240, v194\n"
"v_add_f32 v241, v241, v198\n"
"v_mfma_f32_16x16x32_bf16 v[24:27], v[168:171], v[224:227], v[24:27]\n"
"ds_read_b128 v[144:147], %[kb] offset:26624\n"
"v_add_f32 v242, v242, v210\n"
"v_add_f32 v240, v240, v195\n"
"v_mfma_f32_16x16x32_bf16 v[56:59], v[168:171], v[228:231], v[56:59]\n"
"ds_read_b128 v[148:151], %[kb] offset:26880\n"
"v_add_f32 v241, v241, v199\n"
"v_add_f32 v242, v242, v211\n"
"v_mfma_f32_16x16x32_bf16 v[28:31], v[172:175], v[224:227], v[28:31]\n"
"ds_read_b128 v[152:155], %[kb] offset:27136\n"
"v_add_f32 v240, v240, v200\n"
"v_add_f32 v241, v241, v204\n"
"v_mfma_f32_16x16x32_bf16 v[60:63], v[172:175], v[228:231], v[60:63]\n"
"ds_read_b128 v[156:159], %[kb] offset:27392\n"
"v_add_f32 v240, v240, v201\n"
"v_add_f32 v241, v241, v205\n"
"v_mfma_f32_16x16x32_bf16 v[32:35], v[176:179], v[224:227], v[32:35]\n"
"ds_read_b64_tr_b16 v[160:161], %[vbe] offset:16384\n"
"v_add_f32 v240, v240, v202\n"
"v_add_f32 v241, v241, v206\n"
"v_mfma_f32_16x16x32_bf16 v[64:67], v[176:179], v[228:231], v[64:67]\n"
"ds_read_b64_tr_b16 v[162:163], %[vbe] offset:24576\n"
"v_add_f32 v240, v240, v203\n"
"v_add_f32 v241, v241, v207\n"
"v_mfma_f32_16x16x32_bf16 v[36:39], v[180:183], v[224:227], v[36:39]\n"
"ds_read_b64_tr_b16 v[164:165], v243 offset:16384\n"
"v_mfma_f32_16x16x32_bf16 v[68:71], v[180:183], v[228:231], v[68:71]\n"
"ds_read_b64_tr_b16 v[166:167], v243 offset:24576\n"
"v_mfma_f32_16x16x32_bf16 v[40:43], v[184:187], v[224:227], v[40:43]\n"
"ds_read_b64_tr_b16 v[168:169], %[vbe] offset:16640\n"
"v_mfma_f32_16x16x32_bf16 v[72:75], v[184:187], v[228:231], v[72:75]\n"
"ds_read_b64_tr_b16 v[170:171], %[vbe] offset:24832\n"
"v_mfma_f32_16x16x32_bf16 v[44:47], v[188:191], v[224:227], v[44:47]\n"
"ds_read_b64_tr_b16 v[172:173], v243 offset:16640\n"
"v_mfma_f32_16x16x32_bf16 v[76:79], v[188:191], v[228:231], v[76:79]\n"
"ds_read_b64_tr_b16 v[174:175], v243 offset:24832\n"
"s_waitcnt lgkmcnt(8)\n"
"v_mfma_f32_16x16x32_bf16 v[208:211], v[144:147], v[80:83], v[220:223]\n"
"ds_read_b64_tr_b16 v[176:177], %[vbe] offset:16896\n"
"v_mfma_f32_16x16x32_bf16 v[208:211], v[148:151], v[84:87], v[208:211]\n"
"ds_read_b64_tr_b16 v[178:179], %[vbe] offset:25088\n"
"v_mfma_f32_16x16x32_bf16 v[208:211], v[152:155], v[88:91], v[208:211]\n"
"ds_read_b64_tr_b16 v[180:181], v243 offset:16896\n"
"v_mfma_f32_16x16x32_bf16 v[208:211], v[156:159], v[92:95], v[208:211]\n"
"ds_read_b64_tr_b16 v[182:183], v243 offset:25088\n"
"v_mfma_f32_16x16x32_bf16 v[192:195], v[128:131], v[96:99], v[212:215]\n"
"ds_read_b64_tr_b16 v[184:185], %[vbe] offset:17152\n"
"v_mfma_f32_16x16x32_bf16 v[192:195], v[132:135], v[100:103], v[192:195]\n"
"ds_read_b64_tr_b16 v[186:187], %[vbe] offset:25344\n"
"v_mfma_f32_16x16x32_bf16 v[192:195], v[136:139], v[104:107], v[192:195]\n"
"ds_read_b64_tr_b16 v[188:189], v243 offset:17152\n"
"v_mfma_f32_16x16x32_bf16 v[192:195], v[140:143], v[108:111], v[192:195]\n"
"ds_read_b64_tr_b16 v[190:191], v243 offset:25344\n"
"v_mfma_f32_16x16x32_bf16 v[196:199], v[128:131], v[112:115], v[216:219]\n"
"v_mfma_f32_16x16x32_bf16 v[196:199], v[132:135], v[116:119], v[196:199]\n"
"v_mfma_f32_16x16x32_bf16 v[196:199], v[136:139], v[120:123], v[196:199]\n"
"v_mfma_f32_16x16x32_bf16 v[196:199], v[140:143], v[124:127], v[196:199]\n"
"v_mfma_f32_16x16x32_bf16 v[200:203], v[144:147], v[96:99], v[212:215]\n"
"v_mfma_f32_16x16x32_bf16 v[200:203], v[148:151], v[100:103], v[200:203]\n"
"v_mfma_f32_16x16x32_bf16 v[200:203], v[152:155], v[104:107], v[200:203]\n"
"v_mfma_f32_16x16x32_bf16 v[200:203], v[156:159], v[108:111], v[200:203]\n"
"v_mfma_f32_16x16x32_bf16 v[204:207], v[144:147], v[112:115], v[216:219]\n"
"v_mfma_f32_16x16x32_bf16 v[204:207], v[148:151], v[116:119], v[204:207]\n"
"v_mfma_f32_16x16x32_bf16 v[204:207], v[152:155], v[120:123], v[204:207]\n"
"v_mfma_f32_16x16x32_bf16 v[204:207], v[156:159], v[124:127], v[204:207]\n"
"s_waitcnt lgkmcnt(0)\n"
"s_barrier\n"
"s_setprio 0\n"
"s_lshl_b32 s88, %[osoff], 2\n"
"s_add_u32 s88, s88, 73728\n"
"s_mov_b32 m0, s53\n"
"s_nop 0\n"
"buffer_load_dwordx4 v249, s[76:79], s88 offen lds\n"
"buffer_load_dwordx4 v250, s[76:79], s88 offen offset:1024 lds\n"
"s_lshl_b32 s89, %[osoff], 2\n"
"s_add_u32 s89, s89, 108544\n"
"buffer_load_dwordx4 v249, s[76:79], s89 offen offset:2048 lds\n"
"v_exp_f32 v208, v208\n"
"v_exp_f32 v209, v209\n"
"v_exp_f32 v210, v210\n"
"v_exp_f32 v211, v211\n"
"v_cvt_pk_bf16_f32 v232, v208, v209\n"
"v_cvt_pk_bf16_f32 v233, v210, v211\n"
"ds_write_b64 v247, v[232:233] offset:4104\n"
"v_exp_f32 v192, v192\n"
"v_exp_f32 v193, v193\n"
"v_exp_f32 v194, v194\n"
"v_exp_f32 v195, v195\n"
"v_cvt_pk_bf16_f32 v224, v192, v193\n"
"v_cvt_pk_bf16_f32 v225, v194, v195\n"
"v_exp_f32 v196, v196\n"
"v_exp_f32 v197, v197\n"
"v_exp_f32 v198, v198\n"
"v_exp_f32 v199, v199\n"
"v_cvt_pk_bf16_f32 v228, v196, v197\n"
"v_cvt_pk_bf16_f32 v229, v198, v199\n"
"v_exp_f32 v200, v200\n"
"v_exp_f32 v201, v201\n"
"v_exp_f32 v202, v202\n"
"v_exp_f32 v203, v203\n"
"v_cvt_pk_bf16_f32 v226, v200, v201\n"
"v_cvt_pk_bf16_f32 v227, v202, v203\n"
"v_exp_f32 v204, v204\n"
"v_exp_f32 v205, v205\n"
"v_exp_f32 v206, v206\n"
"v_exp_f32 v207, v207\n"
"v_cvt_pk_bf16_f32 v230, v204, v205\n"
"v_cvt_pk_bf16_f32 v231, v206, v207\n"
"s_waitcnt vmcnt(10) lgkmcnt(0)\n"
"s_barrier\n"
"s_setprio 1\n"
"v_mfma_f32_16x16x32_bf16 v[16:19], v[160:163], v[224:227], v[16:19]\n"
"ds_read_b128 v[128:131], %[kb] offset:34816\n"
"v_add_f32 v240, v240, v192\n"
"v_add_f32_e64 v241, v241, v196\n"
"v_mfma_f32_16x16x32_bf16 v[48:51], v[160:163], v[228:231], v[48:51]\n"
"ds_read_b128 v[132:135], %[kb] offset:35072\n"
"v_add_f32 v242, v242, v208\n"
"v_add_f32 v240, v240, v193\n"
"v_mfma_f32_16x16x32_bf16 v[20:23], v[164:167], v[224:227], v[20:23]\n"
"ds_read_b128 v[136:139], %[kb] offset:35328\n"
"v_add_f32 v241, v241, v197\n"
"v_add_f32 v242, v242, v209\n"
"v_mfma_f32_16x16x32_bf16 v[52:55], v[164:167], v[228:231], v[52:55]\n"
"ds_read_b128 v[140:143], %[kb] offset:35584\n"
"v_add_f32 v240, v240, v194\n"
"v_add_f32 v241, v241, v198\n"
"v_mfma_f32_16x16x32_bf16 v[24:27], v[168:171], v[224:227], v[24:27]\n"
"ds_read_b128 v[144:147], %[kb] offset:43008\n"
"v_add_f32 v242, v242, v210\n"
"v_add_f32 v240, v240, v195\n"
"v_mfma_f32_16x16x32_bf16 v[56:59], v[168:171], v[228:231], v[56:59]\n"
"ds_read_b128 v[148:151], %[kb] offset:43264\n"
"v_add_f32 v241, v241, v199\n"
"v_add_f32 v242, v242, v211\n"
"v_mfma_f32_16x16x32_bf16 v[28:31], v[172:175], v[224:227], v[28:31]\n"
"ds_read_b128 v[152:155], %[kb] offset:43520\n"
"v_add_f32 v240, v240, v200\n"
"v_add_f32 v241, v241, v204\n"
"v_mfma_f32_16x16x32_bf16 v[60:63], v[172:175], v[228:231], v[60:63]\n"
"ds_read_b128 v[156:159], %[kb] offset:43776\n"
"v_add_f32 v240, v240, v201\n"
"v_add_f32 v241, v241, v205\n"
"v_mfma_f32_16x16x32_bf16 v[32:35], v[176:179], v[224:227], v[32:35]\n"
"ds_read_b64_tr_b16 v[160:161], %[vbe] offset:32768\n"
"v_add_f32 v240, v240, v202\n"
"v_add_f32 v241, v241, v206\n"
"v_mfma_f32_16x16x32_bf16 v[64:67], v[176:179], v[228:231], v[64:67]\n"
"ds_read_b64_tr_b16 v[162:163], %[vbe] offset:40960\n"
"v_add_f32 v240, v240, v203\n"
"v_add_f32 v241, v241, v207\n"
"v_mfma_f32_16x16x32_bf16 v[36:39], v[180:183], v[224:227], v[36:39]\n"
"ds_read_b64_tr_b16 v[164:165], v243 offset:32768\n"
"v_mfma_f32_16x16x32_bf16 v[68:71], v[180:183], v[228:231], v[68:71]\n"
"ds_read_b64_tr_b16 v[166:167], v243 offset:40960\n"
"v_mfma_f32_16x16x32_bf16 v[40:43], v[184:187], v[224:227], v[40:43]\n"
"ds_read_b64_tr_b16 v[168:169], %[vbe] offset:33024\n"
"v_mfma_f32_16x16x32_bf16 v[72:75], v[184:187], v[228:231], v[72:75]\n"
"ds_read_b64_tr_b16 v[170:171], %[vbe] offset:41216\n"
"v_mfma_f32_16x16x32_bf16 v[44:47], v[188:191], v[224:227], v[44:47]\n"
"ds_read_b64_tr_b16 v[172:173], v243 offset:33024\n"
"v_mfma_f32_16x16x32_bf16 v[76:79], v[188:191], v[228:231], v[76:79]\n"
"ds_read_b64_tr_b16 v[174:175], v243 offset:41216\n"
"s_waitcnt lgkmcnt(8)\n"
"v_mfma_f32_16x16x32_bf16 v[208:211], v[144:147], v[80:83], v[220:223]\n"
"ds_read_b64_tr_b16 v[176:177], %[vbe] offset:33280\n"
"v_mfma_f32_16x16x32_bf16 v[208:211], v[148:151], v[84:87], v[208:211]\n"
"ds_read_b64_tr_b16 v[178:179], %[vbe] offset:41472\n"
"v_mfma_f32_16x16x32_bf16 v[208:211], v[152:155], v[88:91], v[208:211]\n"
"ds_read_b64_tr_b16 v[180:181], v243 offset:33280\n"
"v_mfma_f32_16x16x32_bf16 v[208:211], v[156:159], v[92:95], v[208:211]\n"
"ds_read_b64_tr_b16 v[182:183], v243 offset:41472\n"
"v_mfma_f32_16x16x32_bf16 v[192:195], v[128:131], v[96:99], v[212:215]\n"
"ds_read_b64_tr_b16 v[184:185], %[vbe] offset:33536\n"
"v_mfma_f32_16x16x32_bf16 v[192:195], v[132:135], v[100:103], v[192:195]\n"
"ds_read_b64_tr_b16 v[186:187], %[vbe] offset:41728\n"
"v_mfma_f32_16x16x32_bf16 v[192:195], v[136:139], v[104:107], v[192:195]\n"
"ds_read_b64_tr_b16 v[188:189], v243 offset:33536\n"
"v_mfma_f32_16x16x32_bf16 v[192:195], v[140:143], v[108:111], v[192:195]\n"
"ds_read_b64_tr_b16 v[190:191], v243 offset:41728\n"
"v_mfma_f32_16x16x32_bf16 v[196:199], v[128:131], v[112:115], v[216:219]\n"
"v_mfma_f32_16x16x32_bf16 v[196:199], v[132:135], v[116:119], v[196:199]\n"
"v_mfma_f32_16x16x32_bf16 v[196:199], v[136:139], v[120:123], v[196:199]\n"
"v_mfma_f32_16x16x32_bf16 v[196:199], v[140:143], v[124:127], v[196:199]\n"
"v_mfma_f32_16x16x32_bf16 v[200:203], v[144:147], v[96:99], v[212:215]\n"
"v_mfma_f32_16x16x32_bf16 v[200:203], v[148:151], v[100:103], v[200:203]\n"
"v_mfma_f32_16x16x32_bf16 v[200:203], v[152:155], v[104:107], v[200:203]\n"
"v_mfma_f32_16x16x32_bf16 v[200:203], v[156:159], v[108:111], v[200:203]\n"
"v_mfma_f32_16x16x32_bf16 v[204:207], v[144:147], v[112:115], v[216:219]\n"
"v_mfma_f32_16x16x32_bf16 v[204:207], v[148:151], v[116:119], v[204:207]\n"
"v_mfma_f32_16x16x32_bf16 v[204:207], v[152:155], v[120:123], v[204:207]\n"
"v_mfma_f32_16x16x32_bf16 v[204:207], v[156:159], v[124:127], v[204:207]\n"
"s_waitcnt lgkmcnt(0)\n"
"s_barrier\n"
"s_setprio 0\n"
"s_lshl_b32 s88, %[os1], 2\n"
"s_mov_b32 m0, s54\n"
"s_nop 0\n"
"buffer_load_dwordx4 v249, s[76:79], s88 offen lds\n"
"buffer_load_dwordx4 v250, s[76:79], s88 offen offset:1024 lds\n"
"s_lshl_b32 s89, %[os1], 2\n"
"s_add_u32 s89, s89, 34816\n"
"buffer_load_dwordx4 v249, s[76:79], s89 offen offset:2048 lds\n"
"s_add_u32 m0, s90, 3072\n"
"s_nop 0\n"
"buffer_load_dwordx4 v252, s[76:79], s89 offen offset:2048 lds\n"
"v_exp_f32 v208, v208\n"
"v_exp_f32 v209, v209\n"
"v_exp_f32 v210, v210\n"
"v_exp_f32_e64 v211, v211\n"
"v_cvt_pk_bf16_f32 v232, v208, v209\n"
"v_cvt_pk_bf16_f32 v233, v210, v211\n"
"ds_write_b64 v247, v[232:233] offset:4104\n"
"v_exp_f32 v192, v192\n"
"v_exp_f32 v193, v193\n"
"v_exp_f32 v194, v194\n"
"v_exp_f32 v195, v195\n"
"v_cvt_pk_bf16_f32 v224, v192, v193\n"
"v_cvt_pk_bf16_f32 v225, v194, v195\n"
"v_exp_f32 v196, v196\n"
"v_exp_f32 v197, v197\n"
"v_exp_f32 v198, v198\n"
"v_exp_f32 v199, v199\n"
"v_cvt_pk_bf16_f32 v228, v196, v197\n"
"v_cvt_pk_bf16_f32 v229, v198, v199\n"
"v_exp_f32 v200, v200\n"
"v_exp_f32 v201, v201\n"
"v_exp_f32 v202, v202\n"
"v_exp_f32 v203, v203\n"
"v_cvt_pk_bf16_f32 v226, v200, v201\n"
"v_cvt_pk_bf16_f32 v227, v202, v203\n"
"v_exp_f32 v204, v204\n"
"v_exp_f32 v205, v205\n"
"v_exp_f32 v206, v206\n"
"v_exp_f32 v207, v207\n"
"v_cvt_pk_bf16_f32 v230, v204, v205\n"
"v_cvt_pk_bf16_f32 v231, v206, v207\n"
"s_waitcnt vmcnt(11) lgkmcnt(0)\n"
"s_barrier\n"
"s_setprio 1\n"
"v_mfma_f32_16x16x32_bf16 v[16:19], v[160:163], v[224:227], v[16:19]\n"
"ds_read_b128 v[128:131], %[kb] offset:51200\n"
"v_add_f32 v240, v240, v192\n"
"v_add_f32_e64 v241, v241, v196\n"
"v_mfma_f32_16x16x32_bf16 v[48:51], v[160:163], v[228:231], v[48:51]\n"
"ds_read_b128 v[132:135], %[kb] offset:51456\n"
"v_add_f32 v242, v242, v208\n"
"v_add_f32 v240, v240, v193\n"
"v_mfma_f32_16x16x32_bf16 v[20:23], v[164:167], v[224:227], v[20:23]\n"
"ds_read_b128 v[136:139], %[kb] offset:51712\n"
"v_add_f32 v241, v241, v197\n"
"v_add_f32 v242, v242, v209\n"
"v_mfma_f32_16x16x32_bf16 v[52:55], v[164:167], v[228:231], v[52:55]\n"
"ds_read_b128 v[140:143], %[kb] offset:51968\n"
"v_add_f32 v240, v240, v194\n"
"v_add_f32 v241, v241, v198\n"
"v_mfma_f32_16x16x32_bf16 v[24:27], v[168:171], v[224:227], v[24:27]\n"
"ds_read_b128 v[144:147], %[kb] offset:59392\n"
"v_add_f32 v242, v242, v210\n"
"v_add_f32 v240, v240, v195\n"
"v_mfma_f32_16x16x32_bf16 v[56:59], v[168:171], v[228:231], v[56:59]\n"
"ds_read_b128 v[148:151], %[kb] offset:59648\n"
"v_add_f32 v241, v241, v199\n"
"v_add_f32 v242, v242, v211\n"
"v_mfma_f32_16x16x32_bf16 v[28:31], v[172:175], v[224:227], v[28:31]\n"
"ds_read_b128 v[152:155], %[kb] offset:59904\n"
"v_add_f32 v240, v240, v200\n"
"v_add_f32 v241, v241, v204\n"
"v_mfma_f32_16x16x32_bf16 v[60:63], v[172:175], v[228:231], v[60:63]\n"
"ds_read_b128 v[156:159], %[kb] offset:60160\n"
"v_add_f32 v240, v240, v201\n"
"v_add_f32 v241, v241, v205\n"
"v_mfma_f32_16x16x32_bf16 v[32:35], v[176:179], v[224:227], v[32:35]\n"
"ds_read_b64_tr_b16 v[160:161], %[vbe] offset:49152\n"
"v_add_f32 v240, v240, v202\n"
"v_add_f32 v241, v241, v206\n"
"v_mfma_f32_16x16x32_bf16 v[64:67], v[176:179], v[228:231], v[64:67]\n"
"ds_read_b64_tr_b16 v[162:163], %[vbe] offset:57344\n"
"v_add_f32 v240, v240, v203\n"
"v_add_f32 v241, v241, v207\n"
"v_mfma_f32_16x16x32_bf16 v[36:39], v[180:183], v[224:227], v[36:39]\n"
"ds_read_b64_tr_b16 v[164:165], v243 offset:49152\n"
"v_mfma_f32_16x16x32_bf16 v[68:71], v[180:183], v[228:231], v[68:71]\n"
"ds_read_b64_tr_b16 v[166:167], v243 offset:57344\n"
"v_mfma_f32_16x16x32_bf16 v[40:43], v[184:187], v[224:227], v[40:43]\n"
"ds_read_b64_tr_b16 v[168:169], %[vbe] offset:49408\n"
"v_mfma_f32_16x16x32_bf16 v[72:75], v[184:187], v[228:231], v[72:75]\n"
"ds_read_b64_tr_b16 v[170:171], %[vbe] offset:57600\n"
"v_mfma_f32_16x16x32_bf16 v[44:47], v[188:191], v[224:227], v[44:47]\n"
"ds_read_b64_tr_b16 v[172:173], v243 offset:49408\n"
"v_mfma_f32_16x16x32_bf16 v[76:79], v[188:191], v[228:231], v[76:79]\n"
"ds_read_b64_tr_b16 v[174:175], v243 offset:57600\n"
"s_waitcnt lgkmcnt(8)\n"
"v_mfma_f32_16x16x32_bf16 v[208:211], v[144:147], v[80:83], v[220:223]\n"
"ds_read_b64_tr_b16 v[176:177], %[vbe] offset:49664\n"
"v_mfma_f32_16x16x32_bf16 v[208:211], v[148:151], v[84:87], v[208:211]\n"
"ds_read_b64_tr_b16 v[178:179], %[vbe] offset:57856\n"
"v_mfma_f32_16x16x32_bf16 v[208:211], v[152:155], v[88:91], v[208:211]\n"
"ds_read_b64_tr_b16 v[180:181], v243 offset:49664\n"
"v_mfma_f32_16x16x32_bf16 v[208:211], v[156:159], v[92:95], v[208:211]\n"
"ds_read_b64_tr_b16 v[182:183], v243 offset:57856\n"
"v_mfma_f32_16x16x32_bf16 v[192:195], v[128:131], v[96:99], v[212:215]\n"
"ds_read_b64_tr_b16 v[184:185], %[vbe] offset:49920\n"
"v_mfma_f32_16x16x32_bf16 v[192:195], v[132:135], v[100:103], v[192:195]\n"
"ds_read_b64_tr_b16 v[186:187], %[vbe] offset:58112\n"
"v_mfma_f32_16x16x32_bf16 v[192:195], v[136:139], v[104:107], v[192:195]\n"
"ds_read_b64_tr_b16 v[188:189], v243 offset:49920\n"
"v_mfma_f32_16x16x32_bf16 v[192:195], v[140:143], v[108:111], v[192:195]\n"
"ds_read_b64_tr_b16 v[190:191], v243 offset:58112\n"
"v_mfma_f32_16x16x32_bf16 v[196:199], v[128:131], v[112:115], v[216:219]\n"
"v_mfma_f32_16x16x32_bf16 v[196:199], v[132:135], v[116:119], v[196:199]\n"
"v_mfma_f32_16x16x32_bf16 v[196:199], v[136:139], v[120:123], v[196:199]\n"
"v_mfma_f32_16x16x32_bf16 v[196:199], v[140:143], v[124:127], v[196:199]\n"
"v_mfma_f32_16x16x32_bf16 v[200:203], v[144:147], v[96:99], v[212:215]\n"
"v_mfma_f32_16x16x32_bf16 v[200:203], v[148:151], v[100:103], v[200:203]\n"
"v_mfma_f32_16x16x32_bf16 v[200:203], v[152:155], v[104:107], v[200:203]\n"
"v_mfma_f32_16x16x32_bf16 v[200:203], v[156:159], v[108:111], v[200:203]\n"
"v_mfma_f32_16x16x32_bf16 v[204:207], v[144:147], v[112:115], v[216:219]\n"
"v_mfma_f32_16x16x32_bf16 v[204:207], v[148:151], v[116:119], v[204:207]\n"
"v_mfma_f32_16x16x32_bf16 v[204:207], v[152:155], v[120:123], v[204:207]\n"
"v_mfma_f32_16x16x32_bf16 v[204:207], v[156:159], v[124:127], v[204:207]\n"
"s_waitcnt lgkmcnt(0)\n"
"s_barrier\n"
"s_setprio 0\n"
"s_branch LT1_%=\n"
".p2align 3\n"
"LF1_%=:\n"
"s_add_u32 s52, %[hb], 4096\n"
"s_add_u32 s53, %[hb], 20480\n"
"s_add_u32 s54, %[hb], 36864\n"
"s_add_u32 s55, %[hb], 53248\n"
"s_lshr_b32 s90, %[hb], 3\n"
"s_add_u32 s90, s90, 131072\n"
"s_movk_i32 s68, 1152\n"
"s_lshr_b32 s69, %[nb256], 3\n"
"s_add_u32 s69, s69, 5632\n"
"v_and_b32 v254, 255, %[g0]\n"
"v_add_u32 v254, %[kq64], v254\n"
"v_and_b32 v254, 255, v254\n"
"v_lshrrev_b32 v255, 8, %[g0]\n"
"v_mul_u32_u24 v255, s68, v255\n"
"v_add_u32 v255, %[nb256], v255\n"
"v_add_u32 v249, v254, v255\n"
"v_xor_b32 v254, 32, v254\n"
"v_add_u32 v250, v254, v255\n"
"v_add_u32 v250, 3584, v250\n"
"v_lshrrev_b32 v254, 5, %[l16]\n"
"v_mul_u32_u24 v254, s68, v254\n"
"v_and_b32 v255, 16, %[l16]\n"
"v_add_u32 v254, v254, v255\n"
"v_add_u32 v252, s69, v254\n"
"s_lshr_b32 s68, %[hb], 11\n"
"s_or_b32 s68, s68, %[hb]\n"
"v_and_b32 v244, 0xffff, %[vbe]\n"
"v_xor_b32 v244, s68, v244\n"
"v_xor_b32 v245, 32, v244\n"
"v_lshrrev_b32 v246, 1, %[l16]\n"
"v_add_u32 v246, s90, v246\n"
"v_add_u32 v228, 6144, %[l16]\n"
"s_add_u32 s68, s90, 16384\n"
"v_add_u32 v247, s68, %[l16]\n"
"s_lshl_b32 s88, %[osoff], 2\n"
"s_mov_b32 m0, s52\n"
"s_nop 0\n"
"buffer_load_dwordx4 v249, s[76:79], s88 offen lds\n"
"buffer_load_dwordx4 v250, s[76:79], s88 offen offset:1024 lds\n"
"s_lshl_b32 s89, %[osoff], 2\n"
"s_add_u32 s89, s89, 34816\n"
"buffer_load_dwordx4 v249, s[76:79], s89 offen offset:2048 lds\n"
"s_add_u32 m0, s90, -1024\n"
"s_nop 0\n"
"buffer_load_dwordx4 v252, s[76:79], s89 offen offset:2048 lds\n"
"s_lshl_b32 s88, %[osoff], 2\n"
"s_add_u32 s88, s88, 73728\n"
"s_mov_b32 m0, s53\n"
"s_nop 0\n"
"buffer_load_dwordx4 v249, s[76:79], s88 offen lds\n"
"buffer_load_dwordx4 v250, s[76:79], s88 offen offset:1024 lds\n"
"s_lshl_b32 s89, %[osoff], 2\n"
"s_add_u32 s89, s89, 108544\n"
"buffer_load_dwordx4 v249, s[76:79], s89 offen offset:2048 lds\n"
"s_lshl_b32 s88, %[os1], 2\n"
"s_mov_b32 m0, s54\n"
"s_nop 0\n"
"buffer_load_dwordx4 v249, s[76:79], s88 offen lds\n"
"buffer_load_dwordx4 v250, s[76:79], s88 offen offset:1024 lds\n"
"s_lshl_b32 s89, %[os1], 2\n"
"s_add_u32 s89, s89, 34816\n"
"buffer_load_dwordx4 v249, s[76:79], s89 offen offset:2048 lds\n"
"s_add_u32 m0, s90, 3072\n"
"s_nop 0\n"
"buffer_load_dwordx4 v252, s[76:79], s89 offen offset:2048 lds\n"
"s_lshl_b32 s88, %[os1], 2\n"
"s_add_u32 s88, s88, 73728\n"
"s_mov_b32 m0, s55\n"
"s_nop 0\n"
"buffer_load_dwordx4 v249, s[76:79], s88 offen lds\n"
"buffer_load_dwordx4 v250, s[76:79], s88 offen offset:1024 lds\n"
"s_lshl_b32 s89, %[os1], 2\n"
"s_add_u32 s89, s89, 108544\n"
"buffer_load_dwordx4 v249, s[76:79], s89 offen offset:2048 lds\n"
"s_add_u32 s60, %[osoff], 2048\n"
"buffer_load_dwordx4 v[144:147], %[l16], %[srdo], s60 offen offset:1024 sc1\n"
"buffer_load_dwordx4 v[128:131], %[l16], %[srdo], s60 offen sc1\n"
"buffer_load_dwordx4 v[160:163], v228, %[srdo], s60 offen sc1\n"
"s_add_u32 s61, %[osoff], 11264\n"
"buffer_load_dwordx4 v[148:151], %[l16], %[srdo], s61 offen offset:1024 sc1\n"
"buffer_load_dwordx4 v[132:135], %[l16], %[srdo], s61 offen sc1\n"
"buffer_load_dwordx4 v[164:167], v228, %[srdo], s61 offen sc1\n"
"s_add_u32 s62, %[osoff], 20480\n"
"buffer_load_dwordx4 v[152:155], %[l16], %[srdo], s62 offen offset:1024 sc1\n"
"buffer_load_dwordx4 v[136:139], %[l16], %[srdo], s62 offen sc1\n"
"buffer_load_dwordx4 v[168:171], v228, %[srdo], s62 offen sc1\n"
"s_add_u32 s63, %[osoff], 29696\n"
"buffer_load_dwordx4 v[156:159], %[l16], %[srdo], s63 offen offset:1024 sc1\n"
"buffer_load_dwordx4 v[140:143], %[l16], %[srdo], s63 offen sc1\n"
"buffer_load_dwordx4 v[172:175], v228, %[srdo], s63 offen sc1\n"
"v_mov_b32 v176, 0\n"
"v_mov_b32 v177, 0\n"
"v_mov_b32 v178, 0\n"
"v_mov_b32 v179, 0\n"
"v_mov_b32 v180, 0\n"
"v_mov_b32 v181, 0\n"
"v_mov_b32 v182, 0\n"
"v_mov_b32 v183, 0\n"
"s_waitcnt vmcnt(0)\n"
"s_barrier\n"
"ds_read_b64_tr_b16 v[100:101], v244 offset:0\n"
"ds_read_b64_tr_b16 v[102:103], v244 offset:8192\n"
"ds_read_b64_tr_b16 v[104:105], v245 offset:0\n"
"ds_read_b64_tr_b16 v[106:107], v245 offset:8192\n"
"ds_read_b64_tr_b16 v[108:109], v244 offset:256\n"
"ds_read_b64_tr_b16 v[110:111], v244 offset:8448\n"
"ds_read_b64_tr_b16 v[112:113], v245 offset:256\n"
"ds_read_b64_tr_b16 v[114:115], v245 offset:8448\n"
"ds_read_b64_tr_b16 v[116:117], v244 offset:512\n"
"ds_read_b64_tr_b16 v[118:119], v244 offset:8704\n"
"ds_read_b64_tr_b16 v[120:121], v245 offset:512\n"
"ds_read_b64_tr_b16 v[122:123], v245 offset:8704\n"
"ds_read_b64_tr_b16 v[124:125], v244 offset:768\n"
"ds_read_b64_tr_b16 v[126:127], v244 offset:8960\n"
"s_waitcnt lgkmcnt(0)\n"
"s_nop 7\n"
"s_branch LO1_%=\n"
".p2align 6\n"
"LA1_%=:\n"
"s_cmp_eq_u32 s61, 0x30000\n"
"s_cbranch_scc0 LNd1_%=\n"
"s_getpc_b64 s[94:95]\n"
"LMd1_%=:\n"
"s_add_u32 s94, s94, (LZZ1_%=-16384-LMd1_%=)&0xffffffff\n"
"s_addc_u32 s95, s95, (LZZ1_%=-16384-LMd1_%=)>>32\n"
"v_lshlrev_b32 v236, 4, %[l16]\n"
"s_lshr_b32 s68, %[hb], 9\n"
"v_add_u32 v236, s68, v236\n"
"s_lshl_b32 exec_lo, 0x11111111, %[wrot]\n"
"s_mov_b32 exec_hi, exec_lo\n"
"global_load_dword v253, v236, s[94:95]\n"
"s_mov_b64 exec, -1\n"
"LNd1_%=:\n"
"s_nop 0\n"
"KA1 0,s52,16384\n"
"KA1 16384,s53,32768\n"
"KA1 32768,s54,49152\n"
"KA1 49152,s55,0\n"
"s_cbranch_scc0 LA1_%=\n"
"s_nop 0\n"
"s_barrier\n"
"s_setprio 1\n"
"v_mfma_f32_16x16x32_bf16 v[16:19], v[160:163], v[224:227], v[16:19]\n"
"ds_read_b128 v[128:131], v244 offset:2048\n"
"v_add_f32 v240, v240, v192\n"
"v_add_f32 v241, v241, v196\n"
"v_mfma_f32_16x16x32_bf16 v[48:51], v[160:163], v[228:231], v[48:51]\n"
"ds_read_b128 v[132:135], v244 offset:2304\n"
"v_add_f32 v242, v242, v208\n"
"v_add_f32 v240, v240, v193\n"
"v_mfma_f32_16x16x32_bf16 v[20:23], v[164:167], v[224:227], v[20:23]\n"
"ds_read_b128 v[136:139], v244 offset:2560\n"
"v_add_f32 v241, v241, v197\n"
"v_add_f32 v242, v242, v209\n"
"v_mfma_f32_16x16x32_bf16 v[52:55], v[164:167], v[228:231], v[52:55]\n"
"ds_read_b128 v[140:143], v244 offset:2816\n"
"v_add_f32 v240, v240, v194\n"
"v_add_f32 v241, v241, v198\n"
"v_mfma_f32_16x16x32_bf16 v[24:27], v[168:171], v[224:227], v[24:27]\n"
"ds_read_b128 v[144:147], v244 offset:10240\n"
"v_add_f32 v242, v242, v210\n"
"v_add_f32 v240, v240, v195\n"
"v_mfma_f32_16x16x32_bf16 v[56:59], v[168:171], v[228:231], v[56:59]\n"
"ds_read_b128 v[148:151], v244 offset:10496\n"
"v_add_f32 v241, v241, v199\n"
"v_add_f32 v242, v242, v211\n"
"v_mfma_f32_16x16x32_bf16 v[28:31], v[172:175], v[224:227], v[28:31]\n"
"ds_read_b128 v[152:155], v244 offset:10752\n"
"v_add_f32 v240, v240, v200\n"
"v_add_f32 v241, v241, v204\n"
"v_mfma_f32_16x16x32_bf16 v[60:63], v[172:175], v[228:231], v[60:63]\n"
"ds_read_b128 v[156:159], v244 offset:11008\n"
"v_add_f32 v240, v240, v201\n"
"v_add_f32 v241, v241, v205\n"
"v_mfma_f32_16x16x32_bf16 v[32:35], v[176:179], v[224:227], v[32:35]\n"
"ds_read_b64_tr_b16 v[160:161], v245 offset:0\n"
"v_add_f32 v240, v240, v202\n"
"v_add_f32 v241, v241, v206\n"
"v_mfma_f32_16x16x32_bf16 v[64:67], v[176:179], v[228:231], v[64:67]\n"
"ds_read_b64_tr_b16 v[162:163], v245 offset:8192\n"
"v_add_f32 v240, v240, v203\n"
"v_add_f32 v241, v241, v207\n"
"v_mfma_f32_16x16x32_bf16 v[36:39], v[180:183], v[224:227], v[36:39]\n"
"ds_read_b64_tr_b16 v[164:165], v246 offset:0\n"
"v_mfma_f32_16x16x32_bf16 v[68:71], v[180:183], v[228:231], v[68:71]\n"
"ds_read_b64_tr_b16 v[166:167], v246 offset:8192\n"
"v_mfma_f32_16x16x32_bf16 v[40:43], v[184:187], v[224:227], v[40:43]\n"
"ds_read_b64_tr_b16 v[168:169], v245 offset:256\n"
"v_mfma_f32_16x16x32_bf16 v[72:75], v[184:187], v[228:231], v[72:75]\n"
"ds_read_b64_tr_b16 v[170:171], v245 offset:8448\n"
"v_mfma_f32_16x16x32_bf16 v[44:47], v[188:191], v[224:227], v[44:47]\n"
"ds_read_b64_tr_b16 v[172:173], v246 offset:256\n"
"v_mfma_f32_16x16x32_bf16 v[76:79], v[188:191], v[228:231], v[76:79]\n"
"ds_read_b64_tr_b16 v[174:175], v246 offset:8448\n"
"s_waitcnt lgkmcnt(8)\n"
"v_mfma_f32_16x16x32_bf16 v[208:211], v[144:147], v[80:83], v[220:223]\n"
"ds_read_b64_tr_b16 v[176:177], v245 offset:512\n"
"v_mfma_f32_16x16x32_bf16 v[208:211], v[148:151], v[84:87], v[208:211]\n"
"ds_read_b64_tr_b16 v[178:179], v245 offset:8704\n"
"v_mfma_f32_16x16x32_bf16 v[208:211], v[152:155], v[88:91], v[208:211]\n"
"ds_read_b64_tr_b16 v[180:181], v246 offset:512\n"
"v_mfma_f32_16x16x32_bf16 v[208:211], v[156:159], v[92:95], v[208:211]\n"
"ds_read_b64_tr_b16 v[182:183], v246 offset:8704\n"
"v_mfma_f32_16x16x32_bf16 v[192:195], v[128:131], v[96:99], v[212:215]\n"
"ds_read_b64_tr_b16 v[184:185], v245 offset:768\n"
"v_mfma_f32_16x16x32_bf16 v[192:195], v[132:135], v[100:103], v[192:195]\n"
"ds_read_b64_tr_b16 v[186:187], v245 offset:8960\n"
"v_mfma_f32_16x16x32_bf16 v[192:195], v[136:139], v[104:107], v[192:195]\n"
"ds_read_b64_tr_b16 v[188:189], v246 offset:768\n"
"v_mfma_f32_16x16x32_bf16 v[192:195], v[140:143], v[108:111], v[192:195]\n"
"ds_read_b64_tr_b16 v[190:191], v246 offset:8960\n"
"v_mfma_f32_16x16x32_bf16 v[196:199], v[128:131], v[112:115], v[216:219]\n"
"v_mfma_f32_16x16x32_bf16 v[196:199], v[132:135], v[116:119], v[196:199]\n"
"v_mfma_f32_16x16x32_bf16 v[196:199], v[136:139], v[120:123], v[196:199]\n"
"v_mfma_f32_16x16x32_bf16 v[196:199], v[140:143], v[124:127], v[196:199]\n"
"v_mfma_f32_16x16x32_bf16 v[200:203], v[144:147], v[96:99], v[212:215]\n"
"v_mfma_f32_16x16x32_bf16 v[200:203], v[148:151], v[100:103], v[200:203]\n"
"v_mfma_f32_16x16x32_bf16 v[200:203], v[152:155], v[104:107], v[200:203]\n"
"v_mfma_f32_16x16x32_bf16 v[200:203], v[156:159], v[108:111], v[200:203]\n"
"v_mfma_f32_16x16x32_bf16 v[204:207], v[144:147], v[112:115], v[216:219]\n"
"v_mfma_f32_16x16x32_bf16 v[204:207], v[148:151], v[116:119], v[204:207]\n"
"v_mfma_f32_16x16x32_bf16 v[204:207], v[152:155], v[120:123], v[204:207]\n"
"v_mfma_f32_16x16x32_bf16 v[204:207], v[156:159], v[124:127], v[204:207]\n"
"s_waitcnt lgkmcnt(0)\n"
"s_barrier\n"
"s_setprio 0\n"
"s_lshl_b32 s88, %[osoff], 2\n"
"s_mov_b32 m0, s52\n"
"s_nop 0\n"
"buffer_load_dwordx4 v249, s[76:79], s88 offen lds\n"
"buffer_load_dwordx4 v250, s[76:79], s88 offen offset:1024 lds\n"
"v_exp_f32 v208, v208\n"
"v_exp_f32 v209, v209\n"
"v_exp_f32 v210, v210\n"
"v_exp_f32_e64 v211, v211\n"
"v_cvt_pk_bf16_f32 v232, v208, v209\n"
"v_cvt_pk_bf16_f32 v233, v210, v211\n"
"ds_write_b64 v247, v[232:233] offset:4104\n"
"v_exp_f32 v192, v192\n"
"v_exp_f32 v193, v193\n"
"v_exp_f32 v194, v194\n"
"v_exp_f32 v195, v195\n"
"v_cvt_pk_bf16_f32 v224, v192, v193\n"
"v_cvt_pk_bf16_f32 v225, v194, v195\n"
"v_exp_f32 v196, v196\n"
"v_exp_f32 v197, v197\n"
"v_exp_f32 v198, v198\n"
"v_exp_f32 v199, v199\n"
"v_cvt_pk_bf16_f32 v228, v196, v197\n"
"v_cvt_pk_bf16_f32 v229, v198, v199\n"
"v_exp_f32 v200, v200\n"
"v_exp_f32 v201, v201\n"
"v_exp_f32 v202, v202\n"
"v_exp_f32 v203, v203\n"
"v_cvt_pk_bf16_f32 v226, v200, v201\n"
"v_cvt_pk_bf16_f32 v227, v202, v203\n"
"v_exp_f32 v204, v204\n"
"v_exp_f32 v205, v205\n"
"v_exp_f32 v206, v206\n"
"v_exp_f32 v207, v207\n"
"v_cvt_pk_bf16_f32 v230, v204, v205\n"
"v_cvt_pk_bf16_f32 v231, v206, v207\n"
"s_waitcnt vmcnt(8) lgkmcnt(0)\n"
"s_barrier\n"
"s_setprio 1\n"
"v_mfma_f32_16x16x32_bf16 v[16:19], v[160:163], v[224:227], v[16:19]\n"
"ds_read_b128 v[128:131], %[kb] offset:18432\n"
"v_add_f32 v240, v240, v192\n"
"v_add_f32_e64 v241, v241, v196\n"
"v_mfma_f32_16x16x32_bf16 v[48:51], v[160:163], v[228:231], v[48:51]\n"
"ds_read_b128 v[132:135], %[kb] offset:18688\n"
"v_add_f32 v242, v242, v208\n"
"v_add_f32 v240, v240, v193\n"
"v_mfma_f32_16x16x32_bf16 v[20:23], v[164:167], v[224:227], v[20:23]\n"
"ds_read_b128 v[136:139], %[kb] offset:18944\n"
"v_add_f32 v241, v241, v197\n"
"v_add_f32 v242, v242, v209\n"
"v_mfma_f32_16x16x32_bf16 v[52:55], v[164:167], v[228:231], v[52:55]\n"
"ds_read_b128 v[140:143], %[kb] offset:19200\n"
"v_add_f32 v240, v240, v194\n"
"v_add_f32 v241, v241, v198\n"
"v_mfma_f32_16x16x32_bf16 v[24:27], v[168:171], v[224:227], v[24:27]\n"
"ds_read_b128 v[144:147], %[kb] offset:26624\n"
"v_add_f32 v242, v242, v210\n"
"v_add_f32 v240, v240, v195\n"
"v_mfma_f32_16x16x32_bf16 v[56:59], v[168:171], v[228:231], v[56:59]\n"
"ds_read_b128 v[148:151], %[kb] offset:26880\n"
"v_add_f32 v241, v241, v199\n"
"v_add_f32 v242, v242, v211\n"
"v_mfma_f32_16x16x32_bf16 v[28:31], v[172:175], v[224:227], v[28:31]\n"
"ds_read_b128 v[152:155], %[kb] offset:27136\n"
"v_add_f32 v240, v240, v200\n"
"v_add_f32 v241, v241, v204\n"
"v_mfma_f32_16x16x32_bf16 v[60:63], v[172:175], v[228:231], v[60:63]\n"
"ds_read_b128 v[156:159], %[kb] offset:27392\n"
"v_add_f32 v240, v240, v201\n"
"v_add_f32 v241, v241, v205\n"
"v_mfma_f32_16x16x32_bf16 v[32:35], v[176:179], v[224:227], v[32:35]\n"
"ds_read_b64_tr_b16 v[160:161], %[vbe] offset:16384\n"
"v_add_f32 v240, v240, v202\n"
"v_add_f32 v241, v241, v206\n"
"v_mfma_f32_16x16x32_bf16 v[64:67], v[176:179], v[228:231], v[64:67]\n"
"ds_read_b64_tr_b16 v[162:163], %[vbe] offset:24576\n"
"v_add_f32 v240, v240, v203\n"
"v_add_f32 v241, v241, v207\n"
"v_mfma_f32_16x16x32_bf16 v[36:39], v[180:183], v[224:227], v[36:39]\n"
"ds_read_b64_tr_b16 v[164:165], v243 offset:16384\n"
"v_mfma_f32_16x16x32_bf16 v[68:71], v[180:183], v[228:231], v[68:71]\n"
"ds_read_b64_tr_b16 v[166:167], v243 offset:24576\n"
"v_mfma_f32_16x16x32_bf16 v[40:43], v[184:187], v[224:227], v[40:43]\n"
"ds_read_b64_tr_b16 v[168:169], %[vbe] offset:16640\n"
"v_mfma_f32_16x16x32_bf16 v[72:75], v[184:187], v[228:231], v[72:75]\n"
"ds_read_b64_tr_b16 v[170:171], %[vbe] offset:24832\n"
"v_mfma_f32_16x16x32_bf16 v[44:47], v[188:191], v[224:227], v[44:47]\n"
"ds_read_b64_tr_b16 v[172:173], v243 offset:16640\n"
"v_mfma_f32_16x16x32_bf16 v[76:79], v[188:191], v[228:231], v[76:79]\n"
"ds_read_b64_tr_b16 v[174:175], v243 offset:24832\n"
"s_waitcnt lgkmcnt(8)\n"
"v_mfma_f32_16x16x32_bf16 v[208:211], v[144:147], v[80:83], v[220:223]\n"
"ds_read_b64_tr_b16 v[176:177], %[vbe] offset:16896\n"
"v_mfma_f32_16x16x32_bf16 v[208:211], v[148:151], v[84:87], v[208:211]\n"
"ds_read_b64_tr_b16 v[178:179], %[vbe] offset:25088\n"
"v_mfma_f32_16x16x32_bf16 v[208:211], v[152:155], v[88:91], v[208:211]\n"
"ds_read_b64_tr_b16 v[180:181], v243 offset:16896\n"
"v_mfma_f32_16x16x32_bf16 v[208:211], v[156:159], v[92:95], v[208:211]\n"
"ds_read_b64_tr_b16 v[182:183], v243 offset:25088\n"
"v_mfma_f32_16x16x32_bf16 v[192:195], v[128:131], v[96:99], v[212:215]\n"
"ds_read_b64_tr_b16 v[184:185], %[vbe] offset:17152\n"
"v_mfma_f32_16x16x32_bf16 v[192:195], v[132:135], v[100:103], v[192:195]\n"
"ds_read_b64_tr_b16 v[186:187], %[vbe] offset:25344\n"
"v_mfma_f32_16x16x32_bf16 v[192:195], v[136:139], v[104:107], v[192:195]\n"
"ds_read_b64_tr_b16 v[188:189], v243 offset:17152\n"
"v_mfma_f32_16x16x32_bf16 v[192:195], v[140:143], v[108:111], v[192:195]\n"
"ds_read_b64_tr_b16 v[190:191], v243 offset:25344\n"
"v_mfma_f32_16x16x32_bf16 v[196:199], v[128:131], v[112:115], v[216:219]\n"
"v_mfma_f32_16x16x32_bf16 v[196:199], v[132:135], v[116:119], v[196:199]\n"
"v_mfma_f32_16x16x32_bf16 v[196:199], v[136:139], v[120:123], v[196:199]\n"
"v_mfma_f32_16x16x32_bf16 v[196:199], v[140:143], v[124:127], v[196:199]\n"
"v_mfma_f32_16x16x32_bf16 v[200:203], v[144:147], v[96:99], v[212:215]\n"
"v_mfma_f32_16x16x32_bf16 v[200:203], v[148:151], v[100:103], v[200:203]\n"
"v_mfma_f32_16x16x32_bf16 v[200:203], v[152:155], v[104:107], v[200:203]\n"
"v_mfma_f32_16x16x32_bf16 v[200:203], v[156:159], v[108:111], v[200:203]\n"
"v_mfma_f32_16x16x32_bf16 v[204:207], v[144:147], v[112:115], v[216:219]\n"
"v_mfma_f32_16x16x32_bf16 v[204:207], v[148:151], v[116:119], v[204:207]\n"
"v_mfma_f32_16x16x32_bf16 v[204:207], v[152:155], v[120:123], v[204:207]\n"
"v_mfma_f32_16x16x32_bf16 v[204:207], v[156:159], v[124:127], v[204:207]\n"
"s_waitcnt lgkmcnt(0)\n"
"s_barrier\n"
"s_setprio 0\n"
"s_lshl_b32 s89, %[osoff], 2\n"
"s_add_u32 s89, s89, 34816\n"
"buffer_load_dwordx4 v249, s[76:79], s89 offen offset:2048 lds\n"
"s_add_u32 m0, s90, -1024\n"
"s_nop 0\n"
"buffer_load_dwordx4 v252, s[76:79], s89 offen offset:2048 lds\n"
"v_exp_f32 v208, v208\n"
"v_exp_f32 v209, v209\n"
"v_exp_f32 v210, v210\n"
"v_exp_f32 v211, v211\n"
"v_cvt_pk_bf16_f32 v232, v208, v209\n"
"v_cvt_pk_bf16_f32 v233, v210, v211\n"
"ds_write_b64 v247, v[232:233] offset:4104\n"
"v_exp_f32 v192, v192\n"
"v_exp_f32 v193, v193\n"
"v_exp_f32 v194, v194\n"
"v_exp_f32 v195, v195\n"
"v_cvt_pk_bf16_f32 v224, v192, v193\n"
"v_cvt_pk_bf16_f32 v225, v194, v195\n"
"v_exp_f32 v196, v196\n"
"v_exp_f32 v197, v197\n"
"v_exp_f32 v198, v198\n"
"v_exp_f32 v199, v199\n"
"v_cvt_pk_bf16_f32 v228, v196, v197\n"
"v_cvt_pk_bf16_f32 v229, v198, v199\n"
"v_exp_f32 v200, v200\n"
"v_exp_f32 v201, v201\n"
"v_exp_f32 v202, v202\n"
"v_exp_f32 v203, v203\n"
"v_cvt_pk_bf16_f32 v226, v200, v201\n"
"v_cvt_pk_bf16_f32 v227, v202, v203\n"
"v_exp_f32 v204, v204\n"
"v_exp_f32 v205, v205\n"
"v_exp_f32 v206, v206\n"
"v_exp_f32 v207, v207\n"
"v_cvt_pk_bf16_f32 v230, v204, v205\n"
"v_cvt_pk_bf16_f32 v231, v206, v207\n"
"s_waitcnt lgkmcnt(0)\n"
"s_barrier\n"
"s_setprio 1\n"
"v_mfma_f32_16x16x32_bf16 v[16:19], v[160:163], v[224:227], v[16:19]\n"
"ds_read_b128 v[128:131], v244 offset:18432\n"
"v_add_f32 v240, v240, v192\n"
"v_add_f32_e64 v241, v241, v196\n"
"v_mfma_f32_16x16x32_bf16 v[48:51], v[160:163], v[228:231], v[48:51]\n"
"ds_read_b128 v[132:135], v244 offset:18688\n"
"v_add_f32 v242, v242, v208\n"
"v_add_f32 v240, v240, v193\n"
"v_mfma_f32_16x16x32_bf16 v[20:23], v[164:167], v[224:227], v[20:23]\n"
"ds_read_b128 v[136:139], v244 offset:18944\n"
"v_add_f32 v241, v241, v197\n"
"v_add_f32 v242, v242, v209\n"
"v_mfma_f32_16x16x32_bf16 v[52:55], v[164:167], v[228:231], v[52:55]\n"
"ds_read_b128 v[140:143], v244 offset:19200\n"
"v_add_f32 v240, v240, v194\n"
"v_add_f32 v241, v241, v198\n"
"v_mfma_f32_16x16x32_bf16 v[24:27], v[168:171], v[224:227], v[24:27]\n"
"ds_read_b128 v[144:147], v244 offset:26624\n"
"v_add_f32 v242, v242, v210\n"
"v_add_f32 v240, v240, v195\n"
"v_mfma_f32_16x16x32_bf16 v[56:59], v[168:171], v[228:231], v[56:59]\n"
"ds_read_b128 v[148:151], v244 offset:26880\n"
"v_add_f32 v241, v241, v199\n"
"v_add_f32 v242, v242, v211\n"
"v_mfma_f32_16x16x32_bf16 v[28:31], v[172:175], v[224:227], v[28:31]\n"
"ds_read_b128 v[152:155], v244 offset:27136\n"
"v_add_f32 v240, v240, v200\n"
"v_add_f32 v241, v241, v204\n"
"v_mfma_f32_16x16x32_bf16 v[60:63], v[172:175], v[228:231], v[60:63]\n"
"ds_read_b128 v[156:159], v244 offset:27392\n"
"v_add_f32 v240, v240, v201\n"
"v_add_f32 v241, v241, v205\n"
"v_mfma_f32_16x16x32_bf16 v[32:35], v[176:179], v[224:227], v[32:35]\n"
"ds_read_b64_tr_b16 v[160:161], v245 offset:16384\n"
"v_add_f32 v240, v240, v202\n"
"v_add_f32 v241, v241, v206\n"
"v_mfma_f32_16x16x32_bf16 v[64:67], v[176:179], v[228:231], v[64:67]\n"
"ds_read_b64_tr_b16 v[162:163], v245 offset:24576\n"
"v_add_f32 v240, v240, v203\n"
"v_add_f32 v241, v241, v207\n"
"v_mfma_f32_16x16x32_bf16 v[36:39], v[180:183], v[224:227], v[36:39]\n"
"ds_read_b64_tr_b16 v[164:165], v246 offset:16384\n"
"v_mfma_f32_16x16x32_bf16 v[68:71], v[180:183], v[228:231], v[68:71]\n"
"ds_read_b64_tr_b16 v[166:167], v246 offset:24576\n"
"v_mfma_f32_16x16x32_bf16 v[40:43], v[184:187], v[224:227], v[40:43]\n"
"ds_read_b64_tr_b16 v[168:169], v245 offset:16640\n"
"v_mfma_f32_16x16x32_bf16 v[72:75], v[184:187], v[228:231], v[72:75]\n"
"ds_read_b64_tr_b16 v[170:171], v245 offset:24832\n"
"v_mfma_f32_16x16x32_bf16 v[44:47], v[188:191], v[224:227], v[44:47]\n"
"ds_read_b64_tr_b16 v[172:173], v246 offset:16640\n"
"v_mfma_f32_16x16x32_bf16 v[76:79], v[188:191], v[228:231], v[76:79]\n"
"ds_read_b64_tr_b16 v[174:175], v246 offset:24832\n"
"s_waitcnt lgkmcnt(8)\n"
"v_mfma_f32_16x16x32_bf16 v[208:211], v[144:147], v[80:83], v[220:223]\n"
"ds_read_b64_tr_b16 v[176:177], v245 offset:16896\n"
"v_mfma_f32_16x16x32_bf16 v[208:211], v[148:151], v[84:87], v[208:211]\n"
"ds_read_b64_tr_b16 v[178:179], v245 offset:25088\n"
"v_mfma_f32_16x16x32_bf16 v[208:211], v[152:155], v[88:91], v[208:211]\n"
"ds_read_b64_tr_b16 v[180:181], v246 offset:16896\n"
"v_mfma_f32_16x16x32_bf16 v[208:211], v[156:159], v[92:95], v[208:211]\n"
"ds_read_b64_tr_b16 v[182:183], v246 offset:25088\n"
"v_mfma_f32_16x16x32_bf16 v[192:195], v[128:131], v[96:99], v[212:215]\n"
"ds_read_b64_tr_b16 v[184:185], v245 offset:17152\n"
"v_mfma_f32_16x16x32_bf16 v[192:195], v[132:135], v[100:103], v[192:195]\n"
"ds_read_b64_tr_b16 v[186:187], v245 offset:25344\n"
"v_mfma_f32_16x16x32_bf16 v[192:195], v[136:139], v[104:107], v[192:195]\n"
"ds_read_b64_tr_b16 v[188:189], v246 offset:17152\n"
"v_mfma_f32_16x16x32_bf16 v[192:195], v[140:143], v[108:111], v[192:195]\n"
"ds_read_b64_tr_b16 v[190:191], v246 offset:25344\n"
"v_mfma_f32_16x16x32_bf16 v[196:199], v[128:131], v[112:115], v[216:219]\n"
"v_mfma_f32_16x16x32_bf16 v[196:199], v[132:135], v[116:119], v[196:199]\n"
"v_mfma_f32_16x16x32_bf16 v[196:199], v[136:139], v[120:123], v[196:199]\n"
"v_mfma_f32_16x16x32_bf16 v[196:199], v[140:143], v[124:127], v[196:199]\n"
"v_mfma_f32_16x16x32_bf16 v[200:203], v[144:147], v[96:99], v[212:215]\n"
"v_mfma_f32_16x16x32_bf16 v[200:203], v[148:151], v[100:103], v[200:203]\n"
"v_mfma_f32_16x16x32_bf16 v[200:203], v[152:155], v[104:107], v[200:203]\n"
"v_mfma_f32_16x16x32_bf16 v[200:203], v[156:159], v[108:111], v[200:203]\n"
"v_mfma_f32_16x16x32_bf16 v[204:207], v[144:147], v[112:115], v[216:219]\n"
"v_mfma_f32_16x16x32_bf16 v[204:207], v[148:151], v[116:119], v[204:207]\n"
"v_mfma_f32_16x16x32_bf16 v[204:207], v[152:155], v[120:123], v[204:207]\n"
"v_mfma_f32_16x16x32_bf16 v[204:207], v[156:159], v[124:127], v[204:207]\n"
"s_waitcnt lgkmcnt(0)\n"
"s_barrier\n"
"s_setprio 0\n"
"s_lshl_b32 s88, %[osoff], 2\n"
"s_add_u32 s88, s88, 73728\n"
"s_mov_b32 m0, s53\n"
"s_nop 0\n"
"buffer_load_dwordx4 v249, s[76:79], s88 offen lds\n"
"buffer_load_dwordx4 v250, s[76:79], s88 offen offset:1024 lds\n"
"v_exp_f32 v208, v208\n"
"v_exp_f32 v209, v209\n"
"v_exp_f32 v210, v210\n"
"v_exp_f32_e64 v211, v211\n"
"v_cvt_pk_bf16_f32 v232, v208, v209\n"
"v_cvt_pk_bf16_f32 v233, v210, v211\n"
"ds_write_b64 v247, v[232:233] offset:4104\n"
"v_exp_f32 v192, v192\n"
"v_exp_f32 v193, v193\n"
"v_exp_f32 v194, v194\n"
"v_exp_f32 v195, v195\n"
"v_cvt_pk_bf16_f32 v224, v192, v193\n"
"v_cvt_pk_bf16_f32 v225, v194, v195\n"
"v_exp_f32 v196, v196\n"
"v_exp_f32 v197, v197\n"
"v_exp_f32 v198, v198\n"
"v_exp_f32 v199, v199\n"
"v_cvt_pk_bf16_f32 v228, v196, v197\n"
"v_cvt_pk_bf16_f32 v229, v198, v199\n"
"v_exp_f32 v200, v200\n"
"v_exp_f32 v201, v201\n"
"v_exp_f32 v202, v202\n"
"v_exp_f32 v203, v203\n"
"v_cvt_pk_bf16_f32 v226, v200, v201\n"
"v_cvt_pk_bf16_f32 v227, v202, v203\n"
"v_exp_f32 v204, v204\n"
"v_exp_f32 v205, v205\n"
"v_exp_f32 v206, v206\n"
"v_exp_f32 v207, v207\n"
"v_cvt_pk_bf16_f32 v230, v204, v205\n"
"v_cvt_pk_bf16_f32 v231, v206, v207\n"
"s_waitcnt vmcnt(9) lgkmcnt(0)\n"
"s_barrier\n"
"s_setprio 1\n"
"v_mfma_f32_16x16x32_bf16 v[16:19], v[160:163], v[224:227], v[16:19]\n"
"ds_read_b128 v[128:131], %[kb] offset:34816\n"
"v_add_f32 v240, v240, v192\n"
"v_add_f32_e64 v241, v241, v196\n"
"v_mfma_f32_16x16x32_bf16 v[48:51], v[160:163], v[228:231], v[48:51]\n"
"ds_read_b128 v[132:135], %[kb] offset:35072\n"
"v_add_f32 v242, v242, v208\n"
"v_add_f32 v240, v240, v193\n"
"v_mfma_f32_16x16x32_bf16 v[20:23], v[164:167], v[224:227], v[20:23]\n"
"ds_read_b128 v[136:139], %[kb] offset:35328\n"
"v_add_f32 v241, v241, v197\n"
"v_add_f32 v242, v242, v209\n"
"v_mfma_f32_16x16x32_bf16 v[52:55], v[164:167], v[228:231], v[52:55]\n"
"ds_read_b128 v[140:143], %[kb] offset:35584\n"
"v_add_f32 v240, v240, v194\n"
"v_add_f32 v241, v241, v198\n"
"v_mfma_f32_16x16x32_bf16 v[24:27], v[168:171], v[224:227], v[24:27]\n"
"ds_read_b128 v[144:147], %[kb] offset:43008\n"
"v_add_f32 v242, v242, v210\n"
"v_add_f32 v240, v240, v195\n"
"v_mfma_f32_16x16x32_bf16 v[56:59], v[168:171], v[228:231], v[56:59]\n"
"ds_read_b128 v[148:151], %[kb] offset:43264\n"
"v_add_f32 v241, v241, v199\n"
"v_add_f32 v242, v242, v211\n"
"v_mfma_f32_16x16x32_bf16 v[28:31], v[172:175], v[224:227], v[28:31]\n"
"ds_read_b128 v[152:155], %[kb] offset:43520\n"
"v_add_f32 v240, v240, v200\n"
"v_add_f32 v241, v241, v204\n"
"v_mfma_f32_16x16x32_bf16 v[60:63], v[172:175], v[228:231], v[60:63]\n"
"ds_read_b128 v[156:159], %[kb] offset:43776\n"
"v_add_f32 v240, v240, v201\n"
"v_add_f32 v241, v241, v205\n"
"v_mfma_f32_16x16x32_bf16 v[32:35], v[176:179], v[224:227], v[32:35]\n"
"ds_read_b64_tr_b16 v[160:161], %[vbe] offset:32768\n"
"v_add_f32 v240, v240, v202\n"
"v_add_f32 v241, v241, v206\n"
"v_mfma_f32_16x16x32_bf16 v[64:67], v[176:179], v[228:231], v[64:67]\n"
"ds_read_b64_tr_b16 v[162:163], %[vbe] offset:40960\n"
"v_add_f32 v240, v240, v203\n"
"v_add_f32 v241, v241, v207\n"
"v_mfma_f32_16x16x32_bf16 v[36:39], v[180:183], v[224:227], v[36:39]\n"
"ds_read_b64_tr_b16 v[164:165], v243 offset:32768\n"
"v_mfma_f32_16x16x32_bf16 v[68:71], v[180:183], v[228:231], v[68:71]\n"
"ds_read_b64_tr_b16 v[166:167], v243 offset:40960\n"
"v_mfma_f32_16x16x32_bf16 v[40:43], v[184:187], v[224:227], v[40:43]\n"
"ds_read_b64_tr_b16 v[168:169], %[vbe] offset:33024\n"
"v_mfma_f32_16x16x32_bf16 v[72:75], v[184:187], v[228:231], v[72:75]\n"
"ds_read_b64_tr_b16 v[170:171], %[vbe] offset:41216\n"
"v_mfma_f32_16x16x32_bf16 v[44:47], v[188:191], v[224:227], v[44:47]\n"
"ds_read_b64_tr_b16 v[172:173], v243 offset:33024\n"
"v_mfma_f32_16x16x32_bf16 v[76:79], v[188:191], v[228:231], v[76:79]\n"
"ds_read_b64_tr_b16 v[174:175], v243 offset:41216\n"
"s_waitcnt lgkmcnt(8)\n"
"v_mfma_f32_16x16x32_bf16 v[208:211], v[144:147], v[80:83], v[220:223]\n"
"ds_read_b64_tr_b16 v[176:177], %[vbe] offset:33280\n"
"v_mfma_f32_16x16x32_bf16 v[208:211], v[148:151], v[84:87], v[208:211]\n"
"ds_read_b64_tr_b16 v[178:179], %[vbe] offset:41472\n"
"v_mfma_f32_16x16x32_bf16 v[208:211], v[152:155], v[88:91], v[208:211]\n"
"ds_read_b64_tr_b16 v[180:181], v243 offset:33280\n"
"v_mfma_f32_16x16x32_bf16 v[208:211], v[156:159], v[92:95], v[208:211]\n"
"ds_read_b64_tr_b16 v[182:183], v243 offset:41472\n"
"v_mfma_f32_16x16x32_bf16 v[192:195], v[128:131], v[96:99], v[212:215]\n"
"ds_read_b64_tr_b16 v[184:185], %[vbe] offset:33536\n"
"v_mfma_f32_16x16x32_bf16 v[192:195], v[132:135], v[100:103], v[192:195]\n"
"ds_read_b64_tr_b16 v[186:187], %[vbe] offset:41728\n"
"v_mfma_f32_16x16x32_bf16 v[192:195], v[136:139], v[104:107], v[192:195]\n"
"ds_read_b64_tr_b16 v[188:189], v243 offset:33536\n"
"v_mfma_f32_16x16x32_bf16 v[192:195], v[140:143], v[108:111], v[192:195]\n"
"ds_read_b64_tr_b16 v[190:191], v243 offset:41728\n"
"v_mfma_f32_16x16x32_bf16 v[196:199], v[128:131], v[112:115], v[216:219]\n"
"v_mfma_f32_16x16x32_bf16 v[196:199], v[132:135], v[116:119], v[196:199]\n"
"v_mfma_f32_16x16x32_bf16 v[196:199], v[136:139], v[120:123], v[196:199]\n"
"v_mfma_f32_16x16x32_bf16 v[196:199], v[140:143], v[124:127], v[196:199]\n"
"v_mfma_f32_16x16x32_bf16 v[200:203], v[144:147], v[96:99], v[212:215]\n"
"v_mfma_f32_16x16x32_bf16 v[200:203], v[148:151], v[100:103], v[200:203]\n"
"v_mfma_f32_16x16x32_bf16 v[200:203], v[152:155], v[104:107], v[200:203]\n"
"v_mfma_f32_16x16x32_bf16 v[200:203], v[156:159], v[108:111], v[200:203]\n"
"v_mfma_f32_16x16x32_bf16 v[204:207], v[144:147], v[112:115], v[216:219]\n"
"v_mfma_f32_16x16x32_bf16 v[204:207], v[148:151], v[116:119], v[204:207]\n"
"v_mfma_f32_16x16x32_bf16 v[204:207], v[152:155], v[120:123], v[204:207]\n"
"v_mfma_f32_16x16x32_bf16 v[204:207], v[156:159], v[124:127], v[204:207]\n"
"s_waitcnt lgkmcnt(0)\n"
"s_barrier\n"
"s_setprio 0\n"
"s_lshl_b32 s89, %[osoff], 2\n"
"s_add_u32 s89, s89, 108544\n"
"buffer_load_dwordx4 v249, s[76:79], s89 offen offset:2048 lds\n"
"v_exp_f32 v208, v208\n"
"v_exp_f32 v209, v209\n"
"v_exp_f32 v210, v210\n"
"v_exp_f32_e64 v211, v211\n"
"v_cvt_pk_bf16_f32 v232, v208, v209\n"
"v_cvt_pk_bf16_f32 v233, v210, v211\n"
"ds_write_b64 v247, v[232:233] offset:4104\n"
"v_exp_f32 v192, v192\n"
"v_exp_f32 v193, v193\n"
"v_exp_f32 v194, v194\n"
"v_exp_f32 v195, v195\n"
"v_cvt_pk_bf16_f32 v224, v192, v193\n"
"v_cvt_pk_bf16_f32 v225, v194, v195\n"
"v_exp_f32 v196, v196\n"
"v_exp_f32 v197, v197\n"
"v_exp_f32 v198, v198\n"
"v_exp_f32 v199, v199\n"
"v_cvt_pk_bf16_f32 v228, v196, v197\n"
"v_cvt_pk_bf16_f32 v229, v198, v199\n"
"v_exp_f32 v200, v200\n"
"v_exp_f32 v201, v201\n"
"v_exp_f32 v202, v202\n"
"v_exp_f32 v203, v203\n"
"v_cvt_pk_bf16_f32 v226, v200, v201\n"
"v_cvt_pk_bf16_f32 v227, v202, v203\n"
"v_exp_f32 v204, v204\n"
"v_exp_f32 v205, v205\n"
"v_exp_f32 v206, v206\n"
"v_exp_f32 v207, v207\n"
"v_cvt_pk_bf16_f32 v230, v204, v205\n"
"v_cvt_pk_bf16_f32 v231, v206, v207\n"
"s_waitcnt lgkmcnt(0)\n"
"s_barrier\n"
"s_setprio 1\n"
"v_mfma_f32_16x16x32_bf16 v[16:19], v[160:163], v[224:227], v[16:19]\n"
"ds_read_b128 v[128:131], v244 offset:34816\n"
"v_add_f32 v240, v240, v192\n"
"v_add_f32_e64 v241, v241, v196\n"
"v_mfma_f32_16x16x32_bf16 v[48:51], v[160:163], v[228:231], v[48:51]\n"
"ds_read_b128 v[132:135], v244 offset:35072\n"
"v_add_f32 v242, v242, v208\n"
"v_add_f32 v240, v240, v193\n"
"v_mfma_f32_16x16x32_bf16 v[20:23], v[164:167], v[224:227], v[20:23]\n"
"ds_read_b128 v[136:139], v244 offset:35328\n"
"v_add_f32 v241, v241, v197\n"
"v_add_f32 v242, v242, v209\n"
"v_mfma_f32_16x16x32_bf16 v[52:55], v[164:167], v[228:231], v[52:55]\n"
"ds_read_b128 v[140:143], v244 offset:35584\n"
"v_add_f32 v240, v240, v194\n"
"v_add_f32 v241, v241, v198\n"
"v_mfma_f32_16x16x32_bf16 v[24:27], v[168:171], v[224:227], v[24:27]\n"
"ds_read_b128 v[144:147], v244 offset:43008\n"
"v_add_f32 v242, v242, v210\n"
"v_add_f32 v240, v240, v195\n"
"v_mfma_f32_16x16x32_bf16 v[56:59], v[168:171], v[228:231], v[56:59]\n"
"ds_read_b128 v[148:151], v244 offset:43264\n"
"v_add_f32 v241, v241, v199\n"
"v_add_f32 v242, v242, v211\n"
"v_mfma_f32_16x16x32_bf16 v[28:31], v[172:175], v[224:227], v[28:31]\n"
"ds_read_b128 v[152:155], v244 offset:43520\n"
"v_add_f32 v240, v240, v200\n"
"v_add_f32 v241, v241, v204\n"
"v_mfma_f32_16x16x32_bf16 v[60:63], v[172:175], v[228:231], v[60:63]\n"
"ds_read_b128 v[156:159], v244 offset:43776\n"
"v_add_f32 v240, v240, v201\n"
"v_add_f32 v241, v241, v205\n"
"v_mfma_f32_16x16x32_bf16 v[32:35], v[176:179], v[224:227], v[32:35]\n"
"ds_read_b64_tr_b16 v[160:161], v245 offset:32768\n"
"v_add_f32 v240, v240, v202\n"
"v_add_f32 v241, v241, v206\n"
"v_mfma_f32_16x16x32_bf16 v[64:67], v[176:179], v[228:231], v[64:67]\n"
"ds_read_b64_tr_b16 v[162:163], v245 offset:40960\n"
"v_add_f32 v240, v240, v203\n"
"v_add_f32 v241, v241, v207\n"
"v_mfma_f32_16x16x32_bf16 v[36:39], v[180:183], v[224:227], v[36:39]\n"
"ds_read_b64_tr_b16 v[164:165], v246 offset:32768\n"
"v_mfma_f32_16x16x32_bf16 v[68:71], v[180:183], v[228:231], v[68:71]\n"
"ds_read_b64_tr_b16 v[166:167], v246 offset:40960\n"
"v_mfma_f32_16x16x32_bf16 v[40:43], v[184:187], v[224:227], v[40:43]\n"
"ds_read_b64_tr_b16 v[168:169], v245 offset:33024\n"
"v_mfma_f32_16x16x32_bf16 v[72:75], v[184:187], v[228:231], v[72:75]\n"
"ds_read_b64_tr_b16 v[170:171], v245 offset:41216\n"
"v_mfma_f32_16x16x32_bf16 v[44:47], v[188:191], v[224:227], v[44:47]\n"
"ds_read_b64_tr_b16 v[172:173], v246 offset:33024\n"
"v_mfma_f32_16x16x32_bf16 v[76:79], v[188:191], v[228:231], v[76:79]\n"
"ds_read_b64_tr_b16 v[174:175], v246 offset:41216\n"
"s_waitcnt lgkmcnt(8)\n"
"v_mfma_f32_16x16x32_bf16 v[208:211], v[144:147], v[80:83], v[220:223]\n"
"ds_read_b64_tr_b16 v[176:177], v245 offset:33280\n"
"v_mfma_f32_16x16x32_bf16 v[208:211], v[148:151], v[84:87], v[208:211]\n"
"ds_read_b64_tr_b16 v[178:179], v245 offset:41472\n"
"v_mfma_f32_16x16x32_bf16 v[208:211], v[152:155], v[88:91], v[208:211]\n"
"ds_read_b64_tr_b16 v[180:181], v246 offset:33280\n"
"v_mfma_f32_16x16x32_bf16 v[208:211], v[156:159], v[92:95], v[208:211]\n"
"ds_read_b64_tr_b16 v[182:183], v246 offset:41472\n"
"v_mfma_f32_16x16x32_bf16 v[192:195], v[128:131], v[96:99], v[212:215]\n"
"ds_read_b64_tr_b16 v[184:185], v245 offset:33536\n"
"v_mfma_f32_16x16x32_bf16 v[192:195], v[132:135], v[100:103], v[192:195]\n"
"ds_read_b64_tr_b16 v[186:187], v245 offset:41728\n"
"v_mfma_f32_16x16x32_bf16 v[192:195], v[136:139], v[104:107], v[192:195]\n"
"ds_read_b64_tr_b16 v[188:189], v246 offset:33536\n"
"v_mfma_f32_16x16x32_bf16 v[192:195], v[140:143], v[108:111], v[192:195]\n"
"ds_read_b64_tr_b16 v[190:191], v246 offset:41728\n"
"v_mfma_f32_16x16x32_bf16 v[196:199], v[128:131], v[112:115], v[216:219]\n"
"v_mfma_f32_16x16x32_bf16 v[196:199], v[132:135], v[116:119], v[196:199]\n"
"v_mfma_f32_16x16x32_bf16 v[196:199], v[136:139], v[120:123], v[196:199]\n"
"v_mfma_f32_16x16x32_bf16 v[196:199], v[140:143], v[124:127], v[196:199]\n"
"v_mfma_f32_16x16x32_bf16 v[200:203], v[144:147], v[96:99], v[212:215]\n"
"v_mfma_f32_16x16x32_bf16 v[200:203], v[148:151], v[100:103], v[200:203]\n"
"v_mfma_f32_16x16x32_bf16 v[200:203], v[152:155], v[104:107], v[200:203]\n"
"v_mfma_f32_16x16x32_bf16 v[200:203], v[156:159], v[108:111], v[200:203]\n"
"v_mfma_f32_16x16x32_bf16 v[204:207], v[144:147], v[112:115], v[216:219]\n"
"v_mfma_f32_16x16x32_bf16 v[204:207], v[148:151], v[116:119], v[204:207]\n"
"v_mfma_f32_16x16x32_bf16 v[204:207], v[152:155], v[120:123], v[204:207]\n"
"v_mfma_f32_16x16x32_bf16 v[204:207], v[156:159], v[124:127], v[204:207]\n"
"s_waitcnt lgkmcnt(0)\n"
"s_barrier\n"
"s_setprio 0\n"
"s_getpc_b64 s[94:95]\n"
"LMa1_%=:\n"
"s_add_u32 s94, s94, (LZZ1_%=-16384-LMa1_%=)&0xffffffff\n"
"s_addc_u32 s95, s95, (LZZ1_%=-16384-LMa1_%=)>>32\n"
"v_lshlrev_b32 v236, 4, %[l16]\n"
"s_lshr_b32 s68, %[hb], 9\n"
"v_add_u32 v236, s68, v236\n"
"s_lshl_b32 exec_lo, 0x11111111, %[wrot]\n"
"s_mov_b32 exec_hi, exec_lo\n"
"global_load_dword v253, v236, s[94:95]\n"
"s_mov_b64 exec, -1\n"
"s_lshl_b32 s88, %[os1], 2\n"
"s_mov_b32 m0, s54\n"
"s_nop 0\n"
"buffer_load_dwordx4 v249, s[76:79], s88 offen lds\n"
"buffer_load_dwordx4 v250, s[76:79], s88 offen offset:1024 lds\n"
"v_exp_f32 v208, v208\n"
"v_exp_f32 v209, v209\n"
"v_exp_f32 v210, v210\n"
"v_exp_f32_e64 v211, v211\n"
"v_cvt_pk_bf16_f32 v232, v208, v209\n"
"v_cvt_pk_bf16_f32 v233, v210, v211\n"
"ds_write_b64 v247, v[232:233] offset:4104\n"
"v_exp_f32 v192, v192\n"
"v_exp_f32 v193, v193\n"
"v_exp_f32 v194, v194\n"
"v_exp_f32 v195, v195\n"
"v_cvt_pk_bf16_f32 v224, v192, v193\n"
"v_cvt_pk_bf16_f32 v225, v194, v195\n"
"v_exp_f32 v196, v196\n"
"v_exp_f32 v197, v197\n"
"v_exp_f32 v198, v198\n"
"v_exp_f32 v199, v199\n"
"v_cvt_pk_bf16_f32 v228, v196, v197\n"
"v_cvt_pk_bf16_f32 v229, v198, v199\n"
"v_exp_f32 v200, v200\n"
"v_exp_f32 v201, v201\n"
"v_exp_f32 v202, v202\n"
"v_exp_f32 v203, v203\n"
"v_cvt_pk_bf16_f32 v226, v200, v201\n"
"v_cvt_pk_bf16_f32 v227, v202, v203\n"
"v_exp_f32 v204, v204\n"
"v_exp_f32 v205, v205\n"
"v_exp_f32 v206, v206\n"
"v_exp_f32 v207, v207\n"
"v_cvt_pk_bf16_f32 v230, v204, v205\n"
"v_cvt_pk_bf16_f32 v231, v206, v207\n"
"s_waitcnt vmcnt(9) lgkmcnt(0)\n"
"s_barrier\n"
"s_setprio 1\n"
"v_mfma_f32_16x16x32_bf16 v[16:19], v[160:163], v[224:227], v[16:19]\n"
"ds_read_b128 v[128:131], %[kb] offset:51200\n"
"v_add_f32 v240, v240, v192\n"
"v_add_f32_e64 v241, v241, v196\n"
"v_mfma_f32_16x16x32_bf16 v[48:51], v[160:163], v[228:231], v[48:51]\n"
"ds_read_b128 v[132:135], %[kb] offset:51456\n"
"v_add_f32 v242, v242, v208\n"
"v_add_f32 v240, v240, v193\n"
"v_mfma_f32_16x16x32_bf16 v[20:23], v[164:167], v[224:227], v[20:23]\n"
"ds_read_b128 v[136:139], %[kb] offset:51712\n"
"v_add_f32 v241, v241, v197\n"
"v_add_f32 v242, v242, v209\n"
"v_mfma_f32_16x16x32_bf16 v[52:55], v[164:167], v[228:231], v[52:55]\n"
"ds_read_b128 v[140:143], %[kb] offset:51968\n"
"v_add_f32 v240, v240, v194\n"
"v_add_f32 v241, v241, v198\n"
"v_mfma_f32_16x16x32_bf16 v[24:27], v[168:171], v[224:227], v[24:27]\n"
"ds_read_b128 v[144:147], %[kb] offset:59392\n"
"v_add_f32 v242, v242, v210\n"
"v_add_f32 v240, v240, v195\n"
"v_mfma_f32_16x16x32_bf16 v[56:59], v[168:171], v[228:231], v[56:59]\n"
"ds_read_b128 v[148:151], %[kb] offset:59648\n"
"v_add_f32 v241, v241, v199\n"
"v_add_f32 v242, v242, v211\n"
"v_mfma_f32_16x16x32_bf16 v[28:31], v[172:175], v[224:227], v[28:31]\n"
"ds_read_b128 v[152:155], %[kb] offset:59904\n"
"v_add_f32 v240, v240, v200\n"
"v_add_f32 v241, v241, v204\n"
"v_mfma_f32_16x16x32_bf16 v[60:63], v[172:175], v[228:231], v[60:63]\n"
"ds_read_b128 v[156:159], %[kb] offset:60160\n"
"v_add_f32 v240, v240, v201\n"
"v_add_f32 v241, v241, v205\n"
"v_mfma_f32_16x16x32_bf16 v[32:35], v[176:179], v[224:227], v[32:35]\n"
"ds_read_b64_tr_b16 v[160:161], %[vbe] offset:49152\n"
"v_add_f32 v240, v240, v202\n"
"v_add_f32 v241, v241, v206\n"
"v_mfma_f32_16x16x32_bf16 v[64:67], v[176:179], v[228:231], v[64:67]\n"
"ds_read_b64_tr_b16 v[162:163], %[vbe] offset:57344\n"
"v_add_f32 v240, v240, v203\n"
"v_add_f32 v241, v241, v207\n"
"v_mfma_f32_16x16x32_bf16 v[36:39], v[180:183], v[224:227], v[36:39]\n"
"ds_read_b64_tr_b16 v[164:165], v243 offset:49152\n"
"v_mfma_f32_16x16x32_bf16 v[68:71], v[180:183], v[228:231], v[68:71]\n"
"ds_read_b64_tr_b16 v[166:167], v243 offset:57344\n"
"v_mfma_f32_16x16x32_bf16 v[40:43], v[184:187], v[224:227], v[40:43]\n"
"ds_read_b64_tr_b16 v[168:169], %[vbe] offset:49408\n"
"v_mfma_f32_16x16x32_bf16 v[72:75], v[184:187], v[228:231], v[72:75]\n"
"ds_read_b64_tr_b16 v[170:171], %[vbe] offset:57600\n"
"v_mfma_f32_16x16x32_bf16 v[44:47], v[188:191], v[224:227], v[44:47]\n"
"ds_read_b64_tr_b16 v[172:173], v243 offset:49408\n"
"v_mfma_f32_16x16x32_bf16 v[76:79], v[188:191], v[228:231], v[76:79]\n"
"ds_read_b64_tr_b16 v[174:175], v243 offset:57600\n"
"s_waitcnt lgkmcnt(8)\n"
"v_mfma_f32_16x16x32_bf16 v[208:211], v[144:147], v[80:83], v[220:223]\n"
"ds_read_b64_tr_b16 v[176:177], %[vbe] offset:49664\n"
"v_mfma_f32_16x16x32_bf16 v[208:211], v[148:151], v[84:87], v[208:211]\n"
"ds_read_b64_tr_b16 v[178:179], %[vbe] offset:57856\n"
"v_mfma_f32_16x16x32_bf16 v[208:211], v[152:155], v[88:91], v[208:211]\n"
"ds_read_b64_tr_b16 v[180:181], v243 offset:49664\n"
"v_mfma_f32_16x16x32_bf16 v[208:211], v[156:159], v[92:95], v[208:211]\n"
"ds_read_b64_tr_b16 v[182:183], v243 offset:57856\n"
"v_mfma_f32_16x16x32_bf16 v[192:195], v[128:131], v[96:99], v[212:215]\n"
"ds_read_b64_tr_b16 v[184:185], %[vbe] offset:49920\n"
"v_mfma_f32_16x16x32_bf16 v[192:195], v[132:135], v[100:103], v[192:195]\n"
"ds_read_b64_tr_b16 v[186:187], %[vbe] offset:58112\n"
"v_mfma_f32_16x16x32_bf16 v[192:195], v[136:139], v[104:107], v[192:195]\n"
"ds_read_b64_tr_b16 v[188:189], v243 offset:49920\n"
"v_mfma_f32_16x16x32_bf16 v[192:195], v[140:143], v[108:111], v[192:195]\n"
"ds_read_b64_tr_b16 v[190:191], v243 offset:58112\n"
"v_mfma_f32_16x16x32_bf16 v[196:199], v[128:131], v[112:115], v[216:219]\n"
"v_mfma_f32_16x16x32_bf16 v[196:199], v[132:135], v[116:119], v[196:199]\n"
"v_mfma_f32_16x16x32_bf16 v[196:199], v[136:139], v[120:123], v[196:199]\n"
"v_mfma_f32_16x16x32_bf16 v[196:199], v[140:143], v[124:127], v[196:199]\n"
"v_mfma_f32_16x16x32_bf16 v[200:203], v[144:147], v[96:99], v[212:215]\n"
"v_mfma_f32_16x16x32_bf16 v[200:203], v[148:151], v[100:103], v[200:203]\n"
"v_mfma_f32_16x16x32_bf16 v[200:203], v[152:155], v[104:107], v[200:203]\n"
"v_mfma_f32_16x16x32_bf16 v[200:203], v[156:159], v[108:111], v[200:203]\n"
"v_mfma_f32_16x16x32_bf16 v[204:207], v[144:147], v[112:115], v[216:219]\n"
"v_mfma_f32_16x16x32_bf16 v[204:207], v[148:151], v[116:119], v[204:207]\n"
"v_mfma_f32_16x16x32_bf16 v[204:207], v[152:155], v[120:123], v[204:207]\n"
"v_mfma_f32_16x16x32_bf16 v[204:207], v[156:159], v[124:127], v[204:207]\n"
"s_waitcnt lgkmcnt(0)\n"
"s_barrier\n"
"s_setprio 0\n"
"s_lshl_b32 s89, %[os1], 2\n"
"s_add_u32 s89, s89, 34816\n"
"buffer_load_dwordx4 v249, s[76:79], s89 offen offset:2048 lds\n"
"s_add_u32 m0, s90, 3072\n"
"s_nop 0\n"
"buffer_load_dwordx4 v252, s[76:79], s89 offen offset:2048 lds\n"
"v_exp_f32 v208, v208\n"
"v_exp_f32 v209, v209\n"
"v_exp_f32 v210, v210\n"
"v_exp_f32 v211, v211\n"
"v_cvt_pk_bf16_f32 v232, v208, v209\n"
"v_cvt_pk_bf16_f32 v233, v210, v211\n"
"ds_write_b64 v247, v[232:233] offset:4104\n"
"v_exp_f32 v192, v192\n"
"v_exp_f32 v193, v193\n"
"v_exp_f32 v194, v194\n"
"v_exp_f32 v195, v195\n"
"v_cvt_pk_bf16_f32 v224, v192, v193\n"
"v_cvt_pk_bf16_f32 v225, v194, v195\n"
"v_exp_f32 v196, v196\n"
"v_exp_f32 v197, v197\n"
"v_exp_f32 v198, v198\n"
"v_exp_f32 v199, v199\n"
"v_cvt_pk_bf16_f32 v228, v196, v197\n"
"v_cvt_pk_bf16_f32 v229, v198, v199\n"
"v_exp_f32 v200, v200\n"
"v_exp_f32 v201, v201\n"
"v_exp_f32 v202, v202\n"
"v_exp_f32 v203, v203\n"
"v_cvt_pk_bf16_f32 v226, v200, v201\n"
"v_cvt_pk_bf16_f32 v227, v202, v203\n"
"v_exp_f32 v204, v204\n"
"v_exp_f32 v205, v205\n"
"v_exp_f32 v206, v206\n"
"v_exp_f32 v207, v207\n"
"v_cvt_pk_bf16_f32 v230, v204, v205\n"
"v_cvt_pk_bf16_f32 v231, v206, v207\n"
"s_waitcnt lgkmcnt(0)\n"
"s_barrier\n"
"s_setprio 1\n"
"v_mfma_f32_16x16x32_bf16 v[16:19], v[160:163], v[224:227], v[16:19]\n"
"ds_read_b128 v[128:131], v244 offset:51200\n"
"v_add_f32 v240, v240, v192\n"
"v_add_f32_e64 v241, v241, v196\n"
"v_mfma_f32_16x16x32_bf16 v[48:51], v[160:163], v[228:231], v[48:51]\n"
"ds_read_b128 v[132:135], v244 offset:51456\n"
"v_add_f32 v242, v242, v208\n"
"v_add_f32 v240, v240, v193\n"
"v_mfma_f32_16x16x32_bf16 v[20:23], v[164:167], v[224:227], v[20:23]\n"
"ds_read_b128 v[136:139], v244 offset:51712\n"
"v_add_f32 v241, v241, v197\n"
"v_add_f32 v242, v242, v209\n"
"v_mfma_f32_16x16x32_bf16 v[52:55], v[164:167], v[228:231], v[52:55]\n"
"ds_read_b128 v[140:143], v244 offset:51968\n"
"v_add_f32 v240, v240, v194\n"
"v_add_f32 v241, v241, v198\n"
"v_mfma_f32_16x16x32_bf16 v[24:27], v[168:171], v[224:227], v[24:27]\n"
"ds_read_b128 v[144:147], v244 offset:59392\n"
"v_add_f32 v242, v242, v210\n"
"v_add_f32 v240, v240, v195\n"
"v_mfma_f32_16x16x32_bf16 v[56:59], v[168:171], v[228:231], v[56:59]\n"
"ds_read_b128 v[148:151], v244 offset:59648\n"
"v_add_f32 v241, v241, v199\n"
"v_add_f32 v242, v242, v211\n"
"v_mfma_f32_16x16x32_bf16 v[28:31], v[172:175], v[224:227], v[28:31]\n"
"ds_read_b128 v[152:155], v244 offset:59904\n"
"v_add_f32 v240, v240, v200\n"
"v_add_f32 v241, v241, v204\n"
"v_mfma_f32_16x16x32_bf16 v[60:63], v[172:175], v[228:231], v[60:63]\n"
"ds_read_b128 v[156:159], v244 offset:60160\n"
"v_add_f32 v240, v240, v201\n"
"v_add_f32 v241, v241, v205\n"
"v_mfma_f32_16x16x32_bf16 v[32:35], v[176:179], v[224:227], v[32:35]\n"
"ds_read_b64_tr_b16 v[160:161], v245 offset:49152\n"
"v_add_f32 v240, v240, v202\n"
"v_add_f32 v241, v241, v206\n"
"v_mfma_f32_16x16x32_bf16 v[64:67], v[176:179], v[228:231], v[64:67]\n"
"ds_read_b64_tr_b16 v[162:163], v245 offset:57344\n"
"v_add_f32 v240, v240, v203\n"
"v_add_f32 v241, v241, v207\n"
"v_mfma_f32_16x16x32_bf16 v[36:39], v[180:183], v[224:227], v[36:39]\n"
"ds_read_b64_tr_b16 v[164:165], v246 offset:49152\n"
"v_mfma_f32_16x16x32_bf16 v[68:71], v[180:183], v[228:231], v[68:71]\n"
"ds_read_b64_tr_b16 v[166:167], v246 offset:57344\n"
"v_mfma_f32_16x16x32_bf16 v[40:43], v[184:187], v[224:227], v[40:43]\n"
"ds_read_b64_tr_b16 v[168:169], v245 offset:49408\n"
"v_mfma_f32_16x16x32_bf16 v[72:75], v[184:187], v[228:231], v[72:75]\n"
"ds_read_b64_tr_b16 v[170:171], v245 offset:57600\n"
"v_mfma_f32_16x16x32_bf16 v[44:47], v[188:191], v[224:227], v[44:47]\n"
"ds_read_b64_tr_b16 v[172:173], v246 offset:49408\n"
"v_mfma_f32_16x16x32_bf16 v[76:79], v[188:191], v[228:231], v[76:79]\n"
"ds_read_b64_tr_b16 v[174:175], v246 offset:57600\n"
"s_waitcnt lgkmcnt(8)\n"
"v_mfma_f32_16x16x32_bf16 v[208:211], v[144:147], v[80:83], v[220:223]\n"
"ds_read_b64_tr_b16 v[176:177], v245 offset:49664\n"
"v_mfma_f32_16x16x32_bf16 v[208:211], v[148:151], v[84:87], v[208:211]\n"
"ds_read_b64_tr_b16 v[178:179], v245 offset:57856\n"
"v_mfma_f32_16x16x32_bf16 v[208:211], v[152:155], v[88:91], v[208:211]\n"
"ds_read_b64_tr_b16 v[180:181], v246 offset:49664\n"
"v_mfma_f32_16x16x32_bf16 v[208:211], v[156:159], v[92:95], v[208:211]\n"
"ds_read_b64_tr_b16 v[182:183], v246 offset:57856\n"
"v_mfma_f32_16x16x32_bf16 v[192:195], v[128:131], v[96:99], v[212:215]\n"
"ds_read_b64_tr_b16 v[184:185], v245 offset:49920\n"
"v_mfma_f32_16x16x32_bf16 v[192:195], v[132:135], v[100:103], v[192:195]\n"
"ds_read_b64_tr_b16 v[186:187], v245 offset:58112\n"
"v_mfma_f32_16x16x32_bf16 v[192:195], v[136:139], v[104:107], v[192:195]\n"
"ds_read_b64_tr_b16 v[188:189], v246 offset:49920\n"
"v_mfma_f32_16x16x32_bf16 v[192:195], v[140:143], v[108:111], v[192:195]\n"
"ds_read_b64_tr_b16 v[190:191], v246 offset:58112\n"
"v_mfma_f32_16x16x32_bf16 v[196:199], v[128:131], v[112:115], v[216:219]\n"
"v_mfma_f32_16x16x32_bf16 v[196:199], v[132:135], v[116:119], v[196:199]\n"
"v_mfma_f32_16x16x32_bf16 v[196:199], v[136:139], v[120:123], v[196:199]\n"
"v_mfma_f32_16x16x32_bf16 v[196:199], v[140:143], v[124:127], v[196:199]\n"
"v_mfma_f32_16x16x32_bf16 v[200:203], v[144:147], v[96:99], v[212:215]\n"
"v_mfma_f32_16x16x32_bf16 v[200:203], v[148:151], v[100:103], v[200:203]\n"
"v_mfma_f32_16x16x32_bf16 v[200:203], v[152:155], v[104:107], v[200:203]\n"
"v_mfma_f32_16x16x32_bf16 v[200:203], v[156:159], v[108:111], v[200:203]\n"
"v_mfma_f32_16x16x32_bf16 v[204:207], v[144:147], v[112:115], v[216:219]\n"
"v_mfma_f32_16x16x32_bf16 v[204:207], v[148:151], v[116:119], v[204:207]\n"
"v_mfma_f32_16x16x32_bf16 v[204:207], v[152:155], v[120:123], v[204:207]\n"
"v_mfma_f32_16x16x32_bf16 v[204:207], v[156:159], v[124:127], v[204:207]\n"
"s_waitcnt lgkmcnt(0)\n"
"s_barrier\n"
"s_setprio 0\n"
"LT1_%=:\n"
"s_lshl_b32 s88, %[os1], 2\n"
"s_add_u32 s88, s88, 73728\n"
"s_mov_b32 m0, s55\n"
"s_nop 0\n"
"buffer_load_dwordx4 v249, s[76:79], s88 offen lds\n"
"buffer_load_dwordx4 v250, s[76:79], s88 offen offset:1024 lds\n"
"s_lshl_b32 s89, %[os1], 2\n"
"s_add_u32 s89, s89, 108544\n"
"buffer_load_dwordx4 v249, s[76:79], s89 offen offset:2048 lds\n"
"v_exp_f32 v208, v208\n"
"v_exp_f32 v209, v209\n"
"v_exp_f32 v210, v210\n"
"v_exp_f32 v211, v211\n"
"v_cvt_pk_bf16_f32 v232, v208, v209\n"
"v_cvt_pk_bf16_f32 v233, v210, v211\n"
"ds_write_b64 v247, v[232:233] offset:4104\n"
"v_exp_f32 v192, v192\n"
"v_add_f32 v242, v242, v208\n"
"v_exp_f32 v193, v193\n"
"v_add_f32 v242, v242, v209\n"
"v_exp_f32 v194, v194\n"
"v_add_f32 v242, v242, v210\n"
"v_exp_f32 v195, v195\n"
"v_add_f32 v242, v242, v211\n"
"v_cvt_pk_bf16_f32 v224, v192, v193\n"
"v_cvt_pk_bf16_f32 v225, v194, v195\n"
"v_exp_f32 v196, v196\n"
"v_mov_b32 v98, v242\n"
"v_exp_f32 v197, v197\n"
"s_nop 0\n"
"v_permlane32_swap_b32 v242, v98\n"
"v_exp_f32 v198, v198\n"
"v_add_f32 v242, v242, v98\n"
"v_exp_f32 v199, v199\n"
"v_cvt_pk_bf16_f32 v228, v196, v197\n"
"v_cvt_pk_bf16_f32 v229, v198, v199\n"
"v_exp_f32 v200, v200\n"
"v_mov_b32 v98, v242\n"
"v_exp_f32 v201, v201\n"
"s_nop 0\n"
"v_permlane16_swap_b32 v242, v98\n"
"v_exp_f32 v202, v202\n"
"v_add_f32 v242, v242, v98\n"
"v_exp_f32 v203, v203\n"
"v_cvt_pk_bf16_f32 v226, v200, v201\n"
"v_cvt_pk_bf16_f32 v227, v202, v203\n"
"ds_write_b32 v247, v242 offset:5124\n"
"v_exp_f32 v204, v204\n"
"v_exp_f32 v205, v205\n"
"v_exp_f32 v206, v206\n"
"v_exp_f32 v207, v207\n"
"v_cvt_pk_bf16_f32 v230, v204, v205\n"
"v_cvt_pk_bf16_f32 v231, v206, v207\n"
"s_waitcnt vmcnt(7) lgkmcnt(0)\n"
"s_barrier\n"
"s_setprio 1\n"
"v_mfma_f32_16x16x32_bf16 v[16:19], v[160:163], v[224:227], v[16:19]\n"
"v_add_f32 v240, v240, v192\n"
"v_add_f32_e64 v241, v241, v196\n"
"v_mfma_f32_16x16x32_bf16 v[48:51], v[160:163], v[228:231], v[48:51]\n"
"v_add_f32 v240, v240, v193\n"
"v_add_f32 v241, v241, v197\n"
"v_mfma_f32_16x16x32_bf16 v[20:23], v[164:167], v[224:227], v[20:23]\n"
"v_add_f32 v240, v240, v194\n"
"v_add_f32 v241, v241, v198\n"
"v_mfma_f32_16x16x32_bf16 v[52:55], v[164:167], v[228:231], v[52:55]\n"
"v_add_f32 v240, v240, v195\n"
"v_add_f32 v241, v241, v199\n"
"v_mfma_f32_16x16x32_bf16 v[24:27], v[168:171], v[224:227], v[24:27]\n"
"v_add_f32 v240, v240, v200\n"
"v_add_f32 v241, v241, v204\n"
"v_mfma_f32_16x16x32_bf16 v[56:59], v[168:171], v[228:231], v[56:59]\n"
"v_add_f32 v240, v240, v201\n"
"v_add_f32 v241, v241, v205\n"
"v_mfma_f32_16x16x32_bf16 v[28:31], v[172:175], v[224:227], v[28:31]\n"
"v_add_f32 v240, v240, v202\n"
"v_add_f32 v241, v241, v206\n"
"v_mfma_f32_16x16x32_bf16 v[60:63], v[172:175], v[228:231], v[60:63]\n"
"v_add_f32 v240, v240, v203\n"
"v_add_f32 v241, v241, v207\n"
"v_mfma_f32_16x16x32_bf16 v[32:35], v[176:179], v[224:227], v[32:35]\n"
"v_mfma_f32_16x16x32_bf16 v[64:67], v[176:179], v[228:231], v[64:67]\n"
"v_mfma_f32_16x16x32_bf16 v[36:39], v[180:183], v[224:227], v[36:39]\n"
"v_mfma_f32_16x16x32_bf16 v[68:71], v[180:183], v[228:231], v[68:71]\n"
"v_mfma_f32_16x16x32_bf16 v[40:43], v[184:187], v[224:227], v[40:43]\n"
"v_mfma_f32_16x16x32_bf16 v[72:75], v[184:187], v[228:231], v[72:75]\n"
"v_mfma_f32_16x16x32_bf16 v[44:47], v[188:191], v[224:227], v[44:47]\n"
"v_mfma_f32_16x16x32_bf16 v[76:79], v[188:191], v[228:231], v[76:79]\n"
"s_setprio 0\n"
"s_cmp_lg_u32 %[hb], 0\n"
"s_cbranch_scc1 LG1_%=\n"
"s_barrier\n"
"LG1_%=:\n"
"v_mov_b32 v206, v240\n"
"v_mov_b32 v207, v241\n"
"s_nop 0\n"
"v_permlane32_swap_b32 v240, v206\n"
"v_permlane32_swap_b32 v241, v207\n"
"v_add_f32 v240, v240, v206\n"
"v_add_f32 v241, v241, v207\n"
"v_mov_b32 v206, v240\n"
"v_mov_b32 v207, v241\n"
"s_nop 0\n"
"v_permlane16_swap_b32 v240, v206\n"
"v_permlane16_swap_b32 v241, v207\n"
"v_add_f32 v240, v240, v206\n"
"v_add_f32 v241, v241, v207\n"
"v_rcp_f32 v192, v240\n"
"v_rcp_f32 v194, v241\n"
"s_lshr_b32 s68, %[hb], 11\n"
"s_or_b32 s68, s68, %[hb]\n"
"v_and_b32 v244, 0xffff, %[vbe]\n"
"v_xor_b32 v244, s68, v244\n"
"v_xor_b32 v245, 32, v244\n"
"v_lshrrev_b32 v246, 1, %[l16]\n"
"v_add_u32 v246, s90, v246\n"
"v_add_u32 v228, 6144, %[l16]\n"
"s_add_u32 s60, %[osoff], 2048\n"
"s_add_u32 s61, %[osoff], 11264\n"
"s_add_u32 s62, %[osoff], 20480\n"
"s_add_u32 s63, %[osoff], 29696\n"
"v_pk_mul_f32 v[16:17], v[16:17], v[192:193] op_sel_hi:[1,0]\n"
"v_pk_mul_f32 v[18:19], v[18:19], v[192:193] op_sel_hi:[1,0]\n"
"v_pk_mul_f32 v[20:21], v[20:21], v[192:193] op_sel_hi:[1,0]\n"
"v_pk_mul_f32 v[22:23], v[22:23], v[192:193] op_sel_hi:[1,0]\n"
"v_cvt_pk_bf16_f32 v128, v16, v17\n"
"v_cvt_pk_bf16_f32 v129, v18, v19\n"
"v_cvt_pk_bf16_f32 v130, v20, v21\n"
"v_cvt_pk_bf16_f32 v131, v22, v23\n"
"buffer_store_dwordx4 v[128:131], %[l16], %[srdo], s60 offen sc1\n"
"v_pk_mul_f32 v[24:25], v[24:25], v[192:193] op_sel_hi:[1,0]\n"
"v_pk_mul_f32 v[26:27], v[26:27], v[192:193] op_sel_hi:[1,0]\n"
"v_pk_mul_f32 v[28:29], v[28:29], v[192:193] op_sel_hi:[1,0]\n"
"v_pk_mul_f32 v[30:31], v[30:31], v[192:193] op_sel_hi:[1,0]\n"
"v_cvt_pk_bf16_f32 v132, v24, v25\n"
"v_cvt_pk_bf16_f32 v133, v26, v27\n"
"v_cvt_pk_bf16_f32 v134, v28, v29\n"
"v_cvt_pk_bf16_f32 v135, v30, v31\n"
"buffer_store_dwordx4 v[132:135], %[l16], %[srdo], s61 offen sc1\n"
"v_pk_mul_f32 v[32:33], v[32:33], v[192:193] op_sel_hi:[1,0]\n"
"v_pk_mul_f32 v[34:35], v[34:35], v[192:193] op_sel_hi:[1,0]\n"
"v_pk_mul_f32 v[36:37], v[36:37], v[192:193] op_sel_hi:[1,0]\n"
"v_pk_mul_f32 v[38:39], v[38:39], v[192:193] op_sel_hi:[1,0]\n"
"v_cvt_pk_bf16_f32 v136, v32, v33\n"
"v_cvt_pk_bf16_f32 v137, v34, v35\n"
"v_cvt_pk_bf16_f32 v138, v36, v37\n"
"v_cvt_pk_bf16_f32 v139, v38, v39\n"
"buffer_store_dwordx4 v[136:139], %[l16], %[srdo], s62 offen sc1\n"
"v_pk_mul_f32 v[40:41], v[40:41], v[192:193] op_sel_hi:[1,0]\n"
"v_pk_mul_f32 v[42:43], v[42:43], v[192:193] op_sel_hi:[1,0]\n"
"v_pk_mul_f32 v[44:45], v[44:45], v[192:193] op_sel_hi:[1,0]\n"
"v_pk_mul_f32 v[46:47], v[46:47], v[192:193] op_sel_hi:[1,0]\n"
"v_cvt_pk_bf16_f32 v140, v40, v41\n"
"v_cvt_pk_bf16_f32 v141, v42, v43\n"
"v_cvt_pk_bf16_f32 v142, v44, v45\n"
"v_cvt_pk_bf16_f32 v143, v46, v47\n"
"buffer_store_dwordx4 v[140:143], %[l16], %[srdo], s63 offen sc1\n"
"v_pk_mul_f32 v[48:49], v[48:49], v[194:195] op_sel_hi:[1,0]\n"
"v_pk_mul_f32 v[50:51], v[50:51], v[194:195] op_sel_hi:[1,0]\n"
"v_pk_mul_f32 v[52:53], v[52:53], v[194:195] op_sel_hi:[1,0]\n"
"v_pk_mul_f32 v[54:55], v[54:55], v[194:195] op_sel_hi:[1,0]\n"
"v_cvt_pk_bf16_f32 v144, v48, v49\n"
"v_cvt_pk_bf16_f32 v145, v50, v51\n"
"v_cvt_pk_bf16_f32 v146, v52, v53\n"
"v_cvt_pk_bf16_f32 v147, v54, v55\n"
"buffer_store_dwordx4 v[144:147], %[l16], %[srdo], s60 offen offset:1024 sc1\n"
"v_pk_mul_f32 v[56:57], v[56:57], v[194:195] op_sel_hi:[1,0]\n"
"v_pk_mul_f32 v[58:59], v[58:59], v[194:195] op_sel_hi:[1,0]\n"
"v_pk_mul_f32 v[60:61], v[60:61], v[194:195] op_sel_hi:[1,0]\n"
"v_pk_mul_f32 v[62:63], v[62:63], v[194:195] op_sel_hi:[1,0]\n"
"v_cvt_pk_bf16_f32 v148, v56, v57\n"
"v_cvt_pk_bf16_f32 v149, v58, v59\n"
"v_cvt_pk_bf16_f32 v150, v60, v61\n"
"v_cvt_pk_bf16_f32 v151, v62, v63\n"
"buffer_store_dwordx4 v[148:151], %[l16], %[srdo], s61 offen offset:1024 sc1\n"
"v_pk_mul_f32 v[64:65], v[64:65], v[194:195] op_sel_hi:[1,0]\n"
"v_pk_mul_f32 v[66:67], v[66:67], v[194:195] op_sel_hi:[1,0]\n"
"v_pk_mul_f32 v[68:69], v[68:69], v[194:195] op_sel_hi:[1,0]\n"
"v_pk_mul_f32 v[70:71], v[70:71], v[194:195] op_sel_hi:[1,0]\n"
"v_cvt_pk_bf16_f32 v152, v64, v65\n"
"v_cvt_pk_bf16_f32 v153, v66, v67\n"
"v_cvt_pk_bf16_f32 v154, v68, v69\n"
"v_cvt_pk_bf16_f32 v155, v70, v71\n"
"buffer_store_dwordx4 v[152:155], %[l16], %[srdo], s62 offen offset:1024 sc1\n"
"v_pk_mul_f32 v[72:73], v[72:73], v[194:195] op_sel_hi:[1,0]\n"
"v_pk_mul_f32 v[74:75], v[74:75], v[194:195] op_sel_hi:[1,0]\n"
"v_pk_mul_f32 v[76:77], v[76:77], v[194:195] op_sel_hi:[1,0]\n"
"v_pk_mul_f32 v[78:79], v[78:79], v[194:195] op_sel_hi:[1,0]\n"
"v_cvt_pk_bf16_f32 v156, v72, v73\n"
"v_cvt_pk_bf16_f32 v157, v74, v75\n"
"v_cvt_pk_bf16_f32 v158, v76, v77\n"
"v_cvt_pk_bf16_f32 v159, v78, v79\n"
"buffer_store_dwordx4 v[156:159], %[l16], %[srdo], s63 offen offset:1024 sc1\n"
"v_max_f32 v202, v240, v241\n"
"v_add_f32 v200, v240, v241\n"
"v_max_f32 v202, v202, v242\n"
"v_add_f32 v201, v242, v242\n"
"v_pk_add_f32 v[200:201], v[200:201], v[16:17]\n"
"ds_read_b64_tr_b16 v[100:101], v244 offset:0\n"
"v_pk_add_f32 v[200:201], v[200:201], v[18:19]\n"
"v_pk_add_f32 v[200:201], v[200:201], v[20:21]\n"
"ds_read_b64_tr_b16 v[102:103], v244 offset:8192\n"
"v_pk_add_f32 v[200:201], v[200:201], v[22:23]\n"
"v_pk_add_f32 v[200:201], v[200:201], v[24:25]\n"
"ds_read_b64_tr_b16 v[104:105], v245 offset:0\n"
"v_pk_add_f32 v[200:201], v[200:201], v[26:27]\n"
"v_pk_add_f32 v[200:201], v[200:201], v[28:29]\n"
"ds_read_b64_tr_b16 v[106:107], v245 offset:8192\n"
"v_pk_add_f32 v[200:201], v[200:201], v[30:31]\n"
"v_pk_add_f32 v[200:201], v[200:201], v[32:33]\n"
"ds_read_b64_tr_b16 v[108:109], v244 offset:256\n"
"v_pk_add_f32 v[200:201], v[200:201], v[34:35]\n"
"v_pk_add_f32 v[200:201], v[200:201], v[36:37]\n"
"ds_read_b64_tr_b16 v[110:111], v244 offset:8448\n"
"v_pk_add_f32 v[200:201], v[200:201], v[38:39]\n"
"v_pk_add_f32 v[200:201], v[200:201], v[40:41]\n"
"ds_read_b64_tr_b16 v[112:113], v245 offset:256\n"
"v_pk_add_f32 v[200:201], v[200:201], v[42:43]\n"
"v_pk_add_f32 v[200:201], v[200:201], v[44:45]\n"
"ds_read_b64_tr_b16 v[114:115], v245 offset:8448\n"
"v_pk_add_f32 v[200:201], v[200:201], v[46:47]\n"
"v_pk_add_f32 v[200:201], v[200:201], v[48:49]\n"
"ds_read_b64_tr_b16 v[116:117], v244 offset:512\n"
"v_pk_add_f32 v[200:201], v[200:201], v[50:51]\n"
"v_pk_add_f32 v[200:201], v[200:201], v[52:53]\n"
"ds_read_b64_tr_b16 v[118:119], v244 offset:8704\n"
"v_pk_add_f32 v[200:201], v[200:201], v[54:55]\n"
"v_pk_add_f32 v[200:201], v[200:201], v[56:57]\n"
"ds_read_b64_tr_b16 v[120:121], v245 offset:512\n"
"v_pk_add_f32 v[200:201], v[200:201], v[58:59]\n"
"v_pk_add_f32 v[200:201], v[200:201], v[60:61]\n"
"ds_read_b64_tr_b16 v[122:123], v245 offset:8704\n"
"v_pk_add_f32 v[200:201], v[200:201], v[62:63]\n"
"v_pk_add_f32 v[200:201], v[200:201], v[64:65]\n"
"ds_read_b64_tr_b16 v[124:125], v244 offset:768\n"
"v_pk_add_f32 v[200:201], v[200:201], v[66:67]\n"
"v_pk_add_f32 v[200:201], v[200:201], v[68:69]\n"
"ds_read_b64_tr_b16 v[126:127], v244 offset:8960\n"
"v_pk_add_f32 v[200:201], v[200:201], v[70:71]\n"
"v_pk_add_f32 v[200:201], v[200:201], v[72:73]\n"
"v_pk_add_f32 v[200:201], v[200:201], v[74:75]\n"
"v_pk_add_f32 v[200:201], v[200:201], v[76:77]\n"
"v_pk_add_f32 v[200:201], v[200:201], v[78:79]\n"
"v_add_f32_e64 v200, v200, v201\n"
"v_mov_b32 v203, 0x207\n"
"v_mov_b32 v205, 0x71800000\n"
"v_xor_b32 v204, 8192, v247\n"
"v_cmp_class_f32 s[68:69], v200, v203\n"
"v_cmp_nlt_f32 vcc, v202, v205\n"
"s_nop 4\n"
"s_or_b64 vcc, vcc, s[68:69]\n"
"s_nop 1\n"
"v_cndmask_b32 v232, 0, 1, vcc\n"
"ds_write_b32 v247, v232 offset:6148\n"
"s_waitcnt lgkmcnt(0)\n"
"s_barrier\n"
"ds_read_b128 v[160:163], v247 offset:0\n"
"ds_read_b128 v[164:167], v247 offset:1024\n"
"ds_read_b128 v[168:171], v247 offset:2048\n"
"ds_read_b128 v[172:175], v247 offset:3072\n"
"ds_read_b128 v[176:179], v247 offset:6144\n"
"ds_read_b128 v[180:183], v204 offset:6144\n"
"LO1_%=:\n"
"v_mfma_f32_16x16x32_bf16 v[52:55], v[100:103], v[144:147], 0\n"
"ds_read_b64_tr_b16 v[224:225], v245 offset:768\n"
"v_mfma_f32_16x16x32_bf16 v[16:19], v[100:103], v[128:131], 0\n"
"ds_read_b64_tr_b16 v[226:227], v245 offset:8960\n"
"v_mfma_f32_16x16x32_bf16 v[56:59], v[104:107], v[144:147], 0\n"
"ds_read_b64_tr_b16 v[100:101], v246 offset:0\n"
"v_mfma_f32_16x16x32_bf16 v[20:23], v[104:107], v[128:131], 0\n"
"ds_read_b64_tr_b16 v[102:103], v246 offset:512\n"
"v_mfma_f32_16x16x32_bf16 v[60:63], v[108:111], v[144:147], 0\n"
"ds_read_b64_tr_b16 v[104:105], v244 offset:2048\n"
"v_mfma_f32_16x16x32_bf16 v[24:27], v[108:111], v[128:131], 0\n"
"ds_read_b64_tr_b16 v[106:107], v244 offset:10240\n"
"s_waitcnt lgkmcnt(11)\n"
"v_mfma_f32_16x16x32_bf16 v[88:91], v[108:111], v[160:163], 0\n"
"s_waitcnt lgkmcnt(7)\n"
"v_or3_b32 v176, v176, v177, v178\n"
"v_mfma_f32_16x16x32_bf16 v[64:67], v[112:115], v[144:147], 0\n"
"s_waitcnt lgkmcnt(6)\n"
"v_or3_b32 v180, v180, v181, v182\n"
"ds_read_b64_tr_b16 v[108:109], v245 offset:2048\n"
"v_mfma_f32_16x16x32_bf16 v[28:31], v[112:115], v[128:131], 0\n"
"v_or3_b32 v176, v176, v179, v183\n"
"ds_read_b64_tr_b16 v[110:111], v245 offset:10240\n"
"v_mfma_f32_16x16x32_bf16 v[92:95], v[112:115], v[160:163], 0\n"
"v_or_b32 v176, v176, v180\n"
"v_mfma_f32_16x16x32_bf16 v[68:71], v[116:119], v[144:147], 0\n"
"v_cmp_ne_u32 vcc, 0, v176\n"
"ds_read_b64_tr_b16 v[112:113], v244 offset:2304\n"
"v_mfma_f32_16x16x32_bf16 v[32:35], v[116:119], v[128:131], 0\n"
"ds_read_b64_tr_b16 v[114:115], v244 offset:10496\n"
"v_mfma_f32_16x16x32_bf16 v[72:75], v[120:123], v[144:147], 0\n"
"ds_read_b64_tr_b16 v[116:117], v245 offset:2304\n"
"v_mfma_f32_16x16x32_bf16 v[36:39], v[120:123], v[128:131], 0\n"
"ds_read_b64_tr_b16 v[118:119], v245 offset:10496\n"
"v_mfma_f32_16x16x32_bf16 v[76:79], v[124:127], v[144:147], 0\n"
"ds_read_b64_tr_b16 v[120:121], v244 offset:2560\n"
"v_mfma_f32_16x16x32_bf16 v[40:43], v[124:127], v[128:131], 0\n"
"ds_read_b64_tr_b16 v[122:123], v244 offset:10752\n"
"s_waitcnt lgkmcnt(12)\n"
"v_mfma_f32_16x16x32_bf16 v[80:83], v[224:227], v[144:147], 0\n"
"ds_read_b64_tr_b16 v[124:125], v245 offset:2560\n"
"v_mfma_f32_16x16x32_bf16 v[44:47], v[224:227], v[128:131], 0\n"
"ds_read_b64_tr_b16 v[126:127], v245 offset:10752\n"
"s_waitcnt lgkmcnt(12)\n"
"v_mfma_f32_16x16x32_bf16 v[84:87], v[100:103], v[144:147], 0\n"
"ds_read_b64_tr_b16 v[224:225], v244 offset:2816\n"
"v_mfma_f32_16x16x32_bf16 v[48:51], v[100:103], v[128:131], 0\n"
"ds_read_b64_tr_b16 v[226:227], v244 offset:11008\n"
"v_mfma_f32_16x16x32_bf16 v[96:99], v[100:103], v[160:163], 0\n"
"s_waitcnt lgkmcnt(12)\n"
"v_mfma_f32_16x16x32_bf16 v[52:55], v[104:107], v[148:151], v[52:55]\n"
"ds_read_b64_tr_b16 v[100:101], v245 offset:2816\n"
"v_mfma_f32_16x16x32_bf16 v[16:19], v[104:107], v[132:135], v[16:19]\n"
"ds_read_b64_tr_b16 v[102:103], v245 offset:11008\n"
"s_waitcnt lgkmcnt(12)\n"
"v_mfma_f32_16x16x32_bf16 v[56:59], v[108:111], v[148:151], v[56:59]\n"
"ds_read_b64_tr_b16 v[104:105], v246 offset:1024\n"
"v_mfma_f32_16x16x32_bf16 v[20:23], v[108:111], v[132:135], v[20:23]\n"
"ds_read_b64_tr_b16 v[106:107], v246 offset:1536\n"
"s_waitcnt lgkmcnt(12)\n"
"v_mfma_f32_16x16x32_bf16 v[60:63], v[112:115], v[148:151], v[60:63]\n"
"v_mfma_f32_16x16x32_bf16 v[24:27], v[112:115], v[132:135], v[24:27]\n"
"v_mfma_f32_16x16x32_bf16 v[88:91], v[112:115], v[164:167], v[88:91]\n"
"s_waitcnt lgkmcnt(10)\n"
"v_mfma_f32_16x16x32_bf16 v[64:67], v[116:119], v[148:151], v[64:67]\n"
"v_mfma_f32_16x16x32_bf16 v[28:31], v[116:119], v[132:135], v[28:31]\n"
"v_mfma_f32_16x16x32_bf16 v[92:95], v[116:119], v[164:167], v[92:95]\n"
"s_waitcnt lgkmcnt(8)\n"
"v_mfma_f32_16x16x32_bf16 v[68:71], v[120:123], v[148:151], v[68:71]\n"
"s_waitcnt vmcnt(0)\n"
"s_cmp_lg_u64 vcc, 0\n"
"s_cbranch_scc1 LBAD_%=\n"
"s_waitcnt lgkmcnt(0)\n"
"s_barrier\n"
"ds_read_b64_tr_b16 v[108:109], v244 offset:16384\n"
"ds_read_b64_tr_b16 v[112:113], v245 offset:16384\n"
"ds_read_b64_tr_b16 v[116:117], v244 offset:16640\n"
"v_mfma_f32_16x16x32_bf16 v[32:35], v[120:123], v[132:135], v[32:35]\n"
"s_lshl_b32 s88, %[os2], 2\n"
"s_mov_b32 m0, s52\n"
"s_nop 0\n"
"buffer_load_dwordx4 v249, s[76:79], s88 offen lds\n"
"ds_read_b64_tr_b16 v[110:111], v244 offset:24576\n"
"ds_read_b64_tr_b16 v[114:115], v245 offset:24576\n"
"ds_read_b64_tr_b16 v[118:119], v244 offset:24832\n"
"v_mfma_f32_16x16x32_bf16 v[72:75], v[124:127], v[148:151], v[72:75]\n"
"buffer_load_dwordx4 v250, s[76:79], s88 offen offset:1024 lds\n"
"ds_read_b64_tr_b16 v[120:121], v245 offset:16640\n"
"v_mfma_f32_16x16x32_bf16 v[36:39], v[124:127], v[132:135], v[36:39]\n"
"s_lshl_b32 s89, %[os2], 2\n"
"s_add_u32 s89, s89, 34816\n"
"buffer_load_dwordx4 v249, s[76:79], s89 offen offset:2048 lds\n"
"ds_read_b64_tr_b16 v[122:123], v245 offset:24832\n"
"v_mfma_f32_16x16x32_bf16 v[76:79], v[224:227], v[148:151], v[76:79]\n"
"buffer_load_dwordx4 v250, s[76:79], s89 offen offset:3072 lds\n"
"ds_read_b64_tr_b16 v[124:125], v244 offset:16896\n"
"s_add_u32 m0, s90, -1024\n"
"v_mfma_f32_16x16x32_bf16 v[40:43], v[224:227], v[132:135], v[40:43]\n"
"buffer_load_dwordx4 v252, s[76:79], s89 offen offset:2048 lds\n"
"ds_read_b64_tr_b16 v[126:127], v244 offset:25088\n"
"v_mfma_f32_16x16x32_bf16 v[80:83], v[100:103], v[148:151], v[80:83]\n"
"ds_read_b64_tr_b16 v[224:225], v245 offset:16896\n"
"v_mfma_f32_16x16x32_bf16 v[44:47], v[100:103], v[132:135], v[44:47]\n"
"ds_read_b64_tr_b16 v[226:227], v245 offset:25088\n"
"v_mfma_f32_16x16x32_bf16 v[84:87], v[104:107], v[148:151], v[84:87]\n"
"ds_read_b64_tr_b16 v[100:101], v244 offset:17152\n"
"v_mfma_f32_16x16x32_bf16 v[48:51], v[104:107], v[132:135], v[48:51]\n"
"ds_read_b64_tr_b16 v[102:103], v244 offset:25344\n"
"v_mfma_f32_16x16x32_bf16 v[96:99], v[104:107], v[164:167], v[96:99]\n"
"s_waitcnt lgkmcnt(10)\n"
"v_mfma_f32_16x16x32_bf16 v[52:55], v[108:111], v[152:155], v[52:55]\n"
"ds_read_b64_tr_b16 v[104:105], v245 offset:17152\n"
"v_mfma_f32_16x16x32_bf16 v[16:19], v[108:111], v[136:139], v[16:19]\n"
"ds_read_b64_tr_b16 v[106:107], v245 offset:25344\n"
"s_waitcnt lgkmcnt(11)\n"
"v_mfma_f32_16x16x32_bf16 v[56:59], v[112:115], v[152:155], v[56:59]\n"
"ds_read_b64_tr_b16 v[108:109], v246 offset:2048\n"
"v_mfma_f32_16x16x32_bf16 v[20:23], v[112:115], v[136:139], v[20:23]\n"
"ds_read_b64_tr_b16 v[110:111], v246 offset:2560\n"
"s_waitcnt lgkmcnt(12)\n"
"v_mfma_f32_16x16x32_bf16 v[60:63], v[116:119], v[152:155], v[60:63]\n"
"ds_read_b64_tr_b16 v[112:113], v244 offset:18432\n"
"v_mfma_f32_16x16x32_bf16 v[24:27], v[116:119], v[136:139], v[24:27]\n"
"ds_read_b64_tr_b16 v[114:115], v244 offset:26624\n"
"v_mfma_f32_16x16x32_bf16 v[88:91], v[116:119], v[168:171], v[88:91]\n"
"s_waitcnt lgkmcnt(12)\n"
"v_mfma_f32_16x16x32_bf16 v[64:67], v[120:123], v[152:155], v[64:67]\n"
"ds_read_b64_tr_b16 v[116:117], v245 offset:18432\n"
"v_mfma_f32_16x16x32_bf16 v[28:31], v[120:123], v[136:139], v[28:31]\n"
"ds_read_b64_tr_b16 v[118:119], v245 offset:26624\n"
"v_mfma_f32_16x16x32_bf16 v[92:95], v[120:123], v[168:171], v[92:95]\n"
"s_waitcnt lgkmcnt(12)\n"
"v_mfma_f32_16x16x32_bf16 v[68:71], v[124:127], v[152:155], v[68:71]\n"
"ds_read_b64_tr_b16 v[120:121], v244 offset:18688\n"
"v_mfma_f32_16x16x32_bf16 v[32:35], v[124:127], v[136:139], v[32:35]\n"
"ds_read_b64_tr_b16 v[122:123], v244 offset:26880\n"
"s_waitcnt lgkmcnt(12)\n"
"v_mfma_f32_16x16x32_bf16 v[72:75], v[224:227], v[152:155], v[72:75]\n"
"ds_read_b64_tr_b16 v[124:125], v245 offset:18688\n"
"v_mfma_f32_16x16x32_bf16 v[36:39], v[224:227], v[136:139], v[36:39]\n"
"ds_read_b64_tr_b16 v[126:127], v245 offset:26880\n"
"s_waitcnt lgkmcnt(12)\n"
"v_mfma_f32_16x16x32_bf16 v[76:79], v[100:103], v[152:155], v[76:79]\n"
"ds_read_b64_tr_b16 v[224:225], v244 offset:18944\n"
"v_mfma_f32_16x16x32_bf16 v[40:43], v[100:103], v[136:139], v[40:43]\n"
"ds_read_b64_tr_b16 v[226:227], v244 offset:27136\n"
"s_waitcnt lgkmcnt(12)\n"
"v_mfma_f32_16x16x32_bf16 v[80:83], v[104:107], v[152:155], v[80:83]\n"
"ds_read_b64_tr_b16 v[100:101], v245 offset:18944\n"
"v_mfma_f32_16x16x32_bf16 v[44:47], v[104:107], v[136:139], v[44:47]\n"
"ds_read_b64_tr_b16 v[102:103], v245 offset:27136\n"
"s_waitcnt lgkmcnt(12)\n"
"v_mfma_f32_16x16x32_bf16 v[84:87], v[108:111], v[152:155], v[84:87]\n"
"ds_read_b64_tr_b16 v[104:105], v244 offset:19200\n"
"v_mfma_f32_16x16x32_bf16 v[48:51], v[108:111], v[136:139], v[48:51]\n"
"ds_read_b64_tr_b16 v[106:107], v244 offset:27392\n"
"v_mfma_f32_16x16x32_bf16 v[96:99], v[108:111], v[168:171], v[96:99]\n"
"s_waitcnt lgkmcnt(12)\n"
"v_mfma_f32_16x16x32_bf16 v[52:55], v[112:115], v[156:159], v[52:55]\n"
"ds_read_b64_tr_b16 v[108:109], v245 offset:19200\n"
"v_mfma_f32_16x16x32_bf16 v[16:19], v[112:115], v[140:143], v[16:19]\n"
"ds_read_b64_tr_b16 v[110:111], v245 offset:27392\n"
"s_waitcnt lgkmcnt(12)\n"
"v_mfma_f32_16x16x32_bf16 v[56:59], v[116:119], v[156:159], v[56:59]\n"
"ds_read_b64_tr_b16 v[112:113], v246 offset:3072\n"
"v_mfma_f32_16x16x32_bf16 v[20:23], v[116:119], v[140:143], v[20:23]\n"
"ds_read_b64_tr_b16 v[114:115], v246 offset:3584\n"
"s_waitcnt lgkmcnt(12)\n"
"v_mfma_f32_16x16x32_bf16 v[60:63], v[120:123], v[156:159], v[60:63]\n"
"v_mfma_f32_16x16x32_bf16 v[24:27], v[120:123], v[140:143], v[24:27]\n"
"v_mfma_f32_16x16x32_bf16 v[88:91], v[120:123], v[172:175], v[88:91]\n"
"s_waitcnt lgkmcnt(10)\n"
"v_mfma_f32_16x16x32_bf16 v[64:67], v[124:127], v[156:159], v[64:67]\n"
"v_mfma_f32_16x16x32_bf16 v[28:31], v[124:127], v[140:143], v[28:31]\n"
"v_mfma_f32_16x16x32_bf16 v[92:95], v[124:127], v[172:175], v[92:95]\n"
"s_waitcnt lgkmcnt(8)\n"
"v_mfma_f32_16x16x32_bf16 v[68:71], v[224:227], v[156:159], v[68:71]\n"
"s_waitcnt lgkmcnt(0)\n"
"s_barrier\n"
"ds_read_b64_tr_b16 v[116:117], v244 offset:32768\n"
"ds_read_b64_tr_b16 v[120:121], v245 offset:32768\n"
"ds_read_b64_tr_b16 v[160:161], v244 offset:33024\n"
"v_mfma_f32_16x16x32_bf16 v[32:35], v[224:227], v[140:143], v[32:35]\n"
"s_lshl_b32 s88, %[os2], 2\n"
"s_add_u32 s88, s88, 73728\n"
"s_mov_b32 m0, s53\n"
"s_nop 0\n"
"buffer_load_dwordx4 v249, s[76:79], s88 offen lds\n"
"ds_read_b64_tr_b16 v[118:119], v244 offset:40960\n"
"ds_read_b64_tr_b16 v[122:123], v245 offset:40960\n"
"ds_read_b64_tr_b16 v[162:163], v244 offset:41216\n"
"v_mfma_f32_16x16x32_bf16 v[72:75], v[100:103], v[156:159], v[72:75]\n"
"buffer_load_dwordx4 v250, s[76:79], s88 offen offset:1024 lds\n"
"ds_read_b64_tr_b16 v[164:165], v245 offset:33024\n"
"v_mfma_f32_16x16x32_bf16 v[36:39], v[100:103], v[140:143], v[36:39]\n"
"s_lshl_b32 s89, %[os2], 2\n"
"s_add_u32 s89, s89, 108544\n"
"buffer_load_dwordx4 v249, s[76:79], s89 offen offset:2048 lds\n"
"ds_read_b64_tr_b16 v[166:167], v245 offset:41216\n"
"v_mfma_f32_16x16x32_bf16 v[76:79], v[104:107], v[156:159], v[76:79]\n"
"buffer_load_dwordx4 v250, s[76:79], s89 offen offset:3072 lds\n"
"ds_read_b64_tr_b16 v[100:101], v244 offset:33280\n"
"v_mfma_f32_16x16x32_bf16 v[40:43], v[104:107], v[140:143], v[40:43]\n"
"ds_read_b64_tr_b16 v[102:103], v244 offset:41472\n"
"v_mfma_f32_16x16x32_bf16 v[80:83], v[108:111], v[156:159], v[80:83]\n"
"ds_read_b64_tr_b16 v[104:105], v245 offset:33280\n"
"v_mfma_f32_16x16x32_bf16 v[44:47], v[108:111], v[140:143], v[44:47]\n"
"ds_read_b64_tr_b16 v[106:107], v245 offset:41472\n"
"v_mfma_f32_16x16x32_bf16 v[84:87], v[112:115], v[156:159], v[84:87]\n"
"ds_read_b64_tr_b16 v[108:109], v244 offset:33536\n"
"v_mfma_f32_16x16x32_bf16 v[48:51], v[112:115], v[140:143], v[48:51]\n"
"ds_read_b64_tr_b16 v[110:111], v244 offset:41728\n"
"v_mfma_f32_16x16x32_bf16 v[96:99], v[112:115], v[172:175], v[96:99]\n"
"s_barrier\n"
"s_add_u32 s60, %[os1], 2048\n"
"buffer_load_dwordx4 v[192:195], %[l16], %[srdo], s60 offen offset:1024 sc1\n"
"buffer_load_dwordx4 v[176:179], %[l16], %[srdo], s60 offen sc1\n"
"s_add_u32 s64, %[os1], 17408\n"
"buffer_load_dwordx4 v[212:215], %[l16], %[srdo], s64 offen sc1\n"
"s_add_u32 s61, %[os1], 11264\n"
"buffer_load_dwordx4 v[196:199], %[l16], %[srdo], s61 offen offset:1024 sc1\n"
"buffer_load_dwordx4 v[180:183], %[l16], %[srdo], s61 offen sc1\n"
"s_add_u32 s62, %[os1], 20480\n"
"buffer_load_dwordx4 v[200:203], %[l16], %[srdo], s62 offen offset:1024 sc1\n"
"buffer_load_dwordx4 v[184:187], %[l16], %[srdo], s62 offen sc1\n"
"s_add_u32 s63, %[os1], 29696\n"
"buffer_load_dwordx4 v[204:207], %[l16], %[srdo], s63 offen offset:1024 sc1\n"
"buffer_load_dwordx4 v[188:191], %[l16], %[srdo], s63 offen sc1\n"
"s_add_u32 s64, %[os2], 17408\n"
"buffer_load_dwordx4 v[240:243], %[l16], %[srdo], s64 offen sc1\n"
"s_waitcnt vmcnt(9) lgkmcnt(10)\n"
"v_mfma_f32_16x16x32_bf16 v[52:55], v[116:119], v[192:195], v[52:55]\n"
"ds_read_b64_tr_b16 v[112:113], v245 offset:33536\n"
"s_waitcnt vmcnt(8)\n"
"v_mfma_f32_16x16x32_bf16 v[16:19], v[116:119], v[176:179], v[16:19]\n"
"s_add_u32 s60, %[os2], 2048\n"
"buffer_load_dwordx4 v[144:147], %[l16], %[srdo], s60 offen offset:1024 sc1\n"
"ds_read_b64_tr_b16 v[114:115], v245 offset:41728\n"
"s_waitcnt lgkmcnt(11)\n"
"v_mfma_f32_16x16x32_bf16 v[56:59], v[120:123], v[192:195], v[56:59]\n"
"ds_read_b64_tr_b16 v[232:233], v246 offset:4096\n"
"v_mfma_f32_16x16x32_bf16 v[20:23], v[120:123], v[176:179], v[20:23]\n"
"buffer_load_dwordx4 v[128:131], %[l16], %[srdo], s60 offen sc1\n"
"ds_read_b64_tr_b16 v[234:235], v246 offset:4608\n"
"s_waitcnt lgkmcnt(12)\n"
"v_mfma_f32_16x16x32_bf16 v[60:63], v[160:163], v[192:195], v[60:63]\n"
"ds_read_b64_tr_b16 v[120:121], v244 offset:34816\n"
"v_mfma_f32_16x16x32_bf16 v[24:27], v[160:163], v[176:179], v[24:27]\n"
"ds_read_b64_tr_b16 v[122:123], v244 offset:43008\n"
"s_waitcnt lgkmcnt(12)\n"
"v_mfma_f32_16x16x32_bf16 v[64:67], v[164:167], v[192:195], v[64:67]\n"
"ds_read_b64_tr_b16 v[124:125], v245 offset:34816\n"
"v_mfma_f32_16x16x32_bf16 v[28:31], v[164:167], v[176:179], v[28:31]\n"
"s_add_u32 s61, %[os2], 11264\n"
"buffer_load_dwordx4 v[148:151], %[l16], %[srdo], s61 offen offset:1024 sc1\n"
"ds_read_b64_tr_b16 v[126:127], v245 offset:43008\n"
"s_waitcnt lgkmcnt(12)\n"
"v_mfma_f32_16x16x32_bf16 v[68:71], v[100:103], v[192:195], v[68:71]\n"
"ds_read_b64_tr_b16 v[168:169], v244 offset:35072\n"
"v_mfma_f32_16x16x32_bf16 v[32:35], v[100:103], v[176:179], v[32:35]\n"
"buffer_load_dwordx4 v[132:135], %[l16], %[srdo], s61 offen sc1\n"
"ds_read_b64_tr_b16 v[170:171], v244 offset:43264\n"
"s_waitcnt lgkmcnt(12)\n"
"v_mfma_f32_16x16x32_bf16 v[72:75], v[104:107], v[192:195], v[72:75]\n"
"ds_read_b64_tr_b16 v[172:173], v245 offset:35072\n"
"v_mfma_f32_16x16x32_bf16 v[36:39], v[104:107], v[176:179], v[36:39]\n"
"ds_read_b64_tr_b16 v[174:175], v245 offset:43264\n"
"s_waitcnt lgkmcnt(12)\n"
"v_mfma_f32_16x16x32_bf16 v[76:79], v[108:111], v[192:195], v[76:79]\n"
"ds_read_b64_tr_b16 v[104:105], v244 offset:35328\n"
"v_mfma_f32_16x16x32_bf16 v[40:43], v[108:111], v[176:179], v[40:43]\n"
"s_add_u32 s62, %[os2], 20480\n"
"buffer_load_dwordx4 v[152:155], %[l16], %[srdo], s62 offen offset:1024 sc1\n"
"ds_read_b64_tr_b16 v[106:107], v244 offset:43520\n"
"s_waitcnt lgkmcnt(12)\n"
"v_mfma_f32_16x16x32_bf16 v[80:83], v[112:115], v[192:195], v[80:83]\n"
"ds_read_b64_tr_b16 v[108:109], v245 offset:35328\n"
"v_mfma_f32_16x16x32_bf16 v[44:47], v[112:115], v[176:179], v[44:47]\n"
"buffer_load_dwordx4 v[136:139], %[l16], %[srdo], s62 offen sc1\n"
"ds_read_b64_tr_b16 v[110:111], v245 offset:43520\n"
"s_waitcnt lgkmcnt(12)\n"
"v_mfma_f32_16x16x32_bf16 v[84:87], v[232:235], v[192:195], v[84:87]\n"
"ds_read_b64_tr_b16 v[112:113], v244 offset:35584\n"
"v_mfma_f32_16x16x32_bf16 v[48:51], v[232:235], v[176:179], v[48:51]\n"
"ds_read_b64_tr_b16 v[114:115], v244 offset:43776\n"
"s_waitcnt vmcnt(12) lgkmcnt(12)\n"
"v_mfma_f32_16x16x32_bf16 v[52:55], v[120:123], v[196:199], v[52:55]\n"
"ds_read_b64_tr_b16 v[116:117], v245 offset:35584\n"
"s_waitcnt vmcnt(11)\n"
"v_mfma_f32_16x16x32_bf16 v[16:19], v[120:123], v[180:183], v[16:19]\n"
"s_add_u32 s63, %[os2], 29696\n"
"buffer_load_dwordx4 v[156:159], %[l16], %[srdo], s63 offen offset:1024 sc1\n"
"ds_read_b64_tr_b16 v[118:119], v245 offset:43776\n"
"s_waitcnt lgkmcnt(12)\n"
"v_mfma_f32_16x16x32_bf16 v[56:59], v[124:127], v[196:199], v[56:59]\n"
"ds_read_b64_tr_b16 v[236:237], v246 offset:5120\n"
"v_mfma_f32_16x16x32_bf16 v[20:23], v[124:127], v[180:183], v[20:23]\n"
"buffer_load_dwordx4 v[140:143], %[l16], %[srdo], s63 offen sc1\n"
"ds_read_b64_tr_b16 v[238:239], v246 offset:5632\n"
"s_waitcnt lgkmcnt(12)\n"
"v_mfma_f32_16x16x32_bf16 v[60:63], v[168:171], v[196:199], v[60:63]\n"
"v_mfma_f32_16x16x32_bf16 v[24:27], v[168:171], v[180:183], v[24:27]\n"
"ds_write_b128 v247, v[212:215] offset:1024\n"
"s_waitcnt lgkmcnt(11)\n"
"v_mfma_f32_16x16x32_bf16 v[64:67], v[172:175], v[196:199], v[64:67]\n"
"v_mfma_f32_16x16x32_bf16 v[28:31], v[172:175], v[180:183], v[28:31]\n"
"s_add_u32 s60, %[os3], 2048\n"
"buffer_load_dwordx4 v[192:195], %[l16], %[srdo], s60 offen offset:1024 sc1\n"
"s_waitcnt lgkmcnt(9)\n"
"v_mfma_f32_16x16x32_bf16 v[68:71], v[104:107], v[196:199], v[68:71]\n"
"s_waitcnt lgkmcnt(0)\n"
"s_barrier\n"
"ds_read_b64_tr_b16 v[124:125], v244 offset:49152\n"
"ds_read_b64_tr_b16 v[224:225], v245 offset:49152\n"
"ds_read_b64_tr_b16 v[100:101], v244 offset:49408\n"
"v_mfma_f32_16x16x32_bf16 v[32:35], v[104:107], v[180:183], v[32:35]\n"
"ds_read_b128 v[208:211], v247 offset:0\n"
"s_lshl_b32 s88, %[os3], 2\n"
"s_mov_b32 m0, s54\n"
"s_nop 0\n"
"buffer_load_dwordx4 v249, s[76:79], s88 offen lds\n"
"buffer_load_dwordx4 v[176:179], %[l16], %[srdo], s60 offen sc1\n"
"ds_read_b64_tr_b16 v[126:127], v244 offset:57344\n"
"ds_read_b64_tr_b16 v[226:227], v245 offset:57344\n"
"ds_read_b64_tr_b16 v[102:103], v244 offset:57600\n"
"v_mfma_f32_16x16x32_bf16 v[72:75], v[108:111], v[196:199], v[72:75]\n"
"ds_read_b128 v[216:219], v247 offset:2048\n"
"buffer_load_dwordx4 v250, s[76:79], s88 offen offset:1024 lds\n"
"ds_read_b64_tr_b16 v[104:105], v245 offset:49408\n"
"v_mfma_f32_16x16x32_bf16 v[36:39], v[108:111], v[180:183], v[36:39]\n"
"ds_read_b128 v[220:223], v247 offset:3072\n"
"s_lshl_b32 s89, %[os3], 2\n"
"s_add_u32 s89, s89, 34816\n"
"buffer_load_dwordx4 v249, s[76:79], s89 offen offset:2048 lds\n"
"ds_read_b64_tr_b16 v[106:107], v245 offset:57600\n"
"v_mfma_f32_16x16x32_bf16 v[76:79], v[112:115], v[196:199], v[76:79]\n"
"buffer_load_dwordx4 v250, s[76:79], s89 offen offset:3072 lds\n"
"ds_read_b64_tr_b16 v[108:109], v244 offset:49664\n"
"s_add_u32 m0, s90, 3072\n"
"v_mfma_f32_16x16x32_bf16 v[40:43], v[112:115], v[180:183], v[40:43]\n"
"buffer_load_dwordx4 v252, s[76:79], s89 offen offset:2048 lds\n"
"ds_read_b64_tr_b16 v[110:111], v244 offset:57856\n"
"v_mfma_f32_16x16x32_bf16 v[80:83], v[116:119], v[196:199], v[80:83]\n"
"ds_read_b64_tr_b16 v[112:113], v245 offset:49664\n"
"v_mfma_f32_16x16x32_bf16 v[44:47], v[116:119], v[180:183], v[44:47]\n"
"ds_read_b64_tr_b16 v[114:115], v245 offset:57856\n"
"v_mfma_f32_16x16x32_bf16 v[84:87], v[236:239], v[196:199], v[84:87]\n"
"ds_read_b64_tr_b16 v[116:117], v244 offset:49920\n"
"v_mfma_f32_16x16x32_bf16 v[48:51], v[236:239], v[180:183], v[48:51]\n"
"ds_read_b64_tr_b16 v[118:119], v244 offset:58112\n"
"s_waitcnt lgkmcnt(13)\n"
"v_mfma_f32_16x16x32_bf16 v[88:91], v[160:163], v[208:211], v[88:91]\n"
"s_waitcnt vmcnt(19) lgkmcnt(12)\n"
"v_mfma_f32_16x16x32_bf16 v[52:55], v[124:127], v[200:203], v[52:55]\n"
"ds_read_b64_tr_b16 v[120:121], v245 offset:49920\n"
"s_waitcnt vmcnt(18)\n"
"v_mfma_f32_16x16x32_bf16 v[16:19], v[124:127], v[184:187], v[16:19]\n"
"s_add_u32 s61, %[os3], 11264\n"
"buffer_load_dwordx4 v[196:199], %[l16], %[srdo], s61 offen offset:1024 sc1\n"
"ds_read_b64_tr_b16 v[122:123], v245 offset:58112\n"
"v_mfma_f32_16x16x32_bf16 v[92:95], v[164:167], v[208:211], v[92:95]\n"
"buffer_load_dwordx4 v[180:183], %[l16], %[srdo], s61 offen sc1\n"
"s_waitcnt lgkmcnt(13)\n"
"v_mfma_f32_16x16x32_bf16 v[56:59], v[224:227], v[200:203], v[56:59]\n"
"ds_read_b64_tr_b16 v[124:125], v246 offset:6144\n"
"v_mfma_f32_16x16x32_bf16 v[20:23], v[224:227], v[184:187], v[20:23]\n"
"ds_read_b64_tr_b16 v[126:127], v246 offset:6656\n"
"v_mfma_f32_16x16x32_bf16 v[96:99], v[232:235], v[208:211], v[96:99]\n"
"s_waitcnt lgkmcnt(14)\n"
"v_mfma_f32_16x16x32_bf16 v[60:63], v[100:103], v[200:203], v[60:63]\n"
"ds_read_b64_tr_b16 v[224:225], v244 offset:51200\n"
"v_mfma_f32_16x16x32_bf16 v[24:27], v[100:103], v[184:187], v[24:27]\n"
"ds_read_b64_tr_b16 v[226:227], v244 offset:59392\n"
"s_waitcnt lgkmcnt(15)\n"
"v_mfma_f32_16x16x32_bf16 v[88:91], v[100:103], v[216:219], v[88:91]\n"
"v_mfma_f32_16x16x32_bf16 v[88:91], v[168:171], v[212:215], v[88:91]\n"
"s_waitcnt lgkmcnt(12)\n"
"v_mfma_f32_16x16x32_bf16 v[64:67], v[104:107], v[200:203], v[64:67]\n"
"ds_read_b64_tr_b16 v[100:101], v245 offset:51200\n"
"v_mfma_f32_16x16x32_bf16 v[28:31], v[104:107], v[184:187], v[28:31]\n"
"ds_read_b64_tr_b16 v[102:103], v245 offset:59392\n"
"v_mfma_f32_16x16x32_bf16 v[92:95], v[104:107], v[216:219], v[92:95]\n"
"v_mfma_f32_16x16x32_bf16 v[92:95], v[172:175], v[212:215], v[92:95]\n"
"s_waitcnt lgkmcnt(12)\n"
"v_mfma_f32_16x16x32_bf16 v[68:71], v[108:111], v[200:203], v[68:71]\n"
"ds_read_b64_tr_b16 v[104:105], v244 offset:51456\n"
"v_mfma_f32_16x16x32_bf16 v[32:35], v[108:111], v[184:187], v[32:35]\n"
"ds_read_b64_tr_b16 v[106:107], v244 offset:59648\n"
"v_mfma_f32_16x16x32_bf16 v[96:99], v[236:239], v[212:215], v[96:99]\n"
"s_waitcnt lgkmcnt(12)\n"
"v_mfma_f32_16x16x32_bf16 v[72:75], v[112:115], v[200:203], v[72:75]\n"
"ds_read_b64_tr_b16 v[108:109], v245 offset:51456\n"
"v_mfma_f32_16x16x32_bf16 v[36:39], v[112:115], v[184:187], v[36:39]\n"
"ds_read_b64_tr_b16 v[110:111], v245 offset:59648\n"
"s_waitcnt lgkmcnt(12)\n"
"v_mfma_f32_16x16x32_bf16 v[76:79], v[116:119], v[200:203], v[76:79]\n"
"ds_read_b64_tr_b16 v[112:113], v244 offset:51712\n"
"v_mfma_f32_16x16x32_bf16 v[40:43], v[116:119], v[184:187], v[40:43]\n"
"ds_read_b64_tr_b16 v[114:115], v244 offset:59904\n"
"s_waitcnt lgkmcnt(12)\n"
"v_mfma_f32_16x16x32_bf16 v[80:83], v[120:123], v[200:203], v[80:83]\n"
"ds_read_b64_tr_b16 v[116:117], v245 offset:51712\n"
"v_mfma_f32_16x16x32_bf16 v[44:47], v[120:123], v[184:187], v[44:47]\n"
"ds_read_b64_tr_b16 v[118:119], v245 offset:59904\n"
"s_waitcnt lgkmcnt(12)\n"
"v_mfma_f32_16x16x32_bf16 v[84:87], v[124:127], v[200:203], v[84:87]\n"
"ds_read_b64_tr_b16 v[120:121], v244 offset:51968\n"
"v_mfma_f32_16x16x32_bf16 v[48:51], v[124:127], v[184:187], v[48:51]\n"
"ds_read_b64_tr_b16 v[122:123], v244 offset:60160\n"
"v_mfma_f32_16x16x32_bf16 v[96:99], v[124:127], v[216:219], v[96:99]\n"
"s_waitcnt vmcnt(19) lgkmcnt(12)\n"
"v_mfma_f32_16x16x32_bf16 v[52:55], v[224:227], v[204:207], v[52:55]\n"
"ds_read_b64_tr_b16 v[124:125], v245 offset:51968\n"
"s_waitcnt vmcnt(18)\n"
"v_mfma_f32_16x16x32_bf16 v[16:19], v[224:227], v[188:191], v[16:19]\n"
"s_add_u32 s62, %[os3], 20480\n"
"buffer_load_dwordx4 v[200:203], %[l16], %[srdo], s62 offen offset:1024 sc1\n"
"ds_read_b64_tr_b16 v[126:127], v245 offset:60160\n"
"s_waitcnt lgkmcnt(12)\n"
"v_mfma_f32_16x16x32_bf16 v[56:59], v[100:103], v[204:207], v[56:59]\n"
"buffer_load_dwordx4 v[184:187], %[l16], %[srdo], s62 offen sc1\n"
"ds_read_b64_tr_b16 v[224:225], v246 offset:7168\n"
"v_mfma_f32_16x16x32_bf16 v[20:23], v[100:103], v[188:191], v[20:23]\n"
"ds_read_b64_tr_b16 v[226:227], v246 offset:7680\n"
"s_waitcnt lgkmcnt(12)\n"
"v_mfma_f32_16x16x32_bf16 v[60:63], v[104:107], v[204:207], v[60:63]\n"
"v_mfma_f32_16x16x32_bf16 v[24:27], v[104:107], v[188:191], v[24:27]\n"
"v_mfma_f32_16x16x32_bf16 v[88:91], v[104:107], v[220:223], v[88:91]\n"
"s_waitcnt lgkmcnt(10)\n"
"v_mfma_f32_16x16x32_bf16 v[64:67], v[108:111], v[204:207], v[64:67]\n"
"s_waitcnt vmcnt(19)\n"
"ds_write_b128 v247, v[240:243] offset:5120\n"
"v_mfma_f32_16x16x32_bf16 v[28:31], v[108:111], v[188:191], v[28:31]\n"
"v_mfma_f32_16x16x32_bf16 v[92:95], v[108:111], v[220:223], v[92:95]\n"
"s_waitcnt lgkmcnt(9)\n"
"v_mfma_f32_16x16x32_bf16 v[68:71], v[112:115], v[204:207], v[68:71]\n"
"s_waitcnt lgkmcnt(0)\n"
"s_barrier\n"
"s_add_u32 s64, %[os3], 17408\n"
"buffer_load_dwordx4 v[240:243], %[l16], %[srdo], s64 offen sc1\n"
"ds_read_b64_tr_b16 v[100:101], v244 offset:0\n"
"ds_read_b64_tr_b16 v[104:105], v245 offset:0\n"
"ds_read_b64_tr_b16 v[108:109], v244 offset:256\n"
"v_mfma_f32_16x16x32_bf16 v[32:35], v[112:115], v[188:191], v[32:35]\n"
"ds_read_b128 v[160:163], v247 offset:4096\n"
"s_lshl_b32 s88, %[os3], 2\n"
"s_add_u32 s88, s88, 73728\n"
"s_mov_b32 m0, s55\n"
"s_nop 0\n"
"buffer_load_dwordx4 v249, s[76:79], s88 offen lds\n"
"ds_read_b64_tr_b16 v[102:103], v244 offset:8192\n"
"ds_read_b64_tr_b16 v[106:107], v245 offset:8192\n"
"ds_read_b64_tr_b16 v[110:111], v244 offset:8448\n"
"v_mfma_f32_16x16x32_bf16 v[72:75], v[116:119], v[204:207], v[72:75]\n"
"ds_read_b128 v[164:167], v247 offset:5120\n"
"buffer_load_dwordx4 v250, s[76:79], s88 offen offset:1024 lds\n"
"ds_read_b64_tr_b16 v[112:113], v245 offset:256\n"
"v_mfma_f32_16x16x32_bf16 v[36:39], v[116:119], v[188:191], v[36:39]\n"
"ds_read_b128 v[168:171], v247 offset:6144\n"
"s_lshl_b32 s89, %[os3], 2\n"
"s_add_u32 s89, s89, 108544\n"
"buffer_load_dwordx4 v249, s[76:79], s89 offen offset:2048 lds\n"
"ds_read_b64_tr_b16 v[114:115], v245 offset:8448\n"
"v_mfma_f32_16x16x32_bf16 v[76:79], v[120:123], v[204:207], v[76:79]\n"
"ds_read_b128 v[172:175], v247 offset:7168\n"
"buffer_load_dwordx4 v250, s[76:79], s89 offen offset:3072 lds\n"
"ds_read_b64_tr_b16 v[116:117], v244 offset:512\n"
"v_mfma_f32_16x16x32_bf16 v[40:43], v[120:123], v[188:191], v[40:43]\n"
"ds_read_b64_tr_b16 v[118:119], v244 offset:8704\n"
"v_mfma_f32_16x16x32_bf16 v[80:83], v[124:127], v[204:207], v[80:83]\n"
"ds_read_b64_tr_b16 v[120:121], v245 offset:512\n"
"v_mfma_f32_16x16x32_bf16 v[44:47], v[124:127], v[188:191], v[44:47]\n"
"ds_read_b64_tr_b16 v[122:123], v245 offset:8704\n"
"v_mfma_f32_16x16x32_bf16 v[84:87], v[224:227], v[204:207], v[84:87]\n"
"ds_read_b64_tr_b16 v[124:125], v244 offset:768\n"
"v_mfma_f32_16x16x32_bf16 v[48:51], v[224:227], v[188:191], v[48:51]\n"
"ds_read_b64_tr_b16 v[126:127], v244 offset:8960\n"
"v_mfma_f32_16x16x32_bf16 v[96:99], v[224:227], v[220:223], v[96:99]\n"
"s_waitcnt vmcnt(23) lgkmcnt(13)\n"
"v_mfma_f32_16x16x32_bf16 v[52:55], v[100:103], v[144:147], v[52:55]\n"
"ds_read_b64_tr_b16 v[224:225], v245 offset:768\n"
"s_waitcnt vmcnt(22)\n"
"v_mfma_f32_16x16x32_bf16 v[16:19], v[100:103], v[128:131], v[16:19]\n"
"s_add_u32 s63, %[os3], 29696\n"
"buffer_load_dwordx4 v[204:207], %[l16], %[srdo], s63 offen offset:1024 sc1\n"
"ds_read_b64_tr_b16 v[226:227], v245 offset:8960\n"
"s_waitcnt lgkmcnt(14)\n"
"v_mfma_f32_16x16x32_bf16 v[56:59], v[104:107], v[144:147], v[56:59]\n"
"buffer_load_dwordx4 v[188:191], %[l16], %[srdo], s63 offen sc1\n"
"ds_read_b64_tr_b16 v[100:101], v246 offset:0\n"
"v_mfma_f32_16x16x32_bf16 v[20:23], v[104:107], v[128:131], v[20:23]\n"
"ds_read_b64_tr_b16 v[102:103], v246 offset:512\n"
"s_waitcnt lgkmcnt(15)\n"
"v_mfma_f32_16x16x32_bf16 v[60:63], v[108:111], v[144:147], v[60:63]\n"
"ds_read_b64_tr_b16 v[104:105], v244 offset:2048\n"
"v_mfma_f32_16x16x32_bf16 v[24:27], v[108:111], v[128:131], v[24:27]\n"
"ds_read_b64_tr_b16 v[106:107], v244 offset:10240\n"
"v_mfma_f32_16x16x32_bf16 v[88:91], v[108:111], v[160:163], v[88:91]\n"
"s_waitcnt lgkmcnt(13)\n"
"v_mfma_f32_16x16x32_bf16 v[64:67], v[112:115], v[144:147], v[64:67]\n"
"ds_read_b64_tr_b16 v[108:109], v245 offset:2048\n"
"v_mfma_f32_16x16x32_bf16 v[28:31], v[112:115], v[128:131], v[28:31]\n"
"ds_read_b64_tr_b16 v[110:111], v245 offset:10240\n"
"v_mfma_f32_16x16x32_bf16 v[92:95], v[112:115], v[160:163], v[92:95]\n"
"s_waitcnt lgkmcnt(12)\n"
"v_mfma_f32_16x16x32_bf16 v[68:71], v[116:119], v[144:147], v[68:71]\n"
"ds_read_b64_tr_b16 v[112:113], v244 offset:2304\n"
"v_mfma_f32_16x16x32_bf16 v[32:35], v[116:119], v[128:131], v[32:35]\n"
"ds_read_b64_tr_b16 v[114:115], v244 offset:10496\n"
"s_waitcnt lgkmcnt(12)\n"
"v_mfma_f32_16x16x32_bf16 v[72:75], v[120:123], v[144:147], v[72:75]\n"
"ds_read_b64_tr_b16 v[116:117], v245 offset:2304\n"
"v_mfma_f32_16x16x32_bf16 v[36:39], v[120:123], v[128:131], v[36:39]\n"
"ds_read_b64_tr_b16 v[118:119], v245 offset:10496\n"
"s_waitcnt lgkmcnt(12)\n"
"v_mfma_f32_16x16x32_bf16 v[76:79], v[124:127], v[144:147], v[76:79]\n"
"ds_read_b64_tr_b16 v[120:121], v244 offset:2560\n"
"v_mfma_f32_16x16x32_bf16 v[40:43], v[124:127], v[128:131], v[40:43]\n"
"ds_read_b64_tr_b16 v[122:123], v244 offset:10752\n"
"s_waitcnt lgkmcnt(12)\n"
"v_mfma_f32_16x16x32_bf16 v[80:83], v[224:227], v[144:147], v[80:83]\n"
"ds_read_b64_tr_b16 v[124:125], v245 offset:2560\n"
"v_mfma_f32_16x16x32_bf16 v[44:47], v[224:227], v[128:131], v[44:47]\n"
"ds_read_b64_tr_b16 v[126:127], v245 offset:10752\n"
"s_waitcnt lgkmcnt(12)\n"
"v_mfma_f32_16x16x32_bf16 v[84:87], v[100:103], v[144:147], v[84:87]\n"
"ds_read_b64_tr_b16 v[224:225], v244 offset:2816\n"
"v_mfma_f32_16x16x32_bf16 v[48:51], v[100:103], v[128:131], v[48:51]\n"
"ds_read_b64_tr_b16 v[226:227], v244 offset:11008\n"
"v_mfma_f32_16x16x32_bf16 v[96:99], v[100:103], v[160:163], v[96:99]\n"
"s_waitcnt vmcnt(23) lgkmcnt(12)\n"
"v_mfma_f32_16x16x32_bf16 v[52:55], v[104:107], v[148:151], v[52:55]\n"
"ds_read_b64_tr_b16 v[100:101], v245 offset:2816\n"
"s_waitcnt vmcnt(22)\n"
"v_mfma_f32_16x16x32_bf16 v[16:19], v[104:107], v[132:135], v[16:19]\n"
"ds_read_b64_tr_b16 v[102:103], v245 offset:11008\n"
"s_waitcnt lgkmcnt(12)\n"
"v_mfma_f32_16x16x32_bf16 v[56:59], v[108:111], v[148:151], v[56:59]\n"
"ds_read_b64_tr_b16 v[104:105], v246 offset:1024\n"
"v_mfma_f32_16x16x32_bf16 v[20:23], v[108:111], v[132:135], v[20:23]\n"
"ds_read_b64_tr_b16 v[106:107], v246 offset:1536\n"
"s_waitcnt lgkmcnt(12)\n"
"v_mfma_f32_16x16x32_bf16 v[60:63], v[112:115], v[148:151], v[60:63]\n"
"v_mfma_f32_16x16x32_bf16 v[24:27], v[112:115], v[132:135], v[24:27]\n"
"v_mfma_f32_16x16x32_bf16 v[88:91], v[112:115], v[164:167], v[88:91]\n"
"s_waitcnt lgkmcnt(10)\n"
"v_mfma_f32_16x16x32_bf16 v[64:67], v[116:119], v[148:151], v[64:67]\n"
"v_mfma_f32_16x16x32_bf16 v[28:31], v[116:119], v[132:135], v[28:31]\n"
"v_mfma_f32_16x16x32_bf16 v[92:95], v[116:119], v[164:167], v[92:95]\n"
"s_waitcnt lgkmcnt(8)\n"
"v_mfma_f32_16x16x32_bf16 v[68:71], v[120:123], v[148:151], v[68:71]\n"
"s_barrier\n"
"ds_read_b64_tr_b16 v[108:109], v244 offset:16384\n"
"ds_read_b64_tr_b16 v[112:113], v245 offset:16384\n"
"ds_read_b64_tr_b16 v[116:117], v244 offset:16640\n"
"v_mfma_f32_16x16x32_bf16 v[32:35], v[120:123], v[132:135], v[32:35]\n"
"ds_read_b64_tr_b16 v[110:111], v244 offset:24576\n"
"ds_read_b64_tr_b16 v[114:115], v245 offset:24576\n"
"ds_read_b64_tr_b16 v[118:119], v244 offset:24832\n"
"s_waitcnt lgkmcnt(12)\n"
"v_mfma_f32_16x16x32_bf16 v[72:75], v[124:127], v[148:151], v[72:75]\n"
"ds_read_b64_tr_b16 v[120:121], v245 offset:16640\n"
"v_mfma_f32_16x16x32_bf16 v[36:39], v[124:127], v[132:135], v[36:39]\n"
"ds_read_b64_tr_b16 v[122:123], v245 offset:24832\n"
"s_waitcnt lgkmcnt(12)\n"
"v_mfma_f32_16x16x32_bf16 v[76:79], v[224:227], v[148:151], v[76:79]\n"
"ds_read_b64_tr_b16 v[124:125], v244 offset:16896\n"
"v_mfma_f32_16x16x32_bf16 v[40:43], v[224:227], v[132:135], v[40:43]\n"
"ds_read_b64_tr_b16 v[126:127], v244 offset:25088\n"
"s_waitcnt lgkmcnt(12)\n"
"v_mfma_f32_16x16x32_bf16 v[80:83], v[100:103], v[148:151], v[80:83]\n"
"ds_read_b64_tr_b16 v[224:225], v245 offset:16896\n"
"v_mfma_f32_16x16x32_bf16 v[44:47], v[100:103], v[132:135], v[44:47]\n"
"ds_read_b64_tr_b16 v[226:227], v245 offset:25088\n"
"s_waitcnt lgkmcnt(12)\n"
"v_mfma_f32_16x16x32_bf16 v[84:87], v[104:107], v[148:151], v[84:87]\n"
"ds_read_b64_tr_b16 v[100:101], v244 offset:17152\n"
"v_mfma_f32_16x16x32_bf16 v[48:51], v[104:107], v[132:135], v[48:51]\n"
"ds_read_b64_tr_b16 v[102:103], v244 offset:25344\n"
"v_mfma_f32_16x16x32_bf16 v[96:99], v[104:107], v[164:167], v[96:99]\n"
"s_waitcnt vmcnt(21) lgkmcnt(10)\n"
"v_mfma_f32_16x16x32_bf16 v[52:55], v[108:111], v[152:155], v[52:55]\n"
"ds_read_b64_tr_b16 v[104:105], v245 offset:17152\n"
"s_waitcnt vmcnt(20)\n"
"v_mfma_f32_16x16x32_bf16 v[16:19], v[108:111], v[136:139], v[16:19]\n"
"ds_read_b64_tr_b16 v[106:107], v245 offset:25344\n"
"s_waitcnt lgkmcnt(11)\n"
"v_mfma_f32_16x16x32_bf16 v[56:59], v[112:115], v[152:155], v[56:59]\n"
"ds_read_b64_tr_b16 v[108:109], v246 offset:2048\n"
"v_mfma_f32_16x16x32_bf16 v[20:23], v[112:115], v[136:139], v[20:23]\n"
"ds_read_b64_tr_b16 v[110:111], v246 offset:2560\n"
"s_waitcnt lgkmcnt(12)\n"
"v_mfma_f32_16x16x32_bf16 v[60:63], v[116:119], v[152:155], v[60:63]\n"
"ds_read_b64_tr_b16 v[112:113], v244 offset:18432\n"
"v_mfma_f32_16x16x32_bf16 v[24:27], v[116:119], v[136:139], v[24:27]\n"
"ds_read_b64_tr_b16 v[114:115], v244 offset:26624\n"
"v_mfma_f32_16x16x32_bf16 v[88:91], v[116:119], v[168:171], v[88:91]\n"
"s_waitcnt lgkmcnt(12)\n"
"v_mfma_f32_16x16x32_bf16 v[64:67], v[120:123], v[152:155], v[64:67]\n"
"ds_read_b64_tr_b16 v[116:117], v245 offset:18432\n"
"v_mfma_f32_16x16x32_bf16 v[28:31], v[120:123], v[136:139], v[28:31]\n"
"ds_read_b64_tr_b16 v[118:119], v245 offset:26624\n"
"v_mfma_f32_16x16x32_bf16 v[92:95], v[120:123], v[168:171], v[92:95]\n"
"s_waitcnt lgkmcnt(12)\n"
"v_mfma_f32_16x16x32_bf16 v[68:71], v[124:127], v[152:155], v[68:71]\n"
"ds_read_b64_tr_b16 v[120:121], v244 offset:18688\n"
"v_mfma_f32_16x16x32_bf16 v[32:35], v[124:127], v[136:139], v[32:35]\n"
"ds_read_b64_tr_b16 v[122:123], v244 offset:26880\n"
"s_waitcnt lgkmcnt(12)\n"
"v_mfma_f32_16x16x32_bf16 v[72:75], v[224:227], v[152:155], v[72:75]\n"
"ds_read_b64_tr_b16 v[124:125], v245 offset:18688\n"
"v_mfma_f32_16x16x32_bf16 v[36:39], v[224:227], v[136:139], v[36:39]\n"
"ds_read_b64_tr_b16 v[126:127], v245 offset:26880\n"
"s_waitcnt lgkmcnt(12)\n"
"v_mfma_f32_16x16x32_bf16 v[76:79], v[100:103], v[152:155], v[76:79]\n"
"ds_read_b64_tr_b16 v[224:225], v244 offset:18944\n"
"v_mfma_f32_16x16x32_bf16 v[40:43], v[100:103], v[136:139], v[40:43]\n"
"ds_read_b64_tr_b16 v[226:227], v244 offset:27136\n"
"s_waitcnt lgkmcnt(12)\n"
"v_mfma_f32_16x16x32_bf16 v[80:83], v[104:107], v[152:155], v[80:83]\n"
"ds_read_b64_tr_b16 v[100:101], v245 offset:18944\n"
"v_mfma_f32_16x16x32_bf16 v[44:47], v[104:107], v[136:139], v[44:47]\n"
"ds_read_b64_tr_b16 v[102:103], v245 offset:27136\n"
"s_waitcnt lgkmcnt(12)\n"
"v_mfma_f32_16x16x32_bf16 v[84:87], v[108:111], v[152:155], v[84:87]\n"
"ds_read_b64_tr_b16 v[104:105], v244 offset:19200\n"
"v_mfma_f32_16x16x32_bf16 v[48:51], v[108:111], v[136:139], v[48:51]\n"
"ds_read_b64_tr_b16 v[106:107], v244 offset:27392\n"
"v_mfma_f32_16x16x32_bf16 v[96:99], v[108:111], v[168:171], v[96:99]\n"
"s_waitcnt vmcnt(19) lgkmcnt(12)\n"
"v_mfma_f32_16x16x32_bf16 v[52:55], v[112:115], v[156:159], v[52:55]\n"
"ds_read_b64_tr_b16 v[108:109], v245 offset:19200\n"
"s_waitcnt vmcnt(18)\n"
"v_mfma_f32_16x16x32_bf16 v[16:19], v[112:115], v[140:143], v[16:19]\n"
"ds_read_b64_tr_b16 v[110:111], v245 offset:27392\n"
"s_waitcnt lgkmcnt(12)\n"
"v_mfma_f32_16x16x32_bf16 v[56:59], v[116:119], v[156:159], v[56:59]\n"
"ds_read_b64_tr_b16 v[112:113], v246 offset:3072\n"
"v_mfma_f32_16x16x32_bf16 v[20:23], v[116:119], v[140:143], v[20:23]\n"
"ds_read_b64_tr_b16 v[114:115], v246 offset:3584\n"
"s_waitcnt lgkmcnt(12)\n"
"v_mfma_f32_16x16x32_bf16 v[60:63], v[120:123], v[156:159], v[60:63]\n"
"v_mfma_f32_16x16x32_bf16 v[24:27], v[120:123], v[140:143], v[24:27]\n"
"v_mfma_f32_16x16x32_bf16 v[88:91], v[120:123], v[172:175], v[88:91]\n"
"s_waitcnt lgkmcnt(10)\n"
"v_mfma_f32_16x16x32_bf16 v[64:67], v[124:127], v[156:159], v[64:67]\n"
"s_waitcnt vmcnt(6)\n"
"ds_write_b128 v247, v[240:243] offset:1024\n"
"v_mfma_f32_16x16x32_bf16 v[28:31], v[124:127], v[140:143], v[28:31]\n"
"v_mfma_f32_16x16x32_bf16 v[92:95], v[124:127], v[172:175], v[92:95]\n"
"s_waitcnt lgkmcnt(9)\n"
"v_mfma_f32_16x16x32_bf16 v[68:71], v[224:227], v[156:159], v[68:71]\n"
"s_waitcnt lgkmcnt(0)\n"
"s_barrier\n"
"ds_read_b64_tr_b16 v[116:117], v244 offset:33024\n"
"ds_read_b64_tr_b16 v[120:121], v245 offset:33024\n"
"ds_read_b64_tr_b16 v[124:125], v244 offset:33280\n"
"v_mfma_f32_16x16x32_bf16 v[32:35], v[224:227], v[140:143], v[32:35]\n"
"ds_read_b128 v[208:211], v247 offset:0\n"
"ds_read_b64_tr_b16 v[118:119], v244 offset:41216\n"
"ds_read_b64_tr_b16 v[122:123], v245 offset:41216\n"
"ds_read_b64_tr_b16 v[126:127], v244 offset:41472\n"
"v_mfma_f32_16x16x32_bf16 v[72:75], v[100:103], v[156:159], v[72:75]\n"
"ds_read_b128 v[212:215], v247 offset:1024\n"
"ds_read_b64_tr_b16 v[224:225], v245 offset:33280\n"
"v_mfma_f32_16x16x32_bf16 v[36:39], v[100:103], v[140:143], v[36:39]\n"
"ds_read_b128 v[216:219], v247 offset:2048\n"
"ds_read_b64_tr_b16 v[226:227], v245 offset:41472\n"
"v_mfma_f32_16x16x32_bf16 v[76:79], v[104:107], v[156:159], v[76:79]\n"
"ds_read_b128 v[220:223], v247 offset:3072\n"
"ds_read_b64_tr_b16 v[100:101], v244 offset:33536\n"
"v_mfma_f32_16x16x32_bf16 v[40:43], v[104:107], v[140:143], v[40:43]\n"
"ds_read_b64_tr_b16 v[102:103], v244 offset:41728\n"
"v_mfma_f32_16x16x32_bf16 v[80:83], v[108:111], v[156:159], v[80:83]\n"
"ds_read_b64_tr_b16 v[104:105], v245 offset:33536\n"
"v_mfma_f32_16x16x32_bf16 v[44:47], v[108:111], v[140:143], v[44:47]\n"
"ds_read_b64_tr_b16 v[106:107], v245 offset:41728\n"
"v_mfma_f32_16x16x32_bf16 v[84:87], v[112:115], v[156:159], v[84:87]\n"
"ds_read_b64_tr_b16 v[108:109], v246 offset:4096\n"
"v_mfma_f32_16x16x32_bf16 v[48:51], v[112:115], v[140:143], v[48:51]\n"
"ds_read_b64_tr_b16 v[110:111], v246 offset:4608\n"
"v_mfma_f32_16x16x32_bf16 v[96:99], v[112:115], v[172:175], v[96:99]\n"
"s_waitcnt lgkmcnt(13)\n"
"v_mfma_f32_16x16x32_bf16 v[60:63], v[116:119], v[192:195], v[60:63]\n"
"ds_read_b64_tr_b16 v[112:113], v244 offset:35072\n"
"v_mfma_f32_16x16x32_bf16 v[24:27], v[116:119], v[176:179], v[24:27]\n"
"ds_read_b64_tr_b16 v[114:115], v244 offset:43264\n"
"v_mfma_f32_16x16x32_bf16 v[88:91], v[116:119], v[208:211], v[88:91]\n"
"s_waitcnt lgkmcnt(14)\n"
"v_mfma_f32_16x16x32_bf16 v[64:67], v[120:123], v[192:195], v[64:67]\n"
"ds_read_b64_tr_b16 v[116:117], v245 offset:35072\n"
"s_add_u32 s68, %[hb], 8192\n"
"v_add_u32 v229, s68, %[l16]\n"
"v_xor_b32 v230, 65536, v229\n"
"v_mfma_f32_16x16x32_bf16 v[28:31], v[120:123], v[176:179], v[28:31]\n"
"ds_read_b64_tr_b16 v[118:119], v245 offset:43264\n"
"v_xor_b32 v234, 8192, v247\n"
"s_lshr_b32 s64, %[kq64], 6\n"
"s_lshr_b32 s65, %[hb], 6\n"
"v_mfma_f32_16x16x32_bf16 v[92:95], v[120:123], v[208:211], v[92:95]\n"
"s_add_u32 s60, s64, 1\n"
"s_and_b32 s60, s60, 3\n"
"s_lshl_b32 s60, s60, 16\n"
"s_waitcnt lgkmcnt(15)\n"
"v_mfma_f32_16x16x32_bf16 v[68:71], v[124:127], v[192:195], v[68:71]\n"
"ds_read_b64_tr_b16 v[120:121], v244 offset:35328\n"
"s_add_u32 s60, s60, %[pbase]\n"
"s_add_u32 s60, s60, 0\n"
"s_add_u32 s61, s64, 2\n"
"v_mfma_f32_16x16x32_bf16 v[32:35], v[124:127], v[176:179], v[32:35]\n"
"ds_read_b64_tr_b16 v[122:123], v244 offset:43520\n"
"s_and_b32 s61, s61, 3\n"
"s_lshl_b32 s61, s61, 16\n"
"s_add_u32 s61, s61, %[pbase]\n"
"s_waitcnt lgkmcnt(13)\n"
"v_mfma_f32_16x16x32_bf16 v[72:75], v[224:227], v[192:195], v[72:75]\n"
"ds_read_b64_tr_b16 v[124:125], v245 offset:35328\n"
"s_add_u32 s61, s61, 21504\n"
"s_add_u32 s62, s64, 3\n"
"s_and_b32 s62, s62, 3\n"
"v_mfma_f32_16x16x32_bf16 v[36:39], v[224:227], v[176:179], v[36:39]\n"
"ds_read_b64_tr_b16 v[126:127], v245 offset:43520\n"
"s_lshl_b32 s62, s62, 16\n"
"s_add_u32 s62, s62, %[pbase]\n"
"s_add_u32 s62, s62, 43008\n"
"s_waitcnt lgkmcnt(12)\n"
"v_mfma_f32_16x16x32_bf16 v[76:79], v[100:103], v[192:195], v[76:79]\n"
"ds_read_b64_tr_b16 v[224:225], v244 offset:35584\n"
"s_lshl_b32 s63, s64, 16\n"
"s_add_u32 s63, s63, %[pbase]\n"
"s_lshl_b32 s66, s65, 1\n"
"v_mfma_f32_16x16x32_bf16 v[40:43], v[100:103], v[176:179], v[40:43]\n"
"ds_read_b64_tr_b16 v[226:227], v244 offset:43776\n"
"s_add_u32 s66, s66, 4096\n"
"v_add_u32_e64 v232, s66, %[l16]\n"
"s_add_u32 s66, s65, 16384\n"
"s_waitcnt lgkmcnt(12)\n"
"v_mfma_f32_16x16x32_bf16 v[80:83], v[104:107], v[192:195], v[80:83]\n"
"ds_read_b64_tr_b16 v[100:101], v245 offset:35584\n"
"v_add_u32 v228, s66, %[l16]\n"
"s_sub_u32 s67, 4, s64\n"
"s_and_b32 s67, s67, 3\n"
"v_mfma_f32_16x16x32_bf16 v[44:47], v[104:107], v[176:179], v[44:47]\n"
"ds_read_b64_tr_b16 v[102:103], v245 offset:43776\n"
"s_mul_i32 s67, s67, 21504\n"
"s_add_u32 s67, s67, %[pbase]\n"
"s_add_u32 s67, s67, 65536\n"
"s_waitcnt lgkmcnt(12)\n"
"v_mfma_f32_16x16x32_bf16 v[84:87], v[108:111], v[192:195], v[84:87]\n"
"ds_read_b64_tr_b16 v[104:105], v246 offset:5120\n"
"s_sub_u32 s66, 3, s64\n"
"s_and_b32 s66, s66, 3\n"
"s_mul_i32 s66, s66, 21504\n"
"v_mfma_f32_16x16x32_bf16 v[48:51], v[108:111], v[176:179], v[48:51]\n"
"ds_read_b64_tr_b16 v[106:107], v246 offset:5632\n"
"s_add_u32 s66, s66, %[pbase]\n"
"s_add_u32 s66, s66, 0\n"
"s_add_u32 s66, s66, 4096\n"
"v_mfma_f32_16x16x32_bf16 v[96:99], v[108:111], v[208:211], v[96:99]\n"
"v_lshrrev_b32 v231, 4, %[l16]\n"
"v_and_b32_e64 v231, 15, v231\n"
"s_movk_i32 s68, 2304\n"
"s_waitcnt lgkmcnt(12)\n"
"v_mfma_f32_16x16x32_bf16 v[60:63], v[112:115], v[196:199], v[60:63]\n"
"v_mul_u32_u24 v231, s68, v231\n"
"v_lshrrev_b32_e64 v233, 8, %[l16]\n"
"v_lshl_add_u32 v231, v233, 3, v231\n"
"v_mfma_f32_16x16x32_bf16 v[24:27], v[112:115], v[180:183], v[24:27]\n"
"v_mfma_f32_16x16x32_bf16 v[88:91], v[112:115], v[212:215], v[88:91]\n"
"s_waitcnt lgkmcnt(10)\n"
"v_mfma_f32_16x16x32_bf16 v[64:67], v[116:119], v[196:199], v[64:67]\n"
"v_mfma_f32_16x16x32_bf16 v[28:31], v[116:119], v[180:183], v[28:31]\n"
"v_mfma_f32_16x16x32_bf16 v[92:95], v[116:119], v[212:215], v[92:95]\n"
"s_waitcnt lgkmcnt(8)\n"
"v_mfma_f32_16x16x32_bf16 v[68:71], v[120:123], v[196:199], v[68:71]\n"
"s_waitcnt vmcnt(2) lgkmcnt(0)\n"
"s_barrier\n"
"ds_read_b64_tr_b16 v[108:109], v244 offset:49408\n"
"ds_read_b64_tr_b16 v[112:113], v245 offset:49408\n"
"ds_read_b64_tr_b16 v[116:117], v244 offset:51456\n"
"v_mfma_f32_16x16x32_bf16 v[32:35], v[120:123], v[180:183], v[32:35]\n"
"ds_read_b64_tr_b16 v[110:111], v244 offset:57600\n"
"ds_read_b64_tr_b16 v[114:115], v245 offset:57600\n"
"ds_read_b64_tr_b16 v[118:119], v244 offset:59648\n"
"v_mfma_f32_16x16x32_bf16 v[72:75], v[124:127], v[196:199], v[72:75]\n"
"ds_read_b64_tr_b16 v[120:121], v245 offset:51456\n"
"v_mfma_f32_16x16x32_bf16 v[36:39], v[124:127], v[180:183], v[36:39]\n"
"ds_read_b64_tr_b16 v[122:123], v245 offset:59648\n"
"v_mfma_f32_16x16x32_bf16 v[76:79], v[224:227], v[196:199], v[76:79]\n"
"ds_read_b64_tr_b16 v[124:125], v244 offset:49664\n"
"v_mfma_f32_16x16x32_bf16 v[40:43], v[224:227], v[180:183], v[40:43]\n"
"ds_read_b64_tr_b16 v[126:127], v244 offset:57856\n"
"v_mfma_f32_16x16x32_bf16 v[80:83], v[100:103], v[196:199], v[80:83]\n"
"ds_read_b64_tr_b16 v[224:225], v245 offset:49664\n"
"v_mfma_f32_16x16x32_bf16 v[44:47], v[100:103], v[180:183], v[44:47]\n"
"ds_read_b64_tr_b16 v[226:227], v245 offset:57856\n"
"v_mfma_f32_16x16x32_bf16 v[84:87], v[104:107], v[196:199], v[84:87]\n"
"ds_read_b64_tr_b16 v[100:101], v244 offset:51712\n"
"v_mfma_f32_16x16x32_bf16 v[48:51], v[104:107], v[180:183], v[48:51]\n"
"ds_read_b64_tr_b16 v[102:103], v244 offset:59904\n"
"v_mfma_f32_16x16x32_bf16 v[96:99], v[104:107], v[212:215], v[96:99]\n"
"s_waitcnt lgkmcnt(10)\n"
"v_mfma_f32_16x16x32_bf16 v[60:63], v[108:111], v[200:203], v[60:63]\n"
"ds_read_b64_tr_b16 v[104:105], v245 offset:51712\n"
"v_mfma_f32_16x16x32_bf16 v[24:27], v[108:111], v[184:187], v[24:27]\n"
"ds_read_b64_tr_b16 v[106:107], v245 offset:59904\n"
"v_mfma_f32_16x16x32_bf16 v[88:91], v[108:111], v[216:219], v[88:91]\n"
"s_waitcnt lgkmcnt(11)\n"
"v_mfma_f32_16x16x32_bf16 v[64:67], v[112:115], v[200:203], v[64:67]\n"
"ds_read_b64_tr_b16 v[108:109], v244 offset:49920\n"
"v_mfma_f32_16x16x32_bf16 v[28:31], v[112:115], v[184:187], v[28:31]\n"
"ds_read_b64_tr_b16 v[110:111], v244 offset:58112\n"
"v_mfma_f32_16x16x32_bf16 v[92:95], v[112:115], v[216:219], v[92:95]\n"
"s_waitcnt vmcnt(1) lgkmcnt(12)\n"
"v_mfma_f32_16x16x32_bf16 v[60:63], v[116:119], v[204:207], v[60:63]\n"
"ds_read_b64_tr_b16 v[112:113], v245 offset:49920\n"
"s_waitcnt vmcnt(0)\n"
"v_mfma_f32_16x16x32_bf16 v[24:27], v[116:119], v[188:191], v[24:27]\n"
"ds_read_b64_tr_b16 v[114:115], v245 offset:58112\n"
"v_mfma_f32_16x16x32_bf16 v[88:91], v[116:119], v[220:223], v[88:91]\n"
"s_waitcnt lgkmcnt(12)\n"
"v_mfma_f32_16x16x32_bf16 v[64:67], v[120:123], v[204:207], v[64:67]\n"
"ds_read_b64_tr_b16 v[116:117], v246 offset:6144\n"
"v_mfma_f32_16x16x32_bf16 v[28:31], v[120:123], v[188:191], v[28:31]\n"
"ds_read_b64_tr_b16 v[118:119], v246 offset:6656\n"
"ds_write_b128 v229, v[60:63] offset:0\n"
"v_mfma_f32_16x16x32_bf16 v[92:95], v[120:123], v[220:223], v[92:95]\n"
"s_waitcnt lgkmcnt(13)\n"
"v_mfma_f32_16x16x32_bf16 v[68:71], v[124:127], v[200:203], v[68:71]\n"
"ds_read_b64_tr_b16 v[120:121], v244 offset:51968\n"
"v_mfma_f32_16x16x32_bf16 v[32:35], v[124:127], v[184:187], v[32:35]\n"
"ds_read_b64_tr_b16 v[122:123], v244 offset:60160\n"
"ds_write_b128 v229, v[64:67] offset:1024\n"
"s_waitcnt lgkmcnt(14)\n"
"v_mfma_f32_16x16x32_bf16 v[72:75], v[224:227], v[200:203], v[72:75]\n"
"ds_read_b64_tr_b16 v[124:125], v245 offset:51968\n"
"v_mfma_f32_16x16x32_bf16 v[36:39], v[224:227], v[184:187], v[36:39]\n"
"ds_read_b64_tr_b16 v[126:127], v245 offset:60160\n"
"ds_write_b128 v229, v[92:95] offset:7168\n"
"s_waitcnt lgkmcnt(15)\n"
"v_mfma_f32_16x16x32_bf16 v[68:71], v[100:103], v[204:207], v[68:71]\n"
"ds_read_b64_tr_b16 v[224:225], v246 offset:7168\n"
"v_mfma_f32_16x16x32_bf16 v[32:35], v[100:103], v[188:191], v[32:35]\n"
"ds_read_b64_tr_b16 v[226:227], v246 offset:7680\n"
"s_waitcnt lgkmcnt(15)\n"
"v_mfma_f32_16x16x32_bf16 v[72:75], v[104:107], v[204:207], v[72:75]\n"
"ds_read_b64_tr_b16 v[100:101], v244 offset:32768\n"
"v_mfma_f32_16x16x32_bf16 v[36:39], v[104:107], v[188:191], v[36:39]\n"
"ds_read_b64_tr_b16 v[102:103], v244 offset:40960\n"
"s_waitcnt lgkmcnt(15)\n"
"v_mfma_f32_16x16x32_bf16 v[76:79], v[108:111], v[200:203], v[76:79]\n"
"ds_read_b64_tr_b16 v[104:105], v245 offset:32768\n"
"ds_write_b128 v229, v[68:71] offset:2048\n"
"v_mfma_f32_16x16x32_bf16 v[40:43], v[108:111], v[184:187], v[40:43]\n"
"ds_read_b64_tr_b16 v[106:107], v245 offset:40960\n"
"s_waitcnt lgkmcnt(15)\n"
"v_mfma_f32_16x16x32_bf16 v[80:83], v[112:115], v[200:203], v[80:83]\n"
"ds_read_b64_tr_b16 v[108:109], v244 offset:34816\n"
"ds_write_b128 v229, v[72:75] offset:3072\n"
"v_mfma_f32_16x16x32_bf16 v[44:47], v[112:115], v[184:187], v[44:47]\n"
"ds_read_b64_tr_b16 v[110:111], v244 offset:43008\n"
"s_waitcnt lgkmcnt(15)\n"
"v_mfma_f32_16x16x32_bf16 v[84:87], v[116:119], v[200:203], v[84:87]\n"
"ds_read_b64_tr_b16 v[112:113], v245 offset:34816\n"
"v_mfma_f32_16x16x32_bf16 v[48:51], v[116:119], v[184:187], v[48:51]\n"
"ds_read_b64_tr_b16 v[114:115], v245 offset:43008\n"
"v_mfma_f32_16x16x32_bf16 v[96:99], v[116:119], v[216:219], v[96:99]\n"
"s_waitcnt lgkmcnt(15)\n"
"v_mfma_f32_16x16x32_bf16 v[76:79], v[120:123], v[204:207], v[76:79]\n"
"ds_read_b64_tr_b16 v[116:117], v244 offset:49152\n"
"s_waitcnt lgkmcnt(4)\n"
"s_barrier\n"
"ds_read_b128 v[128:131], v230 offset:1024\n"
"ds_read_b128 v[132:135], v230 offset:0\n"
"v_mfma_f32_16x16x32_bf16 v[40:43], v[120:123], v[188:191], v[40:43]\n"
"ds_read_b64_tr_b16 v[118:119], v244 offset:57344\n"
"ds_read_b128 v[136:139], v230 offset:7168\n"
"ds_read_b128 v[140:143], v230 offset:3072\n"
"v_mfma_f32_16x16x32_bf16 v[80:83], v[124:127], v[204:207], v[80:83]\n"
"ds_read_b64_tr_b16 v[120:121], v245 offset:49152\n"
"ds_read_b128 v[144:147], v230 offset:2048\n"
"v_mfma_f32_16x16x32_bf16 v[44:47], v[124:127], v[188:191], v[44:47]\n"
"ds_read_b64_tr_b16 v[122:123], v245 offset:57344\n"
"s_waitcnt lgkmcnt(7)\n"
"v_pk_add_f32 v[24:25], v[24:25], v[128:129]\n"
"v_pk_add_f32 v[26:27], v[26:27], v[130:131]\n"
"buffer_store_dwordx4 v[24:27], v232, s[80:83], s60 offen sc1\n"
"v_mfma_f32_16x16x32_bf16 v[84:87], v[224:227], v[204:207], v[84:87]\n"
"ds_read_b64_tr_b16 v[124:125], v244 offset:51200\n"
"s_waitcnt lgkmcnt(7)\n"
"v_pk_add_f32 v[28:29], v[28:29], v[132:133]\n"
"v_pk_add_f32 v[30:31], v[30:31], v[134:135]\n"
"buffer_store_dwordx4 v[28:31], v232, s[80:83], s60 offen offset:1024 sc1\n"
"ds_write_b128 v229, v[76:79] offset:4096\n"
"v_mfma_f32_16x16x32_bf16 v[48:51], v[224:227], v[188:191], v[48:51]\n"
"ds_read_b64_tr_b16 v[126:127], v244 offset:59392\n"
"s_waitcnt lgkmcnt(7)\n"
"v_pk_add_f32 v[88:89], v[88:89], v[136:137]\n"
"v_pk_add_f32 v[90:91], v[90:91], v[138:139]\n"
"buffer_store_dwordx4 v[88:91], v228, s[80:83], s60 offen offset:2048 sc1\n"
"v_mfma_f32_16x16x32_bf16 v[96:99], v[224:227], v[220:223], v[96:99]\n"
"s_waitcnt lgkmcnt(6)\n"
"v_pk_add_f32 v[32:33], v[32:33], v[140:141]\n"
"v_pk_add_f32 v[34:35], v[34:35], v[142:143]\n"
"buffer_store_dwordx4 v[32:35], v232, s[80:83], s61 offen sc1\n"
"ds_write_b128 v229, v[80:83] offset:5120\n"
"v_mfma_f32_16x16x32_bf16 v[52:55], v[100:103], v[192:195], v[52:55]\n"
"ds_read_b64_tr_b16 v[224:225], v245 offset:51200\n"
"s_waitcnt lgkmcnt(6)\n"
"v_pk_add_f32 v[36:37], v[36:37], v[144:145]\n"
"v_pk_add_f32 v[38:39], v[38:39], v[146:147]\n"
"buffer_store_dwordx4 v[36:39], v232, s[80:83], s61 offen offset:1024 sc1\n"
"v_mfma_f32_16x16x32_bf16 v[16:19], v[100:103], v[176:179], v[16:19]\n"
"ds_read_b64_tr_b16 v[226:227], v245 offset:59392\n"
"ds_write_b128 v229, v[84:87] offset:6144\n"
"v_mfma_f32_16x16x32_bf16 v[56:59], v[104:107], v[192:195], v[56:59]\n"
"v_mfma_f32_16x16x32_bf16 v[20:23], v[104:107], v[176:179], v[20:23]\n"
"ds_write_b128 v247, v[96:99] offset:4096\n"
"v_mfma_f32_16x16x32_bf16 v[52:55], v[108:111], v[196:199], v[52:55]\n"
"v_mfma_f32_16x16x32_bf16 v[16:19], v[108:111], v[180:183], v[16:19]\n"
"v_mfma_f32_16x16x32_bf16 v[56:59], v[112:115], v[196:199], v[56:59]\n"
"s_waitcnt lgkmcnt(0)\n"
"s_barrier\n"
"ds_read_b128 v[148:151], v230 offset:5120\n"
"ds_read_b128 v[152:155], v230 offset:4096\n"
"v_mfma_f32_16x16x32_bf16 v[20:23], v[112:115], v[180:183], v[20:23]\n"
"ds_read_b128 v[156:159], v230 offset:6144\n"
"ds_read_b128 v[160:163], v234 offset:4096\n"
"v_mfma_f32_16x16x32_bf16 v[52:55], v[116:119], v[200:203], v[52:55]\n"
"v_mfma_f32_16x16x32_bf16 v[16:19], v[116:119], v[184:187], v[16:19]\n"
"s_waitcnt lgkmcnt(3)\n"
"v_pk_add_f32 v[40:41], v[40:41], v[148:149]\n"
"v_pk_add_f32 v[42:43], v[42:43], v[150:151]\n"
"buffer_store_dwordx4 v[40:43], v232, s[80:83], s62 offen sc1\n"
"v_mfma_f32_16x16x32_bf16 v[56:59], v[120:123], v[200:203], v[56:59]\n"
"s_waitcnt lgkmcnt(2)\n"
"v_pk_add_f32 v[44:45], v[44:45], v[152:153]\n"
"v_pk_add_f32 v[46:47], v[46:47], v[154:155]\n"
"buffer_store_dwordx4 v[44:47], v232, s[80:83], s62 offen offset:1024 sc1\n"
"v_mfma_f32_16x16x32_bf16 v[20:23], v[120:123], v[184:187], v[20:23]\n"
"s_waitcnt lgkmcnt(1)\n"
"v_pk_add_f32 v[48:49], v[48:49], v[156:157]\n"
"v_pk_add_f32 v[50:51], v[50:51], v[158:159]\n"
"s_cmp_eq_u32 s64, 1\n"
"s_cbranch_scc1 LX1_%=\n"
"buffer_store_dwordx4 v[48:51], v228, s[80:83], s67 offen sc1\n"
"LX1_%=:\n"
"v_mfma_f32_16x16x32_bf16 v[52:55], v[124:127], v[204:207], v[52:55]\n"
"s_waitcnt lgkmcnt(0)\n"
"v_pk_add_f32 v[96:97], v[96:97], v[160:161]\n"
"v_pk_add_f32 v[98:99], v[98:99], v[162:163]\n"
"s_cmp_eq_u32 s64, 0\n"
"s_cbranch_scc1 LY1_%=\n"
"s_cmp_lg_u32 %[hb], 0\n"
"s_cbranch_scc1 LY1_%=\n"
"buffer_store_dwordx4 v[96:99], v228, s[80:83], s66 offen sc1\n"
"LY1_%=:\n"
"v_mfma_f32_16x16x32_bf16 v[16:19], v[124:127], v[188:191], v[16:19]\n"
"v_mfma_f32_16x16x32_bf16 v[56:59], v[224:227], v[204:207], v[56:59]\n"
"v_mfma_f32_16x16x32_bf16 v[20:23], v[224:227], v[188:191], v[20:23]\n"
"ds_write_b128 v229, v[52:55] offset:0\n"
"s_nop 5\n"
"ds_write_b128 v229, v[56:59] offset:1024\n"
"s_mul_i32 s60, s65, 36\n"
"s_add_u32 s60, s60, 73728\n"
"s_add_u32 s61, %[nb256], %[kq64]\n"
"s_lshr_b32 s62, %[nb256], 3\n"
"s_add_u32 s62, s62, 1024\n"
"s_lshr_b32 s65, s65, 5\n"
"s_waitcnt vmcnt(0) lgkmcnt(0)\n"
"s_barrier\n"
"ds_read_b128 v[192:195], v230 offset:1024\n"
"ds_read_b128 v[196:199], v230 offset:0\n"
"s_waitcnt lgkmcnt(1)\n"
"v_pk_add_f32 v[16:17], v[16:17], v[192:193]\n"
"v_pk_add_f32 v[18:19], v[18:19], v[194:195]\n"
"s_waitcnt lgkmcnt(0)\n"
"v_pk_add_f32 v[20:21], v[20:21], v[196:197]\n"
"v_pk_add_f32 v[22:23], v[22:23], v[198:199]\n"
"s_barrier\n"
"s_add_u32 s68, s63, 0\n"
"buffer_load_dwordx4 v[128:131], v232, s[80:83], s68 offen sc1\n"
"s_add_u32 s68, s63, 21504\n"
"buffer_load_dwordx4 v[132:135], v232, s[80:83], s68 offen sc1\n"
"s_add_u32 s68, s63, 43008\n"
"buffer_load_dwordx4 v[136:139], v232, s[80:83], s68 offen sc1\n"
"s_add_u32 s68, s63, 0\n"
"buffer_load_dwordx4 v[140:143], v232, s[80:83], s68 offen offset:1024 sc1\n"
"s_add_u32 s68, s63, 21504\n"
"buffer_load_dwordx4 v[144:147], v232, s[80:83], s68 offen offset:1024 sc1\n"
"s_add_u32 s68, s63, 43008\n"
"buffer_load_dwordx4 v[148:151], v232, s[80:83], s68 offen offset:1024 sc1\n"
"s_cmp_lg_u32 s64, 1\n"
"s_cbranch_scc1 LU1_%=\n"
"s_add_u32 s68, s63, 0\n"
"buffer_load_dwordx4 v[164:167], v228, s[80:83], s68 offen sc1\n"
"s_add_u32 s68, s63, 21504\n"
"buffer_load_dwordx4 v[168:171], v228, s[80:83], s68 offen sc1\n"
"s_add_u32 s68, s63, 43008\n"
"buffer_load_dwordx4 v[172:175], v228, s[80:83], s68 offen sc1\n"
"LU1_%=:\n"
"s_or_b32 s68, s64, %[hb]\n"
"s_cbranch_scc1 LV1_%=\n"
"s_add_u32 s68, s63, 4096\n"
"buffer_load_dwordx4 v[176:179], v228, s[80:83], s68 offen sc1\n"
"s_add_u32 s68, s63, 25600\n"
"buffer_load_dwordx4 v[180:183], v228, s[80:83], s68 offen sc1\n"
"s_add_u32 s68, s63, 47104\n"
"buffer_load_dwordx4 v[184:187], v228, s[80:83], s68 offen sc1\n"
"LV1_%=:\n"
"s_waitcnt vmcnt(0)\n"
"v_pk_add_f32 v[128:129], v[128:129], v[132:133]\n"
"v_pk_add_f32 v[130:131], v[130:131], v[134:135]\n"
"v_pk_add_f32 v[16:17], v[16:17], v[136:137]\n"
"v_pk_add_f32 v[18:19], v[18:19], v[138:139]\n"
"v_pk_add_f32 v[16:17], v[16:17], v[128:129]\n"
"v_pk_add_f32 v[18:19], v[18:19], v[130:131]\n"
"v_cvt_pk_bf16_f32 v16, v16, v17\n"
"v_cvt_pk_bf16_f32 v17, v18, v19\n"
"s_add_u32 s68, s60, s61\n"
"s_add_u32 s68, s68, s65\n"
"buffer_store_dwordx2 v[16:17], v231, s[84:87], s68 offen\n"
"v_pk_add_f32 v[140:141], v[140:141], v[144:145]\n"
"v_pk_add_f32 v[142:143], v[142:143], v[146:147]\n"
"v_pk_add_f32 v[20:21], v[20:21], v[148:149]\n"
"v_pk_add_f32 v[22:23], v[22:23], v[150:151]\n"
"v_pk_add_f32 v[20:21], v[20:21], v[140:141]\n"
"v_pk_add_f32 v[22:23], v[22:23], v[142:143]\n"
"v_cvt_pk_bf16_f32 v20, v20, v21\n"
"v_cvt_pk_bf16_f32 v21, v22, v23\n"
"s_xor_b32 s68, s65, 32\n"
"s_add_u32 s68, s68, s60\n"
"s_add_u32 s68, s68, s61\n"
"buffer_store_dwordx2 v[20:21], v231, s[84:87], s68 offen\n"
"s_cmp_lg_u32 s64, 1\n"
"s_cbranch_scc1 LW1_%=\n"
"v_pk_add_f32 v[164:165], v[164:165], v[168:169]\n"
"v_pk_add_f32 v[166:167], v[166:167], v[170:171]\n"
"v_pk_add_f32 v[48:49], v[48:49], v[172:173]\n"
"v_pk_add_f32 v[50:51], v[50:51], v[174:175]\n"
"v_pk_add_f32 v[48:49], v[48:49], v[164:165]\n"
"v_pk_add_f32 v[50:51], v[50:51], v[166:167]\n"
"v_cvt_pk_bf16_f32 v48, v48, v49\n"
"v_cvt_pk_bf16_f32 v49, v50, v51\n"
"s_add_u32 s68, s60, s62\n"
"buffer_store_dwordx2 v[48:49], v231, s[84:87], s68 offen\n"
"LW1_%=:\n"
"s_or_b32 s68, s64, %[hb]\n"
"s_cbranch_scc1 LZ1_%=\n"
"v_pk_add_f32 v[176:177], v[176:177], v[180:181]\n"
"v_pk_add_f32 v[178:179], v[178:179], v[182:183]\n"
"v_pk_add_f32 v[96:97], v[96:97], v[184:185]\n"
"v_pk_add_f32 v[98:99], v[98:99], v[186:187]\n"
"v_pk_add_f32 v[96:97], v[96:97], v[176:177]\n"
"v_pk_add_f32 v[98:99], v[98:99], v[178:179]\n"
"v_cvt_pk_bf16_f32 v96, v96, v97\n"
"v_cvt_pk_bf16_f32 v97, v98, v99\n"
"s_add_u32 s68, s62, 294912\n"
"buffer_store_dwordx2 v[96:97], v231, s[84:87], s68 offen\n"
"LZ1_%=:\n"
"s_branch LEND_%=\n"
"LZZ1_%=:\n"
"LFD_%=:\n"
"s_cmp_eq_u32 %[role], 4\n"
"s_cbranch_scc1 LF0_%=\n"
"s_cmp_eq_u32 %[role], 5\n"
"s_cbranch_scc1 LF1_%=\n"
"s_cmp_eq_u32 %[role], 6\n"
"s_cbranch_scc1 LF2_%=\n"
"s_branch LF3_%=\n"
".macro KB2 p0,p1,p2\n"
"s_barrier\n"
"s_cmp_lt_u32 s61, 0x36000\n"
"s_cbranch_scc0 7f\n"
"s_sleep 3\n"
"7:\n"
"s_sleep 1\n"
"s_setprio 1\n"
"ds_read_b128 v[232:235], v247 offset:4096\n"
"v_mfma_f32_16x16x32_bf16 v[32:35], v[176:179], v[224:227], v[32:35]\n"
"ds_read_b128 v[128:131], %[kb] offset:\\p0+2048\n"
"v_add_f32 v240, v240, v192\n"
"v_add_f32_e64 v241, v241, v196\n"
"v_mfma_f32_16x16x32_bf16 v[64:67], v[176:179], v[228:231], v[64:67]\n"
"ds_read_b128 v[132:135], %[kb] offset:\\p0+2304\n"
"v_add_f32 v240, v240, v193\n"
"v_add_f32 v241, v241, v197\n"
"v_mfma_f32_16x16x32_bf16 v[36:39], v[180:183], v[224:227], v[36:39]\n"
"ds_read_b128 v[136:139], %[kb] offset:\\p0+2560\n"
"v_add_f32 v240, v240, v194\n"
"v_add_f32 v241, v241, v198\n"
"v_mfma_f32_16x16x32_bf16 v[68:71], v[180:183], v[228:231], v[68:71]\n"
"ds_read_b128 v[140:143], %[kb] offset:\\p0+2816\n"
"v_add_f32 v240, v240, v195\n"
"v_add_f32 v241, v241, v199\n"
"v_mfma_f32_16x16x32_bf16 v[40:43], v[184:187], v[224:227], v[40:43]\n"
"ds_read_b128 v[144:147], %[kb] offset:\\p0+10240\n"
"v_add_f32 v240, v240, v200\n"
"v_add_f32 v241, v241, v204\n"
"v_mfma_f32_16x16x32_bf16 v[72:75], v[184:187], v[228:231], v[72:75]\n"
"ds_read_b128 v[148:151], %[kb] offset:\\p0+10496\n"
"v_add_f32 v240, v240, v201\n"
"v_add_f32 v241, v241, v205\n"
"v_mfma_f32_16x16x32_bf16 v[44:47], v[188:191], v[224:227], v[44:47]\n"
"ds_read_b128 v[152:155], %[kb] offset:\\p0+10752\n"
"v_add_f32 v240, v240, v202\n"
"v_add_f32 v241, v241, v206\n"
"v_mfma_f32_16x16x32_bf16 v[76:79], v[188:191], v[228:231], v[76:79]\n"
"ds_read_b128 v[156:159], %[kb] offset:\\p0+11008\n"
"v_add_f32 v240, v240, v203\n"
"v_add_f32 v241, v241, v207\n"
"v_mfma_f32_16x16x32_bf16 v[16:19], v[160:163], v[224:227], v[16:19]\n"
"ds_read_b64_tr_b16 v[176:177], %[vbe] offset:\\p0+512\n"
"v_mfma_f32_16x16x32_bf16 v[48:51], v[160:163], v[228:231], v[48:51]\n"
"ds_read_b64_tr_b16 v[178:179], %[vbe] offset:\\p0+8704\n"
"s_waitcnt lgkmcnt(10)\n"
"v_mfma_f32_16x16x32_bf16 v[80:83], v[160:163], v[232:235], v[80:83]\n"
"ds_read_b64_tr_b16 v[180:181], v243 offset:\\p0+512\n"
"v_mfma_f32_16x16x32_bf16 v[20:23], v[164:167], v[224:227], v[20:23]\n"
"ds_read_b64_tr_b16 v[182:183], v243 offset:\\p0+8704\n"
"v_mfma_f32_16x16x32_bf16 v[52:55], v[164:167], v[228:231], v[52:55]\n"
"ds_read_b64_tr_b16 v[184:185], %[vbe] offset:\\p0+768\n"
"v_mfma_f32_16x16x32_bf16 v[84:87], v[164:167], v[232:235], v[84:87]\n"
"ds_read_b64_tr_b16 v[186:187], %[vbe] offset:\\p0+8960\n"
"v_mfma_f32_16x16x32_bf16 v[24:27], v[168:171], v[224:227], v[24:27]\n"
"ds_read_b64_tr_b16 v[188:189], v243 offset:\\p0+768\n"
"v_mfma_f32_16x16x32_bf16 v[56:59], v[168:171], v[228:231], v[56:59]\n"
"ds_read_b64_tr_b16 v[190:191], v243 offset:\\p0+8960\n"
"v_mfma_f32_16x16x32_bf16 v[88:91], v[168:171], v[232:235], v[88:91]\n"
"ds_read_b64_tr_b16 v[160:161], %[vbe] offset:\\p0+0\n"
"v_mfma_f32_16x16x32_bf16 v[28:31], v[172:175], v[224:227], v[28:31]\n"
"ds_read_b64_tr_b16 v[162:163], %[vbe] offset:\\p0+8192\n"
"v_mfma_f32_16x16x32_bf16 v[60:63], v[172:175], v[228:231], v[60:63]\n"
"ds_read_b64_tr_b16 v[164:165], v243 offset:\\p0+0\n"
"v_mfma_f32_16x16x32_bf16 v[92:95], v[172:175], v[232:235], v[92:95]\n"
"ds_read_b64_tr_b16 v[166:167], v243 offset:\\p0+8192\n"
"s_waitcnt lgkmcnt(12)\n"
"v_mfma_f32_16x16x32_bf16 v[192:195], v[128:131], v[96:99], v[212:215]\n"
"ds_read_b64_tr_b16 v[168:169], %[vbe] offset:\\p0+256\n"
"v_mfma_f32_16x16x32_bf16 v[192:195], v[132:135], v[100:103], v[192:195]\n"
"ds_read_b64_tr_b16 v[170:171], %[vbe] offset:\\p0+8448\n"
"v_mfma_f32_16x16x32_bf16 v[192:195], v[136:139], v[104:107], v[192:195]\n"
"ds_read_b64_tr_b16 v[172:173], v243 offset:\\p0+256\n"
"v_mfma_f32_16x16x32_bf16 v[192:195], v[140:143], v[108:111], v[192:195]\n"
"ds_read_b64_tr_b16 v[174:175], v243 offset:\\p0+8448\n"
"v_mfma_f32_16x16x32_bf16 v[196:199], v[128:131], v[112:115], v[216:219]\n"
"v_mfma_f32_16x16x32_bf16 v[196:199], v[132:135], v[116:119], v[196:199]\n"
"v_mfma_f32_16x16x32_bf16 v[196:199], v[136:139], v[120:123], v[196:199]\n"
"v_mfma_f32_16x16x32_bf16 v[196:199], v[140:143], v[124:127], v[196:199]\n"
"v_mfma_f32_16x16x32_bf16 v[200:203], v[144:147], v[96:99], v[212:215]\n"
"v_mfma_f32_16x16x32_bf16 v[200:203], v[148:151], v[100:103], v[200:203]\n"
"v_mfma_f32_16x16x32_bf16 v[200:203], v[152:155], v[104:107], v[200:203]\n"
"v_mfma_f32_16x16x32_bf16 v[200:203], v[156:159], v[108:111], v[200:203]\n"
"v_mfma_f32_16x16x32_bf16 v[204:207], v[144:147], v[112:115], v[216:219]\n"
"v_mfma_f32_16x16x32_bf16 v[204:207], v[148:151], v[116:119], v[204:207]\n"
"v_mfma_f32_16x16x32_bf16 v[204:207], v[152:155], v[120:123], v[204:207]\n"
"v_mfma_f32_16x16x32_bf16 v[204:207], v[156:159], v[124:127], v[204:207]\n"
"s_waitcnt lgkmcnt(0)\n"
"s_barrier\n"
"s_mov_b32 m0, \\p1\n"
"s_setprio 0\n"
"buffer_load_dwordx4 %[g0], %[srdv], s61 offen nt lds\n"
"buffer_load_dwordx4 %[g1], %[srdv], s61 offen offset:1024 nt lds\n"
"buffer_load_dwordx4 %[g0], %[srdk], s61 offen offset:2048 nt lds\n"
"buffer_load_dwordx4 %[g1], %[srdk], s61 offen offset:3072 nt lds\n"
"s_mov_b32 m0, \\p2\n"
"s_nop 0\n"
"buffer_load_dwordx4 v248, %[srdk], s61 offen offset:3072 nt lds\n"
"s_add_u32 s61, s61, 0x2000\n"
"s_cmp_eq_u32 s61, 0x40000\n"
"v_exp_f32 v192, v192\n"
"v_exp_f32 v193, v193\n"
"v_exp_f32 v194, v194\n"
"v_exp_f32 v195, v195\n"
"v_cvt_pk_bf16_f32 v224, v192, v193\n"
"v_cvt_pk_bf16_f32 v225, v194, v195\n"
"v_exp_f32 v196, v196\n"
"v_exp_f32 v197, v197\n"
"v_exp_f32 v198, v198\n"
"v_exp_f32 v199, v199\n"
"v_cvt_pk_bf16_f32 v228, v196, v197\n"
"v_cvt_pk_bf16_f32 v229, v198, v199\n"
"v_exp_f32 v200, v200\n"
"v_exp_f32 v201, v201\n"
"v_exp_f32 v202, v202\n"
"v_exp_f32 v203, v203\n"
"v_cvt_pk_bf16_f32 v226, v200, v201\n"
"v_cvt_pk_bf16_f32 v227, v202, v203\n"
"v_exp_f32 v204, v204\n"
"v_exp_f32 v205, v205\n"
"v_exp_f32 v206, v206\n"
"v_exp_f32 v207, v207\n"
"v_cvt_pk_bf16_f32 v230, v204, v205\n"
"v_cvt_pk_bf16_f32 v231, v206, v207\n"
"s_waitcnt vmcnt(15)\n"
"s_cbranch_scc1 LQ2_%=\n"
".endm\n"
".macro KA2 p0,p1,p2,p3\n"
"s_barrier\n"
"s_sleep 3\n"
"s_setprio 1\n"
"ds_read_b128 v[232:235], v247 offset:4096\n"
"v_mfma_f32_16x16x32_bf16 v[32:35], v[176:179], v[224:227], v[32:35]\n"
"ds_read_b128 v[128:131], v244 offset:\\p0+2048\n"
"v_add_f32 v240, v240, v192\n"
"v_add_f32_e64 v241, v241, v196\n"
"v_mfma_f32_16x16x32_bf16 v[64:67], v[176:179], v[228:231], v[64:67]\n"
"ds_read_b128 v[132:135], v244 offset:\\p0+2304\n"
"v_add_f32 v240, v240, v193\n"
"v_add_f32 v241, v241, v197\n"
"v_mfma_f32_16x16x32_bf16 v[36:39], v[180:183], v[224:227], v[36:39]\n"
"ds_read_b128 v[136:139], v244 offset:\\p0+2560\n"
"v_add_f32 v240, v240, v194\n"
"v_add_f32 v241, v241, v198\n"
"v_mfma_f32_16x16x32_bf16 v[68:71], v[180:183], v[228:231], v[68:71]\n"
"ds_read_b128 v[140:143], v244 offset:\\p0+2816\n"
"v_add_f32 v240, v240, v195\n"
"v_add_f32 v241, v241, v199\n"
"v_mfma_f32_16x16x32_bf16 v[40:43], v[184:187], v[224:227], v[40:43]\n"
"ds_read_b128 v[144:147], v244 offset:\\p0+10240\n"
"v_add_f32 v240, v240, v200\n"
"v_add_f32 v241, v241, v204\n"
"v_mfma_f32_16x16x32_bf16 v[72:75], v[184:187], v[228:231], v[72:75]\n"
"ds_read_b128 v[148:151], v244 offset:\\p0+10496\n"
"v_add_f32 v240, v240, v201\n"
"v_add_f32 v241, v241, v205\n"
"v_mfma_f32_16x16x32_bf16 v[44:47], v[188:191], v[224:227], v[44:47]\n"
"ds_read_b128 v[152:155], v244 offset:\\p0+10752\n"
"v_add_f32 v240, v240, v202\n"
"v_add_f32 v241, v241, v206\n"
"v_mfma_f32_16x16x32_bf16 v[76:79], v[188:191], v[228:231], v[76:79]\n"
"ds_read_b128 v[156:159], v244 offset:\\p0+11008\n"
"v_add_f32 v240, v240, v203\n"
"v_add_f32 v241, v241, v207\n"
"v_mfma_f32_16x16x32_bf16 v[16:19], v[160:163], v[224:227], v[16:19]\n"
"ds_read_b64_tr_b16 v[176:177], v245 offset:\\p0+512\n"
"v_mfma_f32_16x16x32_bf16 v[48:51], v[160:163], v[228:231], v[48:51]\n"
"ds_read_b64_tr_b16 v[178:179], v245 offset:\\p0+8704\n"
"s_waitcnt lgkmcnt(10)\n"
"v_mfma_f32_16x16x32_bf16 v[80:83], v[160:163], v[232:235], v[80:83]\n"
"ds_read_b64_tr_b16 v[180:181], v246 offset:\\p0+512\n"
"v_mfma_f32_16x16x32_bf16 v[20:23], v[164:167], v[224:227], v[20:23]\n"
"ds_read_b64_tr_b16 v[182:183], v246 offset:\\p0+8704\n"
"v_mfma_f32_16x16x32_bf16 v[52:55], v[164:167], v[228:231], v[52:55]\n"
"ds_read_b64_tr_b16 v[184:185], v245 offset:\\p0+768\n"
"v_mfma_f32_16x16x32_bf16 v[84:87], v[164:167], v[232:235], v[84:87]\n"
"ds_read_b64_tr_b16 v[186:187], v245 offset:\\p0+8960\n"
"v_mfma_f32_16x16x32_bf16 v[24:27], v[168:171], v[224:227], v[24:27]\n"
"ds_read_b64_tr_b16 v[188:189], v246 offset:\\p0+768\n"
"v_mfma_f32_16x16x32_bf16 v[56:59], v[168:171], v[228:231], v[56:59]\n"
"ds_read_b64_tr_b16 v[190:191], v246 offset:\\p0+8960\n"
"v_mfma_f32_16x16x32_bf16 v[88:91], v[168:171], v[232:235], v[88:91]\n"
"ds_read_b64_tr_b16 v[160:161], v245 offset:\\p0+0\n"
"v_mfma_f32_16x16x32_bf16 v[28:31], v[172:175], v[224:227], v[28:31]\n"
"ds_read_b64_tr_b16 v[162:163], v245 offset:\\p0+8192\n"
"v_mfma_f32_16x16x32_bf16 v[60:63], v[172:175], v[228:231], v[60:63]\n"
"ds_read_b64_tr_b16 v[164:165], v246 offset:\\p0+0\n"
"v_mfma_f32_16x16x32_bf16 v[92:95], v[172:175], v[232:235], v[92:95]\n"
"ds_read_b64_tr_b16 v[166:167], v246 offset:\\p0+8192\n"
"s_waitcnt lgkmcnt(12)\n"
"v_mfma_f32_16x16x32_bf16 v[192:195], v[128:131], v[96:99], v[212:215]\n"
"ds_read_b64_tr_b16 v[168:169], v245 offset:\\p0+256\n"
"v_mfma_f32_16x16x32_bf16 v[192:195], v[132:135], v[100:103], v[192:195]\n"
"ds_read_b64_tr_b16 v[170:171], v245 offset:\\p0+8448\n"
"v_mfma_f32_16x16x32_bf16 v[192:195], v[136:139], v[104:107], v[192:195]\n"
"ds_read_b64_tr_b16 v[172:173], v246 offset:\\p0+256\n"
"v_mfma_f32_16x16x32_bf16 v[192:195], v[140:143], v[108:111], v[192:195]\n"
"ds_read_b64_tr_b16 v[174:175], v246 offset:\\p0+8448\n"
"v_mfma_f32_16x16x32_bf16 v[196:199], v[128:131], v[112:115], v[216:219]\n"
"v_mfma_f32_16x16x32_bf16 v[196:199], v[132:135], v[116:119], v[196:199]\n"
"v_mfma_f32_16x16x32_bf16 v[196:199], v[136:139], v[120:123], v[196:199]\n"
"v_mfma_f32_16x16x32_bf16 v[196:199], v[140:143], v[124:127], v[196:199]\n"
"v_mfma_f32_16x16x32_bf16 v[200:203], v[144:147], v[96:99], v[212:215]\n"
"v_mfma_f32_16x16x32_bf16 v[200:203], v[148:151], v[100:103], v[200:203]\n"
"v_mfma_f32_16x16x32_bf16 v[200:203], v[152:155], v[104:107], v[200:203]\n"
"v_mfma_f32_16x16x32_bf16 v[200:203], v[156:159], v[108:111], v[200:203]\n"
"v_mfma_f32_16x16x32_bf16 v[204:207], v[144:147], v[112:115], v[216:219]\n"
"v_mfma_f32_16x16x32_bf16 v[204:207], v[148:151], v[116:119], v[204:207]\n"
"v_mfma_f32_16x16x32_bf16 v[204:207], v[152:155], v[120:123], v[204:207]\n"
"v_mfma_f32_16x16x32_bf16 v[204:207], v[156:159], v[124:127], v[204:207]\n"
"s_waitcnt lgkmcnt(0)\n"
"s_barrier\n"
"s_mov_b32 m0, \\p1\n"
"s_setprio 0\n"
"buffer_load_dwordx4 %[g0], %[srdv], s61 offen nt lds\n"
"buffer_load_dwordx4 %[g1], %[srdv], s61 offen offset:1024 nt lds\n"
"v_exp_f32 v192, v192\n"
"v_exp_f32 v193, v193\n"
"v_exp_f32 v194, v194\n"
"v_exp_f32 v195, v195\n"
"v_cvt_pk_bf16_f32 v224, v192, v193\n"
"v_cvt_pk_bf16_f32 v225, v194, v195\n"
"v_exp_f32 v196, v196\n"
"v_exp_f32 v197, v197\n"
"v_exp_f32 v198, v198\n"
"v_exp_f32 v199, v199\n"
"v_cvt_pk_bf16_f32 v228, v196, v197\n"
"v_cvt_pk_bf16_f32 v229, v198, v199\n"
"v_exp_f32 v200, v200\n"
"v_exp_f32 v201, v201\n"
"v_exp_f32 v202, v202\n"
"v_exp_f32 v203, v203\n"
"v_cvt_pk_bf16_f32 v226, v200, v201\n"
"v_cvt_pk_bf16_f32 v227, v202, v203\n"
"v_exp_f32 v204, v204\n"
"v_exp_f32 v205, v205\n"
"v_exp_f32 v206, v206\n"
"v_exp_f32 v207, v207\n"
"v_cvt_pk_bf16_f32 v230, v204, v205\n"
"v_cvt_pk_bf16_f32 v231, v206, v207\n"
"s_waitcnt vmcnt(12)\n"
"s_barrier\n"
"s_sleep 3\n"
"s_setprio 1\n"
"ds_read_b128 v[232:235], v247 offset:4096\n"
"v_mfma_f32_16x16x32_bf16 v[32:35], v[176:179], v[224:227], v[32:35]\n"
"ds_read_b128 v[128:131], %[kb] offset:\\p2+2048\n"
"v_add_f32 v240, v240, v192\n"
"v_add_f32 v241, v241, v196\n"
"v_mfma_f32_16x16x32_bf16 v[64:67], v[176:179], v[228:231], v[64:67]\n"
"ds_read_b128 v[132:135], %[kb] offset:\\p2+2304\n"
"v_add_f32 v240, v240, v193\n"
"v_add_f32 v241, v241, v197\n"
"v_mfma_f32_16x16x32_bf16 v[36:39], v[180:183], v[224:227], v[36:39]\n"
"ds_read_b128 v[136:139], %[kb] offset:\\p2+2560\n"
"v_add_f32 v240, v240, v194\n"
"v_add_f32 v241, v241, v198\n"
"v_mfma_f32_16x16x32_bf16 v[68:71], v[180:183], v[228:231], v[68:71]\n"
"ds_read_b128 v[140:143], %[kb] offset:\\p2+2816\n"
"v_add_f32 v240, v240, v195\n"
"v_add_f32 v241, v241, v199\n"
"v_mfma_f32_16x16x32_bf16 v[40:43], v[184:187], v[224:227], v[40:43]\n"
"ds_read_b128 v[144:147], %[kb] offset:\\p2+10240\n"
"v_add_f32 v240, v240, v200\n"
"v_add_f32 v241, v241, v204\n"
"v_mfma_f32_16x16x32_bf16 v[72:75], v[184:187], v[228:231], v[72:75]\n"
"ds_read_b128 v[148:151], %[kb] offset:\\p2+10496\n"
"v_add_f32 v240, v240, v201\n"
"v_add_f32 v241, v241, v205\n"
"v_mfma_f32_16x16x32_bf16 v[44:47], v[188:191], v[224:227], v[44:47]\n"
"ds_read_b128 v[152:155], %[kb] offset:\\p2+10752\n"
"v_add_f32 v240, v240, v202\n"
"v_add_f32 v241, v241, v206\n"
"v_mfma_f32_16x16x32_bf16 v[76:79], v[188:191], v[228:231], v[76:79]\n"
"ds_read_b128 v[156:159], %[kb] offset:\\p2+11008\n"
"v_add_f32 v240, v240, v203\n"
"v_add_f32 v241, v241, v207\n"
"v_mfma_f32_16x16x32_bf16 v[16:19], v[160:163], v[224:227], v[16:19]\n"
"ds_read_b64_tr_b16 v[176:177], %[vbe] offset:\\p2+512\n"
"v_mfma_f32_16x16x32_bf16 v[48:51], v[160:163], v[228:231], v[48:51]\n"
"ds_read_b64_tr_b16 v[178:179], %[vbe] offset:\\p2+8704\n"
"s_waitcnt lgkmcnt(10)\n"
"v_mfma_f32_16x16x32_bf16 v[80:83], v[160:163], v[232:235], v[80:83]\n"
"ds_read_b64_tr_b16 v[180:181], v243 offset:\\p2+512\n"
"v_mfma_f32_16x16x32_bf16 v[20:23], v[164:167], v[224:227], v[20:23]\n"
"ds_read_b64_tr_b16 v[182:183], v243 offset:\\p2+8704\n"
"v_mfma_f32_16x16x32_bf16 v[52:55], v[164:167], v[228:231], v[52:55]\n"
"ds_read_b64_tr_b16 v[184:185], %[vbe] offset:\\p2+768\n"
"v_mfma_f32_16x16x32_bf16 v[84:87], v[164:167], v[232:235], v[84:87]\n"
"ds_read_b64_tr_b16 v[186:187], %[vbe] offset:\\p2+8960\n"
"v_mfma_f32_16x16x32_bf16 v[24:27], v[168:171], v[224:227], v[24:27]\n"
"ds_read_b64_tr_b16 v[188:189], v243 offset:\\p2+768\n"
"v_mfma_f32_16x16x32_bf16 v[56:59], v[168:171], v[228:231], v[56:59]\n"
"ds_read_b64_tr_b16 v[190:191], v243 offset:\\p2+8960\n"
"v_mfma_f32_16x16x32_bf16 v[88:91], v[168:171], v[232:235], v[88:91]\n"
"ds_read_b64_tr_b16 v[160:161], %[vbe] offset:\\p2+0\n"
"v_mfma_f32_16x16x32_bf16 v[28:31], v[172:175], v[224:227], v[28:31]\n"
"ds_read_b64_tr_b16 v[162:163], %[vbe] offset:\\p2+8192\n"
"v_mfma_f32_16x16x32_bf16 v[60:63], v[172:175], v[228:231], v[60:63]\n"
"ds_read_b64_tr_b16 v[164:165], v243 offset:\\p2+0\n"
"v_mfma_f32_16x16x32_bf16 v[92:95], v[172:175], v[232:235], v[92:95]\n"
"ds_read_b64_tr_b16 v[166:167], v243 offset:\\p2+8192\n"
"s_waitcnt lgkmcnt(12)\n"
"v_mfma_f32_16x16x32_bf16 v[192:195], v[128:131], v[96:99], v[212:215]\n"
"ds_read_b64_tr_b16 v[168:169], %[vbe] offset:\\p2+256\n"
"v_mfma_f32_16x16x32_bf16 v[192:195], v[132:135], v[100:103], v[192:195]\n"
"ds_read_b64_tr_b16 v[170:171], %[vbe] offset:\\p2+8448\n"
"v_mfma_f32_16x16x32_bf16 v[192:195], v[136:139], v[104:107], v[192:195]\n"
"ds_read_b64_tr_b16 v[172:173], v243 offset:\\p2+256\n"
"v_mfma_f32_16x16x32_bf16 v[192:195], v[140:143], v[108:111], v[192:195]\n"
"ds_read_b64_tr_b16 v[174:175], v243 offset:\\p2+8448\n"
"v_mfma_f32_16x16x32_bf16 v[196:199], v[128:131], v[112:115], v[216:219]\n"
"v_mfma_f32_16x16x32_bf16 v[196:199], v[132:135], v[116:119], v[196:199]\n"
"v_mfma_f32_16x16x32_bf16 v[196:199], v[136:139], v[120:123], v[196:199]\n"
"v_mfma_f32_16x16x32_bf16 v[196:199], v[140:143], v[124:127], v[196:199]\n"
"v_mfma_f32_16x16x32_bf16 v[200:203], v[144:147], v[96:99], v[212:215]\n"
"v_mfma_f32_16x16x32_bf16 v[200:203], v[148:151], v[100:103], v[200:203]\n"
"v_mfma_f32_16x16x32_bf16 v[200:203], v[152:155], v[104:107], v[200:203]\n"
"v_mfma_f32_16x16x32_bf16 v[200:203], v[156:159], v[108:111], v[200:203]\n"
"v_mfma_f32_16x16x32_bf16 v[204:207], v[144:147], v[112:115], v[216:219]\n"
"v_mfma_f32_16x16x32_bf16 v[204:207], v[148:151], v[116:119], v[204:207]\n"
"v_mfma_f32_16x16x32_bf16 v[204:207], v[152:155], v[120:123], v[204:207]\n"
"v_mfma_f32_16x16x32_bf16 v[204:207], v[156:159], v[124:127], v[204:207]\n"
"s_waitcnt lgkmcnt(0)\n"
"s_barrier\n"
"s_mov_b32 m0, \\p1\n"
"s_setprio 0\n"
"buffer_load_dwordx4 %[g0], %[srdk], s61 offen offset:2048 nt lds\n"
"buffer_load_dwordx4 %[g1], %[srdk], s61 offen offset:3072 nt lds\n"
"s_mov_b32 m0, \\p3\n"
"s_nop 0\n"
"buffer_load_dwordx4 v248, %[srdk], s61 offen offset:3072 nt lds\n"
"s_add_u32 s61, s61, 0x4000\n"
"s_cmp_eq_u32 s61, 0x40000\n"
"v_exp_f32 v192, v192\n"
"v_exp_f32 v193, v193\n"
"v_exp_f32 v194, v194\n"
"v_exp_f32 v195, v195\n"
"v_cvt_pk_bf16_f32 v224, v192, v193\n"
"v_cvt_pk_bf16_f32 v225, v194, v195\n"
"v_exp_f32 v196, v196\n"
"v_exp_f32 v197, v197\n"
"v_exp_f32 v198, v198\n"
"v_exp_f32 v199, v199\n"
"v_cvt_pk_bf16_f32 v228, v196, v197\n"
"v_cvt_pk_bf16_f32 v229, v198, v199\n"
"v_exp_f32 v200, v200\n"
"v_exp_f32 v201, v201\n"
"v_exp_f32 v202, v202\n"
"v_exp_f32 v203, v203\n"
"v_cvt_pk_bf16_f32 v226, v200, v201\n"
"v_cvt_pk_bf16_f32 v227, v202, v203\n"
"v_exp_f32 v204, v204\n"
"v_exp_f32 v205, v205\n"
"v_exp_f32 v206, v206\n"
"v_exp_f32 v207, v207\n"
"v_cvt_pk_bf16_f32 v230, v204, v205\n"
"v_cvt_pk_bf16_f32 v231, v206, v207\n"
".endm\n"
".p2align 3\n"
"LR2_%=:\n"
"s_mov_b32 s62, 0\n"
"s_mov_b32 s63, 0\n"
"s_mov_b32 s61, 0\n"
"s_lshr_b32 s60, %[hb], 5\n"
"s_lshr_b32 s64, %[hb], 6\n"
"v_lshrrev_b32 v198, 8, %[g0]\n"
"v_mul_u32_u24 v198, 7936, v198\n"
"v_add_u32 v198, %[g0], v198\n"
"v_xor_b32 v195, 32, v198\n"
"v_add_u32 v195, 31744, v195\n"
"v_xor_b32_e64 v193, 48, %[xoff]\n"
"s_add_u32 m0, %[hb], 36864\n"
"v_add_u32 v193, 8192, v193\n"
"s_add_u32 s62, s62, 0x40000\n"
"s_add_u32 m0, s60, 139264\n"
"s_nop 0\n"
"s_add_u32 s63, s63, 0x80\n"
"s_mul_i32 s91, %[xrot], 6\n"
"s_add_u32 m0, %[hb], 45056\n"
"s_nop 0\n"
"s_add_u32 s62, s62, 0x40000\n"
"s_add_u32 s52, %[hb], 8192\n"
"s_add_u32 s56, %[hb], 0\n"
"s_add_u32 s53, %[hb], 24576\n"
"s_add_u32 s57, %[hb], 16384\n"
"s_add_u32 s54, %[hb], 40960\n"
"s_add_u32 s58, %[hb], 32768\n"
"s_add_u32 s55, %[hb], 57344\n"
"s_add_u32 s59, %[hb], 49152\n"
"s_lshr_b32 s90, %[hb], 3\n"
"s_add_u32 s90, s90, 131072\n"
"v_xor_b32 v192, 32, %[wbe]\n"
"v_xor_b32 v243, 32, %[vbe]\n"
"v_add_u32 v194, 139264, %[xb]\n"
"v_add_u32_e64 v194, s60, v194\n"
"v_xor_b32 v196, 2048, v194\n"
"v_add_u32 v244, %[d1], %[kb]\n"
"v_add_u32 v245, %[d1], %[vbe]\n"
"v_add_u32 v246, %[d1], v243\n"
"s_lshr_b32 s65, %[hb], 3\n"
"s_add_u32 s65, s65, 147456\n"
"v_add_u32_e64 v247, s65, %[l16]\n"
"v_subrev_u32 v248, 4096, %[g1]\n"
"s_mov_b32 s66, 0x3e0293ee\n"
"s_mov_b32 s67, s66\n"
"s_waitcnt vmcnt(2)\n"
"s_barrier\n"
"ds_read_b128 v[96:99], v194 offset:0\n"
"ds_read_b128 v[100:103], v196 offset:0\n"
"ds_read_b128 v[104:107], %[xb] offset:57344\n"
"ds_read_b64_tr_b16 v[128:129], %[wbe] offset:32768\n"
"ds_read_b64_tr_b16 v[130:131], v192 offset:33792\n"
"ds_read_b64_tr_b16 v[132:133], v192 offset:32768\n"
"ds_read_b64_tr_b16 v[134:135], %[wbe] offset:33792\n"
"ds_read_b64_tr_b16 v[136:137], %[wbe] offset:33024\n"
"ds_read_b64_tr_b16 v[138:139], v192 offset:34048\n"
"ds_read_b64_tr_b16 v[140:141], v192 offset:33024\n"
"ds_read_b64_tr_b16 v[142:143], %[wbe] offset:34048\n"
"ds_read_b64_tr_b16 v[144:145], %[wbe] offset:33280\n"
"ds_read_b64_tr_b16 v[146:147], v192 offset:34304\n"
"ds_read_b64_tr_b16 v[148:149], v192 offset:33280\n"
"ds_read_b64_tr_b16 v[150:151], %[wbe] offset:34304\n"
"ds_read_b64_tr_b16 v[152:153], %[wbe] offset:33536\n"
"ds_read_b64_tr_b16 v[154:155], v192 offset:34560\n"
"ds_read_b64_tr_b16 v[156:157], v192 offset:33536\n"
"s_add_u32 m0, s60, 155648\n"
"ds_read_b64_tr_b16 v[158:159], %[wbe] offset:34560\n"
"buffer_load_dwordx4 %[xoff], %[srdx], s63 offen lds\n"
"buffer_load_dwordx4 v193, %[srdx], s63 offen offset:1024 lds\n"
"s_add_u32 s63, s63, 0x80\n"
"s_add_u32 m0, %[hb], 53248\n"
"s_nop 0\n"
"buffer_load_dwordx4 v198, %[srdw], s62 offen lds\n"
"buffer_load_dwordx4 v195, %[srdw], s62 offen offset:1024 lds\n"
"s_add_u32 s62, s62, 0x40000\n"
"s_waitcnt vmcnt(4) lgkmcnt(0)\n"
"s_barrier\n"
"v_mfma_f32_16x16x32_bf16 v[16:19], v[128:131], v[96:99], 0\n"
"ds_read_b128 v[108:111], v194 offset:512\n"
"ds_read_b128 v[112:115], v196 offset:512\n"
"v_mfma_f32_16x16x32_bf16 v[48:51], v[128:131], v[100:103], 0\n"
"ds_read_b128 v[116:119], %[xb] offset:57856\n"
"v_mfma_f32_16x16x32_bf16 v[20:23], v[132:135], v[96:99], 0\n"
"ds_read_b64_tr_b16 v[160:161], %[wbe] offset:40960\n"
"s_add_u32 m0, %[hb], 36864\n"
"v_mfma_f32_16x16x32_bf16 v[52:55], v[132:135], v[100:103], 0\n"
"buffer_load_dwordx4 v198, %[srdw], s62 offen lds\n"
"v_mfma_f32_16x16x32_bf16 v[24:27], v[136:139], v[96:99], 0\n"
"ds_read_b64_tr_b16 v[162:163], v192 offset:41984\n"
"ds_read_b64_tr_b16 v[164:165], v192 offset:40960\n"
"v_mfma_f32_16x16x32_bf16 v[56:59], v[136:139], v[100:103], 0\n"
"ds_read_b64_tr_b16 v[166:167], %[wbe] offset:41984\n"
"v_mfma_f32_16x16x32_bf16 v[28:31], v[140:143], v[96:99], 0\n"
"ds_read_b64_tr_b16 v[168:169], %[wbe] offset:41216\n"
"v_mfma_f32_16x16x32_bf16 v[60:63], v[140:143], v[100:103], 0\n"
"buffer_load_dwordx4 v195, %[srdw], s62 offen offset:1024 lds\n"
"s_add_u32 s62, s62, 0x40000\n"
"v_mfma_f32_16x16x32_bf16 v[32:35], v[144:147], v[96:99], 0\n"
"ds_read_b64_tr_b16 v[170:171], v192 offset:42240\n"
"v_mfma_f32_16x16x32_bf16 v[64:67], v[144:147], v[100:103], 0\n"
"ds_read_b64_tr_b16 v[172:173], v192 offset:41216\n"
"ds_read_b64_tr_b16 v[174:175], %[wbe] offset:42240\n"
"v_mfma_f32_16x16x32_bf16 v[80:83], v[144:147], v[104:107], 0\n"
"ds_read_b64_tr_b16 v[176:177], %[wbe] offset:41472\n"
"v_mfma_f32_16x16x32_bf16 v[36:39], v[148:151], v[96:99], 0\n"
"ds_read_b64_tr_b16 v[178:179], v192 offset:42496\n"
"v_mfma_f32_16x16x32_bf16 v[68:71], v[148:151], v[100:103], 0\n"
"ds_read_b64_tr_b16 v[180:181], v192 offset:41472\n"
"v_mfma_f32_16x16x32_bf16 v[84:87], v[148:151], v[104:107], 0\n"
"ds_read_b64_tr_b16 v[182:183], %[wbe] offset:42496\n"
"ds_read_b64_tr_b16 v[184:185], %[wbe] offset:41728\n"
"v_mfma_f32_16x16x32_bf16 v[40:43], v[152:155], v[96:99], 0\n"
"ds_read_b64_tr_b16 v[186:187], v192 offset:42752\n"
"v_mfma_f32_16x16x32_bf16 v[72:75], v[152:155], v[100:103], 0\n"
"ds_read_b64_tr_b16 v[188:189], v192 offset:41728\n"
"v_mfma_f32_16x16x32_bf16 v[44:47], v[156:159], v[96:99], 0\n"
"ds_read_b64_tr_b16 v[190:191], %[wbe] offset:42752\n"
"v_mfma_f32_16x16x32_bf16 v[76:79], v[156:159], v[100:103], 0\n"
"s_waitcnt vmcnt(2) lgkmcnt(0)\n"
"s_barrier\n"
"v_mfma_f32_16x16x32_bf16 v[16:19], v[160:163], v[108:111], v[16:19]\n"
"ds_read_b128 v[96:99], v194 offset:16384\n"
"ds_read_b128 v[100:103], v196 offset:16384\n"
"v_mfma_f32_16x16x32_bf16 v[48:51], v[160:163], v[112:115], v[48:51]\n"
"ds_read_b128 v[104:107], %[xb] offset:59392\n"
"ds_read_b64_tr_b16 v[128:129], %[wbe] offset:49152\n"
"s_add_u32 m0, s60, 139264\n"
"v_mfma_f32_16x16x32_bf16 v[20:23], v[164:167], v[108:111], v[20:23]\n"
"buffer_load_dwordx4 %[xoff], %[srdx], s63 offen lds\n"
"v_mfma_f32_16x16x32_bf16 v[52:55], v[164:167], v[112:115], v[52:55]\n"
"ds_read_b64_tr_b16 v[130:131], v192 offset:50176\n"
"v_mfma_f32_16x16x32_bf16 v[24:27], v[168:171], v[108:111], v[24:27]\n"
"ds_read_b64_tr_b16 v[132:133], v192 offset:49152\n"
"ds_read_b64_tr_b16 v[134:135], %[wbe] offset:50176\n"
"v_mfma_f32_16x16x32_bf16 v[56:59], v[168:171], v[112:115], v[56:59]\n"
"buffer_load_dwordx4 v193, %[srdx], s63 offen offset:1024 lds\n"
"v_mfma_f32_16x16x32_bf16 v[28:31], v[172:175], v[108:111], v[28:31]\n"
"ds_read_b64_tr_b16 v[136:137], %[wbe] offset:49408\n"
"v_mfma_f32_16x16x32_bf16 v[60:63], v[172:175], v[112:115], v[60:63]\n"
"ds_read_b64_tr_b16 v[138:139], v192 offset:50432\n"
"ds_read_b64_tr_b16 v[140:141], v192 offset:49408\n"
"v_mfma_f32_16x16x32_bf16 v[32:35], v[176:179], v[108:111], v[32:35]\n"
"ds_read_b64_tr_b16 v[142:143], %[wbe] offset:50432\n"
"ds_read_b64_tr_b16 v[144:145], %[wbe] offset:49664\n"
"s_add_u32 m0, s64, 57344\n"
"v_mfma_f32_16x16x32_bf16 v[64:67], v[176:179], v[112:115], v[64:67]\n"
"buffer_load_dwordx4 %[xo9], %[srdx], s63 offen lds\n"
"s_add_u32 s63, s63, 0x80\n"
"v_mfma_f32_16x16x32_bf16 v[80:83], v[176:179], v[116:119], v[80:83]\n"
"ds_read_b64_tr_b16 v[146:147], v192 offset:50688\n"
"v_mfma_f32_16x16x32_bf16 v[36:39], v[180:183], v[108:111], v[36:39]\n"
"ds_read_b64_tr_b16 v[148:149], v192 offset:49664\n"
"ds_read_b64_tr_b16 v[150:151], %[wbe] offset:50688\n"
"s_add_u32 m0, %[hb], 45056\n"
"v_mfma_f32_16x16x32_bf16 v[68:71], v[180:183], v[112:115], v[68:71]\n"
"buffer_load_dwordx4 v198, %[srdw], s62 offen lds\n"
"v_mfma_f32_16x16x32_bf16 v[84:87], v[180:183], v[116:119], v[84:87]\n"
"ds_read_b64_tr_b16 v[152:153], %[wbe] offset:49920\n"
"v_mfma_f32_16x16x32_bf16 v[40:43], v[184:187], v[108:111], v[40:43]\n"
"ds_read_b64_tr_b16 v[154:155], v192 offset:50944\n"
"ds_read_b64_tr_b16 v[156:157], v192 offset:49920\n"
"v_mfma_f32_16x16x32_bf16 v[72:75], v[184:187], v[112:115], v[72:75]\n"
"buffer_load_dwordx4 v195, %[srdw], s62 offen offset:1024 lds\n"
"s_add_u32 s62, s62, 0x40000\n"
"v_mfma_f32_16x16x32_bf16 v[44:47], v[188:191], v[108:111], v[44:47]\n"
"ds_read_b64_tr_b16 v[158:159], %[wbe] offset:50944\n"
"v_mfma_f32_16x16x32_bf16 v[76:79], v[188:191], v[112:115], v[76:79]\n"
"s_waitcnt vmcnt(5) lgkmcnt(0)\n"
"s_barrier\n"
"v_mfma_f32_16x16x32_bf16 v[16:19], v[128:131], v[96:99], v[16:19]\n"
"ds_read_b128 v[108:111], v194 offset:16896\n"
"ds_read_b128 v[112:115], v196 offset:16896\n"
"v_mfma_f32_16x16x32_bf16 v[48:51], v[128:131], v[100:103], v[48:51]\n"
"ds_read_b128 v[116:119], %[xb] offset:59904\n"
"v_mfma_f32_16x16x32_bf16 v[20:23], v[132:135], v[96:99], v[20:23]\n"
"ds_read_b64_tr_b16 v[160:161], %[wbe] offset:32768\n"
"s_add_u32 m0, %[hb], 53248\n"
"v_mfma_f32_16x16x32_bf16 v[52:55], v[132:135], v[100:103], v[52:55]\n"
"buffer_load_dwordx4 v198, %[srdw], s62 offen lds\n"
"v_mfma_f32_16x16x32_bf16 v[24:27], v[136:139], v[96:99], v[24:27]\n"
"ds_read_b64_tr_b16 v[162:163], v192 offset:33792\n"
"ds_read_b64_tr_b16 v[164:165], v192 offset:32768\n"
"v_mfma_f32_16x16x32_bf16 v[56:59], v[136:139], v[100:103], v[56:59]\n"
"ds_read_b64_tr_b16 v[166:167], %[wbe] offset:33792\n"
"v_mfma_f32_16x16x32_bf16 v[28:31], v[140:143], v[96:99], v[28:31]\n"
"ds_read_b64_tr_b16 v[168:169], %[wbe] offset:33024\n"
"v_mfma_f32_16x16x32_bf16 v[60:63], v[140:143], v[100:103], v[60:63]\n"
"buffer_load_dwordx4 v195, %[srdw], s62 offen offset:1024 lds\n"
"s_add_u32 s62, s62, 0x40000\n"
"v_mfma_f32_16x16x32_bf16 v[32:35], v[144:147], v[96:99], v[32:35]\n"
"ds_read_b64_tr_b16 v[170:171], v192 offset:34048\n"
"v_mfma_f32_16x16x32_bf16 v[64:67], v[144:147], v[100:103], v[64:67]\n"
"ds_read_b64_tr_b16 v[172:173], v192 offset:33024\n"
"ds_read_b64_tr_b16 v[174:175], %[wbe] offset:34048\n"
"v_mfma_f32_16x16x32_bf16 v[80:83], v[144:147], v[104:107], v[80:83]\n"
"ds_read_b64_tr_b16 v[176:177], %[wbe] offset:33280\n"
"v_mfma_f32_16x16x32_bf16 v[36:39], v[148:151], v[96:99], v[36:39]\n"
"ds_read_b64_tr_b16 v[178:179], v192 offset:34304\n"
"v_mfma_f32_16x16x32_bf16 v[68:71], v[148:151], v[100:103], v[68:71]\n"
"ds_read_b64_tr_b16 v[180:181], v192 offset:33280\n"
"v_mfma_f32_16x16x32_bf16 v[84:87], v[148:151], v[104:107], v[84:87]\n"
"ds_read_b64_tr_b16 v[182:183], %[wbe] offset:34304\n"
"ds_read_b64_tr_b16 v[184:185], %[wbe] offset:33536\n"
"v_mfma_f32_16x16x32_bf16 v[40:43], v[152:155], v[96:99], v[40:43]\n"
"ds_read_b64_tr_b16 v[186:187], v192 offset:34560\n"
"v_mfma_f32_16x16x32_bf16 v[72:75], v[152:155], v[100:103], v[72:75]\n"
"ds_read_b64_tr_b16 v[188:189], v192 offset:33536\n"
"v_mfma_f32_16x16x32_bf16 v[44:47], v[156:159], v[96:99], v[44:47]\n"
"ds_read_b64_tr_b16 v[190:191], %[wbe] offset:34560\n"
"v_mfma_f32_16x16x32_bf16 v[76:79], v[156:159], v[100:103], v[76:79]\n"
"s_waitcnt vmcnt(2) lgkmcnt(0)\n"
"s_barrier\n"
"v_mfma_f32_16x16x32_bf16 v[16:19], v[160:163], v[108:111], v[16:19]\n"
"ds_read_b128 v[96:99], v194 offset:0\n"
"ds_read_b128 v[100:103], v196 offset:0\n"
"v_mfma_f32_16x16x32_bf16 v[48:51], v[160:163], v[112:115], v[48:51]\n"
"ds_read_b128 v[104:107], %[xb] offset:57344\n"
"v_mfma_f32_16x16x32_bf16 v[20:23], v[164:167], v[108:111], v[20:23]\n"
"ds_read_b64_tr_b16 v[128:129], %[wbe] offset:40960\n"
"ds_read_b64_tr_b16 v[130:131], v192 offset:41984\n"
"s_add_u32 m0, s60, 155648\n"
"v_mfma_f32_16x16x32_bf16 v[52:55], v[164:167], v[112:115], v[52:55]\n"
"buffer_load_dwordx4 %[xoff], %[srdx], s63 offen lds\n"
"v_mfma_f32_16x16x32_bf16 v[24:27], v[168:171], v[108:111], v[24:27]\n"
"ds_read_b64_tr_b16 v[132:133], v192 offset:40960\n"
"v_mfma_f32_16x16x32_bf16 v[56:59], v[168:171], v[112:115], v[56:59]\n"
"ds_read_b64_tr_b16 v[134:135], %[wbe] offset:41984\n"
"ds_read_b64_tr_b16 v[136:137], %[wbe] offset:41216\n"
"v_mfma_f32_16x16x32_bf16 v[28:31], v[172:175], v[108:111], v[28:31]\n"
"ds_read_b64_tr_b16 v[138:139], v192 offset:42240\n"
"v_mfma_f32_16x16x32_bf16 v[60:63], v[172:175], v[112:115], v[60:63]\n"
"buffer_load_dwordx4 v193, %[srdx], s63 offen offset:1024 lds\n"
"s_add_u32 s63, s63, 0x80\n"
"v_mfma_f32_16x16x32_bf16 v[32:35], v[176:179], v[108:111], v[32:35]\n"
"ds_read_b64_tr_b16 v[140:141], v192 offset:41216\n"
"ds_read_b64_tr_b16 v[142:143], %[wbe] offset:42240\n"
"v_mfma_f32_16x16x32_bf16 v[64:67], v[176:179], v[112:115], v[64:67]\n"
"ds_read_b64_tr_b16 v[144:145], %[wbe] offset:41472\n"
"v_mfma_f32_16x16x32_bf16 v[80:83], v[176:179], v[116:119], v[80:83]\n"
"ds_read_b64_tr_b16 v[146:147], v192 offset:42496\n"
"ds_read_b64_tr_b16 v[148:149], v192 offset:41472\n"
"s_add_u32 m0, %[hb], 36864\n"
"v_mfma_f32_16x16x32_bf16 v[36:39], v[180:183], v[108:111], v[36:39]\n"
"buffer_load_dwordx4 v198, %[srdw], s62 offen lds\n"
"v_mfma_f32_16x16x32_bf16 v[68:71], v[180:183], v[112:115], v[68:71]\n"
"ds_read_b64_tr_b16 v[150:151], %[wbe] offset:42496\n"
"v_mfma_f32_16x16x32_bf16 v[84:87], v[180:183], v[116:119], v[84:87]\n"
"ds_read_b64_tr_b16 v[152:153], %[wbe] offset:41728\n"
"ds_read_b64_tr_b16 v[154:155], v192 offset:42752\n"
"v_mfma_f32_16x16x32_bf16 v[40:43], v[184:187], v[108:111], v[40:43]\n"
"ds_read_b64_tr_b16 v[156:157], v192 offset:41728\n"
"v_mfma_f32_16x16x32_bf16 v[72:75], v[184:187], v[112:115], v[72:75]\n"
"buffer_load_dwordx4 v195, %[srdw], s62 offen offset:1024 lds\n"
"s_add_u32 s62, s62, 0x40000\n"
"v_mfma_f32_16x16x32_bf16 v[44:47], v[188:191], v[108:111], v[44:47]\n"
"ds_read_b64_tr_b16 v[158:159], %[wbe] offset:42752\n"
"v_mfma_f32_16x16x32_bf16 v[76:79], v[188:191], v[112:115], v[76:79]\n"
"s_waitcnt vmcnt(4) lgkmcnt(0)\n"
"s_barrier\n"
"v_mfma_f32_16x16x32_bf16 v[16:19], v[128:131], v[96:99], v[16:19]\n"
"ds_read_b128 v[108:111], v194 offset:512\n"
"ds_read_b128 v[112:115], v196 offset:512\n"
"v_mfma_f32_16x16x32_bf16 v[48:51], v[128:131], v[100:103], v[48:51]\n"
"ds_read_b128 v[116:119], %[xb] offset:57856\n"
"v_mfma_f32_16x16x32_bf16 v[20:23], v[132:135], v[96:99], v[20:23]\n"
"ds_read_b64_tr_b16 v[160:161], %[wbe] offset:49152\n"
"ds_read_b64_tr_b16 v[162:163], v192 offset:50176\n"
"s_add_u32 m0, %[hb], 45056\n"
"v_mfma_f32_16x16x32_bf16 v[52:55], v[132:135], v[100:103], v[52:55]\n"
"buffer_load_dwordx4 v198, %[srdw], s62 offen lds\n"
"v_mfma_f32_16x16x32_bf16 v[24:27], v[136:139], v[96:99], v[24:27]\n"
"ds_read_b64_tr_b16 v[164:165], v192 offset:49152\n"
"v_mfma_f32_16x16x32_bf16 v[56:59], v[136:139], v[100:103], v[56:59]\n"
"ds_read_b64_tr_b16 v[166:167], %[wbe] offset:50176\n"
"v_mfma_f32_16x16x32_bf16 v[28:31], v[140:143], v[96:99], v[28:31]\n"
"ds_read_b64_tr_b16 v[168:169], %[wbe] offset:49408\n"
"ds_read_b64_tr_b16 v[170:171], v192 offset:50432\n"
"v_mfma_f32_16x16x32_bf16 v[60:63], v[140:143], v[100:103], v[60:63]\n"
"buffer_load_dwordx4 v195, %[srdw], s62 offen offset:1024 lds\n"
"s_add_u32 s62, s62, 0x40000\n"
"v_mfma_f32_16x16x32_bf16 v[32:35], v[144:147], v[96:99], v[32:35]\n"
"ds_read_b64_tr_b16 v[172:173], v192 offset:49408\n"
"v_mfma_f32_16x16x32_bf16 v[64:67], v[144:147], v[100:103], v[64:67]\n"
"ds_read_b64_tr_b16 v[174:175], %[wbe] offset:50432\n"
"v_mfma_f32_16x16x32_bf16 v[80:83], v[144:147], v[104:107], v[80:83]\n"
"ds_read_b64_tr_b16 v[176:177], %[wbe] offset:49664\n"
"ds_read_b64_tr_b16 v[178:179], v192 offset:50688\n"
"v_mfma_f32_16x16x32_bf16 v[36:39], v[148:151], v[96:99], v[36:39]\n"
"s_bitcmp0_b32 %[xrot], 4\n"
"s_cbranch_scc1 LK2_4_%=\n"
"s_mov_b32 m0, s52\n"
"s_nop 0\n"
"buffer_load_dwordx4 %[g0], %[srdv], s61 offen nt lds\n"
"buffer_load_dwordx4 %[g1], %[srdv], s61 offen offset:1024 nt lds\n"
"buffer_load_dwordx4 %[g0], %[srdk], s61 offen offset:2048 nt lds\n"
"buffer_load_dwordx4 %[g1], %[srdk], s61 offen offset:3072 nt lds\n"
"s_mov_b32 m0, s56\n"
"s_nop 0\n"
"buffer_load_dwordx4 v248, %[srdk], s61 offen offset:3072 nt lds\n"
"s_add_u32 s61, s61, %[kvs]\n"
"LK2_4_%=:\n"
"v_mfma_f32_16x16x32_bf16 v[68:71], v[148:151], v[100:103], v[68:71]\n"
"ds_read_b64_tr_b16 v[180:181], v192 offset:49664\n"
"v_mfma_f32_16x16x32_bf16 v[84:87], v[148:151], v[104:107], v[84:87]\n"
"ds_read_b64_tr_b16 v[182:183], %[wbe] offset:50688\n"
"v_mfma_f32_16x16x32_bf16 v[40:43], v[152:155], v[96:99], v[40:43]\n"
"ds_read_b64_tr_b16 v[184:185], %[wbe] offset:49920\n"
"ds_read_b64_tr_b16 v[186:187], v192 offset:50944\n"
"v_mfma_f32_16x16x32_bf16 v[72:75], v[152:155], v[100:103], v[72:75]\n"
"ds_read_b64_tr_b16 v[188:189], v192 offset:49920\n"
"v_mfma_f32_16x16x32_bf16 v[44:47], v[156:159], v[96:99], v[44:47]\n"
"ds_read_b64_tr_b16 v[190:191], %[wbe] offset:50944\n"
"v_mfma_f32_16x16x32_bf16 v[76:79], v[156:159], v[100:103], v[76:79]\n"
"s_waitcnt vmcnt(7)\n"
"s_bitcmp1_b32 s91, 5\n"
"s_cbranch_scc1 LJ2_5_%=\n"
"s_waitcnt vmcnt(2)\n"
"LJ2_5_%=:\n"
"s_waitcnt lgkmcnt(0)\n"
"s_barrier\n"
"v_mfma_f32_16x16x32_bf16 v[16:19], v[160:163], v[108:111], v[16:19]\n"
"ds_read_b128 v[96:99], v194 offset:16384\n"
"ds_read_b128 v[100:103], v196 offset:16384\n"
"v_mfma_f32_16x16x32_bf16 v[48:51], v[160:163], v[112:115], v[48:51]\n"
"ds_read_b128 v[104:107], %[xb] offset:59392\n"
"ds_read_b64_tr_b16 v[128:129], %[wbe] offset:32768\n"
"s_add_u32 m0, s60, 139264\n"
"v_mfma_f32_16x16x32_bf16 v[20:23], v[164:167], v[108:111], v[20:23]\n"
"buffer_load_dwordx4 %[xoff], %[srdx], s63 offen lds\n"
"v_mfma_f32_16x16x32_bf16 v[52:55], v[164:167], v[112:115], v[52:55]\n"
"ds_read_b64_tr_b16 v[130:131], v192 offset:33792\n"
"v_mfma_f32_16x16x32_bf16 v[24:27], v[168:171], v[108:111], v[24:27]\n"
"ds_read_b64_tr_b16 v[132:133], v192 offset:32768\n"
"ds_read_b64_tr_b16 v[134:135], %[wbe] offset:33792\n"
"v_mfma_f32_16x16x32_bf16 v[56:59], v[168:171], v[112:115], v[56:59]\n"
"buffer_load_dwordx4 v193, %[srdx], s63 offen offset:1024 lds\n"
"s_add_u32 s63, s63, 0x80\n"
"v_mfma_f32_16x16x32_bf16 v[28:31], v[172:175], v[108:111], v[28:31]\n"
"ds_read_b64_tr_b16 v[136:137], %[wbe] offset:33024\n"
"v_mfma_f32_16x16x32_bf16 v[60:63], v[172:175], v[112:115], v[60:63]\n"
"ds_read_b64_tr_b16 v[138:139], v192 offset:34048\n"
"ds_read_b64_tr_b16 v[140:141], v192 offset:33024\n"
"v_mfma_f32_16x16x32_bf16 v[32:35], v[176:179], v[108:111], v[32:35]\n"
"ds_read_b64_tr_b16 v[142:143], %[wbe] offset:34048\n"
"ds_read_b64_tr_b16 v[144:145], %[wbe] offset:33280\n"
"s_add_u32 m0, %[hb], 53248\n"
"v_mfma_f32_16x16x32_bf16 v[64:67], v[176:179], v[112:115], v[64:67]\n"
"buffer_load_dwordx4 v198, %[srdw], s62 offen lds\n"
"v_mfma_f32_16x16x32_bf16 v[80:83], v[176:179], v[116:119], v[80:83]\n"
"ds_read_b64_tr_b16 v[146:147], v192 offset:34304\n"
"v_mfma_f32_16x16x32_bf16 v[36:39], v[180:183], v[108:111], v[36:39]\n"
"ds_read_b64_tr_b16 v[148:149], v192 offset:33280\n"
"ds_read_b64_tr_b16 v[150:151], %[wbe] offset:34304\n"
"v_mfma_f32_16x16x32_bf16 v[68:71], v[180:183], v[112:115], v[68:71]\n"
"buffer_load_dwordx4 v195, %[srdw], s62 offen offset:1024 lds\n"
"s_add_u32 s62, s62, 0x40000\n"
"v_mfma_f32_16x16x32_bf16 v[84:87], v[180:183], v[116:119], v[84:87]\n"
"ds_read_b64_tr_b16 v[152:153], %[wbe] offset:33536\n"
"v_mfma_f32_16x16x32_bf16 v[40:43], v[184:187], v[108:111], v[40:43]\n"
"ds_read_b64_tr_b16 v[154:155], v192 offset:34560\n"
"ds_read_b64_tr_b16 v[156:157], v192 offset:33536\n"
"v_mfma_f32_16x16x32_bf16 v[72:75], v[184:187], v[112:115], v[72:75]\n"
"s_bitcmp0_b32 %[xrot], 5\n"
"s_cbranch_scc1 LK2_5_%=\n"
"s_mov_b32 m0, s52\n"
"s_nop 0\n"
"buffer_load_dwordx4 %[g0], %[srdv], s61 offen nt lds\n"
"buffer_load_dwordx4 %[g1], %[srdv], s61 offen offset:1024 nt lds\n"
"buffer_load_dwordx4 %[g0], %[srdk], s61 offen offset:2048 nt lds\n"
"buffer_load_dwordx4 %[g1], %[srdk], s61 offen offset:3072 nt lds\n"
"s_mov_b32 m0, s56\n"
"s_nop 0\n"
"buffer_load_dwordx4 v248, %[srdk], s61 offen offset:3072 nt lds\n"
"s_add_u32 s61, s61, %[kvs]\n"
"LK2_5_%=:\n"
"v_mfma_f32_16x16x32_bf16 v[44:47], v[188:191], v[108:111], v[44:47]\n"
"ds_read_b64_tr_b16 v[158:159], %[wbe] offset:34560\n"
"v_mfma_f32_16x16x32_bf16 v[76:79], v[188:191], v[112:115], v[76:79]\n"
"s_waitcnt vmcnt(9)\n"
"s_bitcmp1_b32 s91, 6\n"
"s_cbranch_scc1 LJ2_6_%=\n"
"s_waitcnt vmcnt(4)\n"
"LJ2_6_%=:\n"
"s_waitcnt lgkmcnt(0)\n"
"s_barrier\n"
"v_mfma_f32_16x16x32_bf16 v[16:19], v[128:131], v[96:99], v[16:19]\n"
"ds_read_b128 v[108:111], v194 offset:16896\n"
"ds_read_b128 v[112:115], v196 offset:16896\n"
"v_mfma_f32_16x16x32_bf16 v[48:51], v[128:131], v[100:103], v[48:51]\n"
"ds_read_b128 v[116:119], %[xb] offset:59904\n"
"v_mfma_f32_16x16x32_bf16 v[20:23], v[132:135], v[96:99], v[20:23]\n"
"ds_read_b64_tr_b16 v[160:161], %[wbe] offset:40960\n"
"ds_read_b64_tr_b16 v[162:163], v192 offset:41984\n"
"s_add_u32 m0, %[hb], 36864\n"
"v_mfma_f32_16x16x32_bf16 v[52:55], v[132:135], v[100:103], v[52:55]\n"
"buffer_load_dwordx4 v198, %[srdw], s62 offen lds\n"
"v_mfma_f32_16x16x32_bf16 v[24:27], v[136:139], v[96:99], v[24:27]\n"
"ds_read_b64_tr_b16 v[164:165], v192 offset:40960\n"
"v_mfma_f32_16x16x32_bf16 v[56:59], v[136:139], v[100:103], v[56:59]\n"
"ds_read_b64_tr_b16 v[166:167], %[wbe] offset:41984\n"
"v_mfma_f32_16x16x32_bf16 v[28:31], v[140:143], v[96:99], v[28:31]\n"
"ds_read_b64_tr_b16 v[168:169], %[wbe] offset:41216\n"
"ds_read_b64_tr_b16 v[170:171], v192 offset:42240\n"
"v_mfma_f32_16x16x32_bf16 v[60:63], v[140:143], v[100:103], v[60:63]\n"
"buffer_load_dwordx4 v195, %[srdw], s62 offen offset:1024 lds\n"
"s_add_u32 s62, s62, 0x40000\n"
"v_mfma_f32_16x16x32_bf16 v[32:35], v[144:147], v[96:99], v[32:35]\n"
"ds_read_b64_tr_b16 v[172:173], v192 offset:41216\n"
"v_mfma_f32_16x16x32_bf16 v[64:67], v[144:147], v[100:103], v[64:67]\n"
"ds_read_b64_tr_b16 v[174:175], %[wbe] offset:42240\n"
"v_mfma_f32_16x16x32_bf16 v[80:83], v[144:147], v[104:107], v[80:83]\n"
"ds_read_b64_tr_b16 v[176:177], %[wbe] offset:41472\n"
"ds_read_b64_tr_b16 v[178:179], v192 offset:42496\n"
"v_mfma_f32_16x16x32_bf16 v[36:39], v[148:151], v[96:99], v[36:39]\n"
"s_bitcmp0_b32 %[xrot], 6\n"
"s_cbranch_scc1 LK2_6_%=\n"
"s_mov_b32 m0, s52\n"
"s_nop 0\n"
"buffer_load_dwordx4 %[g0], %[srdv], s61 offen nt lds\n"
"buffer_load_dwordx4 %[g1], %[srdv], s61 offen offset:1024 nt lds\n"
"buffer_load_dwordx4 %[g0], %[srdk], s61 offen offset:2048 nt lds\n"
"buffer_load_dwordx4 %[g1], %[srdk], s61 offen offset:3072 nt lds\n"
"s_mov_b32 m0, s56\n"
"s_nop 0\n"
"buffer_load_dwordx4 v248, %[srdk], s61 offen offset:3072 nt lds\n"
"s_add_u32 s61, s61, %[kvs]\n"
"LK2_6_%=:\n"
"v_mfma_f32_16x16x32_bf16 v[68:71], v[148:151], v[100:103], v[68:71]\n"
"ds_read_b64_tr_b16 v[180:181], v192 offset:41472\n"
"v_mfma_f32_16x16x32_bf16 v[84:87], v[148:151], v[104:107], v[84:87]\n"
"ds_read_b64_tr_b16 v[182:183], %[wbe] offset:42496\n"
"v_mfma_f32_16x16x32_bf16 v[40:43], v[152:155], v[96:99], v[40:43]\n"
"ds_read_b64_tr_b16 v[184:185], %[wbe] offset:41728\n"
"ds_read_b64_tr_b16 v[186:187], v192 offset:42752\n"
"v_mfma_f32_16x16x32_bf16 v[72:75], v[152:155], v[100:103], v[72:75]\n"
"ds_read_b64_tr_b16 v[188:189], v192 offset:41728\n"
"v_mfma_f32_16x16x32_bf16 v[44:47], v[156:159], v[96:99], v[44:47]\n"
"ds_read_b64_tr_b16 v[190:191], %[wbe] offset:42752\n"
"v_mfma_f32_16x16x32_bf16 v[76:79], v[156:159], v[100:103], v[76:79]\n"
"s_waitcnt vmcnt(7)\n"
"s_bitcmp1_b32 s91, 7\n"
"s_cbranch_scc1 LJ2_7_%=\n"
"s_waitcnt vmcnt(2)\n"
"LJ2_7_%=:\n"
"s_waitcnt lgkmcnt(0)\n"
"s_barrier\n"
"v_mfma_f32_16x16x32_bf16 v[16:19], v[160:163], v[108:111], v[16:19]\n"
"ds_read_b128 v[96:99], v194 offset:0\n"
"ds_read_b128 v[100:103], v196 offset:0\n"
"v_mfma_f32_16x16x32_bf16 v[48:51], v[160:163], v[112:115], v[48:51]\n"
"ds_read_b128 v[104:107], %[xb] offset:57344\n"
"ds_read_b64_tr_b16 v[128:129], %[wbe] offset:49152\n"
"s_add_u32 m0, s60, 155648\n"
"v_mfma_f32_16x16x32_bf16 v[20:23], v[164:167], v[108:111], v[20:23]\n"
"buffer_load_dwordx4 %[xoff], %[srdx], s63 offen lds\n"
"v_mfma_f32_16x16x32_bf16 v[52:55], v[164:167], v[112:115], v[52:55]\n"
"ds_read_b64_tr_b16 v[130:131], v192 offset:50176\n"
"v_mfma_f32_16x16x32_bf16 v[24:27], v[168:171], v[108:111], v[24:27]\n"
"ds_read_b64_tr_b16 v[132:133], v192 offset:49152\n"
"ds_read_b64_tr_b16 v[134:135], %[wbe] offset:50176\n"
"v_mfma_f32_16x16x32_bf16 v[56:59], v[168:171], v[112:115], v[56:59]\n"
"buffer_load_dwordx4 v193, %[srdx], s63 offen offset:1024 lds\n"
"s_add_u32 s63, s63, 0x80\n"
"v_mfma_f32_16x16x32_bf16 v[28:31], v[172:175], v[108:111], v[28:31]\n"
"ds_read_b64_tr_b16 v[136:137], %[wbe] offset:49408\n"
"v_mfma_f32_16x16x32_bf16 v[60:63], v[172:175], v[112:115], v[60:63]\n"
"ds_read_b64_tr_b16 v[138:139], v192 offset:50432\n"
"ds_read_b64_tr_b16 v[140:141], v192 offset:49408\n"
"v_mfma_f32_16x16x32_bf16 v[32:35], v[176:179], v[108:111], v[32:35]\n"
"ds_read_b64_tr_b16 v[142:143], %[wbe] offset:50432\n"
"ds_read_b64_tr_b16 v[144:145], %[wbe] offset:49664\n"
"s_add_u32 m0, %[hb], 45056\n"
"v_mfma_f32_16x16x32_bf16 v[64:67], v[176:179], v[112:115], v[64:67]\n"
"buffer_load_dwordx4 v198, %[srdw], s62 offen lds\n"
"v_mfma_f32_16x16x32_bf16 v[80:83], v[176:179], v[116:119], v[80:83]\n"
"ds_read_b64_tr_b16 v[146:147], v192 offset:50688\n"
"v_mfma_f32_16x16x32_bf16 v[36:39], v[180:183], v[108:111], v[36:39]\n"
"ds_read_b64_tr_b16 v[148:149], v192 offset:49664\n"
"ds_read_b64_tr_b16 v[150:151], %[wbe] offset:50688\n"
"v_mfma_f32_16x16x32_bf16 v[68:71], v[180:183], v[112:115], v[68:71]\n"
"buffer_load_dwordx4 v195, %[srdw], s62 offen offset:1024 lds\n"
"s_add_u32 s62, s62, 0x40000\n"
"v_mfma_f32_16x16x32_bf16 v[84:87], v[180:183], v[116:119], v[84:87]\n"
"ds_read_b64_tr_b16 v[152:153], %[wbe] offset:49920\n"
"v_mfma_f32_16x16x32_bf16 v[40:43], v[184:187], v[108:111], v[40:43]\n"
"ds_read_b64_tr_b16 v[154:155], v192 offset:50944\n"
"ds_read_b64_tr_b16 v[156:157], v192 offset:49920\n"
"v_mfma_f32_16x16x32_bf16 v[72:75], v[184:187], v[112:115], v[72:75]\n"
"s_bitcmp0_b32 %[xrot], 7\n"
"s_cbranch_scc1 LK2_7_%=\n"
"s_mov_b32 m0, s52\n"
"s_nop 0\n"
"buffer_load_dwordx4 %[g0], %[srdv], s61 offen nt lds\n"
"buffer_load_dwordx4 %[g1], %[srdv], s61 offen offset:1024 nt lds\n"
"buffer_load_dwordx4 %[g0], %[srdk], s61 offen offset:2048 nt lds\n"
"buffer_load_dwordx4 %[g1], %[srdk], s61 offen offset:3072 nt lds\n"
"s_mov_b32 m0, s56\n"
"s_nop 0\n"
"buffer_load_dwordx4 v248, %[srdk], s61 offen offset:3072 nt lds\n"
"s_add_u32 s61, s61, %[kvs]\n"
"LK2_7_%=:\n"
"v_mfma_f32_16x16x32_bf16 v[44:47], v[188:191], v[108:111], v[44:47]\n"
"ds_read_b64_tr_b16 v[158:159], %[wbe] offset:50944\n"
"v_mfma_f32_16x16x32_bf16 v[76:79], v[188:191], v[112:115], v[76:79]\n"
"s_waitcnt vmcnt(9)\n"
"s_bitcmp1_b32 s91, 8\n"
"s_cbranch_scc1 LJ2_8_%=\n"
"s_waitcnt vmcnt(4)\n"
"LJ2_8_%=:\n"
"s_waitcnt lgkmcnt(0)\n"
"s_barrier\n"
"v_mfma_f32_16x16x32_bf16 v[16:19], v[128:131], v[96:99], v[16:19]\n"
"ds_read_b128 v[108:111], v194 offset:512\n"
"ds_read_b128 v[112:115], v196 offset:512\n"
"v_mfma_f32_16x16x32_bf16 v[48:51], v[128:131], v[100:103], v[48:51]\n"
"ds_read_b128 v[116:119], %[xb] offset:57856\n"
"v_mfma_f32_16x16x32_bf16 v[20:23], v[132:135], v[96:99], v[20:23]\n"
"ds_read_b64_tr_b16 v[160:161], %[wbe] offset:32768\n"
"s_add_u32 m0, %[hb], 53248\n"
"v_mfma_f32_16x16x32_bf16 v[52:55], v[132:135], v[100:103], v[52:55]\n"
"buffer_load_dwordx4 v198, %[srdw], s62 offen lds\n"
"v_mfma_f32_16x16x32_bf16 v[24:27], v[136:139], v[96:99], v[24:27]\n"
"ds_read_b64_tr_b16 v[162:163], v192 offset:33792\n"
"ds_read_b64_tr_b16 v[164:165], v192 offset:32768\n"
"v_mfma_f32_16x16x32_bf16 v[56:59], v[136:139], v[100:103], v[56:59]\n"
"ds_read_b64_tr_b16 v[166:167], %[wbe] offset:33792\n"
"v_mfma_f32_16x16x32_bf16 v[28:31], v[140:143], v[96:99], v[28:31]\n"
"ds_read_b64_tr_b16 v[168:169], %[wbe] offset:33024\n"
"v_mfma_f32_16x16x32_bf16 v[60:63], v[140:143], v[100:103], v[60:63]\n"
"buffer_load_dwordx4 v195, %[srdw], s62 offen offset:1024 lds\n"
"s_add_u32 s62, s62, 0x40000\n"
"v_mfma_f32_16x16x32_bf16 v[32:35], v[144:147], v[96:99], v[32:35]\n"
"ds_read_b64_tr_b16 v[170:171], v192 offset:34048\n"
"v_mfma_f32_16x16x32_bf16 v[64:67], v[144:147], v[100:103], v[64:67]\n"
"ds_read_b64_tr_b16 v[172:173], v192 offset:33024\n"
"ds_read_b64_tr_b16 v[174:175], %[wbe] offset:34048\n"
"v_mfma_f32_16x16x32_bf16 v[80:83], v[144:147], v[104:107], v[80:83]\n"
"ds_read_b64_tr_b16 v[176:177], %[wbe] offset:33280\n"
"v_mfma_f32_16x16x32_bf16 v[36:39], v[148:151], v[96:99], v[36:39]\n"
"ds_read_b64_tr_b16 v[178:179], v192 offset:34304\n"
"v_mfma_f32_16x16x32_bf16 v[68:71], v[148:151], v[100:103], v[68:71]\n"
"ds_read_b64_tr_b16 v[180:181], v192 offset:33280\n"
"v_mfma_f32_16x16x32_bf16 v[84:87], v[148:151], v[104:107], v[84:87]\n"
"ds_read_b64_tr_b16 v[182:183], %[wbe] offset:34304\n"
"ds_read_b64_tr_b16 v[184:185], %[wbe] offset:33536\n"
"v_mfma_f32_16x16x32_bf16 v[40:43], v[152:155], v[96:99], v[40:43]\n"
"ds_read_b64_tr_b16 v[186:187], v192 offset:34560\n"
"v_mfma_f32_16x16x32_bf16 v[72:75], v[152:155], v[100:103], v[72:75]\n"
"ds_read_b64_tr_b16 v[188:189], v192 offset:33536\n"
"v_mfma_f32_16x16x32_bf16 v[44:47], v[156:159], v[96:99], v[44:47]\n"
"ds_read_b64_tr_b16 v[190:191], %[wbe] offset:34560\n"
"v_mfma_f32_16x16x32_bf16 v[76:79], v[156:159], v[100:103], v[76:79]\n"
"s_waitcnt vmcnt(7)\n"
"s_bitcmp1_b32 s91, 9\n"
"s_cbranch_scc1 LJ2_9_%=\n"
"s_waitcnt vmcnt(2)\n"
"LJ2_9_%=:\n"
"s_waitcnt lgkmcnt(0)\n"
"s_barrier\n"
"v_mfma_f32_16x16x32_bf16 v[16:19], v[160:163], v[108:111], v[16:19]\n"
"ds_read_b128 v[96:99], v194 offset:16384\n"
"ds_read_b128 v[100:103], v196 offset:16384\n"
"v_mfma_f32_16x16x32_bf16 v[48:51], v[160:163], v[112:115], v[48:51]\n"
"ds_read_b128 v[104:107], %[xb] offset:59392\n"
"ds_read_b64_tr_b16 v[128:129], %[wbe] offset:40960\n"
"s_add_u32 m0, s60, 139264\n"
"v_mfma_f32_16x16x32_bf16 v[20:23], v[164:167], v[108:111], v[20:23]\n"
"buffer_load_dwordx4 %[xoff], %[srdx], s63 offen lds\n"
"v_mfma_f32_16x16x32_bf16 v[52:55], v[164:167], v[112:115], v[52:55]\n"
"ds_read_b64_tr_b16 v[130:131], v192 offset:41984\n"
"v_mfma_f32_16x16x32_bf16 v[24:27], v[168:171], v[108:111], v[24:27]\n"
"ds_read_b64_tr_b16 v[132:133], v192 offset:40960\n"
"ds_read_b64_tr_b16 v[134:135], %[wbe] offset:41984\n"
"v_mfma_f32_16x16x32_bf16 v[56:59], v[168:171], v[112:115], v[56:59]\n"
"buffer_load_dwordx4 v193, %[srdx], s63 offen offset:1024 lds\n"
"v_mfma_f32_16x16x32_bf16 v[28:31], v[172:175], v[108:111], v[28:31]\n"
"ds_read_b64_tr_b16 v[136:137], %[wbe] offset:41216\n"
"v_mfma_f32_16x16x32_bf16 v[60:63], v[172:175], v[112:115], v[60:63]\n"
"ds_read_b64_tr_b16 v[138:139], v192 offset:42240\n"
"ds_read_b64_tr_b16 v[140:141], v192 offset:41216\n"
"v_mfma_f32_16x16x32_bf16 v[32:35], v[176:179], v[108:111], v[32:35]\n"
"ds_read_b64_tr_b16 v[142:143], %[wbe] offset:42240\n"
"ds_read_b64_tr_b16 v[144:145], %[wbe] offset:41472\n"
"s_add_u32 m0, s64, 57344\n"
"v_mfma_f32_16x16x32_bf16 v[64:67], v[176:179], v[112:115], v[64:67]\n"
"buffer_load_dwordx4 %[xo9], %[srdx], s63 offen lds\n"
"s_add_u32 s63, s63, 0x80\n"
"v_mfma_f32_16x16x32_bf16 v[80:83], v[176:179], v[116:119], v[80:83]\n"
"ds_read_b64_tr_b16 v[146:147], v192 offset:42496\n"
"v_mfma_f32_16x16x32_bf16 v[36:39], v[180:183], v[108:111], v[36:39]\n"
"ds_read_b64_tr_b16 v[148:149], v192 offset:41472\n"
"ds_read_b64_tr_b16 v[150:151], %[wbe] offset:42496\n"
"s_add_u32 m0, %[hb], 36864\n"
"v_mfma_f32_16x16x32_bf16 v[68:71], v[180:183], v[112:115], v[68:71]\n"
"buffer_load_dwordx4 v198, %[srdw], s62 offen lds\n"
"v_mfma_f32_16x16x32_bf16 v[84:87], v[180:183], v[116:119], v[84:87]\n"
"ds_read_b64_tr_b16 v[152:153], %[wbe] offset:41728\n"
"v_mfma_f32_16x16x32_bf16 v[40:43], v[184:187], v[108:111], v[40:43]\n"
"ds_read_b64_tr_b16 v[154:155], v192 offset:42752\n"
"ds_read_b64_tr_b16 v[156:157], v192 offset:41728\n"
"v_mfma_f32_16x16x32_bf16 v[72:75], v[184:187], v[112:115], v[72:75]\n"
"buffer_load_dwordx4 v195, %[srdw], s62 offen offset:1024 lds\n"
"s_add_u32 s62, s62, 0x40000\n"
"v_mfma_f32_16x16x32_bf16 v[44:47], v[188:191], v[108:111], v[44:47]\n"
"ds_read_b64_tr_b16 v[158:159], %[wbe] offset:42752\n"
"v_mfma_f32_16x16x32_bf16 v[76:79], v[188:191], v[112:115], v[76:79]\n"
"s_waitcnt vmcnt(5) lgkmcnt(0)\n"
"s_barrier\n"
"v_mfma_f32_16x16x32_bf16 v[16:19], v[128:131], v[96:99], v[16:19]\n"
"ds_read_b128 v[108:111], v194 offset:16896\n"
"ds_read_b128 v[112:115], v196 offset:16896\n"
"v_mfma_f32_16x16x32_bf16 v[48:51], v[128:131], v[100:103], v[48:51]\n"
"ds_read_b128 v[116:119], %[xb] offset:59904\n"
"v_mfma_f32_16x16x32_bf16 v[20:23], v[132:135], v[96:99], v[20:23]\n"
"ds_read_b64_tr_b16 v[160:161], %[wbe] offset:49152\n"
"ds_read_b64_tr_b16 v[162:163], v192 offset:50176\n"
"s_add_u32 m0, %[hb], 45056\n"
"v_mfma_f32_16x16x32_bf16 v[52:55], v[132:135], v[100:103], v[52:55]\n"
"buffer_load_dwordx4 v198, %[srdw], s62 offen lds\n"
"v_mfma_f32_16x16x32_bf16 v[24:27], v[136:139], v[96:99], v[24:27]\n"
"ds_read_b64_tr_b16 v[164:165], v192 offset:49152\n"
"v_mfma_f32_16x16x32_bf16 v[56:59], v[136:139], v[100:103], v[56:59]\n"
"ds_read_b64_tr_b16 v[166:167], %[wbe] offset:50176\n"
"v_mfma_f32_16x16x32_bf16 v[28:31], v[140:143], v[96:99], v[28:31]\n"
"ds_read_b64_tr_b16 v[168:169], %[wbe] offset:49408\n"
"ds_read_b64_tr_b16 v[170:171], v192 offset:50432\n"
"v_mfma_f32_16x16x32_bf16 v[60:63], v[140:143], v[100:103], v[60:63]\n"
"buffer_load_dwordx4 v195, %[srdw], s62 offen offset:1024 lds\n"
"s_add_u32 s62, s62, 0x40000\n"
"v_mfma_f32_16x16x32_bf16 v[32:35], v[144:147], v[96:99], v[32:35]\n"
"ds_read_b64_tr_b16 v[172:173], v192 offset:49408\n"
"v_mfma_f32_16x16x32_bf16 v[64:67], v[144:147], v[100:103], v[64:67]\n"
"ds_read_b64_tr_b16 v[174:175], %[wbe] offset:50432\n"
"v_mfma_f32_16x16x32_bf16 v[80:83], v[144:147], v[104:107], v[80:83]\n"
"ds_read_b64_tr_b16 v[176:177], %[wbe] offset:49664\n"
"ds_read_b64_tr_b16 v[178:179], v192 offset:50688\n"
"v_mfma_f32_16x16x32_bf16 v[36:39], v[148:151], v[96:99], v[36:39]\n"
"s_bitcmp0_b32 %[xrot], 10\n"
"s_cbranch_scc1 LK2_10_%=\n"
"s_mov_b32 m0, s53\n"
"s_nop 0\n"
"buffer_load_dwordx4 %[g0], %[srdv], s61 offen nt lds\n"
"buffer_load_dwordx4 %[g1], %[srdv], s61 offen offset:1024 nt lds\n"
"buffer_load_dwordx4 %[g0], %[srdk], s61 offen offset:2048 nt lds\n"
"buffer_load_dwordx4 %[g1], %[srdk], s61 offen offset:3072 nt lds\n"
"s_mov_b32 m0, s57\n"
"s_nop 0\n"
"buffer_load_dwordx4 v248, %[srdk], s61 offen offset:3072 nt lds\n"
"s_add_u32 s61, s61, %[kvs]\n"
"LK2_10_%=:\n"
"v_mfma_f32_16x16x32_bf16 v[68:71], v[148:151], v[100:103], v[68:71]\n"
"ds_read_b64_tr_b16 v[180:181], v192 offset:49664\n"
"v_mfma_f32_16x16x32_bf16 v[84:87], v[148:151], v[104:107], v[84:87]\n"
"ds_read_b64_tr_b16 v[182:183], %[wbe] offset:50688\n"
"v_mfma_f32_16x16x32_bf16 v[40:43], v[152:155], v[96:99], v[40:43]\n"
"ds_read_b64_tr_b16 v[184:185], %[wbe] offset:49920\n"
"ds_read_b64_tr_b16 v[186:187], v192 offset:50944\n"
"v_mfma_f32_16x16x32_bf16 v[72:75], v[152:155], v[100:103], v[72:75]\n"
"ds_read_b64_tr_b16 v[188:189], v192 offset:49920\n"
"v_mfma_f32_16x16x32_bf16 v[44:47], v[156:159], v[96:99], v[44:47]\n"
"ds_read_b64_tr_b16 v[190:191], %[wbe] offset:50944\n"
"v_mfma_f32_16x16x32_bf16 v[76:79], v[156:159], v[100:103], v[76:79]\n"
"s_waitcnt vmcnt(7)\n"
"s_bitcmp1_b32 s91, 11\n"
"s_cbranch_scc1 LJ2_11_%=\n"
"s_waitcnt vmcnt(2)\n"
"LJ2_11_%=:\n"
"s_waitcnt lgkmcnt(0)\n"
"s_barrier\n"
"v_mfma_f32_16x16x32_bf16 v[16:19], v[160:163], v[108:111], v[16:19]\n"
"ds_read_b128 v[96:99], v194 offset:0\n"
"ds_read_b128 v[100:103], v196 offset:0\n"
"v_mfma_f32_16x16x32_bf16 v[48:51], v[160:163], v[112:115], v[48:51]\n"
"ds_read_b128 v[104:107], %[xb] offset:57344\n"
"ds_read_b64_tr_b16 v[128:129], %[wbe] offset:32768\n"
"s_add_u32 m0, s60, 155648\n"
"v_mfma_f32_16x16x32_bf16 v[20:23], v[164:167], v[108:111], v[20:23]\n"
"buffer_load_dwordx4 %[xoff], %[srdx], s63 offen lds\n"
"v_mfma_f32_16x16x32_bf16 v[52:55], v[164:167], v[112:115], v[52:55]\n"
"ds_read_b64_tr_b16 v[130:131], v192 offset:33792\n"
"v_mfma_f32_16x16x32_bf16 v[24:27], v[168:171], v[108:111], v[24:27]\n"
"ds_read_b64_tr_b16 v[132:133], v192 offset:32768\n"
"ds_read_b64_tr_b16 v[134:135], %[wbe] offset:33792\n"
"v_mfma_f32_16x16x32_bf16 v[56:59], v[168:171], v[112:115], v[56:59]\n"
"buffer_load_dwordx4 v193, %[srdx], s63 offen offset:1024 lds\n"
"s_add_u32 s63, s63, 0x80\n"
"v_mfma_f32_16x16x32_bf16 v[28:31], v[172:175], v[108:111], v[28:31]\n"
"ds_read_b64_tr_b16 v[136:137], %[wbe] offset:33024\n"
"v_mfma_f32_16x16x32_bf16 v[60:63], v[172:175], v[112:115], v[60:63]\n"
"ds_read_b64_tr_b16 v[138:139], v192 offset:34048\n"
"ds_read_b64_tr_b16 v[140:141], v192 offset:33024\n"
"v_mfma_f32_16x16x32_bf16 v[32:35], v[176:179], v[108:111], v[32:35]\n"
"ds_read_b64_tr_b16 v[142:143], %[wbe] offset:34048\n"
"ds_read_b64_tr_b16 v[144:145], %[wbe] offset:33280\n"
"s_add_u32 m0, %[hb], 53248\n"
"v_mfma_f32_16x16x32_bf16 v[64:67], v[176:179], v[112:115], v[64:67]\n"
"buffer_load_dwordx4 v198, %[srdw], s62 offen lds\n"
"v_mfma_f32_16x16x32_bf16 v[80:83], v[176:179], v[116:119], v[80:83]\n"
"ds_read_b64_tr_b16 v[146:147], v192 offset:34304\n"
"v_mfma_f32_16x16x32_bf16 v[36:39], v[180:183], v[108:111], v[36:39]\n"
"ds_read_b64_tr_b16 v[148:149], v192 offset:33280\n"
"ds_read_b64_tr_b16 v[150:151], %[wbe] offset:34304\n"
"v_mfma_f32_16x16x32_bf16 v[68:71], v[180:183], v[112:115], v[68:71]\n"
"buffer_load_dwordx4 v195, %[srdw], s62 offen offset:1024 lds\n"
"s_add_u32 s62, s62, 0x40000\n"
"v_mfma_f32_16x16x32_bf16 v[84:87], v[180:183], v[116:119], v[84:87]\n"
"ds_read_b64_tr_b16 v[152:153], %[wbe] offset:33536\n"
"v_mfma_f32_16x16x32_bf16 v[40:43], v[184:187], v[108:111], v[40:43]\n"
"ds_read_b64_tr_b16 v[154:155], v192 offset:34560\n"
"ds_read_b64_tr_b16 v[156:157], v192 offset:33536\n"
"v_mfma_f32_16x16x32_bf16 v[72:75], v[184:187], v[112:115], v[72:75]\n"
"s_bitcmp0_b32 %[xrot], 11\n"
"s_cbranch_scc1 LK2_11_%=\n"
"s_mov_b32 m0, s53\n"
"s_nop 0\n"
"buffer_load_dwordx4 %[g0], %[srdv], s61 offen nt lds\n"
"buffer_load_dwordx4 %[g1], %[srdv], s61 offen offset:1024 nt lds\n"
"buffer_load_dwordx4 %[g0], %[srdk], s61 offen offset:2048 nt lds\n"
"buffer_load_dwordx4 %[g1], %[srdk], s61 offen offset:3072 nt lds\n"
"s_mov_b32 m0, s57\n"
"s_nop 0\n"
"buffer_load_dwordx4 v248, %[srdk], s61 offen offset:3072 nt lds\n"
"s_add_u32 s61, s61, %[kvs]\n"
"LK2_11_%=:\n"
"v_mfma_f32_16x16x32_bf16 v[44:47], v[188:191], v[108:111], v[44:47]\n"
"ds_read_b64_tr_b16 v[158:159], %[wbe] offset:34560\n"
"v_mfma_f32_16x16x32_bf16 v[76:79], v[188:191], v[112:115], v[76:79]\n"
"s_waitcnt vmcnt(9)\n"
"s_bitcmp1_b32 s91, 12\n"
"s_cbranch_scc1 LJ2_12_%=\n"
"s_waitcnt vmcnt(4)\n"
"LJ2_12_%=:\n"
"s_waitcnt lgkmcnt(0)\n"
"s_barrier\n"
"v_mfma_f32_16x16x32_bf16 v[16:19], v[128:131], v[96:99], v[16:19]\n"
"ds_read_b128 v[108:111], v194 offset:512\n"
"ds_read_b128 v[112:115], v196 offset:512\n"
"v_mfma_f32_16x16x32_bf16 v[48:51], v[128:131], v[100:103], v[48:51]\n"
"ds_read_b128 v[116:119], %[xb] offset:57856\n"
"v_mfma_f32_16x16x32_bf16 v[20:23], v[132:135], v[96:99], v[20:23]\n"
"ds_read_b64_tr_b16 v[160:161], %[wbe] offset:40960\n"
"ds_read_b64_tr_b16 v[162:163], v192 offset:41984\n"
"s_add_u32 m0, %[hb], 36864\n"
"v_mfma_f32_16x16x32_bf16 v[52:55], v[132:135], v[100:103], v[52:55]\n"
"buffer_load_dwordx4 v198, %[srdw], s62 offen lds\n"
"v_mfma_f32_16x16x32_bf16 v[24:27], v[136:139], v[96:99], v[24:27]\n"
"ds_read_b64_tr_b16 v[164:165], v192 offset:40960\n"
"v_mfma_f32_16x16x32_bf16 v[56:59], v[136:139], v[100:103], v[56:59]\n"
"ds_read_b64_tr_b16 v[166:167], %[wbe] offset:41984\n"
"v_mfma_f32_16x16x32_bf16 v[28:31], v[140:143], v[96:99], v[28:31]\n"
"ds_read_b64_tr_b16 v[168:169], %[wbe] offset:41216\n"
"ds_read_b64_tr_b16 v[170:171], v192 offset:42240\n"
"v_mfma_f32_16x16x32_bf16 v[60:63], v[140:143], v[100:103], v[60:63]\n"
"buffer_load_dwordx4 v195, %[srdw], s62 offen offset:1024 lds\n"
"s_add_u32 s62, s62, 0x40000\n"
"v_mfma_f32_16x16x32_bf16 v[32:35], v[144:147], v[96:99], v[32:35]\n"
"ds_read_b64_tr_b16 v[172:173], v192 offset:41216\n"
"v_mfma_f32_16x16x32_bf16 v[64:67], v[144:147], v[100:103], v[64:67]\n"
"ds_read_b64_tr_b16 v[174:175], %[wbe] offset:42240\n"
"v_mfma_f32_16x16x32_bf16 v[80:83], v[144:147], v[104:107], v[80:83]\n"
"ds_read_b64_tr_b16 v[176:177], %[wbe] offset:41472\n"
"ds_read_b64_tr_b16 v[178:179], v192 offset:42496\n"
"v_mfma_f32_16x16x32_bf16 v[36:39], v[148:151], v[96:99], v[36:39]\n"
"s_bitcmp0_b32 %[xrot], 12\n"
"s_cbranch_scc1 LK2_12_%=\n"
"s_mov_b32 m0, s53\n"
"s_nop 0\n"
"buffer_load_dwordx4 %[g0], %[srdv], s61 offen nt lds\n"
"buffer_load_dwordx4 %[g1], %[srdv], s61 offen offset:1024 nt lds\n"
"buffer_load_dwordx4 %[g0], %[srdk], s61 offen offset:2048 nt lds\n"
"buffer_load_dwordx4 %[g1], %[srdk], s61 offen offset:3072 nt lds\n"
"s_mov_b32 m0, s57\n"
"s_nop 0\n"
"buffer_load_dwordx4 v248, %[srdk], s61 offen offset:3072 nt lds\n"
"s_add_u32 s61, s61, %[kvs]\n"
"LK2_12_%=:\n"
"v_mfma_f32_16x16x32_bf16 v[68:71], v[148:151], v[100:103], v[68:71]\n"
"ds_read_b64_tr_b16 v[180:181], v192 offset:41472\n"
"v_mfma_f32_16x16x32_bf16 v[84:87], v[148:151], v[104:107], v[84:87]\n"
"ds_read_b64_tr_b16 v[182:183], %[wbe] offset:42496\n"
"v_mfma_f32_16x16x32_bf16 v[40:43], v[152:155], v[96:99], v[40:43]\n"
"ds_read_b64_tr_b16 v[184:185], %[wbe] offset:41728\n"
"ds_read_b64_tr_b16 v[186:187], v192 offset:42752\n"
"v_mfma_f32_16x16x32_bf16 v[72:75], v[152:155], v[100:103], v[72:75]\n"
"ds_read_b64_tr_b16 v[188:189], v192 offset:41728\n"
"v_mfma_f32_16x16x32_bf16 v[44:47], v[156:159], v[96:99], v[44:47]\n"
"ds_read_b64_tr_b16 v[190:191], %[wbe] offset:42752\n"
"v_mfma_f32_16x16x32_bf16 v[76:79], v[156:159], v[100:103], v[76:79]\n"
"s_waitcnt vmcnt(7)\n"
"s_bitcmp1_b32 s91, 13\n"
"s_cbranch_scc1 LJ2_13_%=\n"
"s_waitcnt vmcnt(2)\n"
"LJ2_13_%=:\n"
"s_waitcnt lgkmcnt(0)\n"
"s_barrier\n"
"v_mfma_f32_16x16x32_bf16 v[16:19], v[160:163], v[108:111], v[16:19]\n"
"ds_read_b128 v[96:99], v194 offset:16384\n"
"ds_read_b128 v[100:103], v196 offset:16384\n"
"v_mfma_f32_16x16x32_bf16 v[48:51], v[160:163], v[112:115], v[48:51]\n"
"ds_read_b128 v[104:107], %[xb] offset:59392\n"
"ds_read_b64_tr_b16 v[128:129], %[wbe] offset:49152\n"
"s_add_u32 m0, s60, 139264\n"
"v_mfma_f32_16x16x32_bf16 v[20:23], v[164:167], v[108:111], v[20:23]\n"
"buffer_load_dwordx4 %[xoff], %[srdx], s63 offen lds\n"
"v_mfma_f32_16x16x32_bf16 v[52:55], v[164:167], v[112:115], v[52:55]\n"
"ds_read_b64_tr_b16 v[130:131], v192 offset:50176\n"
"v_mfma_f32_16x16x32_bf16 v[24:27], v[168:171], v[108:111], v[24:27]\n"
"ds_read_b64_tr_b16 v[132:133], v192 offset:49152\n"
"ds_read_b64_tr_b16 v[134:135], %[wbe] offset:50176\n"
"v_mfma_f32_16x16x32_bf16 v[56:59], v[168:171], v[112:115], v[56:59]\n"
"buffer_load_dwordx4 v193, %[srdx], s63 offen offset:1024 lds\n"
"s_add_u32 s63, s63, 0x80\n"
"v_mfma_f32_16x16x32_bf16 v[28:31], v[172:175], v[108:111], v[28:31]\n"
"ds_read_b64_tr_b16 v[136:137], %[wbe] offset:49408\n"
"v_mfma_f32_16x16x32_bf16 v[60:63], v[172:175], v[112:115], v[60:63]\n"
"ds_read_b64_tr_b16 v[138:139], v192 offset:50432\n"
"ds_read_b64_tr_b16 v[140:141], v192 offset:49408\n"
"v_mfma_f32_16x16x32_bf16 v[32:35], v[176:179], v[108:111], v[32:35]\n"
"ds_read_b64_tr_b16 v[142:143], %[wbe] offset:50432\n"
"ds_read_b64_tr_b16 v[144:145], %[wbe] offset:49664\n"
"s_add_u32 m0, %[hb], 45056\n"
"v_mfma_f32_16x16x32_bf16 v[64:67], v[176:179], v[112:115], v[64:67]\n"
"buffer_load_dwordx4 v198, %[srdw], s62 offen lds\n"
"v_mfma_f32_16x16x32_bf16 v[80:83], v[176:179], v[116:119], v[80:83]\n"
"ds_read_b64_tr_b16 v[146:147], v192 offset:50688\n"
"v_mfma_f32_16x16x32_bf16 v[36:39], v[180:183], v[108:111], v[36:39]\n"
"ds_read_b64_tr_b16 v[148:149], v192 offset:49664\n"
"ds_read_b64_tr_b16 v[150:151], %[wbe] offset:50688\n"
"v_mfma_f32_16x16x32_bf16 v[68:71], v[180:183], v[112:115], v[68:71]\n"
"buffer_load_dwordx4 v195, %[srdw], s62 offen offset:1024 lds\n"
"s_add_u32 s62, s62, 0x40000\n"
"v_mfma_f32_16x16x32_bf16 v[84:87], v[180:183], v[116:119], v[84:87]\n"
"ds_read_b64_tr_b16 v[152:153], %[wbe] offset:49920\n"
"v_mfma_f32_16x16x32_bf16 v[40:43], v[184:187], v[108:111], v[40:43]\n"
"ds_read_b64_tr_b16 v[154:155], v192 offset:50944\n"
"ds_read_b64_tr_b16 v[156:157], v192 offset:49920\n"
"v_mfma_f32_16x16x32_bf16 v[72:75], v[184:187], v[112:115], v[72:75]\n"
"s_bitcmp0_b32 %[xrot], 13\n"
"s_cbranch_scc1 LK2_13_%=\n"
"s_mov_b32 m0, s53\n"
"s_nop 0\n"
"buffer_load_dwordx4 %[g0], %[srdv], s61 offen nt lds\n"
"buffer_load_dwordx4 %[g1], %[srdv], s61 offen offset:1024 nt lds\n"
"buffer_load_dwordx4 %[g0], %[srdk], s61 offen offset:2048 nt lds\n"
"buffer_load_dwordx4 %[g1], %[srdk], s61 offen offset:3072 nt lds\n"
"s_mov_b32 m0, s57\n"
"s_nop 0\n"
"buffer_load_dwordx4 v248, %[srdk], s61 offen offset:3072 nt lds\n"
"s_add_u32 s61, s61, %[kvs]\n"
"LK2_13_%=:\n"
"v_mfma_f32_16x16x32_bf16 v[44:47], v[188:191], v[108:111], v[44:47]\n"
"ds_read_b64_tr_b16 v[158:159], %[wbe] offset:50944\n"
"v_mfma_f32_16x16x32_bf16 v[76:79], v[188:191], v[112:115], v[76:79]\n"
"s_waitcnt vmcnt(9)\n"
"s_bitcmp1_b32 s91, 14\n"
"s_cbranch_scc1 LJ2_14_%=\n"
"s_waitcnt vmcnt(4)\n"
"LJ2_14_%=:\n"
"s_waitcnt lgkmcnt(0)\n"
"s_barrier\n"
"v_mfma_f32_16x16x32_bf16 v[16:19], v[128:131], v[96:99], v[16:19]\n"
"ds_read_b128 v[108:111], v194 offset:16896\n"
"ds_read_b128 v[112:115], v196 offset:16896\n"
"v_mfma_f32_16x16x32_bf16 v[48:51], v[128:131], v[100:103], v[48:51]\n"
"ds_read_b128 v[116:119], %[xb] offset:59904\n"
"v_mfma_f32_16x16x32_bf16 v[20:23], v[132:135], v[96:99], v[20:23]\n"
"ds_read_b64_tr_b16 v[160:161], %[wbe] offset:32768\n"
"s_add_u32 m0, %[hb], 53248\n"
"v_mfma_f32_16x16x32_bf16 v[52:55], v[132:135], v[100:103], v[52:55]\n"
"buffer_load_dwordx4 v198, %[srdw], s62 offen lds\n"
"v_mfma_f32_16x16x32_bf16 v[24:27], v[136:139], v[96:99], v[24:27]\n"
"ds_read_b64_tr_b16 v[162:163], v192 offset:33792\n"
"ds_read_b64_tr_b16 v[164:165], v192 offset:32768\n"
"v_mfma_f32_16x16x32_bf16 v[56:59], v[136:139], v[100:103], v[56:59]\n"
"ds_read_b64_tr_b16 v[166:167], %[wbe] offset:33792\n"
"v_mfma_f32_16x16x32_bf16 v[28:31], v[140:143], v[96:99], v[28:31]\n"
"ds_read_b64_tr_b16 v[168:169], %[wbe] offset:33024\n"
"v_mfma_f32_16x16x32_bf16 v[60:63], v[140:143], v[100:103], v[60:63]\n"
"buffer_load_dwordx4 v195, %[srdw], s62 offen offset:1024 lds\n"
"s_add_u32 s62, s62, 0x40000\n"
"v_mfma_f32_16x16x32_bf16 v[32:35], v[144:147], v[96:99], v[32:35]\n"
"ds_read_b64_tr_b16 v[170:171], v192 offset:34048\n"
"v_mfma_f32_16x16x32_bf16 v[64:67], v[144:147], v[100:103], v[64:67]\n"
"ds_read_b64_tr_b16 v[172:173], v192 offset:33024\n"
"ds_read_b64_tr_b16 v[174:175], %[wbe] offset:34048\n"
"v_mfma_f32_16x16x32_bf16 v[80:83], v[144:147], v[104:107], v[80:83]\n"
"ds_read_b64_tr_b16 v[176:177], %[wbe] offset:33280\n"
"v_mfma_f32_16x16x32_bf16 v[36:39], v[148:151], v[96:99], v[36:39]\n"
"ds_read_b64_tr_b16 v[178:179], v192 offset:34304\n"
"v_mfma_f32_16x16x32_bf16 v[68:71], v[148:151], v[100:103], v[68:71]\n"
"ds_read_b64_tr_b16 v[180:181], v192 offset:33280\n"
"v_mfma_f32_16x16x32_bf16 v[84:87], v[148:151], v[104:107], v[84:87]\n"
"ds_read_b64_tr_b16 v[182:183], %[wbe] offset:34304\n"
"ds_read_b64_tr_b16 v[184:185], %[wbe] offset:33536\n"
"v_mfma_f32_16x16x32_bf16 v[40:43], v[152:155], v[96:99], v[40:43]\n"
"ds_read_b64_tr_b16 v[186:187], v192 offset:34560\n"
"v_mfma_f32_16x16x32_bf16 v[72:75], v[152:155], v[100:103], v[72:75]\n"
"ds_read_b64_tr_b16 v[188:189], v192 offset:33536\n"
"v_mfma_f32_16x16x32_bf16 v[44:47], v[156:159], v[96:99], v[44:47]\n"
"ds_read_b64_tr_b16 v[190:191], %[wbe] offset:34560\n"
"v_mfma_f32_16x16x32_bf16 v[76:79], v[156:159], v[100:103], v[76:79]\n"
"s_waitcnt vmcnt(7)\n"
"s_bitcmp1_b32 s91, 15\n"
"s_cbranch_scc1 LJ2_15_%=\n"
"s_waitcnt vmcnt(2)\n"
"LJ2_15_%=:\n"
"s_waitcnt lgkmcnt(0)\n"
"s_barrier\n"
"v_mfma_f32_16x16x32_bf16 v[16:19], v[160:163], v[108:111], v[16:19]\n"
"ds_read_b128 v[96:99], v194 offset:0\n"
"ds_read_b128 v[100:103], v196 offset:0\n"
"v_mfma_f32_16x16x32_bf16 v[48:51], v[160:163], v[112:115], v[48:51]\n"
"ds_read_b128 v[104:107], %[xb] offset:57344\n"
"v_mfma_f32_16x16x32_bf16 v[20:23], v[164:167], v[108:111], v[20:23]\n"
"ds_read_b64_tr_b16 v[128:129], %[wbe] offset:40960\n"
"v_mfma_f32_16x16x32_bf16 v[52:55], v[164:167], v[112:115], v[52:55]\n"
"ds_read_b64_tr_b16 v[130:131], v192 offset:41984\n"
"v_mfma_f32_16x16x32_bf16 v[24:27], v[168:171], v[108:111], v[24:27]\n"
"ds_read_b64_tr_b16 v[132:133], v192 offset:40960\n"
"v_mfma_f32_16x16x32_bf16 v[56:59], v[168:171], v[112:115], v[56:59]\n"
"ds_read_b64_tr_b16 v[134:135], %[wbe] offset:41984\n"
"v_mfma_f32_16x16x32_bf16 v[28:31], v[172:175], v[108:111], v[28:31]\n"
"ds_read_b64_tr_b16 v[136:137], %[wbe] offset:41216\n"
"v_mfma_f32_16x16x32_bf16 v[60:63], v[172:175], v[112:115], v[60:63]\n"
"ds_read_b64_tr_b16 v[138:139], v192 offset:42240\n"
"v_mfma_f32_16x16x32_bf16 v[32:35], v[176:179], v[108:111], v[32:35]\n"
"ds_read_b64_tr_b16 v[140:141], v192 offset:41216\n"
"ds_read_b64_tr_b16 v[142:143], %[wbe] offset:42240\n"
"v_mfma_f32_16x16x32_bf16 v[64:67], v[176:179], v[112:115], v[64:67]\n"
"ds_read_b64_tr_b16 v[144:145], %[wbe] offset:41472\n"
"v_mfma_f32_16x16x32_bf16 v[80:83], v[176:179], v[116:119], v[80:83]\n"
"ds_read_b64_tr_b16 v[146:147], v192 offset:42496\n"
"v_mfma_f32_16x16x32_bf16 v[36:39], v[180:183], v[108:111], v[36:39]\n"
"ds_read_b64_tr_b16 v[148:149], v192 offset:41472\n"
"v_mfma_f32_16x16x32_bf16 v[68:71], v[180:183], v[112:115], v[68:71]\n"
"ds_read_b64_tr_b16 v[150:151], %[wbe] offset:42496\n"
"v_mfma_f32_16x16x32_bf16 v[84:87], v[180:183], v[116:119], v[84:87]\n"
"ds_read_b64_tr_b16 v[152:153], %[wbe] offset:41728\n"
"v_mfma_f32_16x16x32_bf16 v[40:43], v[184:187], v[108:111], v[40:43]\n"
"ds_read_b64_tr_b16 v[154:155], v192 offset:42752\n"
"v_mfma_f32_16x16x32_bf16 v[72:75], v[184:187], v[112:115], v[72:75]\n"
"ds_read_b64_tr_b16 v[156:157], v192 offset:41728\n"
"v_mfma_f32_16x16x32_bf16 v[44:47], v[188:191], v[108:111], v[44:47]\n"
"ds_read_b64_tr_b16 v[158:159], %[wbe] offset:42752\n"
"v_mfma_f32_16x16x32_bf16 v[76:79], v[188:191], v[112:115], v[76:79]\n"
"s_waitcnt vmcnt(0) lgkmcnt(0)\n"
"s_barrier\n"
"v_mfma_f32_16x16x32_bf16 v[16:19], v[128:131], v[96:99], v[16:19]\n"
"ds_read_b128 v[108:111], v194 offset:512\n"
"ds_read_b128 v[112:115], v196 offset:512\n"
"v_mfma_f32_16x16x32_bf16 v[48:51], v[128:131], v[100:103], v[48:51]\n"
"ds_read_b128 v[116:119], %[xb] offset:57856\n"
"ds_read_b64_tr_b16 v[160:161], %[wbe] offset:49152\n"
"s_mov_b32 m0, s54\n"
"v_mfma_f32_16x16x32_bf16 v[20:23], v[132:135], v[96:99], v[20:23]\n"
"buffer_load_dwordx4 %[g0], %[srdv], s61 offen nt lds\n"
"v_mfma_f32_16x16x32_bf16 v[52:55], v[132:135], v[100:103], v[52:55]\n"
"ds_read_b64_tr_b16 v[162:163], v192 offset:50176\n"
"v_mfma_f32_16x16x32_bf16 v[24:27], v[136:139], v[96:99], v[24:27]\n"
"ds_read_b64_tr_b16 v[164:165], v192 offset:49152\n"
"ds_read_b64_tr_b16 v[166:167], %[wbe] offset:50176\n"
"v_mfma_f32_16x16x32_bf16 v[56:59], v[136:139], v[100:103], v[56:59]\n"
"buffer_load_dwordx4 %[g1], %[srdv], s61 offen offset:1024 nt lds\n"
"v_mfma_f32_16x16x32_bf16 v[28:31], v[140:143], v[96:99], v[28:31]\n"
"ds_read_b64_tr_b16 v[168:169], %[wbe] offset:49408\n"
"v_mfma_f32_16x16x32_bf16 v[60:63], v[140:143], v[100:103], v[60:63]\n"
"ds_read_b64_tr_b16 v[170:171], v192 offset:50432\n"
"ds_read_b64_tr_b16 v[172:173], v192 offset:49408\n"
"v_mfma_f32_16x16x32_bf16 v[32:35], v[144:147], v[96:99], v[32:35]\n"
"ds_read_b64_tr_b16 v[174:175], %[wbe] offset:50432\n"
"ds_read_b64_tr_b16 v[176:177], %[wbe] offset:49664\n"
"v_mfma_f32_16x16x32_bf16 v[64:67], v[144:147], v[100:103], v[64:67]\n"
"buffer_load_dwordx4 %[g0], %[srdk], s61 offen offset:2048 nt lds\n"
"v_mfma_f32_16x16x32_bf16 v[80:83], v[144:147], v[104:107], v[80:83]\n"
"ds_read_b64_tr_b16 v[178:179], v192 offset:50688\n"
"v_mfma_f32_16x16x32_bf16 v[36:39], v[148:151], v[96:99], v[36:39]\n"
"ds_read_b64_tr_b16 v[180:181], v192 offset:49664\n"
"ds_read_b64_tr_b16 v[182:183], %[wbe] offset:50688\n"
"v_mfma_f32_16x16x32_bf16 v[68:71], v[148:151], v[100:103], v[68:71]\n"
"buffer_load_dwordx4 %[g1], %[srdk], s61 offen offset:3072 nt lds\n"
"v_mfma_f32_16x16x32_bf16 v[84:87], v[148:151], v[104:107], v[84:87]\n"
"ds_read_b64_tr_b16 v[184:185], %[wbe] offset:49920\n"
"v_mfma_f32_16x16x32_bf16 v[40:43], v[152:155], v[96:99], v[40:43]\n"
"ds_read_b64_tr_b16 v[186:187], v192 offset:50944\n"
"ds_read_b64_tr_b16 v[188:189], v192 offset:49920\n"
"s_mov_b32 m0, s58\n"
"v_mfma_f32_16x16x32_bf16 v[72:75], v[152:155], v[100:103], v[72:75]\n"
"buffer_load_dwordx4 v248, %[srdk], s61 offen offset:3072 nt lds\n"
"s_add_u32 s61, s61, %[kvs]\n"
"v_mfma_f32_16x16x32_bf16 v[44:47], v[156:159], v[96:99], v[44:47]\n"
"ds_read_b64_tr_b16 v[190:191], %[wbe] offset:50944\n"
"v_mfma_f32_16x16x32_bf16 v[76:79], v[156:159], v[100:103], v[76:79]\n"
"s_waitcnt lgkmcnt(0)\n"
"s_barrier\n"
"v_mfma_f32_16x16x32_bf16 v[16:19], v[160:163], v[108:111], v[16:19]\n"
"ds_read_b128 v[128:131], %[kb] offset:2048\n"
"v_mfma_f32_16x16x32_bf16 v[48:51], v[160:163], v[112:115], v[48:51]\n"
"ds_read_b128 v[132:135], %[kb] offset:2304\n"
"s_mov_b32 m0, s55\n"
"v_mfma_f32_16x16x32_bf16 v[20:23], v[164:167], v[108:111], v[20:23]\n"
"buffer_load_dwordx4 %[g0], %[srdv], s61 offen nt lds\n"
"v_mfma_f32_16x16x32_bf16 v[52:55], v[164:167], v[112:115], v[52:55]\n"
"v_mfma_f32_16x16x32_bf16 v[24:27], v[168:171], v[108:111], v[24:27]\n"
"ds_read_b128 v[136:139], %[kb] offset:2560\n"
"v_mfma_f32_16x16x32_bf16 v[56:59], v[168:171], v[112:115], v[56:59]\n"
"buffer_load_dwordx4 %[g1], %[srdv], s61 offen offset:1024 nt lds\n"
"v_mfma_f32_16x16x32_bf16 v[28:31], v[172:175], v[108:111], v[28:31]\n"
"ds_read_b128 v[140:143], %[kb] offset:2816\n"
"v_mfma_f32_16x16x32_bf16 v[60:63], v[172:175], v[112:115], v[60:63]\n"
"v_mfma_f32_16x16x32_bf16 v[32:35], v[176:179], v[108:111], v[32:35]\n"
"ds_read_b128 v[144:147], %[kb] offset:10240\n"
"v_mfma_f32_16x16x32_bf16 v[64:67], v[176:179], v[112:115], v[64:67]\n"
"buffer_load_dwordx4 %[g0], %[srdk], s61 offen offset:2048 nt lds\n"
"v_mfma_f32_16x16x32_bf16 v[80:83], v[176:179], v[116:119], v[80:83]\n"
"ds_read_b128 v[148:151], %[kb] offset:10496\n"
"v_mfma_f32_16x16x32_bf16 v[36:39], v[180:183], v[108:111], v[36:39]\n"
"v_mfma_f32_16x16x32_bf16 v[68:71], v[180:183], v[112:115], v[68:71]\n"
"buffer_load_dwordx4 %[g1], %[srdk], s61 offen offset:3072 nt lds\n"
"v_mfma_f32_16x16x32_bf16 v[84:87], v[180:183], v[116:119], v[84:87]\n"
"ds_read_b128 v[152:155], %[kb] offset:10752\n"
"v_mfma_f32_16x16x32_bf16 v[40:43], v[184:187], v[108:111], v[40:43]\n"
"ds_read_b128 v[156:159], %[kb] offset:11008\n"
"s_mov_b32 m0, s59\n"
"v_mfma_f32_16x16x32_bf16 v[72:75], v[184:187], v[112:115], v[72:75]\n"
"buffer_load_dwordx4 v248, %[srdk], s61 offen offset:3072 nt lds\n"
"s_add_u32 s61, s61, %[kvs]\n"
"v_mfma_f32_16x16x32_bf16 v[44:47], v[188:191], v[108:111], v[44:47]\n"
"v_mfma_f32_16x16x32_bf16 v[76:79], v[188:191], v[112:115], v[76:79]\n"
"ds_read_b64_tr_b16 v[160:161], %[vbe] offset:0\n"
"ds_read_b64_tr_b16 v[162:163], %[vbe] offset:8192\n"
"ds_read_b64_tr_b16 v[164:165], v243 offset:0\n"
"ds_read_b64_tr_b16 v[166:167], v243 offset:8192\n"
"ds_read_b64_tr_b16 v[168:169], %[vbe] offset:256\n"
"ds_read_b64_tr_b16 v[170:171], %[vbe] offset:8448\n"
"ds_read_b64_tr_b16 v[172:173], v243 offset:256\n"
"ds_read_b64_tr_b16 v[174:175], v243 offset:8448\n"
"ds_read_b64_tr_b16 v[176:177], %[vbe] offset:512\n"
"ds_read_b64_tr_b16 v[178:179], %[vbe] offset:8704\n"
"ds_read_b64_tr_b16 v[180:181], v243 offset:512\n"
"ds_read_b64_tr_b16 v[182:183], v243 offset:8704\n"
"ds_read_b64_tr_b16 v[184:185], %[vbe] offset:768\n"
"ds_read_b64_tr_b16 v[186:187], %[vbe] offset:8960\n"
"ds_read_b64_tr_b16 v[188:189], v243 offset:768\n"
"ds_read_b64_tr_b16 v[190:191], v243 offset:8960\n"
"v_pk_mul_f32 v[80:81], v[80:81], s[66:67]\n"
"v_pk_mul_f32 v[82:83], v[82:83], s[66:67]\n"
"v_pk_mul_f32 v[84:85], v[84:85], s[66:67]\n"
"v_pk_mul_f32 v[86:87], v[86:87], s[66:67]\n"
"v_cvt_pk_bf16_f32 v232, v80, v81\n"
"v_cvt_pk_bf16_f32 v233, v82, v83\n"
"v_cvt_pk_bf16_f32 v234, v84, v85\n"
"v_cvt_pk_bf16_f32 v235, v86, v87\n"
"s_nop 0\n"
"v_permlane32_swap_b32 v232, v234\n"
"v_permlane32_swap_b32 v233, v235\n"
"s_nop 0\n"
"v_permlane16_swap_b32 v232, v234\n"
"v_permlane16_swap_b32 v233, v235\n"
"ds_write_b128 v247, v[232:235] offset:2048\n"
"v_pk_mul_f32 v[16:17], v[16:17], s[66:67]\n"
"v_pk_mul_f32 v[18:19], v[18:19], s[66:67]\n"
"v_pk_mul_f32 v[20:21], v[20:21], s[66:67]\n"
"v_pk_mul_f32 v[22:23], v[22:23], s[66:67]\n"
"v_cvt_pk_bf16_f32 v96, v16, v17\n"
"v_cvt_pk_bf16_f32 v97, v18, v19\n"
"v_cvt_pk_bf16_f32 v98, v20, v21\n"
"v_cvt_pk_bf16_f32 v99, v22, v23\n"
"v_pk_mul_f32 v[24:25], v[24:25], s[66:67]\n"
"v_pk_mul_f32 v[26:27], v[26:27], s[66:67]\n"
"v_pk_mul_f32 v[28:29], v[28:29], s[66:67]\n"
"v_pk_mul_f32 v[30:31], v[30:31], s[66:67]\n"
"v_cvt_pk_bf16_f32 v100, v24, v25\n"
"v_cvt_pk_bf16_f32 v101, v26, v27\n"
"v_cvt_pk_bf16_f32 v102, v28, v29\n"
"v_cvt_pk_bf16_f32 v103, v30, v31\n"
"v_pk_mul_f32 v[32:33], v[32:33], s[66:67]\n"
"v_pk_mul_f32 v[34:35], v[34:35], s[66:67]\n"
"v_pk_mul_f32 v[36:37], v[36:37], s[66:67]\n"
"v_pk_mul_f32 v[38:39], v[38:39], s[66:67]\n"
"v_cvt_pk_bf16_f32 v104, v32, v33\n"
"v_cvt_pk_bf16_f32 v105, v34, v35\n"
"v_cvt_pk_bf16_f32 v106, v36, v37\n"
"v_cvt_pk_bf16_f32 v107, v38, v39\n"
"v_pk_mul_f32 v[40:41], v[40:41], s[66:67]\n"
"v_pk_mul_f32 v[42:43], v[42:43], s[66:67]\n"
"v_pk_mul_f32 v[44:45], v[44:45], s[66:67]\n"
"v_pk_mul_f32 v[46:47], v[46:47], s[66:67]\n"
"v_cvt_pk_bf16_f32 v108, v40, v41\n"
"v_cvt_pk_bf16_f32 v109, v42, v43\n"
"v_cvt_pk_bf16_f32 v110, v44, v45\n"
"v_cvt_pk_bf16_f32 v111, v46, v47\n"
"v_pk_mul_f32 v[48:49], v[48:49], s[66:67]\n"
"v_pk_mul_f32 v[50:51], v[50:51], s[66:67]\n"
"v_pk_mul_f32 v[52:53], v[52:53], s[66:67]\n"
"v_pk_mul_f32 v[54:55], v[54:55], s[66:67]\n"
"v_cvt_pk_bf16_f32 v112, v48, v49\n"
"v_cvt_pk_bf16_f32 v113, v50, v51\n"
"v_cvt_pk_bf16_f32 v114, v52, v53\n"
"v_cvt_pk_bf16_f32 v115, v54, v55\n"
"v_pk_mul_f32 v[56:57], v[56:57], s[66:67]\n"
"v_pk_mul_f32 v[58:59], v[58:59], s[66:67]\n"
"v_pk_mul_f32 v[60:61], v[60:61], s[66:67]\n"
"v_pk_mul_f32 v[62:63], v[62:63], s[66:67]\n"
"v_cvt_pk_bf16_f32 v116, v56, v57\n"
"v_cvt_pk_bf16_f32 v117, v58, v59\n"
"v_cvt_pk_bf16_f32 v118, v60, v61\n"
"v_cvt_pk_bf16_f32 v119, v62, v63\n"
"v_pk_mul_f32 v[64:65], v[64:65], s[66:67]\n"
"v_pk_mul_f32 v[66:67], v[66:67], s[66:67]\n"
"v_pk_mul_f32 v[68:69], v[68:69], s[66:67]\n"
"v_pk_mul_f32 v[70:71], v[70:71], s[66:67]\n"
"v_cvt_pk_bf16_f32 v120, v64, v65\n"
"v_cvt_pk_bf16_f32 v121, v66, v67\n"
"v_cvt_pk_bf16_f32 v122, v68, v69\n"
"v_cvt_pk_bf16_f32 v123, v70, v71\n"
"v_pk_mul_f32 v[72:73], v[72:73], s[66:67]\n"
"v_pk_mul_f32 v[74:75], v[74:75], s[66:67]\n"
"v_pk_mul_f32 v[76:77], v[76:77], s[66:67]\n"
"v_pk_mul_f32 v[78:79], v[78:79], s[66:67]\n"
"v_cvt_pk_bf16_f32 v124, v72, v73\n"
"v_cvt_pk_bf16_f32 v125, v74, v75\n"
"v_cvt_pk_bf16_f32 v126, v76, v77\n"
"v_cvt_pk_bf16_f32 v127, v78, v79\n"
"v_permlane32_swap_b32 v96, v98\n"
"v_permlane32_swap_b32 v97, v99\n"
"v_permlane32_swap_b32 v100, v102\n"
"v_permlane32_swap_b32 v101, v103\n"
"v_permlane32_swap_b32 v104, v106\n"
"v_permlane32_swap_b32 v105, v107\n"
"v_permlane32_swap_b32 v108, v110\n"
"v_permlane32_swap_b32 v109, v111\n"
"v_permlane32_swap_b32 v112, v114\n"
"v_permlane32_swap_b32 v113, v115\n"
"v_permlane32_swap_b32 v116, v118\n"
"v_permlane32_swap_b32 v117, v119\n"
"v_permlane32_swap_b32 v120, v122\n"
"v_permlane32_swap_b32 v121, v123\n"
"v_permlane32_swap_b32 v124, v126\n"
"v_permlane32_swap_b32 v125, v127\n"
"v_permlane16_swap_b32 v96, v98\n"
"v_permlane16_swap_b32 v97, v99\n"
"v_permlane16_swap_b32 v100, v102\n"
"v_permlane16_swap_b32 v101, v103\n"
"v_permlane16_swap_b32 v104, v106\n"
"v_permlane16_swap_b32 v105, v107\n"
"v_permlane16_swap_b32 v108, v110\n"
"v_permlane16_swap_b32 v109, v111\n"
"v_permlane16_swap_b32 v112, v114\n"
"v_permlane16_swap_b32 v113, v115\n"
"v_permlane16_swap_b32 v116, v118\n"
"v_permlane16_swap_b32 v117, v119\n"
"v_permlane16_swap_b32 v120, v122\n"
"v_permlane16_swap_b32 v121, v123\n"
"v_permlane16_swap_b32 v124, v126\n"
"v_permlane16_swap_b32 v125, v127\n"
"s_waitcnt lgkmcnt(0)\n"
"s_barrier\n"
"v_mfma_f32_16x16x32_bf16 v[192:195], v[128:131], v[96:99], 0\n"
"v_mov_b32 v16, 0\n"
"v_mov_b32 v17, 0\n"
"v_mov_b32 v18, 0\n"
"v_mov_b32 v19, 0\n"
"v_mov_b32 v20, 0\n"
"v_mov_b32 v21, 0\n"
"v_mfma_f32_16x16x32_bf16 v[192:195], v[132:135], v[100:103], v[192:195]\n"
"v_mov_b32 v22, 0\n"
"v_mov_b32 v23, 0\n"
"v_mov_b32 v24, 0\n"
"v_mov_b32 v25, 0\n"
"v_mov_b32 v26, 0\n"
"v_mov_b32 v27, 0\n"
"v_mfma_f32_16x16x32_bf16 v[192:195], v[136:139], v[104:107], v[192:195]\n"
"v_mov_b32 v28, 0\n"
"v_mov_b32 v29, 0\n"
"v_mov_b32 v30, 0\n"
"v_mov_b32 v31, 0\n"
"v_mov_b32 v32, 0\n"
"v_mov_b32 v33, 0\n"
"v_mfma_f32_16x16x32_bf16 v[192:195], v[140:143], v[108:111], v[192:195]\n"
"v_mov_b32 v34, 0\n"
"v_mov_b32 v35, 0\n"
"v_mov_b32 v36, 0\n"
"v_mov_b32 v37, 0\n"
"v_mov_b32 v38, 0\n"
"v_mov_b32 v39, 0\n"
"v_mfma_f32_16x16x32_bf16 v[196:199], v[128:131], v[112:115], 0\n"
"v_mov_b32 v40, 0\n"
"v_mov_b32 v41, 0\n"
"v_mov_b32 v42, 0\n"
"v_mov_b32 v43, 0\n"
"v_mov_b32 v44, 0\n"
"v_mov_b32 v45, 0\n"
"v_mfma_f32_16x16x32_bf16 v[196:199], v[132:135], v[116:119], v[196:199]\n"
"v_mov_b32 v46, 0\n"
"v_mov_b32 v47, 0\n"
"v_mov_b32 v48, 0\n"
"v_mov_b32 v49, 0\n"
"v_mov_b32 v50, 0\n"
"v_mov_b32 v51, 0\n"
"v_mfma_f32_16x16x32_bf16 v[196:199], v[136:139], v[120:123], v[196:199]\n"
"v_mov_b32 v52, 0\n"
"v_mov_b32 v53, 0\n"
"v_mov_b32 v54, 0\n"
"v_mov_b32 v55, 0\n"
"v_mov_b32 v56, 0\n"
"v_mov_b32 v57, 0\n"
"v_mfma_f32_16x16x32_bf16 v[196:199], v[140:143], v[124:127], v[196:199]\n"
"v_mov_b32 v58, 0\n"
"v_mov_b32 v59, 0\n"
"v_mov_b32 v60, 0\n"
"v_mov_b32 v61, 0\n"
"v_mov_b32 v62, 0\n"
"v_mov_b32 v63, 0\n"
"v_mfma_f32_16x16x32_bf16 v[200:203], v[144:147], v[96:99], 0\n"
"v_mov_b32 v64, 0\n"
"v_mov_b32 v65, 0\n"
"v_mov_b32 v66, 0\n"
"v_mov_b32 v67, 0\n"
"v_mov_b32 v68, 0\n"
"v_mov_b32 v69, 0\n"
"v_mfma_f32_16x16x32_bf16 v[200:203], v[148:151], v[100:103], v[200:203]\n"
"v_mov_b32 v70, 0\n"
"v_mov_b32 v71, 0\n"
"v_mov_b32 v72, 0\n"
"v_mov_b32 v73, 0\n"
"v_mov_b32 v74, 0\n"
"v_mov_b32 v75, 0\n"
"v_mfma_f32_16x16x32_bf16 v[200:203], v[152:155], v[104:107], v[200:203]\n"
"v_mov_b32 v76, 0\n"
"v_mov_b32 v77, 0\n"
"v_mov_b32 v78, 0\n"
"v_mov_b32 v79, 0\n"
"v_mov_b32 v240, 0\n"
"v_mov_b32 v241, 0\n"
"v_mfma_f32_16x16x32_bf16 v[200:203], v[156:159], v[108:111], v[200:203]\n"
"v_mov_b32 v242, 0\n"
"v_mov_b32 v80, 0\n"
"v_mov_b32 v81, 0\n"
"v_mov_b32 v82, 0\n"
"v_mov_b32 v83, 0\n"
"v_mov_b32 v84, 0\n"
"v_mfma_f32_16x16x32_bf16 v[204:207], v[144:147], v[112:115], 0\n"
"v_mov_b32 v85, 0\n"
"v_mov_b32 v86, 0\n"
"v_mov_b32 v87, 0\n"
"v_mov_b32 v88, 0\n"
"v_mov_b32 v89, 0\n"
"v_mov_b32 v90, 0\n"
"v_mfma_f32_16x16x32_bf16 v[204:207], v[148:151], v[116:119], v[204:207]\n"
"v_mov_b32 v91, 0\n"
"v_mov_b32 v92, 0\n"
"v_mov_b32 v93, 0\n"
"v_mov_b32 v94, 0\n"
"v_mov_b32 v95, 0\n"
"v_mfma_f32_16x16x32_bf16 v[204:207], v[152:155], v[120:123], v[204:207]\n"
"v_mfma_f32_16x16x32_bf16 v[204:207], v[156:159], v[124:127], v[204:207]\n"
"v_max3_f32 v249, v192, v193, v194\n"
"v_max3_f32 v249, v249, v195, v200\n"
"v_max3_f32 v249, v249, v201, v202\n"
"v_max_f32 v249, v249, v203\n"
"v_max3_f32 v251, v196, v197, v198\n"
"s_nop 2\n"
"v_max3_f32 v251, v251, v199, v204\n"
"v_max3_f32 v251, v251, v205, v206\n"
"v_max_f32 v251, v251, v207\n"
"v_mov_b32 v250, v249\n"
"v_mov_b32 v252, v251\n"
"s_nop 0\n"
"v_permlane32_swap_b32 v249, v250\n"
"v_permlane32_swap_b32 v251, v252\n"
"v_max_f32 v249, v249, v250\n"
"v_max_f32 v251, v251, v252\n"
"v_mov_b32 v250, v249\n"
"v_mov_b32 v252, v251\n"
"s_nop 0\n"
"v_permlane16_swap_b32 v249, v250\n"
"v_permlane16_swap_b32 v251, v252\n"
"v_max_f32 v249, v249, v250\n"
"v_max_f32 v251, v251, v252\n"
"v_sub_f32 v212, 0, v249\n"
"v_sub_f32 v213, 0, v249\n"
"v_sub_f32 v214, 0, v249\n"
"v_sub_f32 v215, 0, v249\n"
"v_sub_f32 v192, v192, v249\n"
"v_sub_f32 v193, v193, v249\n"
"v_sub_f32 v194, v194, v249\n"
"v_sub_f32 v195, v195, v249\n"
"v_sub_f32 v200, v200, v249\n"
"v_sub_f32 v201, v201, v249\n"
"v_sub_f32 v202, v202, v249\n"
"v_sub_f32 v203, v203, v249\n"
"v_sub_f32 v216, 0, v251\n"
"v_sub_f32 v217, 0, v251\n"
"v_sub_f32 v218, 0, v251\n"
"v_sub_f32 v219, 0, v251\n"
"v_sub_f32 v196, v196, v251\n"
"v_sub_f32 v197, v197, v251\n"
"v_sub_f32 v198, v198, v251\n"
"v_sub_f32 v199, v199, v251\n"
"v_sub_f32 v204, v204, v251\n"
"v_sub_f32 v205, v205, v251\n"
"v_sub_f32 v206, v206, v251\n"
"v_sub_f32 v207, v207, v251\n"
"s_movk_i32 s68, 1152\n"
"s_lshr_b32 s69, %[nb256], 3\n"
"s_add_u32 s69, s69, 5632\n"
"v_and_b32 v254, 255, %[g0]\n"
"v_add_u32_e64 v254, %[kq64], v254\n"
"v_and_b32 v254, 255, v254\n"
"v_lshrrev_b32_e64 v255, 8, %[g0]\n"
"v_mul_u32_u24 v255, s68, v255\n"
"v_add_u32 v255, %[nb256], v255\n"
"v_add_u32 v249, v254, v255\n"
"v_xor_b32 v254, 32, v254\n"
"v_add_u32 v250, v254, v255\n"
"v_add_u32 v250, 3584, v250\n"
"v_subrev_u32 v251, 18432, v250\n"
"v_lshrrev_b32_e64 v254, 5, %[l16]\n"
"v_mul_u32_u24 v254, s68, v254\n"
"v_and_b32 v255, 16, %[l16]\n"
"v_add_u32 v254, v254, v255\n"
"v_add_u32 v252, s69, v254\n"
"s_cmp_eq_u32 %[hb], 0\n"
"s_cbranch_scc1 LH2_%=\n"
"s_barrier\n"
"LH2_%=:\n"
"v_exp_f32 v192, v192\n"
"v_exp_f32 v193, v193\n"
"v_exp_f32 v194, v194\n"
"v_exp_f32 v195, v195\n"
"v_cvt_pk_bf16_f32 v224, v192, v193\n"
"v_cvt_pk_bf16_f32 v225, v194, v195\n"
"v_exp_f32 v196, v196\n"
"v_exp_f32 v197, v197\n"
"v_exp_f32 v198, v198\n"
"v_exp_f32 v199, v199\n"
"v_cvt_pk_bf16_f32 v228, v196, v197\n"
"v_cvt_pk_bf16_f32 v229, v198, v199\n"
"v_exp_f32 v200, v200\n"
"v_exp_f32 v201, v201\n"
"v_exp_f32 v202, v202\n"
"v_exp_f32 v203, v203\n"
"v_cvt_pk_bf16_f32 v226, v200, v201\n"
"v_cvt_pk_bf16_f32 v227, v202, v203\n"
"v_exp_f32 v204, v204\n"
"v_exp_f32 v205, v205\n"
"v_exp_f32 v206, v206\n"
"v_exp_f32 v207, v207\n"
"v_cvt_pk_bf16_f32 v230, v204, v205\n"
"v_cvt_pk_bf16_f32 v231, v206, v207\n"
"s_cmp_lg_u32 %[isa], 0\n"
"s_cbranch_scc1 LA2_%=\n"
"s_mov_b32 m0, s52\n"
"s_nop 0\n"
"buffer_load_dwordx4 %[g0], %[srdv], s61 offen nt lds\n"
"buffer_load_dwordx4 %[g1], %[srdv], s61 offen offset:1024 nt lds\n"
"buffer_load_dwordx4 %[g0], %[srdk], s61 offen offset:2048 nt lds\n"
"buffer_load_dwordx4 %[g1], %[srdk], s61 offen offset:3072 nt lds\n"
"s_mov_b32 m0, s56\n"
"s_nop 0\n"
"buffer_load_dwordx4 v248, %[srdk], s61 offen offset:3072 nt lds\n"
"s_add_u32 s61, s61, 0x2000\n"
"s_waitcnt vmcnt(15)\n"
".p2align 6\n"
"LB2_%=:\n"
"KB2 16384,s53,s57\n"
"KB2 32768,s54,s58\n"
"KB2 49152,s55,s59\n"
"KB2 0,s52,s56\n"
"s_branch LB2_%=\n"
".p2align 3\n"
"LQ2_%=:\n"
"s_barrier\n"
"s_setprio 1\n"
"ds_read_b128 v[232:235], v247 offset:4096\n"
"v_mfma_f32_16x16x32_bf16 v[32:35], v[176:179], v[224:227], v[32:35]\n"
"ds_read_b128 v[128:131], %[kb] offset:2048\n"
"v_add_f32 v240, v240, v192\n"
"v_add_f32 v241, v241, v196\n"
"v_mfma_f32_16x16x32_bf16 v[64:67], v[176:179], v[228:231], v[64:67]\n"
"ds_read_b128 v[132:135], %[kb] offset:2304\n"
"v_add_f32 v240, v240, v193\n"
"v_add_f32 v241, v241, v197\n"
"v_mfma_f32_16x16x32_bf16 v[36:39], v[180:183], v[224:227], v[36:39]\n"
"ds_read_b128 v[136:139], %[kb] offset:2560\n"
"v_add_f32 v240, v240, v194\n"
"v_add_f32 v241, v241, v198\n"
"v_mfma_f32_16x16x32_bf16 v[68:71], v[180:183], v[228:231], v[68:71]\n"
"ds_read_b128 v[140:143], %[kb] offset:2816\n"
"v_add_f32 v240, v240, v195\n"
"v_add_f32 v241, v241, v199\n"
"v_mfma_f32_16x16x32_bf16 v[40:43], v[184:187], v[224:227], v[40:43]\n"
"ds_read_b128 v[144:147], %[kb] offset:10240\n"
"v_add_f32 v240, v240, v200\n"
"v_add_f32 v241, v241, v204\n"
"v_mfma_f32_16x16x32_bf16 v[72:75], v[184:187], v[228:231], v[72:75]\n"
"ds_read_b128 v[148:151], %[kb] offset:10496\n"
"v_add_f32 v240, v240, v201\n"
"v_add_f32 v241, v241, v205\n"
"v_mfma_f32_16x16x32_bf16 v[44:47], v[188:191], v[224:227], v[44:47]\n"
"ds_read_b128 v[152:155], %[kb] offset:10752\n"
"v_add_f32 v240, v240, v202\n"
"v_add_f32 v241, v241, v206\n"
"v_mfma_f32_16x16x32_bf16 v[76:79], v[188:191], v[228:231], v[76:79]\n"
"ds_read_b128 v[156:159], %[kb] offset:11008\n"
"v_add_f32 v240, v240, v203\n"
"v_add_f32 v241, v241, v207\n"
"v_mfma_f32_16x16x32_bf16 v[16:19], v[160:163], v[224:227], v[16:19]\n"
"ds_read_b64_tr_b16 v[176:177], %[vbe] offset:512\n"
"v_mfma_f32_16x16x32_bf16 v[48:51], v[160:163], v[228:231], v[48:51]\n"
"ds_read_b64_tr_b16 v[178:179], %[vbe] offset:8704\n"
"s_waitcnt lgkmcnt(10)\n"
"v_mfma_f32_16x16x32_bf16 v[80:83], v[160:163], v[232:235], v[80:83]\n"
"ds_read_b64_tr_b16 v[180:181], v243 offset:512\n"
"v_mfma_f32_16x16x32_bf16 v[20:23], v[164:167], v[224:227], v[20:23]\n"
"ds_read_b64_tr_b16 v[182:183], v243 offset:8704\n"
"v_mfma_f32_16x16x32_bf16 v[52:55], v[164:167], v[228:231], v[52:55]\n"
"ds_read_b64_tr_b16 v[184:185], %[vbe] offset:768\n"
"v_mfma_f32_16x16x32_bf16 v[84:87], v[164:167], v[232:235], v[84:87]\n"
"ds_read_b64_tr_b16 v[186:187], %[vbe] offset:8960\n"
"v_mfma_f32_16x16x32_bf16 v[24:27], v[168:171], v[224:227], v[24:27]\n"
"ds_read_b64_tr_b16 v[188:189], v243 offset:768\n"
"v_mfma_f32_16x16x32_bf16 v[56:59], v[168:171], v[228:231], v[56:59]\n"
"ds_read_b64_tr_b16 v[190:191], v243 offset:8960\n"
"v_mfma_f32_16x16x32_bf16 v[88:91], v[168:171], v[232:235], v[88:91]\n"
"ds_read_b64_tr_b16 v[160:161], %[vbe] offset:0\n"
"v_mfma_f32_16x16x32_bf16 v[28:31], v[172:175], v[224:227], v[28:31]\n"
"ds_read_b64_tr_b16 v[162:163], %[vbe] offset:8192\n"
"v_mfma_f32_16x16x32_bf16 v[60:63], v[172:175], v[228:231], v[60:63]\n"
"ds_read_b64_tr_b16 v[164:165], v243 offset:0\n"
"v_mfma_f32_16x16x32_bf16 v[92:95], v[172:175], v[232:235], v[92:95]\n"
"ds_read_b64_tr_b16 v[166:167], v243 offset:8192\n"
"s_waitcnt lgkmcnt(12)\n"
"v_mfma_f32_16x16x32_bf16 v[192:195], v[128:131], v[96:99], v[212:215]\n"
"ds_read_b64_tr_b16 v[168:169], %[vbe] offset:256\n"
"v_mfma_f32_16x16x32_bf16 v[192:195], v[132:135], v[100:103], v[192:195]\n"
"ds_read_b64_tr_b16 v[170:171], %[vbe] offset:8448\n"
"v_mfma_f32_16x16x32_bf16 v[192:195], v[136:139], v[104:107], v[192:195]\n"
"ds_read_b64_tr_b16 v[172:173], v243 offset:256\n"
"v_mfma_f32_16x16x32_bf16 v[192:195], v[140:143], v[108:111], v[192:195]\n"
"ds_read_b64_tr_b16 v[174:175], v243 offset:8448\n"
"v_mfma_f32_16x16x32_bf16 v[196:199], v[128:131], v[112:115], v[216:219]\n"
"v_mfma_f32_16x16x32_bf16 v[196:199], v[132:135], v[116:119], v[196:199]\n"
"v_mfma_f32_16x16x32_bf16 v[196:199], v[136:139], v[120:123], v[196:199]\n"
"v_mfma_f32_16x16x32_bf16 v[196:199], v[140:143], v[124:127], v[196:199]\n"
"v_mfma_f32_16x16x32_bf16 v[200:203], v[144:147], v[96:99], v[212:215]\n"
"v_mfma_f32_16x16x32_bf16 v[200:203], v[148:151], v[100:103], v[200:203]\n"
"v_mfma_f32_16x16x32_bf16 v[200:203], v[152:155], v[104:107], v[200:203]\n"
"v_mfma_f32_16x16x32_bf16 v[200:203], v[156:159], v[108:111], v[200:203]\n"
"v_mfma_f32_16x16x32_bf16 v[204:207], v[144:147], v[112:115], v[216:219]\n"
"v_mfma_f32_16x16x32_bf16 v[204:207], v[148:151], v[116:119], v[204:207]\n"
"v_mfma_f32_16x16x32_bf16 v[204:207], v[152:155], v[120:123], v[204:207]\n"
"v_mfma_f32_16x16x32_bf16 v[204:207], v[156:159], v[124:127], v[204:207]\n"
"s_waitcnt lgkmcnt(0)\n"
"s_barrier\n"
"s_setprio 0\n"
"s_getpc_b64 s[94:95]\n"
"LMb2_%=:\n"
"s_add_u32 s94, s94, (LZZ2_%=-16384-LMb2_%=)&0xffffffff\n"
"s_addc_u32 s95, s95, (LZZ2_%=-16384-LMb2_%=)>>32\n"
"v_lshlrev_b32 v236, 4, %[l16]\n"
"s_lshr_b32 s68, %[hb], 9\n"
"v_add_u32 v236, s68, v236\n"
"s_lshl_b32 exec_lo, 0x11111111, %[wrot]\n"
"s_mov_b32 exec_hi, exec_lo\n"
"global_load_dword v253, v236, s[94:95]\n"
"s_mov_b64 exec, -1\n"
"s_lshl_b32 s88, %[osoff], 2\n"
"s_mov_b32 m0, s52\n"
"s_nop 0\n"
"buffer_load_dwordx4 v249, s[76:79], s88 offen lds\n"
"buffer_load_dwordx4 v250, s[76:79], s88 offen offset:1024 lds\n"
"s_lshl_b32 s89, %[osoff], 2\n"
"s_add_u32 s89, s89, 34816\n"
"buffer_load_dwordx4 v249, s[76:79], s89 offen offset:2048 lds\n"
"buffer_load_dwordx4 v250, s[76:79], s89 offen offset:3072 lds\n"
"s_mov_b32 m0, s56\n"
"s_nop 0\n"
"buffer_load_dwordx4 v251, s[76:79], s89 offen offset:3072 lds\n"
"v_exp_f32 v192, v192\n"
"v_exp_f32 v193, v193\n"
"v_exp_f32 v194, v194\n"
"v_exp_f32_e64 v195, v195\n"
"v_cvt_pk_bf16_f32 v224, v192, v193\n"
"v_cvt_pk_bf16_f32 v225, v194, v195\n"
"v_exp_f32 v196, v196\n"
"v_exp_f32 v197, v197\n"
"v_exp_f32 v198, v198\n"
"v_exp_f32 v199, v199\n"
"v_cvt_pk_bf16_f32 v228, v196, v197\n"
"v_cvt_pk_bf16_f32 v229, v198, v199\n"
"v_exp_f32 v200, v200\n"
"v_exp_f32 v201, v201\n"
"v_exp_f32 v202, v202\n"
"v_exp_f32 v203, v203\n"
"v_cvt_pk_bf16_f32 v226, v200, v201\n"
"v_cvt_pk_bf16_f32 v227, v202, v203\n"
"v_exp_f32 v204, v204\n"
"v_exp_f32 v205, v205\n"
"v_exp_f32 v206, v206\n"
"v_exp_f32 v207, v207\n"
"v_cvt_pk_bf16_f32 v230, v204, v205\n"
"v_cvt_pk_bf16_f32 v231, v206, v207\n"
"s_waitcnt vmcnt(15)\n"
"s_barrier\n"
"s_setprio 1\n"
"ds_read_b128 v[232:235], v247 offset:4096\n"
"v_mfma_f32_16x16x32_bf16 v[32:35], v[176:179], v[224:227], v[32:35]\n"
"ds_read_b128 v[128:131], %[kb] offset:18432\n"
"v_add_f32 v240, v240, v192\n"
"v_add_f32_e64 v241, v241, v196\n"
"v_mfma_f32_16x16x32_bf16 v[64:67], v[176:179], v[228:231], v[64:67]\n"
"ds_read_b128 v[132:135], %[kb] offset:18688\n"
"v_add_f32 v240, v240, v193\n"
"v_add_f32 v241, v241, v197\n"
"v_mfma_f32_16x16x32_bf16 v[36:39], v[180:183], v[224:227], v[36:39]\n"
"ds_read_b128 v[136:139], %[kb] offset:18944\n"
"v_add_f32 v240, v240, v194\n"
"v_add_f32 v241, v241, v198\n"
"v_mfma_f32_16x16x32_bf16 v[68:71], v[180:183], v[228:231], v[68:71]\n"
"ds_read_b128 v[140:143], %[kb] offset:19200\n"
"v_add_f32 v240, v240, v195\n"
"v_add_f32 v241, v241, v199\n"
"v_mfma_f32_16x16x32_bf16 v[40:43], v[184:187], v[224:227], v[40:43]\n"
"ds_read_b128 v[144:147], %[kb] offset:26624\n"
"v_add_f32 v240, v240, v200\n"
"v_add_f32 v241, v241, v204\n"
"v_mfma_f32_16x16x32_bf16 v[72:75], v[184:187], v[228:231], v[72:75]\n"
"ds_read_b128 v[148:151], %[kb] offset:26880\n"
"v_add_f32 v240, v240, v201\n"
"v_add_f32 v241, v241, v205\n"
"v_mfma_f32_16x16x32_bf16 v[44:47], v[188:191], v[224:227], v[44:47]\n"
"ds_read_b128 v[152:155], %[kb] offset:27136\n"
"v_add_f32 v240, v240, v202\n"
"v_add_f32 v241, v241, v206\n"
"v_mfma_f32_16x16x32_bf16 v[76:79], v[188:191], v[228:231], v[76:79]\n"
"ds_read_b128 v[156:159], %[kb] offset:27392\n"
"v_add_f32 v240, v240, v203\n"
"v_add_f32 v241, v241, v207\n"
"v_mfma_f32_16x16x32_bf16 v[16:19], v[160:163], v[224:227], v[16:19]\n"
"ds_read_b64_tr_b16 v[176:177], %[vbe] offset:16896\n"
"v_mfma_f32_16x16x32_bf16 v[48:51], v[160:163], v[228:231], v[48:51]\n"
"ds_read_b64_tr_b16 v[178:179], %[vbe] offset:25088\n"
"s_waitcnt lgkmcnt(10)\n"
"v_mfma_f32_16x16x32_bf16 v[80:83], v[160:163], v[232:235], v[80:83]\n"
"ds_read_b64_tr_b16 v[180:181], v243 offset:16896\n"
"v_mfma_f32_16x16x32_bf16 v[20:23], v[164:167], v[224:227], v[20:23]\n"
"ds_read_b64_tr_b16 v[182:183], v243 offset:25088\n"
"v_mfma_f32_16x16x32_bf16 v[52:55], v[164:167], v[228:231], v[52:55]\n"
"ds_read_b64_tr_b16 v[184:185], %[vbe] offset:17152\n"
"v_mfma_f32_16x16x32_bf16 v[84:87], v[164:167], v[232:235], v[84:87]\n"
"ds_read_b64_tr_b16 v[186:187], %[vbe] offset:25344\n"
"v_mfma_f32_16x16x32_bf16 v[24:27], v[168:171], v[224:227], v[24:27]\n"
"ds_read_b64_tr_b16 v[188:189], v243 offset:17152\n"
"v_mfma_f32_16x16x32_bf16 v[56:59], v[168:171], v[228:231], v[56:59]\n"
"ds_read_b64_tr_b16 v[190:191], v243 offset:25344\n"
"v_mfma_f32_16x16x32_bf16 v[88:91], v[168:171], v[232:235], v[88:91]\n"
"ds_read_b64_tr_b16 v[160:161], %[vbe] offset:16384\n"
"v_mfma_f32_16x16x32_bf16 v[28:31], v[172:175], v[224:227], v[28:31]\n"
"ds_read_b64_tr_b16 v[162:163], %[vbe] offset:24576\n"
"v_mfma_f32_16x16x32_bf16 v[60:63], v[172:175], v[228:231], v[60:63]\n"
"ds_read_b64_tr_b16 v[164:165], v243 offset:16384\n"
"v_mfma_f32_16x16x32_bf16 v[92:95], v[172:175], v[232:235], v[92:95]\n"
"ds_read_b64_tr_b16 v[166:167], v243 offset:24576\n"
"s_waitcnt lgkmcnt(12)\n"
"v_mfma_f32_16x16x32_bf16 v[192:195], v[128:131], v[96:99], v[212:215]\n"
"ds_read_b64_tr_b16 v[168:169], %[vbe] offset:16640\n"
"v_mfma_f32_16x16x32_bf16 v[192:195], v[132:135], v[100:103], v[192:195]\n"
"ds_read_b64_tr_b16 v[170:171], %[vbe] offset:24832\n"
"v_mfma_f32_16x16x32_bf16 v[192:195], v[136:139], v[104:107], v[192:195]\n"
"ds_read_b64_tr_b16 v[172:173], v243 offset:16640\n"
"v_mfma_f32_16x16x32_bf16 v[192:195], v[140:143], v[108:111], v[192:195]\n"
"ds_read_b64_tr_b16 v[174:175], v243 offset:24832\n"
"v_mfma_f32_16x16x32_bf16 v[196:199], v[128:131], v[112:115], v[216:219]\n"
"v_mfma_f32_16x16x32_bf16 v[196:199], v[132:135], v[116:119], v[196:199]\n"
"v_mfma_f32_16x16x32_bf16 v[196:199], v[136:139], v[120:123], v[196:199]\n"
"v_mfma_f32_16x16x32_bf16 v[196:199], v[140:143], v[124:127], v[196:199]\n"
"v_mfma_f32_16x16x32_bf16 v[200:203], v[144:147], v[96:99], v[212:215]\n"
"v_mfma_f32_16x16x32_bf16 v[200:203], v[148:151], v[100:103], v[200:203]\n"
"v_mfma_f32_16x16x32_bf16 v[200:203], v[152:155], v[104:107], v[200:203]\n"
"v_mfma_f32_16x16x32_bf16 v[200:203], v[156:159], v[108:111], v[200:203]\n"
"v_mfma_f32_16x16x32_bf16 v[204:207], v[144:147], v[112:115], v[216:219]\n"
"v_mfma_f32_16x16x32_bf16 v[204:207], v[148:151], v[116:119], v[204:207]\n"
"v_mfma_f32_16x16x32_bf16 v[204:207], v[152:155], v[120:123], v[204:207]\n"
"v_mfma_f32_16x16x32_bf16 v[204:207], v[156:159], v[124:127], v[204:207]\n"
"s_waitcnt lgkmcnt(0)\n"
"s_barrier\n"
"s_setprio 0\n"
"s_lshl_b32 s88, %[osoff], 2\n"
"s_add_u32 s88, s88, 73728\n"
"s_mov_b32 m0, s53\n"
"s_nop 0\n"
"buffer_load_dwordx4 v249, s[76:79], s88 offen lds\n"
"buffer_load_dwordx4 v250, s[76:79], s88 offen offset:1024 lds\n"
"s_add_u32 m0, s90, 2048\n"
"s_nop 0\n"
"buffer_load_dwordx4 v252, s[76:79], s88 offen lds\n"
"s_lshl_b32 s89, %[osoff], 2\n"
"s_add_u32 s89, s89, 108544\n"
"s_mov_b32 m0, s53\n"
"s_nop 0\n"
"buffer_load_dwordx4 v249, s[76:79], s89 offen offset:2048 lds\n"
"buffer_load_dwordx4 v250, s[76:79], s89 offen offset:3072 lds\n"
"s_mov_b32 m0, s57\n"
"s_nop 0\n"
"buffer_load_dwordx4 v251, s[76:79], s89 offen offset:3072 lds\n"
"v_exp_f32 v192, v192\n"
"v_exp_f32 v193, v193\n"
"v_exp_f32 v194, v194\n"
"v_exp_f32 v195, v195\n"
"v_cvt_pk_bf16_f32 v224, v192, v193\n"
"v_cvt_pk_bf16_f32 v225, v194, v195\n"
"v_exp_f32 v196, v196\n"
"v_exp_f32 v197, v197\n"
"v_exp_f32 v198, v198\n"
"v_exp_f32 v199, v199\n"
"v_cvt_pk_bf16_f32 v228, v196, v197\n"
"v_cvt_pk_bf16_f32 v229, v198, v199\n"
"v_exp_f32 v200, v200\n"
"v_exp_f32 v201, v201\n"
"v_exp_f32 v202, v202\n"
"v_exp_f32 v203, v203\n"
"v_cvt_pk_bf16_f32 v226, v200, v201\n"
"v_cvt_pk_bf16_f32 v227, v202, v203\n"
"v_exp_f32 v204, v204\n"
"v_exp_f32 v205, v205\n"
"v_exp_f32 v206, v206\n"
"v_exp_f32 v207, v207\n"
"v_cvt_pk_bf16_f32 v230, v204, v205\n"
"v_cvt_pk_bf16_f32 v231, v206, v207\n"
"s_waitcnt vmcnt(16)\n"
"s_barrier\n"
"s_setprio 1\n"
"ds_read_b128 v[232:235], v247 offset:4096\n"
"v_mfma_f32_16x16x32_bf16 v[32:35], v[176:179], v[224:227], v[32:35]\n"
"ds_read_b128 v[128:131], %[kb] offset:34816\n"
"v_add_f32 v240, v240, v192\n"
"v_add_f32_e64 v241, v241, v196\n"
"v_mfma_f32_16x16x32_bf16 v[64:67], v[176:179], v[228:231], v[64:67]\n"
"ds_read_b128 v[132:135], %[kb] offset:35072\n"
"v_add_f32 v240, v240, v193\n"
"v_add_f32 v241, v241, v197\n"
"v_mfma_f32_16x16x32_bf16 v[36:39], v[180:183], v[224:227], v[36:39]\n"
"ds_read_b128 v[136:139], %[kb] offset:35328\n"
"v_add_f32 v240, v240, v194\n"
"v_add_f32 v241, v241, v198\n"
"v_mfma_f32_16x16x32_bf16 v[68:71], v[180:183], v[228:231], v[68:71]\n"
"ds_read_b128 v[140:143], %[kb] offset:35584\n"
"v_add_f32 v240, v240, v195\n"
"v_add_f32 v241, v241, v199\n"
"v_mfma_f32_16x16x32_bf16 v[40:43], v[184:187], v[224:227], v[40:43]\n"
"ds_read_b128 v[144:147], %[kb] offset:43008\n"
"v_add_f32 v240, v240, v200\n"
"v_add_f32 v241, v241, v204\n"
"v_mfma_f32_16x16x32_bf16 v[72:75], v[184:187], v[228:231], v[72:75]\n"
"ds_read_b128 v[148:151], %[kb] offset:43264\n"
"v_add_f32 v240, v240, v201\n"
"v_add_f32 v241, v241, v205\n"
"v_mfma_f32_16x16x32_bf16 v[44:47], v[188:191], v[224:227], v[44:47]\n"
"ds_read_b128 v[152:155], %[kb] offset:43520\n"
"v_add_f32 v240, v240, v202\n"
"v_add_f32 v241, v241, v206\n"
"v_mfma_f32_16x16x32_bf16 v[76:79], v[188:191], v[228:231], v[76:79]\n"
"ds_read_b128 v[156:159], %[kb] offset:43776\n"
"v_add_f32 v240, v240, v203\n"
"v_add_f32 v241, v241, v207\n"
"v_mfma_f32_16x16x32_bf16 v[16:19], v[160:163], v[224:227], v[16:19]\n"
"ds_read_b64_tr_b16 v[176:177], %[vbe] offset:33280\n"
"v_mfma_f32_16x16x32_bf16 v[48:51], v[160:163], v[228:231], v[48:51]\n"
"ds_read_b64_tr_b16 v[178:179], %[vbe] offset:41472\n"
"s_waitcnt lgkmcnt(10)\n"
"v_mfma_f32_16x16x32_bf16 v[80:83], v[160:163], v[232:235], v[80:83]\n"
"ds_read_b64_tr_b16 v[180:181], v243 offset:33280\n"
"v_mfma_f32_16x16x32_bf16 v[20:23], v[164:167], v[224:227], v[20:23]\n"
"ds_read_b64_tr_b16 v[182:183], v243 offset:41472\n"
"v_mfma_f32_16x16x32_bf16 v[52:55], v[164:167], v[228:231], v[52:55]\n"
"ds_read_b64_tr_b16 v[184:185], %[vbe] offset:33536\n"
"v_mfma_f32_16x16x32_bf16 v[84:87], v[164:167], v[232:235], v[84:87]\n"
"ds_read_b64_tr_b16 v[186:187], %[vbe] offset:41728\n"
"v_mfma_f32_16x16x32_bf16 v[24:27], v[168:171], v[224:227], v[24:27]\n"
"ds_read_b64_tr_b16 v[188:189], v243 offset:33536\n"
"v_mfma_f32_16x16x32_bf16 v[56:59], v[168:171], v[228:231], v[56:59]\n"
"ds_read_b64_tr_b16 v[190:191], v243 offset:41728\n"
"v_mfma_f32_16x16x32_bf16 v[88:91], v[168:171], v[232:235], v[88:91]\n"
"ds_read_b64_tr_b16 v[160:161], %[vbe] offset:32768\n"
"v_mfma_f32_16x16x32_bf16 v[28:31], v[172:175], v[224:227], v[28:31]\n"
"ds_read_b64_tr_b16 v[162:163], %[vbe] offset:40960\n"
"v_mfma_f32_16x16x32_bf16 v[60:63], v[172:175], v[228:231], v[60:63]\n"
"ds_read_b64_tr_b16 v[164:165], v243 offset:32768\n"
"v_mfma_f32_16x16x32_bf16 v[92:95], v[172:175], v[232:235], v[92:95]\n"
"ds_read_b64_tr_b16 v[166:167], v243 offset:40960\n"
"s_waitcnt lgkmcnt(12)\n"
"v_mfma_f32_16x16x32_bf16 v[192:195], v[128:131], v[96:99], v[212:215]\n"
"ds_read_b64_tr_b16 v[168:169], %[vbe] offset:33024\n"
"v_mfma_f32_16x16x32_bf16 v[192:195], v[132:135], v[100:103], v[192:195]\n"
"ds_read_b64_tr_b16 v[170:171], %[vbe] offset:41216\n"
"v_mfma_f32_16x16x32_bf16 v[192:195], v[136:139], v[104:107], v[192:195]\n"
"ds_read_b64_tr_b16 v[172:173], v243 offset:33024\n"
"v_mfma_f32_16x16x32_bf16 v[192:195], v[140:143], v[108:111], v[192:195]\n"
"ds_read_b64_tr_b16 v[174:175], v243 offset:41216\n"
"v_mfma_f32_16x16x32_bf16 v[196:199], v[128:131], v[112:115], v[216:219]\n"
"v_mfma_f32_16x16x32_bf16 v[196:199], v[132:135], v[116:119], v[196:199]\n"
"v_mfma_f32_16x16x32_bf16 v[196:199], v[136:139], v[120:123], v[196:199]\n"
"v_mfma_f32_16x16x32_bf16 v[196:199], v[140:143], v[124:127], v[196:199]\n"
"v_mfma_f32_16x16x32_bf16 v[200:203], v[144:147], v[96:99], v[212:215]\n"
"v_mfma_f32_16x16x32_bf16 v[200:203], v[148:151], v[100:103], v[200:203]\n"
"v_mfma_f32_16x16x32_bf16 v[200:203], v[152:155], v[104:107], v[200:203]\n"
"v_mfma_f32_16x16x32_bf16 v[200:203], v[156:159], v[108:111], v[200:203]\n"
"v_mfma_f32_16x16x32_bf16 v[204:207], v[144:147], v[112:115], v[216:219]\n"
"v_mfma_f32_16x16x32_bf16 v[204:207], v[148:151], v[116:119], v[204:207]\n"
"v_mfma_f32_16x16x32_bf16 v[204:207], v[152:155], v[120:123], v[204:207]\n"
"v_mfma_f32_16x16x32_bf16 v[204:207], v[156:159], v[124:127], v[204:207]\n"
"s_waitcnt lgkmcnt(0)\n"
"s_barrier\n"
"s_setprio 0\n"
"s_lshl_b32 s88, %[os1], 2\n"
"s_mov_b32 m0, s54\n"
"s_nop 0\n"
"buffer_load_dwordx4 v249, s[76:79], s88 offen lds\n"
"buffer_load_dwordx4 v250, s[76:79], s88 offen offset:1024 lds\n"
"s_lshl_b32 s89, %[os1], 2\n"
"s_add_u32 s89, s89, 34816\n"
"buffer_load_dwordx4 v249, s[76:79], s89 offen offset:2048 lds\n"
"buffer_load_dwordx4 v250, s[76:79], s89 offen offset:3072 lds\n"
"s_mov_b32 m0, s58\n"
"s_nop 0\n"
"buffer_load_dwordx4 v251, s[76:79], s89 offen offset:3072 lds\n"
"v_exp_f32 v192, v192\n"
"v_exp_f32 v193, v193\n"
"v_exp_f32 v194, v194\n"
"v_exp_f32_e64 v195, v195\n"
"v_cvt_pk_bf16_f32 v224, v192, v193\n"
"v_cvt_pk_bf16_f32 v225, v194, v195\n"
"v_exp_f32 v196, v196\n"
"v_exp_f32 v197, v197\n"
"v_exp_f32 v198, v198\n"
"v_exp_f32 v199, v199\n"
"v_cvt_pk_bf16_f32 v228, v196, v197\n"
"v_cvt_pk_bf16_f32 v229, v198, v199\n"
"v_exp_f32 v200, v200\n"
"v_exp_f32 v201, v201\n"
"v_exp_f32 v202, v202\n"
"v_exp_f32 v203, v203\n"
"v_cvt_pk_bf16_f32 v226, v200, v201\n"
"v_cvt_pk_bf16_f32 v227, v202, v203\n"
"v_exp_f32 v204, v204\n"
"v_exp_f32 v205, v205\n"
"v_exp_f32 v206, v206\n"
"v_exp_f32 v207, v207\n"
"v_cvt_pk_bf16_f32 v230, v204, v205\n"
"v_cvt_pk_bf16_f32 v231, v206, v207\n"
"s_waitcnt vmcnt(16)\n"
"s_barrier\n"
"s_setprio 1\n"
"ds_read_b128 v[232:235], v247 offset:4096\n"
"v_mfma_f32_16x16x32_bf16 v[32:35], v[176:179], v[224:227], v[32:35]\n"
"ds_read_b128 v[128:131], %[kb] offset:51200\n"
"v_add_f32 v240, v240, v192\n"
"v_add_f32_e64 v241, v241, v196\n"
"v_mfma_f32_16x16x32_bf16 v[64:67], v[176:179], v[228:231], v[64:67]\n"
"ds_read_b128 v[132:135], %[kb] offset:51456\n"
"v_add_f32 v240, v240, v193\n"
"v_add_f32 v241, v241, v197\n"
"v_mfma_f32_16x16x32_bf16 v[36:39], v[180:183], v[224:227], v[36:39]\n"
"ds_read_b128 v[136:139], %[kb] offset:51712\n"
"v_add_f32 v240, v240, v194\n"
"v_add_f32 v241, v241, v198\n"
"v_mfma_f32_16x16x32_bf16 v[68:71], v[180:183], v[228:231], v[68:71]\n"
"ds_read_b128 v[140:143], %[kb] offset:51968\n"
"v_add_f32 v240, v240, v195\n"
"v_add_f32 v241, v241, v199\n"
"v_mfma_f32_16x16x32_bf16 v[40:43], v[184:187], v[224:227], v[40:43]\n"
"ds_read_b128 v[144:147], %[kb] offset:59392\n"
"v_add_f32 v240, v240, v200\n"
"v_add_f32 v241, v241, v204\n"
"v_mfma_f32_16x16x32_bf16 v[72:75], v[184:187], v[228:231], v[72:75]\n"
"ds_read_b128 v[148:151], %[kb] offset:59648\n"
"v_add_f32 v240, v240, v201\n"
"v_add_f32 v241, v241, v205\n"
"v_mfma_f32_16x16x32_bf16 v[44:47], v[188:191], v[224:227], v[44:47]\n"
"ds_read_b128 v[152:155], %[kb] offset:59904\n"
"v_add_f32 v240, v240, v202\n"
"v_add_f32 v241, v241, v206\n"
"v_mfma_f32_16x16x32_bf16 v[76:79], v[188:191], v[228:231], v[76:79]\n"
"ds_read_b128 v[156:159], %[kb] offset:60160\n"
"v_add_f32 v240, v240, v203\n"
"v_add_f32 v241, v241, v207\n"
"v_mfma_f32_16x16x32_bf16 v[16:19], v[160:163], v[224:227], v[16:19]\n"
"ds_read_b64_tr_b16 v[176:177], %[vbe] offset:49664\n"
"v_mfma_f32_16x16x32_bf16 v[48:51], v[160:163], v[228:231], v[48:51]\n"
"ds_read_b64_tr_b16 v[178:179], %[vbe] offset:57856\n"
"s_waitcnt lgkmcnt(10)\n"
"v_mfma_f32_16x16x32_bf16 v[80:83], v[160:163], v[232:235], v[80:83]\n"
"ds_read_b64_tr_b16 v[180:181], v243 offset:49664\n"
"v_mfma_f32_16x16x32_bf16 v[20:23], v[164:167], v[224:227], v[20:23]\n"
"ds_read_b64_tr_b16 v[182:183], v243 offset:57856\n"
"v_mfma_f32_16x16x32_bf16 v[52:55], v[164:167], v[228:231], v[52:55]\n"
"ds_read_b64_tr_b16 v[184:185], %[vbe] offset:49920\n"
"v_mfma_f32_16x16x32_bf16 v[84:87], v[164:167], v[232:235], v[84:87]\n"
"ds_read_b64_tr_b16 v[186:187], %[vbe] offset:58112\n"
"v_mfma_f32_16x16x32_bf16 v[24:27], v[168:171], v[224:227], v[24:27]\n"
"ds_read_b64_tr_b16 v[188:189], v243 offset:49920\n"
"v_mfma_f32_16x16x32_bf16 v[56:59], v[168:171], v[228:231], v[56:59]\n"
"ds_read_b64_tr_b16 v[190:191], v243 offset:58112\n"
"v_mfma_f32_16x16x32_bf16 v[88:91], v[168:171], v[232:235], v[88:91]\n"
"ds_read_b64_tr_b16 v[160:161], %[vbe] offset:49152\n"
"v_mfma_f32_16x16x32_bf16 v[28:31], v[172:175], v[224:227], v[28:31]\n"
"ds_read_b64_tr_b16 v[162:163], %[vbe] offset:57344\n"
"v_mfma_f32_16x16x32_bf16 v[60:63], v[172:175], v[228:231], v[60:63]\n"
"ds_read_b64_tr_b16 v[164:165], v243 offset:49152\n"
"v_mfma_f32_16x16x32_bf16 v[92:95], v[172:175], v[232:235], v[92:95]\n"
"ds_read_b64_tr_b16 v[166:167], v243 offset:57344\n"
"s_waitcnt lgkmcnt(12)\n"
"v_mfma_f32_16x16x32_bf16 v[192:195], v[128:131], v[96:99], v[212:215]\n"
"ds_read_b64_tr_b16 v[168:169], %[vbe] offset:49408\n"
"v_mfma_f32_16x16x32_bf16 v[192:195], v[132:135], v[100:103], v[192:195]\n"
"ds_read_b64_tr_b16 v[170:171], %[vbe] offset:57600\n"
"v_mfma_f32_16x16x32_bf16 v[192:195], v[136:139], v[104:107], v[192:195]\n"
"ds_read_b64_tr_b16 v[172:173], v243 offset:49408\n"
"v_mfma_f32_16x16x32_bf16 v[192:195], v[140:143], v[108:111], v[192:195]\n"
"ds_read_b64_tr_b16 v[174:175], v243 offset:57600\n"
"v_mfma_f32_16x16x32_bf16 v[196:199], v[128:131], v[112:115], v[216:219]\n"
"v_mfma_f32_16x16x32_bf16 v[196:199], v[132:135], v[116:119], v[196:199]\n"
"v_mfma_f32_16x16x32_bf16 v[196:199], v[136:139], v[120:123], v[196:199]\n"
"v_mfma_f32_16x16x32_bf16 v[196:199], v[140:143], v[124:127], v[196:199]\n"
"v_mfma_f32_16x16x32_bf16 v[200:203], v[144:147], v[96:99], v[212:215]\n"
"v_mfma_f32_16x16x32_bf16 v[200:203], v[148:151], v[100:103], v[200:203]\n"
"v_mfma_f32_16x16x32_bf16 v[200:203], v[152:155], v[104:107], v[200:203]\n"
"v_mfma_f32_16x16x32_bf16 v[200:203], v[156:159], v[108:111], v[200:203]\n"
"v_mfma_f32_16x16x32_bf16 v[204:207], v[144:147], v[112:115], v[216:219]\n"
"v_mfma_f32_16x16x32_bf16 v[204:207], v[148:151], v[116:119], v[204:207]\n"
"v_mfma_f32_16x16x32_bf16 v[204:207], v[152:155], v[120:123], v[204:207]\n"
"v_mfma_f32_16x16x32_bf16 v[204:207], v[156:159], v[124:127], v[204:207]\n"
"s_waitcnt lgkmcnt(0)\n"
"s_barrier\n"
"s_setprio 0\n"
"s_branch LT2_%=\n"
".p2align 3\n"
"LF2_%=:\n"
"s_add_u32 s52, %[hb], 8192\n"
"s_add_u32 s56, %[hb], 0\n"
"s_add_u32 s53, %[hb], 24576\n"
"s_add_u32 s57, %[hb], 16384\n"
"s_add_u32 s54, %[hb], 40960\n"
"s_add_u32 s58, %[hb], 32768\n"
"s_add_u32 s55, %[hb], 57344\n"
"s_add_u32 s59, %[hb], 49152\n"
"s_lshr_b32 s90, %[hb], 3\n"
"s_add_u32 s90, s90, 131072\n"
"s_movk_i32 s68, 1152\n"
"s_lshr_b32 s69, %[nb256], 3\n"
"s_add_u32 s69, s69, 5632\n"
"v_and_b32 v254, 255, %[g0]\n"
"v_add_u32 v254, %[kq64], v254\n"
"v_and_b32 v254, 255, v254\n"
"v_lshrrev_b32 v255, 8, %[g0]\n"
"v_mul_u32_u24 v255, s68, v255\n"
"v_add_u32 v255, %[nb256], v255\n"
"v_add_u32 v249, v254, v255\n"
"v_xor_b32 v254, 32, v254\n"
"v_add_u32 v250, v254, v255\n"
"v_add_u32 v250, 3584, v250\n"
"v_subrev_u32 v251, 18432, v250\n"
"v_lshrrev_b32 v254, 5, %[l16]\n"
"v_mul_u32_u24 v254, s68, v254\n"
"v_and_b32 v255, 16, %[l16]\n"
"v_add_u32 v254, v254, v255\n"
"v_add_u32 v252, s69, v254\n"
"s_lshr_b32 s68, %[hb], 11\n"
"s_or_b32 s68, s68, %[hb]\n"
"v_and_b32 v244, 0xffff, %[vbe]\n"
"v_xor_b32 v244, s68, v244\n"
"v_xor_b32 v245, 32, v244\n"
"v_lshrrev_b32 v246, 1, %[l16]\n"
"v_add_u32 v246, s90, v246\n"
"v_add_u32 v228, 4096, %[l16]\n"
"s_add_u32 s68, s90, 16384\n"
"v_add_u32 v247, s68, %[l16]\n"
"s_lshl_b32 s88, %[osoff], 2\n"
"s_mov_b32 m0, s52\n"
"s_nop 0\n"
"buffer_load_dwordx4 v249, s[76:79], s88 offen lds\n"
"buffer_load_dwordx4 v250, s[76:79], s88 offen offset:1024 lds\n"
"s_lshl_b32 s89, %[osoff], 2\n"
"s_add_u32 s89, s89, 34816\n"
"buffer_load_dwordx4 v249, s[76:79], s89 offen offset:2048 lds\n"
"buffer_load_dwordx4 v250, s[76:79], s89 offen offset:3072 lds\n"
"s_mov_b32 m0, s56\n"
"s_nop 0\n"
"buffer_load_dwordx4 v251, s[76:79], s89 offen offset:3072 lds\n"
"s_lshl_b32 s88, %[osoff], 2\n"
"s_add_u32 s88, s88, 73728\n"
"s_mov_b32 m0, s53\n"
"s_nop 0\n"
"buffer_load_dwordx4 v249, s[76:79], s88 offen lds\n"
"buffer_load_dwordx4 v250, s[76:79], s88 offen offset:1024 lds\n"
"s_add_u32 m0, s90, 2048\n"
"s_nop 0\n"
"buffer_load_dwordx4 v252, s[76:79], s88 offen lds\n"
"s_lshl_b32 s89, %[osoff], 2\n"
"s_add_u32 s89, s89, 108544\n"
"s_mov_b32 m0, s53\n"
"s_nop 0\n"
"buffer_load_dwordx4 v249, s[76:79], s89 offen offset:2048 lds\n"
"buffer_load_dwordx4 v250, s[76:79], s89 offen offset:3072 lds\n"
"s_mov_b32 m0, s57\n"
"s_nop 0\n"
"buffer_load_dwordx4 v251, s[76:79], s89 offen offset:3072 lds\n"
"s_lshl_b32 s88, %[os1], 2\n"
"s_mov_b32 m0, s54\n"
"s_nop 0\n"
"buffer_load_dwordx4 v249, s[76:79], s88 offen lds\n"
"buffer_load_dwordx4 v250, s[76:79], s88 offen offset:1024 lds\n"
"s_lshl_b32 s89, %[os1], 2\n"
"s_add_u32 s89, s89, 34816\n"
"buffer_load_dwordx4 v249, s[76:79], s89 offen offset:2048 lds\n"
"buffer_load_dwordx4 v250, s[76:79], s89 offen offset:3072 lds\n"
"s_mov_b32 m0, s58\n"
"s_nop 0\n"
"buffer_load_dwordx4 v251, s[76:79], s89 offen offset:3072 lds\n"
"s_lshl_b32 s88, %[os1], 2\n"
"s_add_u32 s88, s88, 73728\n"
"s_mov_b32 m0, s55\n"
"s_nop 0\n"
"buffer_load_dwordx4 v249, s[76:79], s88 offen lds\n"
"buffer_load_dwordx4 v250, s[76:79], s88 offen offset:1024 lds\n"
"s_add_u32 m0, s90, 6144\n"
"s_nop 0\n"
"buffer_load_dwordx4 v252, s[76:79], s88 offen lds\n"
"s_lshl_b32 s89, %[os1], 2\n"
"s_add_u32 s89, s89, 108544\n"
"s_mov_b32 m0, s55\n"
"s_nop 0\n"
"buffer_load_dwordx4 v249, s[76:79], s89 offen offset:2048 lds\n"
"buffer_load_dwordx4 v250, s[76:79], s89 offen offset:3072 lds\n"
"s_mov_b32 m0, s59\n"
"s_nop 0\n"
"buffer_load_dwordx4 v251, s[76:79], s89 offen offset:3072 lds\n"
"s_add_u32 s60, %[osoff], 4096\n"
"buffer_load_dwordx4 v[144:147], %[l16], %[srdo], s60 offen offset:1024 sc1\n"
"buffer_load_dwordx4 v[128:131], %[l16], %[srdo], s60 offen sc1\n"
"buffer_load_dwordx4 v[160:163], v228, %[srdo], s60 offen sc1\n"
"s_add_u32 s61, %[osoff], 13312\n"
"buffer_load_dwordx4 v[148:151], %[l16], %[srdo], s61 offen offset:1024 sc1\n"
"buffer_load_dwordx4 v[132:135], %[l16], %[srdo], s61 offen sc1\n"
"buffer_load_dwordx4 v[164:167], v228, %[srdo], s61 offen sc1\n"
"s_add_u32 s62, %[osoff], 22528\n"
"buffer_load_dwordx4 v[152:155], %[l16], %[srdo], s62 offen offset:1024 sc1\n"
"buffer_load_dwordx4 v[136:139], %[l16], %[srdo], s62 offen sc1\n"
"buffer_load_dwordx4 v[168:171], v228, %[srdo], s62 offen sc1\n"
"s_add_u32 s63, %[osoff], 31744\n"
"buffer_load_dwordx4 v[156:159], %[l16], %[srdo], s63 offen offset:1024 sc1\n"
"buffer_load_dwordx4 v[140:143], %[l16], %[srdo], s63 offen sc1\n"
"buffer_load_dwordx4 v[172:175], v228, %[srdo], s63 offen sc1\n"
"v_mov_b32 v176, 0\n"
"v_mov_b32 v177, 0\n"
"v_mov_b32 v178, 0\n"
"v_mov_b32 v179, 0\n"
"v_mov_b32 v180, 0\n"
"v_mov_b32 v181, 0\n"
"v_mov_b32 v182, 0\n"
"v_mov_b32 v183, 0\n"
"s_waitcnt vmcnt(0)\n"
"s_barrier\n"
"ds_read_b64_tr_b16 v[100:101], v244 offset:0\n"
"ds_read_b64_tr_b16 v[102:103], v244 offset:8192\n"
"ds_read_b64_tr_b16 v[104:105], v245 offset:0\n"
"ds_read_b64_tr_b16 v[106:107], v245 offset:8192\n"
"ds_read_b64_tr_b16 v[108:109], v244 offset:256\n"
"ds_read_b64_tr_b16 v[110:111], v244 offset:8448\n"
"ds_read_b64_tr_b16 v[112:113], v245 offset:256\n"
"ds_read_b64_tr_b16 v[114:115], v245 offset:8448\n"
"ds_read_b64_tr_b16 v[116:117], v244 offset:512\n"
"ds_read_b64_tr_b16 v[118:119], v244 offset:8704\n"
"ds_read_b64_tr_b16 v[120:121], v245 offset:512\n"
"ds_read_b64_tr_b16 v[122:123], v245 offset:8704\n"
"ds_read_b64_tr_b16 v[124:125], v244 offset:768\n"
"ds_read_b64_tr_b16 v[126:127], v244 offset:8960\n"
"s_waitcnt lgkmcnt(0)\n"
"s_nop 7\n"
"s_branch LO2_%=\n"
".p2align 6\n"
"LA2_%=:\n"
"s_cmp_eq_u32 s61, 0x30000\n"
"s_cbranch_scc0 LNd2_%=\n"
"s_getpc_b64 s[94:95]\n"
"LMd2_%=:\n"
"s_add_u32 s94, s94, (LZZ2_%=-16384-LMd2_%=)&0xffffffff\n"
"s_addc_u32 s95, s95, (LZZ2_%=-16384-LMd2_%=)>>32\n"
"v_lshlrev_b32 v236, 4, %[l16]\n"
"s_lshr_b32 s68, %[hb], 9\n"
"v_add_u32 v236, s68, v236\n"
"s_lshl_b32 exec_lo, 0x11111111, %[wrot]\n"
"s_mov_b32 exec_hi, exec_lo\n"
"global_load_dword v253, v236, s[94:95]\n"
"s_mov_b64 exec, -1\n"
"LNd2_%=:\n"
"s_nop 0\n"
"KA2 0,s52,16384,s56\n"
"KA2 16384,s53,32768,s57\n"
"KA2 32768,s54,49152,s58\n"
"KA2 49152,s55,0,s59\n"
"s_cbranch_scc0 LA2_%=\n"
"s_nop 0\n"
"s_barrier\n"
"s_setprio 1\n"
"ds_read_b128 v[232:235], v247 offset:4096\n"
"v_mfma_f32_16x16x32_bf16 v[32:35], v[176:179], v[224:227], v[32:35]\n"
"ds_read_b128 v[128:131], v244 offset:2048\n"
"v_add_f32 v240, v240, v192\n"
"v_add_f32 v241, v241, v196\n"
"v_mfma_f32_16x16x32_bf16 v[64:67], v[176:179], v[228:231], v[64:67]\n"
"ds_read_b128 v[132:135], v244 offset:2304\n"
"v_add_f32 v240, v240, v193\n"
"v_add_f32 v241, v241, v197\n"
"v_mfma_f32_16x16x32_bf16 v[36:39], v[180:183], v[224:227], v[36:39]\n"
"ds_read_b128 v[136:139], v244 offset:2560\n"
"v_add_f32 v240, v240, v194\n"
"v_add_f32 v241, v241, v198\n"
"v_mfma_f32_16x16x32_bf16 v[68:71], v[180:183], v[228:231], v[68:71]\n"
"ds_read_b128 v[140:143], v244 offset:2816\n"
"v_add_f32 v240, v240, v195\n"
"v_add_f32 v241, v241, v199\n"
"v_mfma_f32_16x16x32_bf16 v[40:43], v[184:187], v[224:227], v[40:43]\n"
"ds_read_b128 v[144:147], v244 offset:10240\n"
"v_add_f32 v240, v240, v200\n"
"v_add_f32 v241, v241, v204\n"
"v_mfma_f32_16x16x32_bf16 v[72:75], v[184:187], v[228:231], v[72:75]\n"
"ds_read_b128 v[148:151], v244 offset:10496\n"
"v_add_f32 v240, v240, v201\n"
"v_add_f32 v241, v241, v205\n"
"v_mfma_f32_16x16x32_bf16 v[44:47], v[188:191], v[224:227], v[44:47]\n"
"ds_read_b128 v[152:155], v244 offset:10752\n"
"v_add_f32 v240, v240, v202\n"
"v_add_f32 v241, v241, v206\n"
"v_mfma_f32_16x16x32_bf16 v[76:79], v[188:191], v[228:231], v[76:79]\n"
"ds_read_b128 v[156:159], v244 offset:11008\n"
"v_add_f32 v240, v240, v203\n"
"v_add_f32 v241, v241, v207\n"
"v_mfma_f32_16x16x32_bf16 v[16:19], v[160:163], v[224:227], v[16:19]\n"
"ds_read_b64_tr_b16 v[176:177], v245 offset:512\n"
"v_mfma_f32_16x16x32_bf16 v[48:51], v[160:163], v[228:231], v[48:51]\n"
"ds_read_b64_tr_b16 v[178:179], v245 offset:8704\n"
"s_waitcnt lgkmcnt(10)\n"
"v_mfma_f32_16x16x32_bf16 v[80:83], v[160:163], v[232:235], v[80:83]\n"
"ds_read_b64_tr_b16 v[180:181], v246 offset:512\n"
"v_mfma_f32_16x16x32_bf16 v[20:23], v[164:167], v[224:227], v[20:23]\n"
"ds_read_b64_tr_b16 v[182:183], v246 offset:8704\n"
"v_mfma_f32_16x16x32_bf16 v[52:55], v[164:167], v[228:231], v[52:55]\n"
"ds_read_b64_tr_b16 v[184:185], v245 offset:768\n"
"v_mfma_f32_16x16x32_bf16 v[84:87], v[164:167], v[232:235], v[84:87]\n"
"ds_read_b64_tr_b16 v[186:187], v245 offset:8960\n"
"v_mfma_f32_16x16x32_bf16 v[24:27], v[168:171], v[224:227], v[24:27]\n"
"ds_read_b64_tr_b16 v[188:189], v246 offset:768\n"
"v_mfma_f32_16x16x32_bf16 v[56:59], v[168:171], v[228:231], v[56:59]\n"
"ds_read_b64_tr_b16 v[190:191], v246 offset:8960\n"
"v_mfma_f32_16x16x32_bf16 v[88:91], v[168:171], v[232:235], v[88:91]\n"
"ds_read_b64_tr_b16 v[160:161], v245 offset:0\n"
"v_mfma_f32_16x16x32_bf16 v[28:31], v[172:175], v[224:227], v[28:31]\n"
"ds_read_b64_tr_b16 v[162:163], v245 offset:8192\n"
"v_mfma_f32_16x16x32_bf16 v[60:63], v[172:175], v[228:231], v[60:63]\n"
"ds_read_b64_tr_b16 v[164:165], v246 offset:0\n"
"v_mfma_f32_16x16x32_bf16 v[92:95], v[172:175], v[232:235], v[92:95]\n"
"ds_read_b64_tr_b16 v[166:167], v246 offset:8192\n"
"s_waitcnt lgkmcnt(12)\n"
"v_mfma_f32_16x16x32_bf16 v[192:195], v[128:131], v[96:99], v[212:215]\n"
"ds_read_b64_tr_b16 v[168:169], v245 offset:256\n"
"v_mfma_f32_16x16x32_bf16 v[192:195], v[132:135], v[100:103], v[192:195]\n"
"ds_read_b64_tr_b16 v[170:171], v245 offset:8448\n"
"v_mfma_f32_16x16x32_bf16 v[192:195], v[136:139], v[104:107], v[192:195]\n"
"ds_read_b64_tr_b16 v[172:173], v246 offset:256\n"
"v_mfma_f32_16x16x32_bf16 v[192:195], v[140:143], v[108:111], v[192:195]\n"
"ds_read_b64_tr_b16 v[174:175], v246 offset:8448\n"
"v_mfma_f32_16x16x32_bf16 v[196:199], v[128:131], v[112:115], v[216:219]\n"
"v_mfma_f32_16x16x32_bf16 v[196:199], v[132:135], v[116:119], v[196:199]\n"
"v_mfma_f32_16x16x32_bf16 v[196:199], v[136:139], v[120:123], v[196:199]\n"
"v_mfma_f32_16x16x32_bf16 v[196:199], v[140:143], v[124:127], v[196:199]\n"
"v_mfma_f32_16x16x32_bf16 v[200:203], v[144:147], v[96:99], v[212:215]\n"
"v_mfma_f32_16x16x32_bf16 v[200:203], v[148:151], v[100:103], v[200:203]\n"
"v_mfma_f32_16x16x32_bf16 v[200:203], v[152:155], v[104:107], v[200:203]\n"
"v_mfma_f32_16x16x32_bf16 v[200:203], v[156:159], v[108:111], v[200:203]\n"
"v_mfma_f32_16x16x32_bf16 v[204:207], v[144:147], v[112:115], v[216:219]\n"
"v_mfma_f32_16x16x32_bf16 v[204:207], v[148:151], v[116:119], v[204:207]\n"
"v_mfma_f32_16x16x32_bf16 v[204:207], v[152:155], v[120:123], v[204:207]\n"
"v_mfma_f32_16x16x32_bf16 v[204:207], v[156:159], v[124:127], v[204:207]\n"
"s_waitcnt lgkmcnt(0)\n"
"s_barrier\n"
"s_setprio 0\n"
"s_lshl_b32 s88, %[osoff], 2\n"
"s_mov_b32 m0, s52\n"
"s_nop 0\n"
"buffer_load_dwordx4 v249, s[76:79], s88 offen lds\n"
"buffer_load_dwordx4 v250, s[76:79], s88 offen offset:1024 lds\n"
"v_exp_f32 v192, v192\n"
"v_exp_f32 v193, v193\n"
"v_exp_f32 v194, v194\n"
"v_exp_f32 v195, v195\n"
"v_cvt_pk_bf16_f32 v224, v192, v193\n"
"v_cvt_pk_bf16_f32 v225, v194, v195\n"
"v_exp_f32 v196, v196\n"
"v_exp_f32 v197, v197\n"
"v_exp_f32 v198, v198\n"
"v_exp_f32 v199, v199\n"
"v_cvt_pk_bf16_f32 v228, v196, v197\n"
"v_cvt_pk_bf16_f32 v229, v198, v199\n"
"v_exp_f32 v200, v200\n"
"v_exp_f32 v201, v201\n"
"v_exp_f32 v202, v202\n"
"v_exp_f32 v203, v203\n"
"v_cvt_pk_bf16_f32 v226, v200, v201\n"
"v_cvt_pk_bf16_f32 v227, v202, v203\n"
"v_exp_f32 v204, v204\n"
"v_exp_f32 v205, v205\n"
"v_exp_f32 v206, v206\n"
"v_exp_f32 v207, v207\n"
"v_cvt_pk_bf16_f32 v230, v204, v205\n"
"v_cvt_pk_bf16_f32 v231, v206, v207\n"
"s_waitcnt vmcnt(12)\n"
"s_barrier\n"
"s_setprio 1\n"
"ds_read_b128 v[232:235], v247 offset:4096\n"
"v_mfma_f32_16x16x32_bf16 v[32:35], v[176:179], v[224:227], v[32:35]\n"
"ds_read_b128 v[128:131], %[kb] offset:18432\n"
"v_add_f32 v240, v240, v192\n"
"v_add_f32_e64 v241, v241, v196\n"
"v_mfma_f32_16x16x32_bf16 v[64:67], v[176:179], v[228:231], v[64:67]\n"
"ds_read_b128 v[132:135], %[kb] offset:18688\n"
"v_add_f32 v240, v240, v193\n"
"v_add_f32 v241, v241, v197\n"
"v_mfma_f32_16x16x32_bf16 v[36:39], v[180:183], v[224:227], v[36:39]\n"
"ds_read_b128 v[136:139], %[kb] offset:18944\n"
"v_add_f32 v240, v240, v194\n"
"v_add_f32 v241, v241, v198\n"
"v_mfma_f32_16x16x32_bf16 v[68:71], v[180:183], v[228:231], v[68:71]\n"
"ds_read_b128 v[140:143], %[kb] offset:19200\n"
"v_add_f32 v240, v240, v195\n"
"v_add_f32 v241, v241, v199\n"
"v_mfma_f32_16x16x32_bf16 v[40:43], v[184:187], v[224:227], v[40:43]\n"
"ds_read_b128 v[144:147], %[kb] offset:26624\n"
"v_add_f32 v240, v240, v200\n"
"v_add_f32 v241, v241, v204\n"
"v_mfma_f32_16x16x32_bf16 v[72:75], v[184:187], v[228:231], v[72:75]\n"
"ds_read_b128 v[148:151], %[kb] offset:26880\n"
"v_add_f32 v240, v240, v201\n"
"v_add_f32 v241, v241, v205\n"
"v_mfma_f32_16x16x32_bf16 v[44:47], v[188:191], v[224:227], v[44:47]\n"
"ds_read_b128 v[152:155], %[kb] offset:27136\n"
"v_add_f32 v240, v240, v202\n"
"v_add_f32 v241, v241, v206\n"
"v_mfma_f32_16x16x32_bf16 v[76:79], v[188:191], v[228:231], v[76:79]\n"
"ds_read_b128 v[156:159], %[kb] offset:27392\n"
"v_add_f32 v240, v240, v203\n"
"v_add_f32 v241, v241, v207\n"
"v_mfma_f32_16x16x32_bf16 v[16:19], v[160:163], v[224:227], v[16:19]\n"
"ds_read_b64_tr_b16 v[176:177], %[vbe] offset:16896\n"
"v_mfma_f32_16x16x32_bf16 v[48:51], v[160:163], v[228:231], v[48:51]\n"
"ds_read_b64_tr_b16 v[178:179], %[vbe] offset:25088\n"
"s_waitcnt lgkmcnt(10)\n"
"v_mfma_f32_16x16x32_bf16 v[80:83], v[160:163], v[232:235], v[80:83]\n"
"ds_read_b64_tr_b16 v[180:181], v243 offset:16896\n"
"v_mfma_f32_16x16x32_bf16 v[20:23], v[164:167], v[224:227], v[20:23]\n"
"ds_read_b64_tr_b16 v[182:183], v243 offset:25088\n"
"v_mfma_f32_16x16x32_bf16 v[52:55], v[164:167], v[228:231], v[52:55]\n"
"ds_read_b64_tr_b16 v[184:185], %[vbe] offset:17152\n"
"v_mfma_f32_16x16x32_bf16 v[84:87], v[164:167], v[232:235], v[84:87]\n"
"ds_read_b64_tr_b16 v[186:187], %[vbe] offset:25344\n"
"v_mfma_f32_16x16x32_bf16 v[24:27], v[168:171], v[224:227], v[24:27]\n"
"ds_read_b64_tr_b16 v[188:189], v243 offset:17152\n"
"v_mfma_f32_16x16x32_bf16 v[56:59], v[168:171], v[228:231], v[56:59]\n"
"ds_read_b64_tr_b16 v[190:191], v243 offset:25344\n"
"v_mfma_f32_16x16x32_bf16 v[88:91], v[168:171], v[232:235], v[88:91]\n"
"ds_read_b64_tr_b16 v[160:161], %[vbe] offset:16384\n"
"v_mfma_f32_16x16x32_bf16 v[28:31], v[172:175], v[224:227], v[28:31]\n"
"ds_read_b64_tr_b16 v[162:163], %[vbe] offset:24576\n"
"v_mfma_f32_16x16x32_bf16 v[60:63], v[172:175], v[228:231], v[60:63]\n"
"ds_read_b64_tr_b16 v[164:165], v243 offset:16384\n"
"v_mfma_f32_16x16x32_bf16 v[92:95], v[172:175], v[232:235], v[92:95]\n"
"ds_read_b64_tr_b16 v[166:167], v243 offset:24576\n"
"s_waitcnt lgkmcnt(12)\n"
"v_mfma_f32_16x16x32_bf16 v[192:195], v[128:131], v[96:99], v[212:215]\n"
"ds_read_b64_tr_b16 v[168:169], %[vbe] offset:16640\n"
"v_mfma_f32_16x16x32_bf16 v[192:195], v[132:135], v[100:103], v[192:195]\n"
"ds_read_b64_tr_b16 v[170:171], %[vbe] offset:24832\n"
"v_mfma_f32_16x16x32_bf16 v[192:195], v[136:139], v[104:107], v[192:195]\n"
"ds_read_b64_tr_b16 v[172:173], v243 offset:16640\n"
"v_mfma_f32_16x16x32_bf16 v[192:195], v[140:143], v[108:111], v[192:195]\n"
"ds_read_b64_tr_b16 v[174:175], v243 offset:24832\n"
"v_mfma_f32_16x16x32_bf16 v[196:199], v[128:131], v[112:115], v[216:219]\n"
"v_mfma_f32_16x16x32_bf16 v[196:199], v[132:135], v[116:119], v[196:199]\n"
"v_mfma_f32_16x16x32_bf16 v[196:199], v[136:139], v[120:123], v[196:199]\n"
"v_mfma_f32_16x16x32_bf16 v[196:199], v[140:143], v[124:127], v[196:199]\n"
"v_mfma_f32_16x16x32_bf16 v[200:203], v[144:147], v[96:99], v[212:215]\n"
"v_mfma_f32_16x16x32_bf16 v[200:203], v[148:151], v[100:103], v[200:203]\n"
"v_mfma_f32_16x16x32_bf16 v[200:203], v[152:155], v[104:107], v[200:203]\n"
"v_mfma_f32_16x16x32_bf16 v[200:203], v[156:159], v[108:111], v[200:203]\n"
"v_mfma_f32_16x16x32_bf16 v[204:207], v[144:147], v[112:115], v[216:219]\n"
"v_mfma_f32_16x16x32_bf16 v[204:207], v[148:151], v[116:119], v[204:207]\n"
"v_mfma_f32_16x16x32_bf16 v[204:207], v[152:155], v[120:123], v[204:207]\n"
"v_mfma_f32_16x16x32_bf16 v[204:207], v[156:159], v[124:127], v[204:207]\n"
"s_waitcnt lgkmcnt(0)\n"
"s_barrier\n"
"s_setprio 0\n"
"s_lshl_b32 s89, %[osoff], 2\n"
"s_add_u32 s89, s89, 34816\n"
"buffer_load_dwordx4 v249, s[76:79], s89 offen offset:2048 lds\n"
"buffer_load_dwordx4 v250, s[76:79], s89 offen offset:3072 lds\n"
"s_mov_b32 m0, s56\n"
"s_nop 0\n"
"buffer_load_dwordx4 v251, s[76:79], s89 offen offset:3072 lds\n"
"v_exp_f32 v192, v192\n"
"v_exp_f32 v193, v193\n"
"v_exp_f32 v194, v194\n"
"v_exp_f32 v195, v195\n"
"v_cvt_pk_bf16_f32 v224, v192, v193\n"
"v_cvt_pk_bf16_f32 v225, v194, v195\n"
"v_exp_f32 v196, v196\n"
"v_exp_f32 v197, v197\n"
"v_exp_f32 v198, v198\n"
"v_exp_f32 v199, v199\n"
"v_cvt_pk_bf16_f32 v228, v196, v197\n"
"v_cvt_pk_bf16_f32 v229, v198, v199\n"
"v_exp_f32 v200, v200\n"
"v_exp_f32 v201, v201\n"
"v_exp_f32 v202, v202\n"
"v_exp_f32 v203, v203\n"
"v_cvt_pk_bf16_f32 v226, v200, v201\n"
"v_cvt_pk_bf16_f32 v227, v202, v203\n"
"v_exp_f32 v204, v204\n"
"v_exp_f32 v205, v205\n"
"v_exp_f32 v206, v206\n"
"v_exp_f32 v207, v207\n"
"v_cvt_pk_bf16_f32 v230, v204, v205\n"
"v_cvt_pk_bf16_f32 v231, v206, v207\n"
"s_barrier\n"
"s_setprio 1\n"
"ds_read_b128 v[232:235], v247 offset:4096\n"
"v_mfma_f32_16x16x32_bf16 v[32:35], v[176:179], v[224:227], v[32:35]\n"
"ds_read_b128 v[128:131], v244 offset:18432\n"
"v_add_f32 v240, v240, v192\n"
"v_add_f32 v241, v241, v196\n"
"v_mfma_f32_16x16x32_bf16 v[64:67], v[176:179], v[228:231], v[64:67]\n"
"ds_read_b128 v[132:135], v244 offset:18688\n"
"v_add_f32 v240, v240, v193\n"
"v_add_f32 v241, v241, v197\n"
"v_mfma_f32_16x16x32_bf16 v[36:39], v[180:183], v[224:227], v[36:39]\n"
"ds_read_b128 v[136:139], v244 offset:18944\n"
"v_add_f32 v240, v240, v194\n"
"v_add_f32 v241, v241, v198\n"
"v_mfma_f32_16x16x32_bf16 v[68:71], v[180:183], v[228:231], v[68:71]\n"
"ds_read_b128 v[140:143], v244 offset:19200\n"
"v_add_f32 v240, v240, v195\n"
"v_add_f32 v241, v241, v199\n"
"v_mfma_f32_16x16x32_bf16 v[40:43], v[184:187], v[224:227], v[40:43]\n"
"ds_read_b128 v[144:147], v244 offset:26624\n"
"v_add_f32 v240, v240, v200\n"
"v_add_f32 v241, v241, v204\n"
"v_mfma_f32_16x16x32_bf16 v[72:75], v[184:187], v[228:231], v[72:75]\n"
"ds_read_b128 v[148:151], v244 offset:26880\n"
"v_add_f32 v240, v240, v201\n"
"v_add_f32 v241, v241, v205\n"
"v_mfma_f32_16x16x32_bf16 v[44:47], v[188:191], v[224:227], v[44:47]\n"
"ds_read_b128 v[152:155], v244 offset:27136\n"
"v_add_f32 v240, v240, v202\n"
"v_add_f32 v241, v241, v206\n"
"v_mfma_f32_16x16x32_bf16 v[76:79], v[188:191], v[228:231], v[76:79]\n"
"ds_read_b128 v[156:159], v244 offset:27392\n"
"v_add_f32 v240, v240, v203\n"
"v_add_f32 v241, v241, v207\n"
"v_mfma_f32_16x16x32_bf16 v[16:19], v[160:163], v[224:227], v[16:19]\n"
"ds_read_b64_tr_b16 v[176:177], v245 offset:16896\n"
"v_mfma_f32_16x16x32_bf16 v[48:51], v[160:163], v[228:231], v[48:51]\n"
"ds_read_b64_tr_b16 v[178:179], v245 offset:25088\n"
"s_waitcnt lgkmcnt(10)\n"
"v_mfma_f32_16x16x32_bf16 v[80:83], v[160:163], v[232:235], v[80:83]\n"
"ds_read_b64_tr_b16 v[180:181], v246 offset:16896\n"
"v_mfma_f32_16x16x32_bf16 v[20:23], v[164:167], v[224:227], v[20:23]\n"
"ds_read_b64_tr_b16 v[182:183], v246 offset:25088\n"
"v_mfma_f32_16x16x32_bf16 v[52:55], v[164:167], v[228:231], v[52:55]\n"
"ds_read_b64_tr_b16 v[184:185], v245 offset:17152\n"
"v_mfma_f32_16x16x32_bf16 v[84:87], v[164:167], v[232:235], v[84:87]\n"
"ds_read_b64_tr_b16 v[186:187], v245 offset:25344\n"
"v_mfma_f32_16x16x32_bf16 v[24:27], v[168:171], v[224:227], v[24:27]\n"
"ds_read_b64_tr_b16 v[188:189], v246 offset:17152\n"
"v_mfma_f32_16x16x32_bf16 v[56:59], v[168:171], v[228:231], v[56:59]\n"
"ds_read_b64_tr_b16 v[190:191], v246 offset:25344\n"
"v_mfma_f32_16x16x32_bf16 v[88:91], v[168:171], v[232:235], v[88:91]\n"
"ds_read_b64_tr_b16 v[160:161], v245 offset:16384\n"
"v_mfma_f32_16x16x32_bf16 v[28:31], v[172:175], v[224:227], v[28:31]\n"
"ds_read_b64_tr_b16 v[162:163], v245 offset:24576\n"
"v_mfma_f32_16x16x32_bf16 v[60:63], v[172:175], v[228:231], v[60:63]\n"
"ds_read_b64_tr_b16 v[164:165], v246 offset:16384\n"
"v_mfma_f32_16x16x32_bf16 v[92:95], v[172:175], v[232:235], v[92:95]\n"
"ds_read_b64_tr_b16 v[166:167], v246 offset:24576\n"
"s_waitcnt lgkmcnt(12)\n"
"v_mfma_f32_16x16x32_bf16 v[192:195], v[128:131], v[96:99], v[212:215]\n"
"ds_read_b64_tr_b16 v[168:169], v245 offset:16640\n"
"v_mfma_f32_16x16x32_bf16 v[192:195], v[132:135], v[100:103], v[192:195]\n"
"ds_read_b64_tr_b16 v[170:171], v245 offset:24832\n"
"v_mfma_f32_16x16x32_bf16 v[192:195], v[136:139], v[104:107], v[192:195]\n"
"ds_read_b64_tr_b16 v[172:173], v246 offset:16640\n"
"v_mfma_f32_16x16x32_bf16 v[192:195], v[140:143], v[108:111], v[192:195]\n"
"ds_read_b64_tr_b16 v[174:175], v246 offset:24832\n"
"v_mfma_f32_16x16x32_bf16 v[196:199], v[128:131], v[112:115], v[216:219]\n"
"v_mfma_f32_16x16x32_bf16 v[196:199], v[132:135], v[116:119], v[196:199]\n"
"v_mfma_f32_16x16x32_bf16 v[196:199], v[136:139], v[120:123], v[196:199]\n"
"v_mfma_f32_16x16x32_bf16 v[196:199], v[140:143], v[124:127], v[196:199]\n"
"v_mfma_f32_16x16x32_bf16 v[200:203], v[144:147], v[96:99], v[212:215]\n"
"v_mfma_f32_16x16x32_bf16 v[200:203], v[148:151], v[100:103], v[200:203]\n"
"v_mfma_f32_16x16x32_bf16 v[200:203], v[152:155], v[104:107], v[200:203]\n"
"v_mfma_f32_16x16x32_bf16 v[200:203], v[156:159], v[108:111], v[200:203]\n"
"v_mfma_f32_16x16x32_bf16 v[204:207], v[144:147], v[112:115], v[216:219]\n"
"v_mfma_f32_16x16x32_bf16 v[204:207], v[148:151], v[116:119], v[204:207]\n"
"v_mfma_f32_16x16x32_bf16 v[204:207], v[152:155], v[120:123], v[204:207]\n"
"v_mfma_f32_16x16x32_bf16 v[204:207], v[156:159], v[124:127], v[204:207]\n"
"s_waitcnt lgkmcnt(0)\n"
"s_barrier\n"
"s_setprio 0\n"
"s_lshl_b32 s88, %[osoff], 2\n"
"s_add_u32 s88, s88, 73728\n"
"s_mov_b32 m0, s53\n"
"s_nop 0\n"
"buffer_load_dwordx4 v249, s[76:79], s88 offen lds\n"
"buffer_load_dwordx4 v250, s[76:79], s88 offen offset:1024 lds\n"
"s_add_u32 m0, s90, 2048\n"
"s_nop 0\n"
"buffer_load_dwordx4 v252, s[76:79], s88 offen lds\n"
"v_exp_f32 v192, v192\n"
"v_exp_f32 v193, v193\n"
"v_exp_f32 v194, v194\n"
"v_exp_f32_e64 v195, v195\n"
"v_cvt_pk_bf16_f32 v224, v192, v193\n"
"v_cvt_pk_bf16_f32 v225, v194, v195\n"
"v_exp_f32 v196, v196\n"
"v_exp_f32 v197, v197\n"
"v_exp_f32 v198, v198\n"
"v_exp_f32 v199, v199\n"
"v_cvt_pk_bf16_f32 v228, v196, v197\n"
"v_cvt_pk_bf16_f32 v229, v198, v199\n"
"v_exp_f32 v200, v200\n"
"v_exp_f32 v201, v201\n"
"v_exp_f32 v202, v202\n"
"v_exp_f32 v203, v203\n"
"v_cvt_pk_bf16_f32 v226, v200, v201\n"
"v_cvt_pk_bf16_f32 v227, v202, v203\n"
"v_exp_f32 v204, v204\n"
"v_exp_f32 v205, v205\n"
"v_exp_f32 v206, v206\n"
"v_exp_f32 v207, v207\n"
"v_cvt_pk_bf16_f32 v230, v204, v205\n"
"v_cvt_pk_bf16_f32 v231, v206, v207\n"
"s_waitcnt vmcnt(13)\n"
"s_barrier\n"
"s_setprio 1\n"
"ds_read_b128 v[232:235], v247 offset:4096\n"
"v_mfma_f32_16x16x32_bf16 v[32:35], v[176:179], v[224:227], v[32:35]\n"
"ds_read_b128 v[128:131], %[kb] offset:34816\n"
"v_add_f32 v240, v240, v192\n"
"v_add_f32_e64 v241, v241, v196\n"
"v_mfma_f32_16x16x32_bf16 v[64:67], v[176:179], v[228:231], v[64:67]\n"
"ds_read_b128 v[132:135], %[kb] offset:35072\n"
"v_add_f32 v240, v240, v193\n"
"v_add_f32 v241, v241, v197\n"
"v_mfma_f32_16x16x32_bf16 v[36:39], v[180:183], v[224:227], v[36:39]\n"
"ds_read_b128 v[136:139], %[kb] offset:35328\n"
"v_add_f32 v240, v240, v194\n"
"v_add_f32 v241, v241, v198\n"
"v_mfma_f32_16x16x32_bf16 v[68:71], v[180:183], v[228:231], v[68:71]\n"
"ds_read_b128 v[140:143], %[kb] offset:35584\n"
"v_add_f32 v240, v240, v195\n"
"v_add_f32 v241, v241, v199\n"
"v_mfma_f32_16x16x32_bf16 v[40:43], v[184:187], v[224:227], v[40:43]\n"
"ds_read_b128 v[144:147], %[kb] offset:43008\n"
"v_add_f32 v240, v240, v200\n"
"v_add_f32 v241, v241, v204\n"
"v_mfma_f32_16x16x32_bf16 v[72:75], v[184:187], v[228:231], v[72:75]\n"
"ds_read_b128 v[148:151], %[kb] offset:43264\n"
"v_add_f32 v240, v240, v201\n"
"v_add_f32 v241, v241, v205\n"
"v_mfma_f32_16x16x32_bf16 v[44:47], v[188:191], v[224:227], v[44:47]\n"
"ds_read_b128 v[152:155], %[kb] offset:43520\n"
"v_add_f32 v240, v240, v202\n"
"v_add_f32 v241, v241, v206\n"
"v_mfma_f32_16x16x32_bf16 v[76:79], v[188:191], v[228:231], v[76:79]\n"
"ds_read_b128 v[156:159], %[kb] offset:43776\n"
"v_add_f32 v240, v240, v203\n"
"v_add_f32 v241, v241, v207\n"
"v_mfma_f32_16x16x32_bf16 v[16:19], v[160:163], v[224:227], v[16:19]\n"
"ds_read_b64_tr_b16 v[176:177], %[vbe] offset:33280\n"
"v_mfma_f32_16x16x32_bf16 v[48:51], v[160:163], v[228:231], v[48:51]\n"
"ds_read_b64_tr_b16 v[178:179], %[vbe] offset:41472\n"
"s_waitcnt lgkmcnt(10)\n"
"v_mfma_f32_16x16x32_bf16 v[80:83], v[160:163], v[232:235], v[80:83]\n"
"ds_read_b64_tr_b16 v[180:181], v243 offset:33280\n"
"v_mfma_f32_16x16x32_bf16 v[20:23], v[164:167], v[224:227], v[20:23]\n"
"ds_read_b64_tr_b16 v[182:183], v243 offset:41472\n"
"v_mfma_f32_16x16x32_bf16 v[52:55], v[164:167], v[228:231], v[52:55]\n"
"ds_read_b64_tr_b16 v[184:185], %[vbe] offset:33536\n"
"v_mfma_f32_16x16x32_bf16 v[84:87], v[164:167], v[232:235], v[84:87]\n"
"ds_read_b64_tr_b16 v[186:187], %[vbe] offset:41728\n"
"v_mfma_f32_16x16x32_bf16 v[24:27], v[168:171], v[224:227], v[24:27]\n"
"ds_read_b64_tr_b16 v[188:189], v243 offset:33536\n"
"v_mfma_f32_16x16x32_bf16 v[56:59], v[168:171], v[228:231], v[56:59]\n"
"ds_read_b64_tr_b16 v[190:191], v243 offset:41728\n"
"v_mfma_f32_16x16x32_bf16 v[88:91], v[168:171], v[232:235], v[88:91]\n"
"ds_read_b64_tr_b16 v[160:161], %[vbe] offset:32768\n"
"v_mfma_f32_16x16x32_bf16 v[28:31], v[172:175], v[224:227], v[28:31]\n"
"ds_read_b64_tr_b16 v[162:163], %[vbe] offset:40960\n"
"v_mfma_f32_16x16x32_bf16 v[60:63], v[172:175], v[228:231], v[60:63]\n"
"ds_read_b64_tr_b16 v[164:165], v243 offset:32768\n"
"v_mfma_f32_16x16x32_bf16 v[92:95], v[172:175], v[232:235], v[92:95]\n"
"ds_read_b64_tr_b16 v[166:167], v243 offset:40960\n"
"s_waitcnt lgkmcnt(12)\n"
"v_mfma_f32_16x16x32_bf16 v[192:195], v[128:131], v[96:99], v[212:215]\n"
"ds_read_b64_tr_b16 v[168:169], %[vbe] offset:33024\n"
"v_mfma_f32_16x16x32_bf16 v[192:195], v[132:135], v[100:103], v[192:195]\n"
"ds_read_b64_tr_b16 v[170:171], %[vbe] offset:41216\n"
"v_mfma_f32_16x16x32_bf16 v[192:195], v[136:139], v[104:107], v[192:195]\n"
"ds_read_b64_tr_b16 v[172:173], v243 offset:33024\n"
"v_mfma_f32_16x16x32_bf16 v[192:195], v[140:143], v[108:111], v[192:195]\n"
"ds_read_b64_tr_b16 v[174:175], v243 offset:41216\n"
"v_mfma_f32_16x16x32_bf16 v[196:199], v[128:131], v[112:115], v[216:219]\n"
"v_mfma_f32_16x16x32_bf16 v[196:199], v[132:135], v[116:119], v[196:199]\n"
"v_mfma_f32_16x16x32_bf16 v[196:199], v[136:139], v[120:123], v[196:199]\n"
"v_mfma_f32_16x16x32_bf16 v[196:199], v[140:143], v[124:127], v[196:199]\n"
"v_mfma_f32_16x16x32_bf16 v[200:203], v[144:147], v[96:99], v[212:215]\n"
"v_mfma_f32_16x16x32_bf16 v[200:203], v[148:151], v[100:103], v[200:203]\n"
"v_mfma_f32_16x16x32_bf16 v[200:203], v[152:155], v[104:107], v[200:203]\n"
"v_mfma_f32_16x16x32_bf16 v[200:203], v[156:159], v[108:111], v[200:203]\n"
"v_mfma_f32_16x16x32_bf16 v[204:207], v[144:147], v[112:115], v[216:219]\n"
"v_mfma_f32_16x16x32_bf16 v[204:207], v[148:151], v[116:119], v[204:207]\n"
"v_mfma_f32_16x16x32_bf16 v[204:207], v[152:155], v[120:123], v[204:207]\n"
"v_mfma_f32_16x16x32_bf16 v[204:207], v[156:159], v[124:127], v[204:207]\n"
"s_waitcnt lgkmcnt(0)\n"
"s_barrier\n"
"s_setprio 0\n"
"s_lshl_b32 s89, %[osoff], 2\n"
"s_add_u32 s89, s89, 108544\n"
"s_mov_b32 m0, s53\n"
"s_nop 0\n"
"buffer_load_dwordx4 v249, s[76:79], s89 offen offset:2048 lds\n"
"buffer_load_dwordx4 v250, s[76:79], s89 offen offset:3072 lds\n"
"s_mov_b32 m0, s57\n"
"s_nop 0\n"
"buffer_load_dwordx4 v251, s[76:79], s89 offen offset:3072 lds\n"
"v_exp_f32 v192, v192\n"
"v_exp_f32 v193, v193\n"
"v_exp_f32 v194, v194\n"
"v_exp_f32 v195, v195\n"
"v_cvt_pk_bf16_f32 v224, v192, v193\n"
"v_cvt_pk_bf16_f32 v225, v194, v195\n"
"v_exp_f32 v196, v196\n"
"v_exp_f32 v197, v197\n"
"v_exp_f32 v198, v198\n"
"v_exp_f32 v199, v199\n"
"v_cvt_pk_bf16_f32 v228, v196, v197\n"
"v_cvt_pk_bf16_f32 v229, v198, v199\n"
"v_exp_f32 v200, v200\n"
"v_exp_f32 v201, v201\n"
"v_exp_f32 v202, v202\n"
"v_exp_f32 v203, v203\n"
"v_cvt_pk_bf16_f32 v226, v200, v201\n"
"v_cvt_pk_bf16_f32 v227, v202, v203\n"
"v_exp_f32 v204, v204\n"
"v_exp_f32 v205, v205\n"
"v_exp_f32 v206, v206\n"
"v_exp_f32 v207, v207\n"
"v_cvt_pk_bf16_f32 v230, v204, v205\n"
"v_cvt_pk_bf16_f32 v231, v206, v207\n"
"s_barrier\n"
"s_setprio 1\n"
"ds_read_b128 v[232:235], v247 offset:4096\n"
"v_mfma_f32_16x16x32_bf16 v[32:35], v[176:179], v[224:227], v[32:35]\n"
"ds_read_b128 v[128:131], v244 offset:34816\n"
"v_add_f32 v240, v240, v192\n"
"v_add_f32 v241, v241, v196\n"
"v_mfma_f32_16x16x32_bf16 v[64:67], v[176:179], v[228:231], v[64:67]\n"
"ds_read_b128 v[132:135], v244 offset:35072\n"
"v_add_f32 v240, v240, v193\n"
"v_add_f32 v241, v241, v197\n"
"v_mfma_f32_16x16x32_bf16 v[36:39], v[180:183], v[224:227], v[36:39]\n"
"ds_read_b128 v[136:139], v244 offset:35328\n"
"v_add_f32 v240, v240, v194\n"
"v_add_f32 v241, v241, v198\n"
"v_mfma_f32_16x16x32_bf16 v[68:71], v[180:183], v[228:231], v[68:71]\n"
"ds_read_b128 v[140:143], v244 offset:35584\n"
"v_add_f32 v240, v240, v195\n"
"v_add_f32 v241, v241, v199\n"
"v_mfma_f32_16x16x32_bf16 v[40:43], v[184:187], v[224:227], v[40:43]\n"
"ds_read_b128 v[144:147], v244 offset:43008\n"
"v_add_f32 v240, v240, v200\n"
"v_add_f32 v241, v241, v204\n"
"v_mfma_f32_16x16x32_bf16 v[72:75], v[184:187], v[228:231], v[72:75]\n"
"ds_read_b128 v[148:151], v244 offset:43264\n"
"v_add_f32 v240, v240, v201\n"
"v_add_f32 v241, v241, v205\n"
"v_mfma_f32_16x16x32_bf16 v[44:47], v[188:191], v[224:227], v[44:47]\n"
"ds_read_b128 v[152:155], v244 offset:43520\n"
"v_add_f32 v240, v240, v202\n"
"v_add_f32 v241, v241, v206\n"
"v_mfma_f32_16x16x32_bf16 v[76:79], v[188:191], v[228:231], v[76:79]\n"
"ds_read_b128 v[156:159], v244 offset:43776\n"
"v_add_f32 v240, v240, v203\n"
"v_add_f32 v241, v241, v207\n"
"v_mfma_f32_16x16x32_bf16 v[16:19], v[160:163], v[224:227], v[16:19]\n"
"ds_read_b64_tr_b16 v[176:177], v245 offset:33280\n"
"v_mfma_f32_16x16x32_bf16 v[48:51], v[160:163], v[228:231], v[48:51]\n"
"ds_read_b64_tr_b16 v[178:179], v245 offset:41472\n"
"s_waitcnt lgkmcnt(10)\n"
"v_mfma_f32_16x16x32_bf16 v[80:83], v[160:163], v[232:235], v[80:83]\n"
"ds_read_b64_tr_b16 v[180:181], v246 offset:33280\n"
"v_mfma_f32_16x16x32_bf16 v[20:23], v[164:167], v[224:227], v[20:23]\n"
"ds_read_b64_tr_b16 v[182:183], v246 offset:41472\n"
"v_mfma_f32_16x16x32_bf16 v[52:55], v[164:167], v[228:231], v[52:55]\n"
"ds_read_b64_tr_b16 v[184:185], v245 offset:33536\n"
"v_mfma_f32_16x16x32_bf16 v[84:87], v[164:167], v[232:235], v[84:87]\n"
"ds_read_b64_tr_b16 v[186:187], v245 offset:41728\n"
"v_mfma_f32_16x16x32_bf16 v[24:27], v[168:171], v[224:227], v[24:27]\n"
"ds_read_b64_tr_b16 v[188:189], v246 offset:33536\n"
"v_mfma_f32_16x16x32_bf16 v[56:59], v[168:171], v[228:231], v[56:59]\n"
"ds_read_b64_tr_b16 v[190:191], v246 offset:41728\n"
"v_mfma_f32_16x16x32_bf16 v[88:91], v[168:171], v[232:235], v[88:91]\n"
"ds_read_b64_tr_b16 v[160:161], v245 offset:32768\n"
"v_mfma_f32_16x16x32_bf16 v[28:31], v[172:175], v[224:227], v[28:31]\n"
"ds_read_b64_tr_b16 v[162:163], v245 offset:40960\n"
"v_mfma_f32_16x16x32_bf16 v[60:63], v[172:175], v[228:231], v[60:63]\n"
"ds_read_b64_tr_b16 v[164:165], v246 offset:32768\n"
"v_mfma_f32_16x16x32_bf16 v[92:95], v[172:175], v[232:235], v[92:95]\n"
"ds_read_b64_tr_b16 v[166:167], v246 offset:40960\n"
"s_waitcnt lgkmcnt(12)\n"
"v_mfma_f32_16x16x32_bf16 v[192:195], v[128:131], v[96:99], v[212:215]\n"
"ds_read_b64_tr_b16 v[168:169], v245 offset:33024\n"
"v_mfma_f32_16x16x32_bf16 v[192:195], v[132:135], v[100:103], v[192:195]\n"
"ds_read_b64_tr_b16 v[170:171], v245 offset:41216\n"
"v_mfma_f32_16x16x32_bf16 v[192:195], v[136:139], v[104:107], v[192:195]\n"
"ds_read_b64_tr_b16 v[172:173], v246 offset:33024\n"
"v_mfma_f32_16x16x32_bf16 v[192:195], v[140:143], v[108:111], v[192:195]\n"
"ds_read_b64_tr_b16 v[174:175], v246 offset:41216\n"
"v_mfma_f32_16x16x32_bf16 v[196:199], v[128:131], v[112:115], v[216:219]\n"
"v_mfma_f32_16x16x32_bf16 v[196:199], v[132:135], v[116:119], v[196:199]\n"
"v_mfma_f32_16x16x32_bf16 v[196:199], v[136:139], v[120:123], v[196:199]\n"
"v_mfma_f32_16x16x32_bf16 v[196:199], v[140:143], v[124:127], v[196:199]\n"
"v_mfma_f32_16x16x32_bf16 v[200:203], v[144:147], v[96:99], v[212:215]\n"
"v_mfma_f32_16x16x32_bf16 v[200:203], v[148:151], v[100:103], v[200:203]\n"
"v_mfma_f32_16x16x32_bf16 v[200:203], v[152:155], v[104:107], v[200:203]\n"
"v_mfma_f32_16x16x32_bf16 v[200:203], v[156:159], v[108:111], v[200:203]\n"
"v_mfma_f32_16x16x32_bf16 v[204:207], v[144:147], v[112:115], v[216:219]\n"
"v_mfma_f32_16x16x32_bf16 v[204:207], v[148:151], v[116:119], v[204:207]\n"
"v_mfma_f32_16x16x32_bf16 v[204:207], v[152:155], v[120:123], v[204:207]\n"
"v_mfma_f32_16x16x32_bf16 v[204:207], v[156:159], v[124:127], v[204:207]\n"
"s_waitcnt lgkmcnt(0)\n"
"s_barrier\n"
"s_setprio 0\n"
"s_getpc_b64 s[94:95]\n"
"LMa2_%=:\n"
"s_add_u32 s94, s94, (LZZ2_%=-16384-LMa2_%=)&0xffffffff\n"
"s_addc_u32 s95, s95, (LZZ2_%=-16384-LMa2_%=)>>32\n"
"v_lshlrev_b32 v236, 4, %[l16]\n"
"s_lshr_b32 s68, %[hb], 9\n"
"v_add_u32 v236, s68, v236\n"
"s_lshl_b32 exec_lo, 0x11111111, %[wrot]\n"
"s_mov_b32 exec_hi, exec_lo\n"
"global_load_dword v253, v236, s[94:95]\n"
"s_mov_b64 exec, -1\n"
"s_lshl_b32 s88, %[os1], 2\n"
"s_mov_b32 m0, s54\n"
"s_nop 0\n"
"buffer_load_dwordx4 v249, s[76:79], s88 offen lds\n"
"buffer_load_dwordx4 v250, s[76:79], s88 offen offset:1024 lds\n"
"v_exp_f32 v192, v192\n"
"v_exp_f32 v193, v193\n"
"v_exp_f32 v194, v194\n"
"v_exp_f32 v195, v195\n"
"v_cvt_pk_bf16_f32 v224, v192, v193\n"
"v_cvt_pk_bf16_f32 v225, v194, v195\n"
"v_exp_f32 v196, v196\n"
"v_exp_f32 v197, v197\n"
"v_exp_f32 v198, v198\n"
"v_exp_f32 v199, v199\n"
"v_cvt_pk_bf16_f32 v228, v196, v197\n"
"v_cvt_pk_bf16_f32 v229, v198, v199\n"
"v_exp_f32 v200, v200\n"
"v_exp_f32 v201, v201\n"
"v_exp_f32 v202, v202\n"
"v_exp_f32 v203, v203\n"
"v_cvt_pk_bf16_f32 v226, v200, v201\n"
"v_cvt_pk_bf16_f32 v227, v202, v203\n"
"v_exp_f32 v204, v204\n"
"v_exp_f32 v205, v205\n"
"v_exp_f32 v206, v206\n"
"v_exp_f32 v207, v207\n"
"v_cvt_pk_bf16_f32 v230, v204, v205\n"
"v_cvt_pk_bf16_f32 v231, v206, v207\n"
"s_waitcnt vmcnt(13)\n"
"s_barrier\n"
"s_setprio 1\n"
"ds_read_b128 v[232:235], v247 offset:4096\n"
"v_mfma_f32_16x16x32_bf16 v[32:35], v[176:179], v[224:227], v[32:35]\n"
"ds_read_b128 v[128:131], %[kb] offset:51200\n"
"v_add_f32 v240, v240, v192\n"
"v_add_f32_e64 v241, v241, v196\n"
"v_mfma_f32_16x16x32_bf16 v[64:67], v[176:179], v[228:231], v[64:67]\n"
"ds_read_b128 v[132:135], %[kb] offset:51456\n"
"v_add_f32 v240, v240, v193\n"
"v_add_f32 v241, v241, v197\n"
"v_mfma_f32_16x16x32_bf16 v[36:39], v[180:183], v[224:227], v[36:39]\n"
"ds_read_b128 v[136:139], %[kb] offset:51712\n"
"v_add_f32 v240, v240, v194\n"
"v_add_f32 v241, v241, v198\n"
"v_mfma_f32_16x16x32_bf16 v[68:71], v[180:183], v[228:231], v[68:71]\n"
"ds_read_b128 v[140:143], %[kb] offset:51968\n"
"v_add_f32 v240, v240, v195\n"
"v_add_f32 v241, v241, v199\n"
"v_mfma_f32_16x16x32_bf16 v[40:43], v[184:187], v[224:227], v[40:43]\n"
"ds_read_b128 v[144:147], %[kb] offset:59392\n"
"v_add_f32 v240, v240, v200\n"
"v_add_f32 v241, v241, v204\n"
"v_mfma_f32_16x16x32_bf16 v[72:75], v[184:187], v[228:231], v[72:75]\n"
"ds_read_b128 v[148:151], %[kb] offset:59648\n"
"v_add_f32 v240, v240, v201\n"
"v_add_f32 v241, v241, v205\n"
"v_mfma_f32_16x16x32_bf16 v[44:47], v[188:191], v[224:227], v[44:47]\n"
"ds_read_b128 v[152:155], %[kb] offset:59904\n"
"v_add_f32 v240, v240, v202\n"
"v_add_f32 v241, v241, v206\n"
"v_mfma_f32_16x16x32_bf16 v[76:79], v[188:191], v[228:231], v[76:79]\n"
"ds_read_b128 v[156:159], %[kb] offset:60160\n"
"v_add_f32 v240, v240, v203\n"
"v_add_f32 v241, v241, v207\n"
"v_mfma_f32_16x16x32_bf16 v[16:19], v[160:163], v[224:227], v[16:19]\n"
"ds_read_b64_tr_b16 v[176:177], %[vbe] offset:49664\n"
"v_mfma_f32_16x16x32_bf16 v[48:51], v[160:163], v[228:231], v[48:51]\n"
"ds_read_b64_tr_b16 v[178:179], %[vbe] offset:57856\n"
"s_waitcnt lgkmcnt(10)\n"
"v_mfma_f32_16x16x32_bf16 v[80:83], v[160:163], v[232:235], v[80:83]\n"
"ds_read_b64_tr_b16 v[180:181], v243 offset:49664\n"
"v_mfma_f32_16x16x32_bf16 v[20:23], v[164:167], v[224:227], v[20:23]\n"
"ds_read_b64_tr_b16 v[182:183], v243 offset:57856\n"
"v_mfma_f32_16x16x32_bf16 v[52:55], v[164:167], v[228:231], v[52:55]\n"
"ds_read_b64_tr_b16 v[184:185], %[vbe] offset:49920\n"
"v_mfma_f32_16x16x32_bf16 v[84:87], v[164:167], v[232:235], v[84:87]\n"
"ds_read_b64_tr_b16 v[186:187], %[vbe] offset:58112\n"
"v_mfma_f32_16x16x32_bf16 v[24:27], v[168:171], v[224:227], v[24:27]\n"
"ds_read_b64_tr_b16 v[188:189], v243 offset:49920\n"
"v_mfma_f32_16x16x32_bf16 v[56:59], v[168:171], v[228:231], v[56:59]\n"
"ds_read_b64_tr_b16 v[190:191], v243 offset:58112\n"
"v_mfma_f32_16x16x32_bf16 v[88:91], v[168:171], v[232:235], v[88:91]\n"
"ds_read_b64_tr_b16 v[160:161], %[vbe] offset:49152\n"
"v_mfma_f32_16x16x32_bf16 v[28:31], v[172:175], v[224:227], v[28:31]\n"
"ds_read_b64_tr_b16 v[162:163], %[vbe] offset:57344\n"
"v_mfma_f32_16x16x32_bf16 v[60:63], v[172:175], v[228:231], v[60:63]\n"
"ds_read_b64_tr_b16 v[164:165], v243 offset:49152\n"
"v_mfma_f32_16x16x32_bf16 v[92:95], v[172:175], v[232:235], v[92:95]\n"
"ds_read_b64_tr_b16 v[166:167], v243 offset:57344\n"
"s_waitcnt lgkmcnt(12)\n"
"v_mfma_f32_16x16x32_bf16 v[192:195], v[128:131], v[96:99], v[212:215]\n"
"ds_read_b64_tr_b16 v[168:169], %[vbe] offset:49408\n"
"v_mfma_f32_16x16x32_bf16 v[192:195], v[132:135], v[100:103], v[192:195]\n"
"ds_read_b64_tr_b16 v[170:171], %[vbe] offset:57600\n"
"v_mfma_f32_16x16x32_bf16 v[192:195], v[136:139], v[104:107], v[192:195]\n"
"ds_read_b64_tr_b16 v[172:173], v243 offset:49408\n"
"v_mfma_f32_16x16x32_bf16 v[192:195], v[140:143], v[108:111], v[192:195]\n"
"ds_read_b64_tr_b16 v[174:175], v243 offset:57600\n"
"v_mfma_f32_16x16x32_bf16 v[196:199], v[128:131], v[112:115], v[216:219]\n"
"v_mfma_f32_16x16x32_bf16 v[196:199], v[132:135], v[116:119], v[196:199]\n"
"v_mfma_f32_16x16x32_bf16 v[196:199], v[136:139], v[120:123], v[196:199]\n"
"v_mfma_f32_16x16x32_bf16 v[196:199], v[140:143], v[124:127], v[196:199]\n"
"v_mfma_f32_16x16x32_bf16 v[200:203], v[144:147], v[96:99], v[212:215]\n"
"v_mfma_f32_16x16x32_bf16 v[200:203], v[148:151], v[100:103], v[200:203]\n"
"v_mfma_f32_16x16x32_bf16 v[200:203], v[152:155], v[104:107], v[200:203]\n"
"v_mfma_f32_16x16x32_bf16 v[200:203], v[156:159], v[108:111], v[200:203]\n"
"v_mfma_f32_16x16x32_bf16 v[204:207], v[144:147], v[112:115], v[216:219]\n"
"v_mfma_f32_16x16x32_bf16 v[204:207], v[148:151], v[116:119], v[204:207]\n"
"v_mfma_f32_16x16x32_bf16 v[204:207], v[152:155], v[120:123], v[204:207]\n"
"v_mfma_f32_16x16x32_bf16 v[204:207], v[156:159], v[124:127], v[204:207]\n"
"s_waitcnt lgkmcnt(0)\n"
"s_barrier\n"
"s_setprio 0\n"
"s_lshl_b32 s89, %[os1], 2\n"
"s_add_u32 s89, s89, 34816\n"
"buffer_load_dwordx4 v249, s[76:79], s89 offen offset:2048 lds\n"
"buffer_load_dwordx4 v250, s[76:79], s89 offen offset:3072 lds\n"
"s_mov_b32 m0, s58\n"
"s_nop 0\n"
"buffer_load_dwordx4 v251, s[76:79], s89 offen offset:3072 lds\n"
"v_exp_f32 v192, v192\n"
"v_exp_f32 v193, v193\n"
"v_exp_f32 v194, v194\n"
"v_exp_f32 v195, v195\n"
"v_cvt_pk_bf16_f32 v224, v192, v193\n"
"v_cvt_pk_bf16_f32 v225, v194, v195\n"
"v_exp_f32 v196, v196\n"
"v_exp_f32 v197, v197\n"
"v_exp_f32 v198, v198\n"
"v_exp_f32 v199, v199\n"
"v_cvt_pk_bf16_f32 v228, v196, v197\n"
"v_cvt_pk_bf16_f32 v229, v198, v199\n"
"v_exp_f32 v200, v200\n"
"v_exp_f32 v201, v201\n"
"v_exp_f32 v202, v202\n"
"v_exp_f32 v203, v203\n"
"v_cvt_pk_bf16_f32 v226, v200, v201\n"
"v_cvt_pk_bf16_f32 v227, v202, v203\n"
"v_exp_f32 v204, v204\n"
"v_exp_f32 v205, v205\n"
"v_exp_f32 v206, v206\n"
"v_exp_f32 v207, v207\n"
"v_cvt_pk_bf16_f32 v230, v204, v205\n"
"v_cvt_pk_bf16_f32 v231, v206, v207\n"
"s_barrier\n"
"s_setprio 1\n"
"ds_read_b128 v[232:235], v247 offset:4096\n"
"v_mfma_f32_16x16x32_bf16 v[32:35], v[176:179], v[224:227], v[32:35]\n"
"ds_read_b128 v[128:131], v244 offset:51200\n"
"v_add_f32 v240, v240, v192\n"
"v_add_f32 v241, v241, v196\n"
"v_mfma_f32_16x16x32_bf16 v[64:67], v[176:179], v[228:231], v[64:67]\n"
"ds_read_b128 v[132:135], v244 offset:51456\n"
"v_add_f32 v240, v240, v193\n"
"v_add_f32 v241, v241, v197\n"
"v_mfma_f32_16x16x32_bf16 v[36:39], v[180:183], v[224:227], v[36:39]\n"
"ds_read_b128 v[136:139], v244 offset:51712\n"
"v_add_f32 v240, v240, v194\n"
"v_add_f32 v241, v241, v198\n"
"v_mfma_f32_16x16x32_bf16 v[68:71], v[180:183], v[228:231], v[68:71]\n"
"ds_read_b128 v[140:143], v244 offset:51968\n"
"v_add_f32 v240, v240, v195\n"
"v_add_f32 v241, v241, v199\n"
"v_mfma_f32_16x16x32_bf16 v[40:43], v[184:187], v[224:227], v[40:43]\n"
"ds_read_b128 v[144:147], v244 offset:59392\n"
"v_add_f32 v240, v240, v200\n"
"v_add_f32 v241, v241, v204\n"
"v_mfma_f32_16x16x32_bf16 v[72:75], v[184:187], v[228:231], v[72:75]\n"
"ds_read_b128 v[148:151], v244 offset:59648\n"
"v_add_f32 v240, v240, v201\n"
"v_add_f32 v241, v241, v205\n"
"v_mfma_f32_16x16x32_bf16 v[44:47], v[188:191], v[224:227], v[44:47]\n"
"ds_read_b128 v[152:155], v244 offset:59904\n"
"v_add_f32 v240, v240, v202\n"
"v_add_f32 v241, v241, v206\n"
"v_mfma_f32_16x16x32_bf16 v[76:79], v[188:191], v[228:231], v[76:79]\n"
"ds_read_b128 v[156:159], v244 offset:60160\n"
"v_add_f32 v240, v240, v203\n"
"v_add_f32 v241, v241, v207\n"
"v_mfma_f32_16x16x32_bf16 v[16:19], v[160:163], v[224:227], v[16:19]\n"
"ds_read_b64_tr_b16 v[176:177], v245 offset:49664\n"
"v_mfma_f32_16x16x32_bf16 v[48:51], v[160:163], v[228:231], v[48:51]\n"
"ds_read_b64_tr_b16 v[178:179], v245 offset:57856\n"
"s_waitcnt lgkmcnt(10)\n"
"v_mfma_f32_16x16x32_bf16 v[80:83], v[160:163], v[232:235], v[80:83]\n"
"ds_read_b64_tr_b16 v[180:181], v246 offset:49664\n"
"v_mfma_f32_16x16x32_bf16 v[20:23], v[164:167], v[224:227], v[20:23]\n"
"ds_read_b64_tr_b16 v[182:183], v246 offset:57856\n"
"v_mfma_f32_16x16x32_bf16 v[52:55], v[164:167], v[228:231], v[52:55]\n"
"ds_read_b64_tr_b16 v[184:185], v245 offset:49920\n"
"v_mfma_f32_16x16x32_bf16 v[84:87], v[164:167], v[232:235], v[84:87]\n"
"ds_read_b64_tr_b16 v[186:187], v245 offset:58112\n"
"v_mfma_f32_16x16x32_bf16 v[24:27], v[168:171], v[224:227], v[24:27]\n"
"ds_read_b64_tr_b16 v[188:189], v246 offset:49920\n"
"v_mfma_f32_16x16x32_bf16 v[56:59], v[168:171], v[228:231], v[56:59]\n"
"ds_read_b64_tr_b16 v[190:191], v246 offset:58112\n"
"v_mfma_f32_16x16x32_bf16 v[88:91], v[168:171], v[232:235], v[88:91]\n"
"ds_read_b64_tr_b16 v[160:161], v245 offset:49152\n"
"v_mfma_f32_16x16x32_bf16 v[28:31], v[172:175], v[224:227], v[28:31]\n"
"ds_read_b64_tr_b16 v[162:163], v245 offset:57344\n"
"v_mfma_f32_16x16x32_bf16 v[60:63], v[172:175], v[228:231], v[60:63]\n"
"ds_read_b64_tr_b16 v[164:165], v246 offset:49152\n"
"v_mfma_f32_16x16x32_bf16 v[92:95], v[172:175], v[232:235], v[92:95]\n"
"ds_read_b64_tr_b16 v[166:167], v246 offset:57344\n"
"s_waitcnt lgkmcnt(12)\n"
"v_mfma_f32_16x16x32_bf16 v[192:195], v[128:131], v[96:99], v[212:215]\n"
"ds_read_b64_tr_b16 v[168:169], v245 offset:49408\n"
"v_mfma_f32_16x16x32_bf16 v[192:195], v[132:135], v[100:103], v[192:195]\n"
"ds_read_b64_tr_b16 v[170:171], v245 offset:57600\n"
"v_mfma_f32_16x16x32_bf16 v[192:195], v[136:139], v[104:107], v[192:195]\n"
"ds_read_b64_tr_b16 v[172:173], v246 offset:49408\n"
"v_mfma_f32_16x16x32_bf16 v[192:195], v[140:143], v[108:111], v[192:195]\n"
"ds_read_b64_tr_b16 v[174:175], v246 offset:57600\n"
"v_mfma_f32_16x16x32_bf16 v[196:199], v[128:131], v[112:115], v[216:219]\n"
"v_mfma_f32_16x16x32_bf16 v[196:199], v[132:135], v[116:119], v[196:199]\n"
"v_mfma_f32_16x16x32_bf16 v[196:199], v[136:139], v[120:123], v[196:199]\n"
"v_mfma_f32_16x16x32_bf16 v[196:199], v[140:143], v[124:127], v[196:199]\n"
"v_mfma_f32_16x16x32_bf16 v[200:203], v[144:147], v[96:99], v[212:215]\n"
"v_mfma_f32_16x16x32_bf16 v[200:203], v[148:151], v[100:103], v[200:203]\n"
"v_mfma_f32_16x16x32_bf16 v[200:203], v[152:155], v[104:107], v[200:203]\n"
"v_mfma_f32_16x16x32_bf16 v[200:203], v[156:159], v[108:111], v[200:203]\n"
"v_mfma_f32_16x16x32_bf16 v[204:207], v[144:147], v[112:115], v[216:219]\n"
"v_mfma_f32_16x16x32_bf16 v[204:207], v[148:151], v[116:119], v[204:207]\n"
"v_mfma_f32_16x16x32_bf16 v[204:207], v[152:155], v[120:123], v[204:207]\n"
"v_mfma_f32_16x16x32_bf16 v[204:207], v[156:159], v[124:127], v[204:207]\n"
"s_waitcnt lgkmcnt(0)\n"
"s_barrier\n"
"s_setprio 0\n"
"s_nop 0\n"
"LT2_%=:\n"
"s_lshl_b32 s88, %[os1], 2\n"
"s_add_u32 s88, s88, 73728\n"
"s_mov_b32 m0, s55\n"
"s_nop 0\n"
"buffer_load_dwordx4 v249, s[76:79], s88 offen lds\n"
"buffer_load_dwordx4 v250, s[76:79], s88 offen offset:1024 lds\n"
"s_add_u32 m0, s90, 6144\n"
"s_nop 0\n"
"buffer_load_dwordx4 v252, s[76:79], s88 offen lds\n"
"s_lshl_b32 s89, %[os1], 2\n"
"s_add_u32 s89, s89, 108544\n"
"s_mov_b32 m0, s55\n"
"s_nop 0\n"
"buffer_load_dwordx4 v249, s[76:79], s89 offen offset:2048 lds\n"
"buffer_load_dwordx4 v250, s[76:79], s89 offen offset:3072 lds\n"
"s_mov_b32 m0, s59\n"
"s_nop 0\n"
"buffer_load_dwordx4 v251, s[76:79], s89 offen offset:3072 lds\n"
"v_exp_f32 v192, v192\n"
"v_exp_f32 v193, v193\n"
"v_exp_f32 v194, v194\n"
"v_exp_f32_e64 v195, v195\n"
"v_cvt_pk_bf16_f32 v224, v192, v193\n"
"v_cvt_pk_bf16_f32 v225, v194, v195\n"
"v_exp_f32 v196, v196\n"
"v_exp_f32 v197, v197\n"
"v_exp_f32 v198, v198\n"
"v_exp_f32 v199, v199\n"
"v_cvt_pk_bf16_f32 v228, v196, v197\n"
"v_cvt_pk_bf16_f32 v229, v198, v199\n"
"v_exp_f32 v200, v200\n"
"v_exp_f32 v201, v201\n"
"v_exp_f32 v202, v202\n"
"v_exp_f32 v203, v203\n"
"v_cvt_pk_bf16_f32 v226, v200, v201\n"
"v_cvt_pk_bf16_f32 v227, v202, v203\n"
"v_exp_f32 v204, v204\n"
"v_exp_f32 v205, v205\n"
"v_exp_f32 v206, v206\n"
"v_exp_f32 v207, v207\n"
"v_cvt_pk_bf16_f32 v230, v204, v205\n"
"v_cvt_pk_bf16_f32 v231, v206, v207\n"
"s_waitcnt vmcnt(11)\n"
"s_barrier\n"
"s_setprio 1\n"
"ds_read_b128 v[232:235], v247 offset:4096\n"
"v_mfma_f32_16x16x32_bf16 v[32:35], v[176:179], v[224:227], v[32:35]\n"
"ds_read_b64 v[96:97], v247 offset:5120\n"
"v_add_f32 v240, v240, v192\n"
"v_add_f32_e64 v241, v241, v196\n"
"v_mfma_f32_16x16x32_bf16 v[64:67], v[176:179], v[228:231], v[64:67]\n"
"v_add_f32 v240, v240, v193\n"
"v_add_f32 v241, v241, v197\n"
"v_mfma_f32_16x16x32_bf16 v[36:39], v[180:183], v[224:227], v[36:39]\n"
"v_add_f32 v240, v240, v194\n"
"v_add_f32 v241, v241, v198\n"
"v_mfma_f32_16x16x32_bf16 v[68:71], v[180:183], v[228:231], v[68:71]\n"
"v_add_f32 v240, v240, v195\n"
"v_add_f32 v241, v241, v199\n"
"v_mfma_f32_16x16x32_bf16 v[40:43], v[184:187], v[224:227], v[40:43]\n"
"v_add_f32 v240, v240, v200\n"
"v_add_f32 v241, v241, v204\n"
"v_mfma_f32_16x16x32_bf16 v[72:75], v[184:187], v[228:231], v[72:75]\n"
"v_add_f32 v240, v240, v201\n"
"v_add_f32 v241, v241, v205\n"
"v_mfma_f32_16x16x32_bf16 v[44:47], v[188:191], v[224:227], v[44:47]\n"
"v_add_f32 v240, v240, v202\n"
"v_add_f32 v241, v241, v206\n"
"v_mfma_f32_16x16x32_bf16 v[76:79], v[188:191], v[228:231], v[76:79]\n"
"v_add_f32 v240, v240, v203\n"
"v_add_f32 v241, v241, v207\n"
"v_mfma_f32_16x16x32_bf16 v[16:19], v[160:163], v[224:227], v[16:19]\n"
"v_mfma_f32_16x16x32_bf16 v[48:51], v[160:163], v[228:231], v[48:51]\n"
"s_waitcnt lgkmcnt(1)\n"
"v_mfma_f32_16x16x32_bf16 v[80:83], v[160:163], v[232:235], v[80:83]\n"
"v_mfma_f32_16x16x32_bf16 v[20:23], v[164:167], v[224:227], v[20:23]\n"
"v_mfma_f32_16x16x32_bf16 v[52:55], v[164:167], v[228:231], v[52:55]\n"
"v_mfma_f32_16x16x32_bf16 v[84:87], v[164:167], v[232:235], v[84:87]\n"
"v_mfma_f32_16x16x32_bf16 v[24:27], v[168:171], v[224:227], v[24:27]\n"
"v_mfma_f32_16x16x32_bf16 v[56:59], v[168:171], v[228:231], v[56:59]\n"
"v_mfma_f32_16x16x32_bf16 v[88:91], v[168:171], v[232:235], v[88:91]\n"
"v_mfma_f32_16x16x32_bf16 v[28:31], v[172:175], v[224:227], v[28:31]\n"
"v_mfma_f32_16x16x32_bf16 v[60:63], v[172:175], v[228:231], v[60:63]\n"
"v_mfma_f32_16x16x32_bf16 v[92:95], v[172:175], v[232:235], v[92:95]\n"
"s_setprio 0\n"
"s_cmp_lg_u32 %[hb], 0\n"
"s_cbranch_scc1 LG2_%=\n"
"s_barrier\n"
"LG2_%=:\n"
"v_mov_b32 v206, v240\n"
"v_mov_b32 v207, v241\n"
"s_nop 0\n"
"v_permlane32_swap_b32 v240, v206\n"
"v_permlane32_swap_b32 v241, v207\n"
"v_add_f32 v240, v240, v206\n"
"v_add_f32 v241, v241, v207\n"
"v_mov_b32 v206, v240\n"
"v_mov_b32 v207, v241\n"
"s_nop 0\n"
"v_permlane16_swap_b32 v240, v206\n"
"v_permlane16_swap_b32 v241, v207\n"
"v_add_f32 v240, v240, v206\n"
"v_add_f32 v241, v241, v207\n"
"v_rcp_f32 v192, v240\n"
"v_rcp_f32 v194, v241\n"
"s_waitcnt lgkmcnt(0)\n"
"v_add_f32 v96, v96, v97\n"
"v_rcp_f32 v196, v96\n"
"s_lshr_b32 s68, %[hb], 11\n"
"s_or_b32 s68, s68, %[hb]\n"
"v_and_b32 v244, 0xffff, %[vbe]\n"
"v_xor_b32 v244, s68, v244\n"
"v_xor_b32 v245, 32, v244\n"
"v_lshrrev_b32 v246, 1, %[l16]\n"
"v_add_u32 v246, s90, v246\n"
"v_add_u32 v228, 4096, %[l16]\n"
"s_add_u32 s60, %[osoff], 4096\n"
"s_add_u32 s61, %[osoff], 13312\n"
"s_add_u32 s62, %[osoff], 22528\n"
"s_add_u32 s63, %[osoff], 31744\n"
"v_pk_mul_f32 v[80:81], v[80:81], v[196:197] op_sel_hi:[1,0]\n"
"v_pk_mul_f32 v[82:83], v[82:83], v[196:197] op_sel_hi:[1,0]\n"
"v_pk_mul_f32 v[84:85], v[84:85], v[196:197] op_sel_hi:[1,0]\n"
"v_pk_mul_f32 v[86:87], v[86:87], v[196:197] op_sel_hi:[1,0]\n"
"v_cvt_pk_bf16_f32 v160, v80, v81\n"
"v_cvt_pk_bf16_f32 v161, v82, v83\n"
"v_cvt_pk_bf16_f32 v162, v84, v85\n"
"v_cvt_pk_bf16_f32 v163, v86, v87\n"
"buffer_store_dwordx4 v[160:163], v228, %[srdo], s60 offen sc1\n"
"ds_write_b128 v247, v[160:163] offset:0\n"
"v_pk_mul_f32 v[88:89], v[88:89], v[196:197] op_sel_hi:[1,0]\n"
"v_pk_mul_f32 v[90:91], v[90:91], v[196:197] op_sel_hi:[1,0]\n"
"v_pk_mul_f32 v[92:93], v[92:93], v[196:197] op_sel_hi:[1,0]\n"
"v_pk_mul_f32 v[94:95], v[94:95], v[196:197] op_sel_hi:[1,0]\n"
"v_cvt_pk_bf16_f32 v164, v88, v89\n"
"v_cvt_pk_bf16_f32 v165, v90, v91\n"
"v_cvt_pk_bf16_f32 v166, v92, v93\n"
"v_cvt_pk_bf16_f32 v167, v94, v95\n"
"buffer_store_dwordx4 v[164:167], v228, %[srdo], s61 offen sc1\n"
"ds_write_b128 v247, v[164:167] offset:1024\n"
"v_pk_mul_f32 v[16:17], v[16:17], v[192:193] op_sel_hi:[1,0]\n"
"v_pk_mul_f32 v[18:19], v[18:19], v[192:193] op_sel_hi:[1,0]\n"
"v_pk_mul_f32 v[20:21], v[20:21], v[192:193] op_sel_hi:[1,0]\n"
"v_pk_mul_f32 v[22:23], v[22:23], v[192:193] op_sel_hi:[1,0]\n"
"v_cvt_pk_bf16_f32 v128, v16, v17\n"
"v_cvt_pk_bf16_f32 v129, v18, v19\n"
"v_cvt_pk_bf16_f32 v130, v20, v21\n"
"v_cvt_pk_bf16_f32 v131, v22, v23\n"
"buffer_store_dwordx4 v[128:131], %[l16], %[srdo], s60 offen sc1\n"
"v_pk_mul_f32 v[24:25], v[24:25], v[192:193] op_sel_hi:[1,0]\n"
"v_pk_mul_f32 v[26:27], v[26:27], v[192:193] op_sel_hi:[1,0]\n"
"v_pk_mul_f32 v[28:29], v[28:29], v[192:193] op_sel_hi:[1,0]\n"
"v_pk_mul_f32 v[30:31], v[30:31], v[192:193] op_sel_hi:[1,0]\n"
"v_cvt_pk_bf16_f32 v132, v24, v25\n"
"v_cvt_pk_bf16_f32 v133, v26, v27\n"
"v_cvt_pk_bf16_f32 v134, v28, v29\n"
"v_cvt_pk_bf16_f32 v135, v30, v31\n"
"buffer_store_dwordx4 v[132:135], %[l16], %[srdo], s61 offen sc1\n"
"v_pk_mul_f32 v[32:33], v[32:33], v[192:193] op_sel_hi:[1,0]\n"
"v_pk_mul_f32 v[34:35], v[34:35], v[192:193] op_sel_hi:[1,0]\n"
"v_pk_mul_f32 v[36:37], v[36:37], v[192:193] op_sel_hi:[1,0]\n"
"v_pk_mul_f32 v[38:39], v[38:39], v[192:193] op_sel_hi:[1,0]\n"
"v_cvt_pk_bf16_f32 v136, v32, v33\n"
"v_cvt_pk_bf16_f32 v137, v34, v35\n"
"v_cvt_pk_bf16_f32 v138, v36, v37\n"
"v_cvt_pk_bf16_f32 v139, v38, v39\n"
"buffer_store_dwordx4 v[136:139], %[l16], %[srdo], s62 offen sc1\n"
"v_pk_mul_f32 v[40:41], v[40:41], v[192:193] op_sel_hi:[1,0]\n"
"v_pk_mul_f32 v[42:43], v[42:43], v[192:193] op_sel_hi:[1,0]\n"
"v_pk_mul_f32 v[44:45], v[44:45], v[192:193] op_sel_hi:[1,0]\n"
"v_pk_mul_f32 v[46:47], v[46:47], v[192:193] op_sel_hi:[1,0]\n"
"v_cvt_pk_bf16_f32 v140, v40, v41\n"
"v_cvt_pk_bf16_f32 v141, v42, v43\n"
"v_cvt_pk_bf16_f32 v142, v44, v45\n"
"v_cvt_pk_bf16_f32 v143, v46, v47\n"
"buffer_store_dwordx4 v[140:143], %[l16], %[srdo], s63 offen sc1\n"
"v_pk_mul_f32 v[48:49], v[48:49], v[194:195] op_sel_hi:[1,0]\n"
"v_pk_mul_f32 v[50:51], v[50:51], v[194:195] op_sel_hi:[1,0]\n"
"v_pk_mul_f32 v[52:53], v[52:53], v[194:195] op_sel_hi:[1,0]\n"
"v_pk_mul_f32 v[54:55], v[54:55], v[194:195] op_sel_hi:[1,0]\n"
"v_cvt_pk_bf16_f32 v144, v48, v49\n"
"v_cvt_pk_bf16_f32 v145, v50, v51\n"
"v_cvt_pk_bf16_f32 v146, v52, v53\n"
"v_cvt_pk_bf16_f32 v147, v54, v55\n"
"buffer_store_dwordx4 v[144:147], %[l16], %[srdo], s60 offen offset:1024 sc1\n"
"v_pk_mul_f32 v[56:57], v[56:57], v[194:195] op_sel_hi:[1,0]\n"
"v_pk_mul_f32 v[58:59], v[58:59], v[194:195] op_sel_hi:[1,0]\n"
"v_pk_mul_f32 v[60:61], v[60:61], v[194:195] op_sel_hi:[1,0]\n"
"v_pk_mul_f32 v[62:63], v[62:63], v[194:195] op_sel_hi:[1,0]\n"
"v_cvt_pk_bf16_f32 v148, v56, v57\n"
"v_cvt_pk_bf16_f32 v149, v58, v59\n"
"v_cvt_pk_bf16_f32 v150, v60, v61\n"
"v_cvt_pk_bf16_f32 v151, v62, v63\n"
"buffer_store_dwordx4 v[148:151], %[l16], %[srdo], s61 offen offset:1024 sc1\n"
"v_pk_mul_f32 v[64:65], v[64:65], v[194:195] op_sel_hi:[1,0]\n"
"v_pk_mul_f32 v[66:67], v[66:67], v[194:195] op_sel_hi:[1,0]\n"
"v_pk_mul_f32 v[68:69], v[68:69], v[194:195] op_sel_hi:[1,0]\n"
"v_pk_mul_f32 v[70:71], v[70:71], v[194:195] op_sel_hi:[1,0]\n"
"v_cvt_pk_bf16_f32 v152, v64, v65\n"
"v_cvt_pk_bf16_f32 v153, v66, v67\n"
"v_cvt_pk_bf16_f32 v154, v68, v69\n"
"v_cvt_pk_bf16_f32 v155, v70, v71\n"
"buffer_store_dwordx4 v[152:155], %[l16], %[srdo], s62 offen offset:1024 sc1\n"
"v_pk_mul_f32 v[72:73], v[72:73], v[194:195] op_sel_hi:[1,0]\n"
"v_pk_mul_f32 v[74:75], v[74:75], v[194:195] op_sel_hi:[1,0]\n"
"v_pk_mul_f32 v[76:77], v[76:77], v[194:195] op_sel_hi:[1,0]\n"
"v_pk_mul_f32 v[78:79], v[78:79], v[194:195] op_sel_hi:[1,0]\n"
"v_cvt_pk_bf16_f32 v156, v72, v73\n"
"v_cvt_pk_bf16_f32 v157, v74, v75\n"
"v_cvt_pk_bf16_f32 v158, v76, v77\n"
"v_cvt_pk_bf16_f32 v159, v78, v79\n"
"buffer_store_dwordx4 v[156:159], %[l16], %[srdo], s63 offen offset:1024 sc1\n"
"v_max_f32 v202, v240, v241\n"
"v_add_f32_e64 v200, v240, v241\n"
"v_mov_b32 v201, 0\n"
"v_pk_add_f32 v[200:201], v[200:201], v[16:17]\n"
"ds_read_b64_tr_b16 v[100:101], v244 offset:0\n"
"v_pk_add_f32 v[200:201], v[200:201], v[18:19]\n"
"v_pk_add_f32 v[200:201], v[200:201], v[20:21]\n"
"ds_read_b64_tr_b16 v[102:103], v244 offset:8192\n"
"v_pk_add_f32 v[200:201], v[200:201], v[22:23]\n"
"v_pk_add_f32 v[200:201], v[200:201], v[24:25]\n"
"ds_read_b64_tr_b16 v[104:105], v245 offset:0\n"
"v_pk_add_f32 v[200:201], v[200:201], v[26:27]\n"
"v_pk_add_f32 v[200:201], v[200:201], v[28:29]\n"
"ds_read_b64_tr_b16 v[106:107], v245 offset:8192\n"
"v_pk_add_f32 v[200:201], v[200:201], v[30:31]\n"
"v_pk_add_f32 v[200:201], v[200:201], v[32:33]\n"
"ds_read_b64_tr_b16 v[108:109], v244 offset:256\n"
"v_pk_add_f32 v[200:201], v[200:201], v[34:35]\n"
"v_pk_add_f32 v[200:201], v[200:201], v[36:37]\n"
"ds_read_b64_tr_b16 v[110:111], v244 offset:8448\n"
"v_pk_add_f32 v[200:201], v[200:201], v[38:39]\n"
"v_pk_add_f32 v[200:201], v[200:201], v[40:41]\n"
"ds_read_b64_tr_b16 v[112:113], v245 offset:256\n"
"v_pk_add_f32 v[200:201], v[200:201], v[42:43]\n"
"v_pk_add_f32 v[200:201], v[200:201], v[44:45]\n"
"ds_read_b64_tr_b16 v[114:115], v245 offset:8448\n"
"v_pk_add_f32 v[200:201], v[200:201], v[46:47]\n"
"v_pk_add_f32 v[200:201], v[200:201], v[48:49]\n"
"ds_read_b64_tr_b16 v[116:117], v244 offset:512\n"
"v_pk_add_f32 v[200:201], v[200:201], v[50:51]\n"
"v_pk_add_f32 v[200:201], v[200:201], v[52:53]\n"
"ds_read_b64_tr_b16 v[118:119], v244 offset:8704\n"
"v_pk_add_f32 v[200:201], v[200:201], v[54:55]\n"
"v_pk_add_f32 v[200:201], v[200:201], v[56:57]\n"
"ds_read_b64_tr_b16 v[120:121], v245 offset:512\n"
"v_pk_add_f32 v[200:201], v[200:201], v[58:59]\n"
"v_pk_add_f32 v[200:201], v[200:201], v[60:61]\n"
"ds_read_b64_tr_b16 v[122:123], v245 offset:8704\n"
"v_pk_add_f32 v[200:201], v[200:201], v[62:63]\n"
"v_pk_add_f32 v[200:201], v[200:201], v[64:65]\n"
"ds_read_b64_tr_b16 v[124:125], v244 offset:768\n"
"v_pk_add_f32 v[200:201], v[200:201], v[66:67]\n"
"v_pk_add_f32 v[200:201], v[200:201], v[68:69]\n"
"ds_read_b64_tr_b16 v[126:127], v244 offset:8960\n"
"v_pk_add_f32 v[200:201], v[200:201], v[70:71]\n"
"v_pk_add_f32 v[200:201], v[200:201], v[72:73]\n"
"v_pk_add_f32 v[200:201], v[200:201], v[74:75]\n"
"v_pk_add_f32 v[200:201], v[200:201], v[76:77]\n"
"v_pk_add_f32 v[200:201], v[200:201], v[78:79]\n"
"v_pk_add_f32 v[200:201], v[200:201], v[80:81]\n"
"v_pk_add_f32 v[200:201], v[200:201], v[82:83]\n"
"v_pk_add_f32 v[200:201], v[200:201], v[84:85]\n"
"v_pk_add_f32 v[200:201], v[200:201], v[86:87]\n"
"v_pk_add_f32 v[200:201], v[200:201], v[88:89]\n"
"v_pk_add_f32 v[200:201], v[200:201], v[90:91]\n"
"v_pk_add_f32 v[200:201], v[200:201], v[92:93]\n"
"v_pk_add_f32 v[200:201], v[200:201], v[94:95]\n"
"v_add_f32_e64 v200, v200, v201\n"
"v_mov_b32 v203, 0x207\n"
"v_mov_b32 v205, 0x71800000\n"
"v_xor_b32 v204, 8192, v247\n"
"v_cmp_class_f32 s[68:69], v200, v203\n"
"v_cmp_nlt_f32 vcc, v202, v205\n"
"s_nop 4\n"
"s_or_b64 vcc, vcc, s[68:69]\n"
"s_nop 1\n"
"v_cndmask_b32 v232, 0, 1, vcc\n"
"ds_write_b32 v247, v232 offset:6152\n"
"s_waitcnt lgkmcnt(0)\n"
"s_barrier\n"
"ds_read_b128 v[168:171], v247 offset:2048\n"
"ds_read_b128 v[172:175], v247 offset:3072\n"
"ds_read_b128 v[176:179], v247 offset:6144\n"
"ds_read_b128 v[180:183], v204 offset:6144\n"
"LO2_%=:\n"
"v_mfma_f32_16x16x32_bf16 v[52:55], v[100:103], v[144:147], 0\n"
"ds_read_b64_tr_b16 v[224:225], v245 offset:768\n"
"v_mfma_f32_16x16x32_bf16 v[16:19], v[100:103], v[128:131], 0\n"
"ds_read_b64_tr_b16 v[226:227], v245 offset:8960\n"
"v_mfma_f32_16x16x32_bf16 v[56:59], v[104:107], v[144:147], 0\n"
"ds_read_b64_tr_b16 v[100:101], v246 offset:0\n"
"v_mfma_f32_16x16x32_bf16 v[20:23], v[104:107], v[128:131], 0\n"
"ds_read_b64_tr_b16 v[102:103], v246 offset:512\n"
"v_mfma_f32_16x16x32_bf16 v[60:63], v[108:111], v[144:147], 0\n"
"ds_read_b64_tr_b16 v[104:105], v244 offset:2048\n"
"v_mfma_f32_16x16x32_bf16 v[24:27], v[108:111], v[128:131], 0\n"
"ds_read_b64_tr_b16 v[106:107], v244 offset:10240\n"
"v_mfma_f32_16x16x32_bf16 v[64:67], v[112:115], v[144:147], 0\n"
"s_waitcnt lgkmcnt(7)\n"
"v_or3_b32 v176, v176, v177, v178\n"
"ds_read_b64_tr_b16 v[108:109], v245 offset:2048\n"
"v_mfma_f32_16x16x32_bf16 v[28:31], v[112:115], v[128:131], 0\n"
"s_waitcnt lgkmcnt(7)\n"
"v_or3_b32 v180, v180, v181, v182\n"
"ds_read_b64_tr_b16 v[110:111], v245 offset:10240\n"
"v_mfma_f32_16x16x32_bf16 v[68:71], v[116:119], v[144:147], 0\n"
"v_or3_b32 v176, v176, v179, v183\n"
"ds_read_b64_tr_b16 v[112:113], v244 offset:2304\n"
"v_mfma_f32_16x16x32_bf16 v[32:35], v[116:119], v[128:131], 0\n"
"v_or_b32_e64 v176, v176, v180\n"
"ds_read_b64_tr_b16 v[114:115], v244 offset:10496\n"
"v_mfma_f32_16x16x32_bf16 v[88:91], v[116:119], v[160:163], 0\n"
"v_cmp_ne_u32 vcc, 0, v176\n"
"v_mfma_f32_16x16x32_bf16 v[72:75], v[120:123], v[144:147], 0\n"
"ds_read_b64_tr_b16 v[116:117], v245 offset:2304\n"
"v_mfma_f32_16x16x32_bf16 v[36:39], v[120:123], v[128:131], 0\n"
"ds_read_b64_tr_b16 v[118:119], v245 offset:10496\n"
"v_mfma_f32_16x16x32_bf16 v[92:95], v[120:123], v[160:163], 0\n"
"v_mfma_f32_16x16x32_bf16 v[76:79], v[124:127], v[144:147], 0\n"
"ds_read_b64_tr_b16 v[120:121], v244 offset:2560\n"
"v_mfma_f32_16x16x32_bf16 v[40:43], v[124:127], v[128:131], 0\n"
"ds_read_b64_tr_b16 v[122:123], v244 offset:10752\n"
"s_waitcnt lgkmcnt(12)\n"
"v_mfma_f32_16x16x32_bf16 v[80:83], v[224:227], v[144:147], 0\n"
"ds_read_b64_tr_b16 v[124:125], v245 offset:2560\n"
"v_mfma_f32_16x16x32_bf16 v[44:47], v[224:227], v[128:131], 0\n"
"ds_read_b64_tr_b16 v[126:127], v245 offset:10752\n"
"s_waitcnt lgkmcnt(12)\n"
"v_mfma_f32_16x16x32_bf16 v[84:87], v[100:103], v[144:147], 0\n"
"ds_read_b64_tr_b16 v[224:225], v244 offset:2816\n"
"v_mfma_f32_16x16x32_bf16 v[48:51], v[100:103], v[128:131], 0\n"
"ds_read_b64_tr_b16 v[226:227], v244 offset:11008\n"
"s_waitcnt lgkmcnt(12)\n"
"v_mfma_f32_16x16x32_bf16 v[52:55], v[104:107], v[148:151], v[52:55]\n"
"ds_read_b64_tr_b16 v[100:101], v245 offset:2816\n"
"v_mfma_f32_16x16x32_bf16 v[16:19], v[104:107], v[132:135], v[16:19]\n"
"ds_read_b64_tr_b16 v[102:103], v245 offset:11008\n"
"s_waitcnt lgkmcnt(12)\n"
"v_mfma_f32_16x16x32_bf16 v[56:59], v[108:111], v[148:151], v[56:59]\n"
"ds_read_b64_tr_b16 v[104:105], v246 offset:1024\n"
"v_mfma_f32_16x16x32_bf16 v[20:23], v[108:111], v[132:135], v[20:23]\n"
"ds_read_b64_tr_b16 v[106:107], v246 offset:1536\n"
"s_waitcnt lgkmcnt(12)\n"
"v_mfma_f32_16x16x32_bf16 v[60:63], v[112:115], v[148:151], v[60:63]\n"
"v_mfma_f32_16x16x32_bf16 v[24:27], v[112:115], v[132:135], v[24:27]\n"
"s_waitcnt lgkmcnt(10)\n"
"v_mfma_f32_16x16x32_bf16 v[64:67], v[116:119], v[148:151], v[64:67]\n"
"v_mfma_f32_16x16x32_bf16 v[28:31], v[116:119], v[132:135], v[28:31]\n"
"s_waitcnt lgkmcnt(8)\n"
"v_mfma_f32_16x16x32_bf16 v[68:71], v[120:123], v[148:151], v[68:71]\n"
"s_waitcnt vmcnt(0)\n"
"s_cmp_lg_u64 vcc, 0\n"
"s_cbranch_scc1 LBAD_%=\n"
"s_waitcnt lgkmcnt(0)\n"
"s_barrier\n"
"ds_read_b64_tr_b16 v[108:109], v244 offset:16384\n"
"ds_read_b64_tr_b16 v[112:113], v245 offset:16384\n"
"ds_read_b64_tr_b16 v[116:117], v244 offset:16640\n"
"v_mfma_f32_16x16x32_bf16 v[32:35], v[120:123], v[132:135], v[32:35]\n"
"s_lshl_b32 s88, %[os2], 2\n"
"s_mov_b32 m0, s52\n"
"s_nop 0\n"
"buffer_load_dwordx4 v249, s[76:79], s88 offen lds\n"
"ds_read_b64_tr_b16 v[110:111], v244 offset:24576\n"
"ds_read_b64_tr_b16 v[114:115], v245 offset:24576\n"
"ds_read_b64_tr_b16 v[118:119], v244 offset:24832\n"
"v_mfma_f32_16x16x32_bf16 v[88:91], v[120:123], v[164:167], v[88:91]\n"
"buffer_load_dwordx4 v250, s[76:79], s88 offen offset:1024 lds\n"
"v_mfma_f32_16x16x32_bf16 v[72:75], v[124:127], v[148:151], v[72:75]\n"
"s_lshl_b32 s89, %[os2], 2\n"
"s_add_u32 s89, s89, 34816\n"
"buffer_load_dwordx4 v249, s[76:79], s89 offen offset:2048 lds\n"
"ds_read_b64_tr_b16 v[120:121], v245 offset:16640\n"
"v_mfma_f32_16x16x32_bf16 v[36:39], v[124:127], v[132:135], v[36:39]\n"
"buffer_load_dwordx4 v250, s[76:79], s89 offen offset:3072 lds\n"
"ds_read_b64_tr_b16 v[122:123], v245 offset:24832\n"
"v_mfma_f32_16x16x32_bf16 v[92:95], v[124:127], v[164:167], v[92:95]\n"
"v_mfma_f32_16x16x32_bf16 v[76:79], v[224:227], v[148:151], v[76:79]\n"
"ds_read_b64_tr_b16 v[124:125], v244 offset:16896\n"
"v_mfma_f32_16x16x32_bf16 v[40:43], v[224:227], v[132:135], v[40:43]\n"
"ds_read_b64_tr_b16 v[126:127], v244 offset:25088\n"
"v_mfma_f32_16x16x32_bf16 v[80:83], v[100:103], v[148:151], v[80:83]\n"
"ds_read_b64_tr_b16 v[224:225], v245 offset:16896\n"
"v_mfma_f32_16x16x32_bf16 v[44:47], v[100:103], v[132:135], v[44:47]\n"
"ds_read_b64_tr_b16 v[226:227], v245 offset:25088\n"
"v_mfma_f32_16x16x32_bf16 v[84:87], v[104:107], v[148:151], v[84:87]\n"
"ds_read_b64_tr_b16 v[100:101], v244 offset:17152\n"
"v_mfma_f32_16x16x32_bf16 v[48:51], v[104:107], v[132:135], v[48:51]\n"
"ds_read_b64_tr_b16 v[102:103], v244 offset:25344\n"
"s_waitcnt lgkmcnt(10)\n"
"v_mfma_f32_16x16x32_bf16 v[52:55], v[108:111], v[152:155], v[52:55]\n"
"ds_read_b64_tr_b16 v[104:105], v245 offset:17152\n"
"v_mfma_f32_16x16x32_bf16 v[16:19], v[108:111], v[136:139], v[16:19]\n"
"ds_read_b64_tr_b16 v[106:107], v245 offset:25344\n"
"s_waitcnt lgkmcnt(11)\n"
"v_mfma_f32_16x16x32_bf16 v[56:59], v[112:115], v[152:155], v[56:59]\n"
"ds_read_b64_tr_b16 v[108:109], v246 offset:2048\n"
"v_mfma_f32_16x16x32_bf16 v[20:23], v[112:115], v[136:139], v[20:23]\n"
"ds_read_b64_tr_b16 v[110:111], v246 offset:2560\n"
"s_waitcnt lgkmcnt(12)\n"
"v_mfma_f32_16x16x32_bf16 v[60:63], v[116:119], v[152:155], v[60:63]\n"
"ds_read_b64_tr_b16 v[112:113], v244 offset:18432\n"
"v_mfma_f32_16x16x32_bf16 v[24:27], v[116:119], v[136:139], v[24:27]\n"
"ds_read_b64_tr_b16 v[114:115], v244 offset:26624\n"
"s_waitcnt lgkmcnt(12)\n"
"v_mfma_f32_16x16x32_bf16 v[64:67], v[120:123], v[152:155], v[64:67]\n"
"ds_read_b64_tr_b16 v[116:117], v245 offset:18432\n"
"v_mfma_f32_16x16x32_bf16 v[28:31], v[120:123], v[136:139], v[28:31]\n"
"ds_read_b64_tr_b16 v[118:119], v245 offset:26624\n"
"s_waitcnt lgkmcnt(12)\n"
"v_mfma_f32_16x16x32_bf16 v[68:71], v[124:127], v[152:155], v[68:71]\n"
"ds_read_b64_tr_b16 v[120:121], v244 offset:18688\n"
"v_mfma_f32_16x16x32_bf16 v[32:35], v[124:127], v[136:139], v[32:35]\n"
"ds_read_b64_tr_b16 v[122:123], v244 offset:26880\n"
"v_mfma_f32_16x16x32_bf16 v[88:91], v[124:127], v[168:171], v[88:91]\n"
"s_waitcnt lgkmcnt(12)\n"
"v_mfma_f32_16x16x32_bf16 v[72:75], v[224:227], v[152:155], v[72:75]\n"
"ds_read_b64_tr_b16 v[124:125], v245 offset:18688\n"
"v_mfma_f32_16x16x32_bf16 v[36:39], v[224:227], v[136:139], v[36:39]\n"
"ds_read_b64_tr_b16 v[126:127], v245 offset:26880\n"
"v_mfma_f32_16x16x32_bf16 v[92:95], v[224:227], v[168:171], v[92:95]\n"
"s_waitcnt lgkmcnt(12)\n"
"v_mfma_f32_16x16x32_bf16 v[76:79], v[100:103], v[152:155], v[76:79]\n"
"ds_read_b64_tr_b16 v[224:225], v244 offset:18944\n"
"v_mfma_f32_16x16x32_bf16 v[40:43], v[100:103], v[136:139], v[40:43]\n"
"ds_read_b64_tr_b16 v[226:227], v244 offset:27136\n"
"s_waitcnt lgkmcnt(12)\n"
"v_mfma_f32_16x16x32_bf16 v[80:83], v[104:107], v[152:155], v[80:83]\n"
"ds_read_b64_tr_b16 v[100:101], v245 offset:18944\n"
"v_mfma_f32_16x16x32_bf16 v[44:47], v[104:107], v[136:139], v[44:47]\n"
"ds_read_b64_tr_b16 v[102:103], v245 offset:27136\n"
"s_waitcnt lgkmcnt(12)\n"
"v_mfma_f32_16x16x32_bf16 v[84:87], v[108:111], v[152:155], v[84:87]\n"
"ds_read_b64_tr_b16 v[104:105], v244 offset:19200\n"
"v_mfma_f32_16x16x32_bf16 v[48:51], v[108:111], v[136:139], v[48:51]\n"
"ds_read_b64_tr_b16 v[106:107], v244 offset:27392\n"
"s_waitcnt lgkmcnt(12)\n"
"v_mfma_f32_16x16x32_bf16 v[52:55], v[112:115], v[156:159], v[52:55]\n"
"ds_read_b64_tr_b16 v[108:109], v245 offset:19200\n"
"v_mfma_f32_16x16x32_bf16 v[16:19], v[112:115], v[140:143], v[16:19]\n"
"ds_read_b64_tr_b16 v[110:111], v245 offset:27392\n"
"s_waitcnt lgkmcnt(12)\n"
"v_mfma_f32_16x16x32_bf16 v[56:59], v[116:119], v[156:159], v[56:59]\n"
"ds_read_b64_tr_b16 v[112:113], v246 offset:3072\n"
"v_mfma_f32_16x16x32_bf16 v[20:23], v[116:119], v[140:143], v[20:23]\n"
"ds_read_b64_tr_b16 v[114:115], v246 offset:3584\n"
"s_waitcnt lgkmcnt(12)\n"
"v_mfma_f32_16x16x32_bf16 v[60:63], v[120:123], v[156:159], v[60:63]\n"
"v_mfma_f32_16x16x32_bf16 v[24:27], v[120:123], v[140:143], v[24:27]\n"
"s_waitcnt lgkmcnt(10)\n"
"v_mfma_f32_16x16x32_bf16 v[64:67], v[124:127], v[156:159], v[64:67]\n"
"v_mfma_f32_16x16x32_bf16 v[28:31], v[124:127], v[140:143], v[28:31]\n"
"s_waitcnt lgkmcnt(8)\n"
"v_mfma_f32_16x16x32_bf16 v[68:71], v[224:227], v[156:159], v[68:71]\n"
"s_waitcnt lgkmcnt(0)\n"
"s_barrier\n"
"ds_read_b64_tr_b16 v[116:117], v244 offset:32768\n"
"ds_read_b64_tr_b16 v[120:121], v245 offset:32768\n"
"ds_read_b64_tr_b16 v[124:125], v244 offset:33024\n"
"v_mfma_f32_16x16x32_bf16 v[32:35], v[224:227], v[140:143], v[32:35]\n"
"s_lshl_b32 s88, %[os2], 2\n"
"s_add_u32 s88, s88, 73728\n"
"s_mov_b32 m0, s53\n"
"s_nop 0\n"
"buffer_load_dwordx4 v249, s[76:79], s88 offen lds\n"
"ds_read_b64_tr_b16 v[118:119], v244 offset:40960\n"
"ds_read_b64_tr_b16 v[122:123], v245 offset:40960\n"
"ds_read_b64_tr_b16 v[126:127], v244 offset:41216\n"
"v_mfma_f32_16x16x32_bf16 v[88:91], v[224:227], v[172:175], v[88:91]\n"
"buffer_load_dwordx4 v250, s[76:79], s88 offen offset:1024 lds\n"
"s_add_u32 m0, s90, 2048\n"
"v_mfma_f32_16x16x32_bf16 v[72:75], v[100:103], v[156:159], v[72:75]\n"
"buffer_load_dwordx4 v252, s[76:79], s88 offen lds\n"
"ds_read_b64_tr_b16 v[224:225], v245 offset:33024\n"
"v_mfma_f32_16x16x32_bf16 v[36:39], v[100:103], v[140:143], v[36:39]\n"
"s_lshl_b32 s89, %[os2], 2\n"
"s_add_u32 s89, s89, 108544\n"
"s_mov_b32 m0, s53\n"
"s_nop 0\n"
"buffer_load_dwordx4 v249, s[76:79], s89 offen offset:2048 lds\n"
"ds_read_b64_tr_b16 v[226:227], v245 offset:41216\n"
"v_mfma_f32_16x16x32_bf16 v[92:95], v[100:103], v[172:175], v[92:95]\n"
"buffer_load_dwordx4 v250, s[76:79], s89 offen offset:3072 lds\n"
"v_mfma_f32_16x16x32_bf16 v[76:79], v[104:107], v[156:159], v[76:79]\n"
"ds_read_b64_tr_b16 v[160:161], v244 offset:33280\n"
"v_mfma_f32_16x16x32_bf16 v[40:43], v[104:107], v[140:143], v[40:43]\n"
"ds_read_b64_tr_b16 v[162:163], v244 offset:41472\n"
"v_mfma_f32_16x16x32_bf16 v[80:83], v[108:111], v[156:159], v[80:83]\n"
"ds_read_b64_tr_b16 v[164:165], v245 offset:33280\n"
"v_mfma_f32_16x16x32_bf16 v[44:47], v[108:111], v[140:143], v[44:47]\n"
"ds_read_b64_tr_b16 v[166:167], v245 offset:41472\n"
"v_mfma_f32_16x16x32_bf16 v[84:87], v[112:115], v[156:159], v[84:87]\n"
"ds_read_b64_tr_b16 v[108:109], v244 offset:33536\n"
"v_mfma_f32_16x16x32_bf16 v[48:51], v[112:115], v[140:143], v[48:51]\n"
"s_barrier\n"
"s_add_u32 s60, %[os1], 4096\n"
"buffer_load_dwordx4 v[192:195], %[l16], %[srdo], s60 offen offset:1024 sc1\n"
"buffer_load_dwordx4 v[176:179], %[l16], %[srdo], s60 offen sc1\n"
"s_add_u32 s64, %[os1], 26624\n"
"buffer_load_dwordx4 v[216:219], %[l16], %[srdo], s64 offen sc1\n"
"s_add_u32 s61, %[os1], 13312\n"
"buffer_load_dwordx4 v[196:199], %[l16], %[srdo], s61 offen offset:1024 sc1\n"
"buffer_load_dwordx4 v[180:183], %[l16], %[srdo], s61 offen sc1\n"
"s_add_u32 s62, %[os1], 22528\n"
"buffer_load_dwordx4 v[200:203], %[l16], %[srdo], s62 offen offset:1024 sc1\n"
"buffer_load_dwordx4 v[184:187], %[l16], %[srdo], s62 offen sc1\n"
"s_add_u32 s63, %[os1], 31744\n"
"buffer_load_dwordx4 v[204:207], %[l16], %[srdo], s63 offen offset:1024 sc1\n"
"buffer_load_dwordx4 v[188:191], %[l16], %[srdo], s63 offen sc1\n"
"s_add_u32 s64, %[os2], 26624\n"
"buffer_load_dwordx4 v[240:243], %[l16], %[srdo], s64 offen sc1\n"
"ds_read_b64_tr_b16 v[110:111], v244 offset:41728\n"
"s_waitcnt vmcnt(9) lgkmcnt(10)\n"
"v_mfma_f32_16x16x32_bf16 v[52:55], v[116:119], v[192:195], v[52:55]\n"
"ds_read_b64_tr_b16 v[112:113], v245 offset:33536\n"
"s_waitcnt vmcnt(8)\n"
"v_mfma_f32_16x16x32_bf16 v[16:19], v[116:119], v[176:179], v[16:19]\n"
"s_add_u32 s60, %[os2], 4096\n"
"buffer_load_dwordx4 v[144:147], %[l16], %[srdo], s60 offen offset:1024 sc1\n"
"ds_read_b64_tr_b16 v[114:115], v245 offset:41728\n"
"s_waitcnt lgkmcnt(11)\n"
"v_mfma_f32_16x16x32_bf16 v[56:59], v[120:123], v[192:195], v[56:59]\n"
"ds_read_b64_tr_b16 v[116:117], v246 offset:4096\n"
"v_mfma_f32_16x16x32_bf16 v[20:23], v[120:123], v[176:179], v[20:23]\n"
"buffer_load_dwordx4 v[128:131], %[l16], %[srdo], s60 offen sc1\n"
"ds_read_b64_tr_b16 v[118:119], v246 offset:4608\n"
"s_waitcnt lgkmcnt(12)\n"
"v_mfma_f32_16x16x32_bf16 v[60:63], v[124:127], v[192:195], v[60:63]\n"
"ds_read_b64_tr_b16 v[120:121], v244 offset:34816\n"
"v_mfma_f32_16x16x32_bf16 v[24:27], v[124:127], v[176:179], v[24:27]\n"
"ds_read_b64_tr_b16 v[122:123], v244 offset:43008\n"
"s_waitcnt lgkmcnt(12)\n"
"v_mfma_f32_16x16x32_bf16 v[64:67], v[224:227], v[192:195], v[64:67]\n"
"ds_read_b64_tr_b16 v[124:125], v245 offset:34816\n"
"v_mfma_f32_16x16x32_bf16 v[28:31], v[224:227], v[176:179], v[28:31]\n"
"s_add_u32 s61, %[os2], 13312\n"
"buffer_load_dwordx4 v[148:151], %[l16], %[srdo], s61 offen offset:1024 sc1\n"
"ds_read_b64_tr_b16 v[126:127], v245 offset:43008\n"
"s_waitcnt lgkmcnt(12)\n"
"v_mfma_f32_16x16x32_bf16 v[68:71], v[160:163], v[192:195], v[68:71]\n"
"ds_read_b64_tr_b16 v[224:225], v244 offset:35072\n"
"v_mfma_f32_16x16x32_bf16 v[32:35], v[160:163], v[176:179], v[32:35]\n"
"buffer_load_dwordx4 v[132:135], %[l16], %[srdo], s61 offen sc1\n"
"ds_read_b64_tr_b16 v[226:227], v244 offset:43264\n"
"s_waitcnt lgkmcnt(12)\n"
"v_mfma_f32_16x16x32_bf16 v[72:75], v[164:167], v[192:195], v[72:75]\n"
"ds_read_b64_tr_b16 v[100:101], v245 offset:35072\n"
"v_mfma_f32_16x16x32_bf16 v[36:39], v[164:167], v[176:179], v[36:39]\n"
"ds_read_b64_tr_b16 v[102:103], v245 offset:43264\n"
"s_waitcnt lgkmcnt(12)\n"
"v_mfma_f32_16x16x32_bf16 v[76:79], v[108:111], v[192:195], v[76:79]\n"
"ds_read_b64_tr_b16 v[168:169], v244 offset:35328\n"
"v_mfma_f32_16x16x32_bf16 v[40:43], v[108:111], v[176:179], v[40:43]\n"
"s_add_u32 s62, %[os2], 22528\n"
"buffer_load_dwordx4 v[152:155], %[l16], %[srdo], s62 offen offset:1024 sc1\n"
"ds_read_b64_tr_b16 v[170:171], v244 offset:43520\n"
"s_waitcnt lgkmcnt(12)\n"
"v_mfma_f32_16x16x32_bf16 v[80:83], v[112:115], v[192:195], v[80:83]\n"
"ds_read_b64_tr_b16 v[172:173], v245 offset:35328\n"
"v_mfma_f32_16x16x32_bf16 v[44:47], v[112:115], v[176:179], v[44:47]\n"
"buffer_load_dwordx4 v[136:139], %[l16], %[srdo], s62 offen sc1\n"
"ds_read_b64_tr_b16 v[174:175], v245 offset:43520\n"
"s_waitcnt lgkmcnt(12)\n"
"v_mfma_f32_16x16x32_bf16 v[84:87], v[116:119], v[192:195], v[84:87]\n"
"ds_read_b64_tr_b16 v[112:113], v244 offset:35584\n"
"v_mfma_f32_16x16x32_bf16 v[48:51], v[116:119], v[176:179], v[48:51]\n"
"ds_read_b64_tr_b16 v[114:115], v244 offset:43776\n"
"s_waitcnt vmcnt(12) lgkmcnt(12)\n"
"v_mfma_f32_16x16x32_bf16 v[52:55], v[120:123], v[196:199], v[52:55]\n"
"ds_read_b64_tr_b16 v[116:117], v245 offset:35584\n"
"s_waitcnt vmcnt(11)\n"
"v_mfma_f32_16x16x32_bf16 v[16:19], v[120:123], v[180:183], v[16:19]\n"
"s_add_u32 s63, %[os2], 31744\n"
"buffer_load_dwordx4 v[156:159], %[l16], %[srdo], s63 offen offset:1024 sc1\n"
"ds_read_b64_tr_b16 v[118:119], v245 offset:43776\n"
"s_waitcnt lgkmcnt(12)\n"
"v_mfma_f32_16x16x32_bf16 v[56:59], v[124:127], v[196:199], v[56:59]\n"
"ds_read_b64_tr_b16 v[120:121], v246 offset:5120\n"
"v_mfma_f32_16x16x32_bf16 v[20:23], v[124:127], v[180:183], v[20:23]\n"
"buffer_load_dwordx4 v[140:143], %[l16], %[srdo], s63 offen sc1\n"
"ds_read_b64_tr_b16 v[122:123], v246 offset:5632\n"
"s_waitcnt lgkmcnt(12)\n"
"v_mfma_f32_16x16x32_bf16 v[60:63], v[224:227], v[196:199], v[60:63]\n"
"v_mfma_f32_16x16x32_bf16 v[24:27], v[224:227], v[180:183], v[24:27]\n"
"ds_write_b128 v247, v[216:219] offset:2048\n"
"s_waitcnt lgkmcnt(11)\n"
"v_mfma_f32_16x16x32_bf16 v[64:67], v[100:103], v[196:199], v[64:67]\n"
"v_mfma_f32_16x16x32_bf16 v[28:31], v[100:103], v[180:183], v[28:31]\n"
"s_add_u32 s60, %[os3], 4096\n"
"buffer_load_dwordx4 v[192:195], %[l16], %[srdo], s60 offen offset:1024 sc1\n"
"s_waitcnt lgkmcnt(9)\n"
"v_mfma_f32_16x16x32_bf16 v[68:71], v[168:171], v[196:199], v[68:71]\n"
"s_waitcnt lgkmcnt(0)\n"
"s_barrier\n"
"ds_read_b64_tr_b16 v[124:125], v244 offset:49152\n"
"ds_read_b64_tr_b16 v[224:225], v245 offset:49152\n"
"ds_read_b64_tr_b16 v[100:101], v244 offset:49408\n"
"v_mfma_f32_16x16x32_bf16 v[32:35], v[168:171], v[180:183], v[32:35]\n"
"ds_read_b128 v[208:211], v247 offset:0\n"
"s_lshl_b32 s88, %[os3], 2\n"
"s_mov_b32 m0, s54\n"
"s_nop 0\n"
"buffer_load_dwordx4 v249, s[76:79], s88 offen lds\n"
"buffer_load_dwordx4 v[176:179], %[l16], %[srdo], s60 offen sc1\n"
"ds_read_b64_tr_b16 v[126:127], v244 offset:57344\n"
"ds_read_b64_tr_b16 v[226:227], v245 offset:57344\n"
"ds_read_b64_tr_b16 v[102:103], v244 offset:57600\n"
"v_mfma_f32_16x16x32_bf16 v[72:75], v[172:175], v[196:199], v[72:75]\n"
"ds_read_b128 v[212:215], v247 offset:1024\n"
"buffer_load_dwordx4 v250, s[76:79], s88 offen offset:1024 lds\n"
"ds_read_b64_tr_b16 v[104:105], v245 offset:49408\n"
"v_mfma_f32_16x16x32_bf16 v[36:39], v[172:175], v[180:183], v[36:39]\n"
"ds_read_b128 v[220:223], v247 offset:3072\n"
"s_lshl_b32 s89, %[os3], 2\n"
"s_add_u32 s89, s89, 34816\n"
"buffer_load_dwordx4 v249, s[76:79], s89 offen offset:2048 lds\n"
"ds_read_b64_tr_b16 v[106:107], v245 offset:57600\n"
"v_mfma_f32_16x16x32_bf16 v[76:79], v[112:115], v[196:199], v[76:79]\n"
"buffer_load_dwordx4 v250, s[76:79], s89 offen offset:3072 lds\n"
"ds_read_b64_tr_b16 v[108:109], v244 offset:49664\n"
"v_mfma_f32_16x16x32_bf16 v[40:43], v[112:115], v[180:183], v[40:43]\n"
"ds_read_b64_tr_b16 v[110:111], v244 offset:57856\n"
"v_mfma_f32_16x16x32_bf16 v[80:83], v[116:119], v[196:199], v[80:83]\n"
"ds_read_b64_tr_b16 v[112:113], v245 offset:49664\n"
"v_mfma_f32_16x16x32_bf16 v[44:47], v[116:119], v[180:183], v[44:47]\n"
"ds_read_b64_tr_b16 v[114:115], v245 offset:57856\n"
"v_mfma_f32_16x16x32_bf16 v[84:87], v[120:123], v[196:199], v[84:87]\n"
"ds_read_b64_tr_b16 v[116:117], v244 offset:49920\n"
"v_mfma_f32_16x16x32_bf16 v[48:51], v[120:123], v[180:183], v[48:51]\n"
"ds_read_b64_tr_b16 v[118:119], v244 offset:58112\n"
"s_waitcnt lgkmcnt(13)\n"
"v_mfma_f32_16x16x32_bf16 v[88:91], v[160:163], v[208:211], v[88:91]\n"
"s_waitcnt vmcnt(18) lgkmcnt(12)\n"
"v_mfma_f32_16x16x32_bf16 v[52:55], v[124:127], v[200:203], v[52:55]\n"
"ds_read_b64_tr_b16 v[120:121], v245 offset:49920\n"
"s_waitcnt vmcnt(17)\n"
"v_mfma_f32_16x16x32_bf16 v[16:19], v[124:127], v[184:187], v[16:19]\n"
"s_add_u32 s61, %[os3], 13312\n"
"buffer_load_dwordx4 v[196:199], %[l16], %[srdo], s61 offen offset:1024 sc1\n"
"ds_read_b64_tr_b16 v[122:123], v245 offset:58112\n"
"v_mfma_f32_16x16x32_bf16 v[92:95], v[164:167], v[208:211], v[92:95]\n"
"buffer_load_dwordx4 v[180:183], %[l16], %[srdo], s61 offen sc1\n"
"s_waitcnt lgkmcnt(13)\n"
"v_mfma_f32_16x16x32_bf16 v[56:59], v[224:227], v[200:203], v[56:59]\n"
"ds_read_b64_tr_b16 v[124:125], v246 offset:6144\n"
"v_mfma_f32_16x16x32_bf16 v[20:23], v[224:227], v[184:187], v[20:23]\n"
"ds_read_b64_tr_b16 v[126:127], v246 offset:6656\n"
"s_waitcnt lgkmcnt(13)\n"
"v_mfma_f32_16x16x32_bf16 v[88:91], v[168:171], v[212:215], v[88:91]\n"
"v_mfma_f32_16x16x32_bf16 v[60:63], v[100:103], v[200:203], v[60:63]\n"
"ds_read_b64_tr_b16 v[224:225], v244 offset:51200\n"
"v_mfma_f32_16x16x32_bf16 v[24:27], v[100:103], v[184:187], v[24:27]\n"
"ds_read_b64_tr_b16 v[226:227], v244 offset:59392\n"
"v_mfma_f32_16x16x32_bf16 v[92:95], v[172:175], v[212:215], v[92:95]\n"
"s_waitcnt lgkmcnt(12)\n"
"v_mfma_f32_16x16x32_bf16 v[64:67], v[104:107], v[200:203], v[64:67]\n"
"ds_read_b64_tr_b16 v[100:101], v245 offset:51200\n"
"v_mfma_f32_16x16x32_bf16 v[28:31], v[104:107], v[184:187], v[28:31]\n"
"ds_read_b64_tr_b16 v[102:103], v245 offset:59392\n"
"s_waitcnt lgkmcnt(12)\n"
"v_mfma_f32_16x16x32_bf16 v[68:71], v[108:111], v[200:203], v[68:71]\n"
"ds_read_b64_tr_b16 v[104:105], v244 offset:51456\n"
"v_mfma_f32_16x16x32_bf16 v[32:35], v[108:111], v[184:187], v[32:35]\n"
"ds_read_b64_tr_b16 v[106:107], v244 offset:59648\n"
"v_mfma_f32_16x16x32_bf16 v[88:91], v[108:111], v[216:219], v[88:91]\n"
"s_waitcnt lgkmcnt(12)\n"
"v_mfma_f32_16x16x32_bf16 v[72:75], v[112:115], v[200:203], v[72:75]\n"
"ds_read_b64_tr_b16 v[108:109], v245 offset:51456\n"
"v_mfma_f32_16x16x32_bf16 v[36:39], v[112:115], v[184:187], v[36:39]\n"
"ds_read_b64_tr_b16 v[110:111], v245 offset:59648\n"
"v_mfma_f32_16x16x32_bf16 v[92:95], v[112:115], v[216:219], v[92:95]\n"
"s_waitcnt lgkmcnt(12)\n"
"v_mfma_f32_16x16x32_bf16 v[76:79], v[116:119], v[200:203], v[76:79]\n"
"ds_read_b64_tr_b16 v[112:113], v244 offset:51712\n"
"v_mfma_f32_16x16x32_bf16 v[40:43], v[116:119], v[184:187], v[40:43]\n"
"ds_read_b64_tr_b16 v[114:115], v244 offset:59904\n"
"s_waitcnt lgkmcnt(12)\n"
"v_mfma_f32_16x16x32_bf16 v[80:83], v[120:123], v[200:203], v[80:83]\n"
"ds_read_b64_tr_b16 v[116:117], v245 offset:51712\n"
"v_mfma_f32_16x16x32_bf16 v[44:47], v[120:123], v[184:187], v[44:47]\n"
"ds_read_b64_tr_b16 v[118:119], v245 offset:59904\n"
"s_waitcnt lgkmcnt(12)\n"
"v_mfma_f32_16x16x32_bf16 v[84:87], v[124:127], v[200:203], v[84:87]\n"
"ds_read_b64_tr_b16 v[120:121], v244 offset:51968\n"
"v_mfma_f32_16x16x32_bf16 v[48:51], v[124:127], v[184:187], v[48:51]\n"
"ds_read_b64_tr_b16 v[122:123], v244 offset:60160\n"
"s_waitcnt vmcnt(18) lgkmcnt(12)\n"
"v_mfma_f32_16x16x32_bf16 v[52:55], v[224:227], v[204:207], v[52:55]\n"
"ds_read_b64_tr_b16 v[124:125], v245 offset:51968\n"
"s_waitcnt vmcnt(17)\n"
"v_mfma_f32_16x16x32_bf16 v[16:19], v[224:227], v[188:191], v[16:19]\n"
"s_add_u32 s62, %[os3], 22528\n"
"buffer_load_dwordx4 v[200:203], %[l16], %[srdo], s62 offen offset:1024 sc1\n"
"ds_read_b64_tr_b16 v[126:127], v245 offset:60160\n"
"s_waitcnt lgkmcnt(12)\n"
"v_mfma_f32_16x16x32_bf16 v[56:59], v[100:103], v[204:207], v[56:59]\n"
"buffer_load_dwordx4 v[184:187], %[l16], %[srdo], s62 offen sc1\n"
"ds_read_b64_tr_b16 v[224:225], v246 offset:7168\n"
"v_mfma_f32_16x16x32_bf16 v[20:23], v[100:103], v[188:191], v[20:23]\n"
"ds_read_b64_tr_b16 v[226:227], v246 offset:7680\n"
"s_waitcnt lgkmcnt(12)\n"
"v_mfma_f32_16x16x32_bf16 v[60:63], v[104:107], v[204:207], v[60:63]\n"
"v_mfma_f32_16x16x32_bf16 v[24:27], v[104:107], v[188:191], v[24:27]\n"
"s_waitcnt vmcnt(18)\n"
"ds_write_b128 v247, v[240:243] offset:6144\n"
"s_waitcnt lgkmcnt(11)\n"
"v_mfma_f32_16x16x32_bf16 v[64:67], v[108:111], v[204:207], v[64:67]\n"
"v_mfma_f32_16x16x32_bf16 v[28:31], v[108:111], v[188:191], v[28:31]\n"
"s_waitcnt lgkmcnt(9)\n"
"v_mfma_f32_16x16x32_bf16 v[68:71], v[112:115], v[204:207], v[68:71]\n"
"s_waitcnt lgkmcnt(0)\n"
"s_barrier\n"
"s_add_u32 s64, %[os3], 26624\n"
"buffer_load_dwordx4 v[240:243], %[l16], %[srdo], s64 offen sc1\n"
"ds_read_b64_tr_b16 v[100:101], v244 offset:0\n"
"ds_read_b64_tr_b16 v[104:105], v245 offset:0\n"
"ds_read_b64_tr_b16 v[108:109], v244 offset:256\n"
"v_mfma_f32_16x16x32_bf16 v[32:35], v[112:115], v[188:191], v[32:35]\n"
"ds_read_b128 v[160:163], v247 offset:4096\n"
"s_lshl_b32 s88, %[os3], 2\n"
"s_add_u32 s88, s88, 73728\n"
"s_mov_b32 m0, s55\n"
"s_nop 0\n"
"buffer_load_dwordx4 v249, s[76:79], s88 offen lds\n"
"ds_read_b64_tr_b16 v[102:103], v244 offset:8192\n"
"ds_read_b64_tr_b16 v[106:107], v245 offset:8192\n"
"ds_read_b64_tr_b16 v[110:111], v244 offset:8448\n"
"v_mfma_f32_16x16x32_bf16 v[88:91], v[112:115], v[220:223], v[88:91]\n"
"ds_read_b128 v[164:167], v247 offset:5120\n"
"buffer_load_dwordx4 v250, s[76:79], s88 offen offset:1024 lds\n"
"v_mfma_f32_16x16x32_bf16 v[72:75], v[116:119], v[204:207], v[72:75]\n"
"s_add_u32 m0, s90, 6144\n"
"ds_read_b128 v[168:171], v247 offset:6144\n"
"buffer_load_dwordx4 v252, s[76:79], s88 offen lds\n"
"ds_read_b64_tr_b16 v[112:113], v245 offset:256\n"
"v_mfma_f32_16x16x32_bf16 v[36:39], v[116:119], v[188:191], v[36:39]\n"
"ds_read_b128 v[172:175], v247 offset:7168\n"
"s_lshl_b32 s89, %[os3], 2\n"
"s_add_u32 s89, s89, 108544\n"
"s_mov_b32 m0, s55\n"
"s_nop 0\n"
"buffer_load_dwordx4 v249, s[76:79], s89 offen offset:2048 lds\n"
"ds_read_b64_tr_b16 v[114:115], v245 offset:8448\n"
"v_mfma_f32_16x16x32_bf16 v[92:95], v[116:119], v[220:223], v[92:95]\n"
"buffer_load_dwordx4 v250, s[76:79], s89 offen offset:3072 lds\n"
"v_mfma_f32_16x16x32_bf16 v[76:79], v[120:123], v[204:207], v[76:79]\n"
"ds_read_b64_tr_b16 v[116:117], v244 offset:512\n"
"v_mfma_f32_16x16x32_bf16 v[40:43], v[120:123], v[188:191], v[40:43]\n"
"ds_read_b64_tr_b16 v[118:119], v244 offset:8704\n"
"v_mfma_f32_16x16x32_bf16 v[80:83], v[124:127], v[204:207], v[80:83]\n"
"ds_read_b64_tr_b16 v[120:121], v245 offset:512\n"
"v_mfma_f32_16x16x32_bf16 v[44:47], v[124:127], v[188:191], v[44:47]\n"
"ds_read_b64_tr_b16 v[122:123], v245 offset:8704\n"
"v_mfma_f32_16x16x32_bf16 v[84:87], v[224:227], v[204:207], v[84:87]\n"
"ds_read_b64_tr_b16 v[124:125], v244 offset:768\n"
"v_mfma_f32_16x16x32_bf16 v[48:51], v[224:227], v[188:191], v[48:51]\n"
"ds_read_b64_tr_b16 v[126:127], v244 offset:8960\n"
"s_waitcnt vmcnt(23) lgkmcnt(13)\n"
"v_mfma_f32_16x16x32_bf16 v[52:55], v[100:103], v[144:147], v[52:55]\n"
"ds_read_b64_tr_b16 v[224:225], v245 offset:768\n"
"s_waitcnt vmcnt(22)\n"
"v_mfma_f32_16x16x32_bf16 v[16:19], v[100:103], v[128:131], v[16:19]\n"
"s_add_u32 s63, %[os3], 31744\n"
"buffer_load_dwordx4 v[204:207], %[l16], %[srdo], s63 offen offset:1024 sc1\n"
"ds_read_b64_tr_b16 v[226:227], v245 offset:8960\n"
"s_waitcnt lgkmcnt(14)\n"
"v_mfma_f32_16x16x32_bf16 v[56:59], v[104:107], v[144:147], v[56:59]\n"
"buffer_load_dwordx4 v[188:191], %[l16], %[srdo], s63 offen sc1\n"
"ds_read_b64_tr_b16 v[100:101], v246 offset:0\n"
"v_mfma_f32_16x16x32_bf16 v[20:23], v[104:107], v[128:131], v[20:23]\n"
"ds_read_b64_tr_b16 v[102:103], v246 offset:512\n"
"s_waitcnt lgkmcnt(15)\n"
"v_mfma_f32_16x16x32_bf16 v[60:63], v[108:111], v[144:147], v[60:63]\n"
"ds_read_b64_tr_b16 v[104:105], v244 offset:2048\n"
"v_mfma_f32_16x16x32_bf16 v[24:27], v[108:111], v[128:131], v[24:27]\n"
"ds_read_b64_tr_b16 v[106:107], v244 offset:10240\n"
"s_waitcnt lgkmcnt(12)\n"
"v_mfma_f32_16x16x32_bf16 v[64:67], v[112:115], v[144:147], v[64:67]\n"
"ds_read_b64_tr_b16 v[108:109], v245 offset:2048\n"
"v_mfma_f32_16x16x32_bf16 v[28:31], v[112:115], v[128:131], v[28:31]\n"
"ds_read_b64_tr_b16 v[110:111], v245 offset:10240\n"
"s_waitcnt lgkmcnt(12)\n"
"v_mfma_f32_16x16x32_bf16 v[68:71], v[116:119], v[144:147], v[68:71]\n"
"ds_read_b64_tr_b16 v[112:113], v244 offset:2304\n"
"v_mfma_f32_16x16x32_bf16 v[32:35], v[116:119], v[128:131], v[32:35]\n"
"ds_read_b64_tr_b16 v[114:115], v244 offset:10496\n"
"v_mfma_f32_16x16x32_bf16 v[88:91], v[116:119], v[160:163], v[88:91]\n"
"s_waitcnt lgkmcnt(12)\n"
"v_mfma_f32_16x16x32_bf16 v[72:75], v[120:123], v[144:147], v[72:75]\n"
"ds_read_b64_tr_b16 v[116:117], v245 offset:2304\n"
"v_mfma_f32_16x16x32_bf16 v[36:39], v[120:123], v[128:131], v[36:39]\n"
"ds_read_b64_tr_b16 v[118:119], v245 offset:10496\n"
"v_mfma_f32_16x16x32_bf16 v[92:95], v[120:123], v[160:163], v[92:95]\n"
"s_waitcnt lgkmcnt(12)\n"
"v_mfma_f32_16x16x32_bf16 v[76:79], v[124:127], v[144:147], v[76:79]\n"
"ds_read_b64_tr_b16 v[120:121], v244 offset:2560\n"
"v_mfma_f32_16x16x32_bf16 v[40:43], v[124:127], v[128:131], v[40:43]\n"
"ds_read_b64_tr_b16 v[122:123], v244 offset:10752\n"
"s_waitcnt lgkmcnt(12)\n"
"v_mfma_f32_16x16x32_bf16 v[80:83], v[224:227], v[144:147], v[80:83]\n"
"ds_read_b64_tr_b16 v[124:125], v245 offset:2560\n"
"v_mfma_f32_16x16x32_bf16 v[44:47], v[224:227], v[128:131], v[44:47]\n"
"ds_read_b64_tr_b16 v[126:127], v245 offset:10752\n"
"s_waitcnt lgkmcnt(12)\n"
"v_mfma_f32_16x16x32_bf16 v[84:87], v[100:103], v[144:147], v[84:87]\n"
"ds_read_b64_tr_b16 v[224:225], v244 offset:2816\n"
"v_mfma_f32_16x16x32_bf16 v[48:51], v[100:103], v[128:131], v[48:51]\n"
"ds_read_b64_tr_b16 v[226:227], v244 offset:11008\n"
"s_waitcnt vmcnt(23) lgkmcnt(12)\n"
"v_mfma_f32_16x16x32_bf16 v[52:55], v[104:107], v[148:151], v[52:55]\n"
"ds_read_b64_tr_b16 v[100:101], v245 offset:2816\n"
"s_waitcnt vmcnt(22)\n"
"v_mfma_f32_16x16x32_bf16 v[16:19], v[104:107], v[132:135], v[16:19]\n"
"ds_read_b64_tr_b16 v[102:103], v245 offset:11008\n"
"s_waitcnt lgkmcnt(12)\n"
"v_mfma_f32_16x16x32_bf16 v[56:59], v[108:111], v[148:151], v[56:59]\n"
"ds_read_b64_tr_b16 v[104:105], v246 offset:1024\n"
"v_mfma_f32_16x16x32_bf16 v[20:23], v[108:111], v[132:135], v[20:23]\n"
"ds_read_b64_tr_b16 v[106:107], v246 offset:1536\n"
"s_waitcnt lgkmcnt(12)\n"
"v_mfma_f32_16x16x32_bf16 v[60:63], v[112:115], v[148:151], v[60:63]\n"
"v_mfma_f32_16x16x32_bf16 v[24:27], v[112:115], v[132:135], v[24:27]\n"
"s_waitcnt lgkmcnt(10)\n"
"v_mfma_f32_16x16x32_bf16 v[64:67], v[116:119], v[148:151], v[64:67]\n"
"v_mfma_f32_16x16x32_bf16 v[28:31], v[116:119], v[132:135], v[28:31]\n"
"s_waitcnt lgkmcnt(8)\n"
"v_mfma_f32_16x16x32_bf16 v[68:71], v[120:123], v[148:151], v[68:71]\n"
"s_barrier\n"
"ds_read_b64_tr_b16 v[108:109], v244 offset:16384\n"
"ds_read_b64_tr_b16 v[112:113], v245 offset:16384\n"
"ds_read_b64_tr_b16 v[116:117], v244 offset:16640\n"
"v_mfma_f32_16x16x32_bf16 v[32:35], v[120:123], v[132:135], v[32:35]\n"
"ds_read_b64_tr_b16 v[110:111], v244 offset:24576\n"
"ds_read_b64_tr_b16 v[114:115], v245 offset:24576\n"
"ds_read_b64_tr_b16 v[118:119], v244 offset:24832\n"
"v_mfma_f32_16x16x32_bf16 v[88:91], v[120:123], v[164:167], v[88:91]\n"
"s_waitcnt lgkmcnt(12)\n"
"v_mfma_f32_16x16x32_bf16 v[72:75], v[124:127], v[148:151], v[72:75]\n"
"ds_read_b64_tr_b16 v[120:121], v245 offset:16640\n"
"v_mfma_f32_16x16x32_bf16 v[36:39], v[124:127], v[132:135], v[36:39]\n"
"ds_read_b64_tr_b16 v[122:123], v245 offset:24832\n"
"v_mfma_f32_16x16x32_bf16 v[92:95], v[124:127], v[164:167], v[92:95]\n"
"s_waitcnt lgkmcnt(12)\n"
"v_mfma_f32_16x16x32_bf16 v[76:79], v[224:227], v[148:151], v[76:79]\n"
"ds_read_b64_tr_b16 v[124:125], v244 offset:16896\n"
"v_mfma_f32_16x16x32_bf16 v[40:43], v[224:227], v[132:135], v[40:43]\n"
"ds_read_b64_tr_b16 v[126:127], v244 offset:25088\n"
"s_waitcnt lgkmcnt(12)\n"
"v_mfma_f32_16x16x32_bf16 v[80:83], v[100:103], v[148:151], v[80:83]\n"
"ds_read_b64_tr_b16 v[224:225], v245 offset:16896\n"
"v_mfma_f32_16x16x32_bf16 v[44:47], v[100:103], v[132:135], v[44:47]\n"
"ds_read_b64_tr_b16 v[226:227], v245 offset:25088\n"
"s_waitcnt lgkmcnt(12)\n"
"v_mfma_f32_16x16x32_bf16 v[84:87], v[104:107], v[148:151], v[84:87]\n"
"ds_read_b64_tr_b16 v[100:101], v244 offset:17152\n"
"v_mfma_f32_16x16x32_bf16 v[48:51], v[104:107], v[132:135], v[48:51]\n"
"ds_read_b64_tr_b16 v[102:103], v244 offset:25344\n"
"s_waitcnt vmcnt(21) lgkmcnt(10)\n"
"v_mfma_f32_16x16x32_bf16 v[52:55], v[108:111], v[152:155], v[52:55]\n"
"ds_read_b64_tr_b16 v[104:105], v245 offset:17152\n"
"s_waitcnt vmcnt(20)\n"
"v_mfma_f32_16x16x32_bf16 v[16:19], v[108:111], v[136:139], v[16:19]\n"
"ds_read_b64_tr_b16 v[106:107], v245 offset:25344\n"
"s_waitcnt lgkmcnt(11)\n"
"v_mfma_f32_16x16x32_bf16 v[56:59], v[112:115], v[152:155], v[56:59]\n"
"ds_read_b64_tr_b16 v[108:109], v246 offset:2048\n"
"v_mfma_f32_16x16x32_bf16 v[20:23], v[112:115], v[136:139], v[20:23]\n"
"ds_read_b64_tr_b16 v[110:111], v246 offset:2560\n"
"s_waitcnt lgkmcnt(12)\n"
"v_mfma_f32_16x16x32_bf16 v[60:63], v[116:119], v[152:155], v[60:63]\n"
"ds_read_b64_tr_b16 v[112:113], v244 offset:18432\n"
"v_mfma_f32_16x16x32_bf16 v[24:27], v[116:119], v[136:139], v[24:27]\n"
"ds_read_b64_tr_b16 v[114:115], v244 offset:26624\n"
"s_waitcnt lgkmcnt(12)\n"
"v_mfma_f32_16x16x32_bf16 v[64:67], v[120:123], v[152:155], v[64:67]\n"
"ds_read_b64_tr_b16 v[116:117], v245 offset:18432\n"
"v_mfma_f32_16x16x32_bf16 v[28:31], v[120:123], v[136:139], v[28:31]\n"
"ds_read_b64_tr_b16 v[118:119], v245 offset:26624\n"
"s_waitcnt lgkmcnt(12)\n"
"v_mfma_f32_16x16x32_bf16 v[68:71], v[124:127], v[152:155], v[68:71]\n"
"ds_read_b64_tr_b16 v[120:121], v244 offset:18688\n"
"v_mfma_f32_16x16x32_bf16 v[32:35], v[124:127], v[136:139], v[32:35]\n"
"ds_read_b64_tr_b16 v[122:123], v244 offset:26880\n"
"v_mfma_f32_16x16x32_bf16 v[88:91], v[124:127], v[168:171], v[88:91]\n"
"s_waitcnt lgkmcnt(12)\n"
"v_mfma_f32_16x16x32_bf16 v[72:75], v[224:227], v[152:155], v[72:75]\n"
"ds_read_b64_tr_b16 v[124:125], v245 offset:18688\n"
"v_mfma_f32_16x16x32_bf16 v[36:39], v[224:227], v[136:139], v[36:39]\n"
"ds_read_b64_tr_b16 v[126:127], v245 offset:26880\n"
"v_mfma_f32_16x16x32_bf16 v[92:95], v[224:227], v[168:171], v[92:95]\n"
"s_waitcnt lgkmcnt(12)\n"
"v_mfma_f32_16x16x32_bf16 v[76:79], v[100:103], v[152:155], v[76:79]\n"
"ds_read_b64_tr_b16 v[224:225], v244 offset:18944\n"
"v_mfma_f32_16x16x32_bf16 v[40:43], v[100:103], v[136:139], v[40:43]\n"
"ds_read_b64_tr_b16 v[226:227], v244 offset:27136\n"
"s_waitcnt lgkmcnt(12)\n"
"v_mfma_f32_16x16x32_bf16 v[80:83], v[104:107], v[152:155], v[80:83]\n"
"ds_read_b64_tr_b16 v[100:101], v245 offset:18944\n"
"v_mfma_f32_16x16x32_bf16 v[44:47], v[104:107], v[136:139], v[44:47]\n"
"ds_read_b64_tr_b16 v[102:103], v245 offset:27136\n"
"s_waitcnt lgkmcnt(12)\n"
"v_mfma_f32_16x16x32_bf16 v[84:87], v[108:111], v[152:155], v[84:87]\n"
"ds_read_b64_tr_b16 v[104:105], v244 offset:19200\n"
"v_mfma_f32_16x16x32_bf16 v[48:51], v[108:111], v[136:139], v[48:51]\n"
"ds_read_b64_tr_b16 v[106:107], v244 offset:27392\n"
"s_waitcnt vmcnt(19) lgkmcnt(12)\n"
"v_mfma_f32_16x16x32_bf16 v[52:55], v[112:115], v[156:159], v[52:55]\n"
"ds_read_b64_tr_b16 v[108:109], v245 offset:19200\n"
"s_waitcnt vmcnt(18)\n"
"v_mfma_f32_16x16x32_bf16 v[16:19], v[112:115], v[140:143], v[16:19]\n"
"ds_read_b64_tr_b16 v[110:111], v245 offset:27392\n"
"s_waitcnt lgkmcnt(12)\n"
"v_mfma_f32_16x16x32_bf16 v[56:59], v[116:119], v[156:159], v[56:59]\n"
"ds_read_b64_tr_b16 v[112:113], v246 offset:3072\n"
"v_mfma_f32_16x16x32_bf16 v[20:23], v[116:119], v[140:143], v[20:23]\n"
"ds_read_b64_tr_b16 v[114:115], v246 offset:3584\n"
"s_waitcnt lgkmcnt(12)\n"
"v_mfma_f32_16x16x32_bf16 v[60:63], v[120:123], v[156:159], v[60:63]\n"
"v_mfma_f32_16x16x32_bf16 v[24:27], v[120:123], v[140:143], v[24:27]\n"
"s_waitcnt vmcnt(7)\n"
"ds_write_b128 v247, v[240:243] offset:2048\n"
"s_waitcnt lgkmcnt(11)\n"
"v_mfma_f32_16x16x32_bf16 v[64:67], v[124:127], v[156:159], v[64:67]\n"
"v_mfma_f32_16x16x32_bf16 v[28:31], v[124:127], v[140:143], v[28:31]\n"
"s_waitcnt lgkmcnt(9)\n"
"v_mfma_f32_16x16x32_bf16 v[68:71], v[224:227], v[156:159], v[68:71]\n"
"s_waitcnt lgkmcnt(0)\n"
"s_barrier\n"
"ds_read_b64_tr_b16 v[116:117], v244 offset:33024\n"
"ds_read_b64_tr_b16 v[120:121], v245 offset:33024\n"
"ds_read_b64_tr_b16 v[124:125], v244 offset:33280\n"
"v_mfma_f32_16x16x32_bf16 v[32:35], v[224:227], v[140:143], v[32:35]\n"
"ds_read_b128 v[208:211], v247 offset:0\n"
"ds_read_b64_tr_b16 v[118:119], v244 offset:41216\n"
"ds_read_b64_tr_b16 v[122:123], v245 offset:41216\n"
"ds_read_b64_tr_b16 v[126:127], v244 offset:41472\n"
"v_mfma_f32_16x16x32_bf16 v[88:91], v[224:227], v[172:175], v[88:91]\n"
"ds_read_b128 v[212:215], v247 offset:1024\n"
"v_mfma_f32_16x16x32_bf16 v[72:75], v[100:103], v[156:159], v[72:75]\n"
"ds_read_b128 v[216:219], v247 offset:2048\n"
"ds_read_b64_tr_b16 v[224:225], v245 offset:33280\n"
"v_mfma_f32_16x16x32_bf16 v[36:39], v[100:103], v[140:143], v[36:39]\n"
"ds_read_b128 v[220:223], v247 offset:3072\n"
"ds_read_b64_tr_b16 v[226:227], v245 offset:41472\n"
"v_mfma_f32_16x16x32_bf16 v[92:95], v[100:103], v[172:175], v[92:95]\n"
"v_mfma_f32_16x16x32_bf16 v[76:79], v[104:107], v[156:159], v[76:79]\n"
"ds_read_b64_tr_b16 v[100:101], v244 offset:33536\n"
"v_mfma_f32_16x16x32_bf16 v[40:43], v[104:107], v[140:143], v[40:43]\n"
"ds_read_b64_tr_b16 v[102:103], v244 offset:41728\n"
"v_mfma_f32_16x16x32_bf16 v[80:83], v[108:111], v[156:159], v[80:83]\n"
"ds_read_b64_tr_b16 v[104:105], v245 offset:33536\n"
"v_mfma_f32_16x16x32_bf16 v[44:47], v[108:111], v[140:143], v[44:47]\n"
"ds_read_b64_tr_b16 v[106:107], v245 offset:41728\n"
"v_mfma_f32_16x16x32_bf16 v[84:87], v[112:115], v[156:159], v[84:87]\n"
"ds_read_b64_tr_b16 v[108:109], v246 offset:4096\n"
"v_mfma_f32_16x16x32_bf16 v[48:51], v[112:115], v[140:143], v[48:51]\n"
"ds_read_b64_tr_b16 v[110:111], v246 offset:4608\n"
"s_waitcnt lgkmcnt(13)\n"
"v_mfma_f32_16x16x32_bf16 v[60:63], v[116:119], v[192:195], v[60:63]\n"
"ds_read_b64_tr_b16 v[112:113], v244 offset:35072\n"
"v_mfma_f32_16x16x32_bf16 v[24:27], v[116:119], v[176:179], v[24:27]\n"
"ds_read_b64_tr_b16 v[114:115], v244 offset:43264\n"
"s_waitcnt lgkmcnt(14)\n"
"v_mfma_f32_16x16x32_bf16 v[64:67], v[120:123], v[192:195], v[64:67]\n"
"ds_read_b64_tr_b16 v[116:117], v245 offset:35072\n"
"s_add_u32 s68, %[hb], 16384\n"
"v_add_u32_e64 v229, s68, %[l16]\n"
"v_xor_b32 v230, 65536, v229\n"
"v_mfma_f32_16x16x32_bf16 v[28:31], v[120:123], v[176:179], v[28:31]\n"
"ds_read_b64_tr_b16 v[118:119], v245 offset:43264\n"
"v_xor_b32 v234, 8192, v247\n"
"s_lshr_b32 s64, %[kq64], 6\n"
"s_lshr_b32 s65, %[hb], 6\n"
"s_waitcnt lgkmcnt(15)\n"
"v_mfma_f32_16x16x32_bf16 v[68:71], v[124:127], v[192:195], v[68:71]\n"
"ds_read_b64_tr_b16 v[120:121], v244 offset:35328\n"
"s_add_u32 s60, s64, 1\n"
"s_and_b32 s60, s60, 3\n"
"s_lshl_b32 s60, s60, 16\n"
"v_mfma_f32_16x16x32_bf16 v[32:35], v[124:127], v[176:179], v[32:35]\n"
"ds_read_b64_tr_b16 v[122:123], v244 offset:43520\n"
"s_add_u32 s60, s60, %[pbase]\n"
"s_add_u32 s60, s60, 0\n"
"s_add_u32 s61, s64, 2\n"
"v_mfma_f32_16x16x32_bf16 v[88:91], v[124:127], v[208:211], v[88:91]\n"
"s_and_b32 s61, s61, 3\n"
"s_lshl_b32 s61, s61, 16\n"
"s_add_u32 s61, s61, %[pbase]\n"
"s_waitcnt lgkmcnt(12)\n"
"v_mfma_f32_16x16x32_bf16 v[72:75], v[224:227], v[192:195], v[72:75]\n"
"ds_read_b64_tr_b16 v[124:125], v245 offset:35328\n"
"s_add_u32 s61, s61, 21504\n"
"s_add_u32 s62, s64, 3\n"
"s_and_b32 s62, s62, 3\n"
"v_mfma_f32_16x16x32_bf16 v[36:39], v[224:227], v[176:179], v[36:39]\n"
"ds_read_b64_tr_b16 v[126:127], v245 offset:43520\n"
"s_lshl_b32 s62, s62, 16\n"
"s_add_u32 s62, s62, %[pbase]\n"
"s_add_u32 s62, s62, 43008\n"
"v_mfma_f32_16x16x32_bf16 v[92:95], v[224:227], v[208:211], v[92:95]\n"
"s_lshl_b32 s63, s64, 16\n"
"s_add_u32 s63, s63, %[pbase]\n"
"s_lshl_b32 s66, s65, 1\n"
"s_waitcnt lgkmcnt(12)\n"
"v_mfma_f32_16x16x32_bf16 v[76:79], v[100:103], v[192:195], v[76:79]\n"
"ds_read_b64_tr_b16 v[224:225], v244 offset:35584\n"
"s_add_u32 s66, s66, 8192\n"
"v_add_u32_e64 v232, s66, %[l16]\n"
"s_add_u32 s66, s65, 16384\n"
"v_mfma_f32_16x16x32_bf16 v[40:43], v[100:103], v[176:179], v[40:43]\n"
"ds_read_b64_tr_b16 v[226:227], v244 offset:43776\n"
"v_add_u32 v228, s66, %[l16]\n"
"s_sub_u32 s67, 5, s64\n"
"s_and_b32 s67, s67, 3\n"
"s_waitcnt lgkmcnt(12)\n"
"v_mfma_f32_16x16x32_bf16 v[80:83], v[104:107], v[192:195], v[80:83]\n"
"ds_read_b64_tr_b16 v[100:101], v245 offset:35584\n"
"s_mul_i32 s67, s67, 21504\n"
"s_add_u32 s67, s67, %[pbase]\n"
"s_add_u32 s67, s67, 131072\n"
"v_mfma_f32_16x16x32_bf16 v[44:47], v[104:107], v[176:179], v[44:47]\n"
"ds_read_b64_tr_b16 v[102:103], v245 offset:43776\n"
"v_lshrrev_b32 v231, 4, %[l16]\n"
"v_and_b32_e64 v231, 15, v231\n"
"s_movk_i32 s68, 2304\n"
"s_waitcnt lgkmcnt(12)\n"
"v_mfma_f32_16x16x32_bf16 v[84:87], v[108:111], v[192:195], v[84:87]\n"
"ds_read_b64_tr_b16 v[104:105], v246 offset:5120\n"
"v_mul_u32_u24 v231, s68, v231\n"
"v_lshrrev_b32_e64 v233, 8, %[l16]\n"
"v_lshl_add_u32 v231, v233, 3, v231\n"
"v_mfma_f32_16x16x32_bf16 v[48:51], v[108:111], v[176:179], v[48:51]\n"
"ds_read_b64_tr_b16 v[106:107], v246 offset:5632\n"
"s_waitcnt lgkmcnt(12)\n"
"v_mfma_f32_16x16x32_bf16 v[60:63], v[112:115], v[196:199], v[60:63]\n"
"v_mfma_f32_16x16x32_bf16 v[24:27], v[112:115], v[180:183], v[24:27]\n"
"s_waitcnt lgkmcnt(10)\n"
"v_mfma_f32_16x16x32_bf16 v[64:67], v[116:119], v[196:199], v[64:67]\n"
"v_mfma_f32_16x16x32_bf16 v[28:31], v[116:119], v[180:183], v[28:31]\n"
"s_waitcnt lgkmcnt(8)\n"
"v_mfma_f32_16x16x32_bf16 v[68:71], v[120:123], v[196:199], v[68:71]\n"
"s_waitcnt vmcnt(2) lgkmcnt(0)\n"
"s_barrier\n"
"ds_read_b64_tr_b16 v[108:109], v244 offset:49408\n"
"ds_read_b64_tr_b16 v[112:113], v245 offset:49408\n"
"ds_read_b64_tr_b16 v[116:117], v244 offset:51456\n"
"v_mfma_f32_16x16x32_bf16 v[32:35], v[120:123], v[180:183], v[32:35]\n"
"ds_read_b64_tr_b16 v[110:111], v244 offset:57600\n"
"ds_read_b64_tr_b16 v[114:115], v245 offset:57600\n"
"ds_read_b64_tr_b16 v[118:119], v244 offset:59648\n"
"v_mfma_f32_16x16x32_bf16 v[88:91], v[120:123], v[212:215], v[88:91]\n"
"v_mfma_f32_16x16x32_bf16 v[72:75], v[124:127], v[196:199], v[72:75]\n"
"ds_read_b64_tr_b16 v[120:121], v245 offset:51456\n"
"v_mfma_f32_16x16x32_bf16 v[36:39], v[124:127], v[180:183], v[36:39]\n"
"ds_read_b64_tr_b16 v[122:123], v245 offset:59648\n"
"v_mfma_f32_16x16x32_bf16 v[92:95], v[124:127], v[212:215], v[92:95]\n"
"v_mfma_f32_16x16x32_bf16 v[76:79], v[224:227], v[196:199], v[76:79]\n"
"ds_read_b64_tr_b16 v[124:125], v244 offset:49664\n"
"v_mfma_f32_16x16x32_bf16 v[40:43], v[224:227], v[180:183], v[40:43]\n"
"ds_read_b64_tr_b16 v[126:127], v244 offset:57856\n"
"v_mfma_f32_16x16x32_bf16 v[80:83], v[100:103], v[196:199], v[80:83]\n"
"ds_read_b64_tr_b16 v[224:225], v245 offset:49664\n"
"v_mfma_f32_16x16x32_bf16 v[44:47], v[100:103], v[180:183], v[44:47]\n"
"ds_read_b64_tr_b16 v[226:227], v245 offset:57856\n"
"v_mfma_f32_16x16x32_bf16 v[84:87], v[104:107], v[196:199], v[84:87]\n"
"ds_read_b64_tr_b16 v[100:101], v244 offset:51712\n"
"v_mfma_f32_16x16x32_bf16 v[48:51], v[104:107], v[180:183], v[48:51]\n"
"ds_read_b64_tr_b16 v[102:103], v244 offset:59904\n"
"s_waitcnt lgkmcnt(10)\n"
"v_mfma_f32_16x16x32_bf16 v[60:63], v[108:111], v[200:203], v[60:63]\n"
"ds_read_b64_tr_b16 v[104:105], v245 offset:51712\n"
"v_mfma_f32_16x16x32_bf16 v[24:27], v[108:111], v[184:187], v[24:27]\n"
"ds_read_b64_tr_b16 v[106:107], v245 offset:59904\n"
"s_waitcnt lgkmcnt(11)\n"
"v_mfma_f32_16x16x32_bf16 v[64:67], v[112:115], v[200:203], v[64:67]\n"
"ds_read_b64_tr_b16 v[108:109], v244 offset:49920\n"
"v_mfma_f32_16x16x32_bf16 v[28:31], v[112:115], v[184:187], v[28:31]\n"
"ds_read_b64_tr_b16 v[110:111], v244 offset:58112\n"
"s_waitcnt vmcnt(1) lgkmcnt(12)\n"
"v_mfma_f32_16x16x32_bf16 v[60:63], v[116:119], v[204:207], v[60:63]\n"
"ds_read_b64_tr_b16 v[112:113], v245 offset:49920\n"
"s_waitcnt vmcnt(0)\n"
"v_mfma_f32_16x16x32_bf16 v[24:27], v[116:119], v[188:191], v[24:27]\n"
"ds_read_b64_tr_b16 v[114:115], v245 offset:58112\n"
"s_waitcnt lgkmcnt(12)\n"
"v_mfma_f32_16x16x32_bf16 v[64:67], v[120:123], v[204:207], v[64:67]\n"
"ds_read_b64_tr_b16 v[116:117], v246 offset:6144\n"
"v_mfma_f32_16x16x32_bf16 v[28:31], v[120:123], v[188:191], v[28:31]\n"
"ds_read_b64_tr_b16 v[118:119], v246 offset:6656\n"
"s_waitcnt lgkmcnt(12)\n"
"v_mfma_f32_16x16x32_bf16 v[68:71], v[124:127], v[200:203], v[68:71]\n"
"ds_read_b64_tr_b16 v[120:121], v244 offset:51968\n"
"ds_write_b128 v229, v[60:63] offset:0\n"
"v_mfma_f32_16x16x32_bf16 v[32:35], v[124:127], v[184:187], v[32:35]\n"
"ds_read_b64_tr_b16 v[122:123], v244 offset:60160\n"
"v_mfma_f32_16x16x32_bf16 v[88:91], v[124:127], v[216:219], v[88:91]\n"
"ds_write_b128 v229, v[64:67] offset:1024\n"
"s_waitcnt lgkmcnt(14)\n"
"v_mfma_f32_16x16x32_bf16 v[72:75], v[224:227], v[200:203], v[72:75]\n"
"ds_read_b64_tr_b16 v[124:125], v245 offset:51968\n"
"v_mfma_f32_16x16x32_bf16 v[36:39], v[224:227], v[184:187], v[36:39]\n"
"ds_read_b64_tr_b16 v[126:127], v245 offset:60160\n"
"v_mfma_f32_16x16x32_bf16 v[92:95], v[224:227], v[216:219], v[92:95]\n"
"s_waitcnt lgkmcnt(14)\n"
"v_mfma_f32_16x16x32_bf16 v[68:71], v[100:103], v[204:207], v[68:71]\n"
"ds_read_b64_tr_b16 v[224:225], v246 offset:7168\n"
"v_mfma_f32_16x16x32_bf16 v[32:35], v[100:103], v[188:191], v[32:35]\n"
"ds_read_b64_tr_b16 v[226:227], v246 offset:7680\n"
"v_mfma_f32_16x16x32_bf16 v[88:91], v[100:103], v[220:223], v[88:91]\n"
"s_waitcnt lgkmcnt(14)\n"
"v_mfma_f32_16x16x32_bf16 v[72:75], v[104:107], v[204:207], v[72:75]\n"
"ds_read_b64_tr_b16 v[100:101], v244 offset:32768\n"
"v_mfma_f32_16x16x32_bf16 v[36:39], v[104:107], v[188:191], v[36:39]\n"
"ds_read_b64_tr_b16 v[102:103], v244 offset:40960\n"
"ds_write_b128 v229, v[68:71] offset:2048\n"
"v_mfma_f32_16x16x32_bf16 v[92:95], v[104:107], v[220:223], v[92:95]\n"
"s_waitcnt lgkmcnt(15)\n"
"v_mfma_f32_16x16x32_bf16 v[76:79], v[108:111], v[200:203], v[76:79]\n"
"ds_read_b64_tr_b16 v[104:105], v245 offset:32768\n"
"v_mfma_f32_16x16x32_bf16 v[40:43], v[108:111], v[184:187], v[40:43]\n"
"ds_read_b64_tr_b16 v[106:107], v245 offset:40960\n"
"ds_write_b128 v229, v[72:75] offset:3072\n"
"s_waitcnt lgkmcnt(15)\n"
"v_mfma_f32_16x16x32_bf16 v[80:83], v[112:115], v[200:203], v[80:83]\n"
"ds_read_b64_tr_b16 v[108:109], v244 offset:34816\n"
"v_mfma_f32_16x16x32_bf16 v[44:47], v[112:115], v[184:187], v[44:47]\n"
"ds_read_b64_tr_b16 v[110:111], v244 offset:43008\n"
"ds_write_b128 v229, v[92:95] offset:7168\n"
"s_waitcnt lgkmcnt(15)\n"
"v_mfma_f32_16x16x32_bf16 v[84:87], v[116:119], v[200:203], v[84:87]\n"
"ds_read_b64_tr_b16 v[112:113], v245 offset:34816\n"
"v_mfma_f32_16x16x32_bf16 v[48:51], v[116:119], v[184:187], v[48:51]\n"
"ds_read_b64_tr_b16 v[114:115], v245 offset:43008\n"
"s_waitcnt lgkmcnt(15)\n"
"v_mfma_f32_16x16x32_bf16 v[76:79], v[120:123], v[204:207], v[76:79]\n"
"ds_read_b64_tr_b16 v[116:117], v244 offset:49152\n"
"s_waitcnt lgkmcnt(3)\n"
"s_barrier\n"
"ds_read_b128 v[128:131], v230 offset:1024\n"
"ds_read_b128 v[132:135], v230 offset:0\n"
"v_mfma_f32_16x16x32_bf16 v[40:43], v[120:123], v[188:191], v[40:43]\n"
"ds_read_b64_tr_b16 v[118:119], v244 offset:57344\n"
"ds_read_b128 v[136:139], v230 offset:3072\n"
"ds_read_b128 v[140:143], v230 offset:2048\n"
"v_mfma_f32_16x16x32_bf16 v[80:83], v[124:127], v[204:207], v[80:83]\n"
"ds_read_b64_tr_b16 v[120:121], v245 offset:49152\n"
"ds_read_b128 v[144:147], v230 offset:7168\n"
"v_mfma_f32_16x16x32_bf16 v[44:47], v[124:127], v[188:191], v[44:47]\n"
"ds_read_b64_tr_b16 v[122:123], v245 offset:57344\n"
"s_waitcnt lgkmcnt(7)\n"
"v_pk_add_f32 v[24:25], v[24:25], v[128:129]\n"
"v_pk_add_f32 v[26:27], v[26:27], v[130:131]\n"
"buffer_store_dwordx4 v[24:27], v232, s[80:83], s60 offen sc1\n"
"v_mfma_f32_16x16x32_bf16 v[84:87], v[224:227], v[204:207], v[84:87]\n"
"ds_read_b64_tr_b16 v[124:125], v244 offset:51200\n"
"s_waitcnt lgkmcnt(7)\n"
"v_pk_add_f32 v[28:29], v[28:29], v[132:133]\n"
"v_pk_add_f32 v[30:31], v[30:31], v[134:135]\n"
"buffer_store_dwordx4 v[28:31], v232, s[80:83], s60 offen offset:1024 sc1\n"
"ds_write_b128 v229, v[76:79] offset:4096\n"
"v_mfma_f32_16x16x32_bf16 v[48:51], v[224:227], v[188:191], v[48:51]\n"
"ds_read_b64_tr_b16 v[126:127], v244 offset:59392\n"
"s_waitcnt lgkmcnt(7)\n"
"v_pk_add_f32 v[32:33], v[32:33], v[136:137]\n"
"v_pk_add_f32 v[34:35], v[34:35], v[138:139]\n"
"buffer_store_dwordx4 v[32:35], v232, s[80:83], s61 offen sc1\n"
"v_mfma_f32_16x16x32_bf16 v[52:55], v[100:103], v[192:195], v[52:55]\n"
"ds_read_b64_tr_b16 v[224:225], v245 offset:51200\n"
"s_waitcnt lgkmcnt(7)\n"
"v_pk_add_f32 v[36:37], v[36:37], v[140:141]\n"
"v_pk_add_f32 v[38:39], v[38:39], v[142:143]\n"
"buffer_store_dwordx4 v[36:39], v232, s[80:83], s61 offen offset:1024 sc1\n"
"ds_write_b128 v229, v[80:83] offset:5120\n"
"v_mfma_f32_16x16x32_bf16 v[16:19], v[100:103], v[176:179], v[16:19]\n"
"ds_read_b64_tr_b16 v[226:227], v245 offset:59392\n"
"s_waitcnt lgkmcnt(7)\n"
"v_pk_add_f32 v[88:89], v[88:89], v[144:145]\n"
"v_pk_add_f32 v[90:91], v[90:91], v[146:147]\n"
"buffer_store_dwordx4 v[88:91], v228, s[80:83], s61 offen offset:2048 sc1\n"
"v_mfma_f32_16x16x32_bf16 v[56:59], v[104:107], v[192:195], v[56:59]\n"
"ds_write_b128 v229, v[84:87] offset:6144\n"
"v_mfma_f32_16x16x32_bf16 v[20:23], v[104:107], v[176:179], v[20:23]\n"
"v_mfma_f32_16x16x32_bf16 v[52:55], v[108:111], v[196:199], v[52:55]\n"
"v_mfma_f32_16x16x32_bf16 v[16:19], v[108:111], v[180:183], v[16:19]\n"
"v_mfma_f32_16x16x32_bf16 v[56:59], v[112:115], v[196:199], v[56:59]\n"
"s_waitcnt lgkmcnt(0)\n"
"s_barrier\n"
"ds_read_b128 v[148:151], v230 offset:5120\n"
"ds_read_b128 v[152:155], v230 offset:4096\n"
"v_mfma_f32_16x16x32_bf16 v[20:23], v[112:115], v[180:183], v[20:23]\n"
"ds_read_b128 v[156:159], v230 offset:6144\n"
"v_mfma_f32_16x16x32_bf16 v[52:55], v[116:119], v[200:203], v[52:55]\n"
"v_mfma_f32_16x16x32_bf16 v[16:19], v[116:119], v[184:187], v[16:19]\n"
"s_waitcnt lgkmcnt(2)\n"
"v_pk_add_f32 v[40:41], v[40:41], v[148:149]\n"
"v_pk_add_f32 v[42:43], v[42:43], v[150:151]\n"
"buffer_store_dwordx4 v[40:43], v232, s[80:83], s62 offen sc1\n"
"v_mfma_f32_16x16x32_bf16 v[56:59], v[120:123], v[200:203], v[56:59]\n"
"s_waitcnt lgkmcnt(1)\n"
"v_pk_add_f32 v[44:45], v[44:45], v[152:153]\n"
"v_pk_add_f32 v[46:47], v[46:47], v[154:155]\n"
"buffer_store_dwordx4 v[44:47], v232, s[80:83], s62 offen offset:1024 sc1\n"
"v_mfma_f32_16x16x32_bf16 v[20:23], v[120:123], v[184:187], v[20:23]\n"
"s_waitcnt lgkmcnt(0)\n"
"v_pk_add_f32 v[48:49], v[48:49], v[156:157]\n"
"v_pk_add_f32 v[50:51], v[50:51], v[158:159]\n"
"s_cmp_eq_u32 s64, 2\n"
"s_cbranch_scc1 LX2_%=\n"
"buffer_store_dwordx4 v[48:51], v228, s[80:83], s67 offen sc1\n"
"LX2_%=:\n"
"v_mfma_f32_16x16x32_bf16 v[52:55], v[124:127], v[204:207], v[52:55]\n"
"v_mfma_f32_16x16x32_bf16 v[16:19], v[124:127], v[188:191], v[16:19]\n"
"v_mfma_f32_16x16x32_bf16 v[56:59], v[224:227], v[204:207], v[56:59]\n"
"v_mfma_f32_16x16x32_bf16 v[20:23], v[224:227], v[188:191], v[20:23]\n"
"s_nop 4\n"
"ds_write_b128 v229, v[52:55] offset:0\n"
"s_nop 0\n"
"ds_write_b128 v229, v[56:59] offset:1024\n"
"s_mul_i32 s60, s65, 36\n"
"s_add_u32 s60, s60, 147456\n"
"s_add_u32 s61, %[nb256], %[kq64]\n"
"s_lshr_b32 s62, %[nb256], 3\n"
"s_add_u32 s62, s62, 1024\n"
"s_lshr_b32 s65, s65, 5\n"
"s_waitcnt vmcnt(0) lgkmcnt(0)\n"
"s_barrier\n"
"ds_read_b128 v[192:195], v230 offset:1024\n"
"ds_read_b128 v[196:199], v230 offset:0\n"
"s_waitcnt lgkmcnt(1)\n"
"v_pk_add_f32 v[16:17], v[16:17], v[192:193]\n"
"v_pk_add_f32 v[18:19], v[18:19], v[194:195]\n"
"s_waitcnt lgkmcnt(0)\n"
"v_pk_add_f32 v[20:21], v[20:21], v[196:197]\n"
"v_pk_add_f32 v[22:23], v[22:23], v[198:199]\n"
"s_barrier\n"
"s_add_u32 s68, s63, 0\n"
"buffer_load_dwordx4 v[128:131], v232, s[80:83], s68 offen sc1\n"
"s_add_u32 s68, s63, 21504\n"
"buffer_load_dwordx4 v[132:135], v232, s[80:83], s68 offen sc1\n"
"s_add_u32 s68, s63, 43008\n"
"buffer_load_dwordx4 v[136:139], v232, s[80:83], s68 offen sc1\n"
"s_add_u32 s68, s63, 0\n"
"buffer_load_dwordx4 v[140:143], v232, s[80:83], s68 offen offset:1024 sc1\n"
"s_add_u32 s68, s63, 21504\n"
"buffer_load_dwordx4 v[144:147], v232, s[80:83], s68 offen offset:1024 sc1\n"
"s_add_u32 s68, s63, 43008\n"
"buffer_load_dwordx4 v[148:151], v232, s[80:83], s68 offen offset:1024 sc1\n"
"s_cmp_lg_u32 s64, 2\n"
"s_cbranch_scc1 LU2_%=\n"
"s_add_u32 s68, s63, 0\n"
"buffer_load_dwordx4 v[164:167], v228, s[80:83], s68 offen sc1\n"
"s_add_u32 s68, s63, 21504\n"
"buffer_load_dwordx4 v[168:171], v228, s[80:83], s68 offen sc1\n"
"s_add_u32 s68, s63, 43008\n"
"buffer_load_dwordx4 v[172:175], v228, s[80:83], s68 offen sc1\n"
"LU2_%=:\n"
"s_waitcnt vmcnt(0)\n"
"v_pk_add_f32 v[128:129], v[128:129], v[132:133]\n"
"v_pk_add_f32 v[130:131], v[130:131], v[134:135]\n"
"v_pk_add_f32 v[16:17], v[16:17], v[136:137]\n"
"v_pk_add_f32 v[18:19], v[18:19], v[138:139]\n"
"v_pk_add_f32 v[16:17], v[16:17], v[128:129]\n"
"v_pk_add_f32 v[18:19], v[18:19], v[130:131]\n"
"v_cvt_pk_bf16_f32 v16, v16, v17\n"
"v_cvt_pk_bf16_f32 v17, v18, v19\n"
"s_add_u32 s68, s60, s61\n"
"s_add_u32 s68, s68, s65\n"
"buffer_store_dwordx2 v[16:17], v231, s[84:87], s68 offen\n"
"v_pk_add_f32 v[140:141], v[140:141], v[144:145]\n"
"v_pk_add_f32 v[142:143], v[142:143], v[146:147]\n"
"v_pk_add_f32 v[20:21], v[20:21], v[148:149]\n"
"v_pk_add_f32 v[22:23], v[22:23], v[150:151]\n"
"v_pk_add_f32 v[20:21], v[20:21], v[140:141]\n"
"v_pk_add_f32 v[22:23], v[22:23], v[142:143]\n"
"v_cvt_pk_bf16_f32 v20, v20, v21\n"
"v_cvt_pk_bf16_f32 v21, v22, v23\n"
"s_xor_b32 s68, s65, 32\n"
"s_add_u32 s68, s68, s60\n"
"s_add_u32 s68, s68, s61\n"
"buffer_store_dwordx2 v[20:21], v231, s[84:87], s68 offen\n"
"s_cmp_lg_u32 s64, 2\n"
"s_cbranch_scc1 LW2_%=\n"
"v_pk_add_f32 v[164:165], v[164:165], v[168:169]\n"
"v_pk_add_f32 v[166:167], v[166:167], v[170:171]\n"
"v_pk_add_f32 v[48:49], v[48:49], v[172:173]\n"
"v_pk_add_f32 v[50:51], v[50:51], v[174:175]\n"
"v_pk_add_f32 v[48:49], v[48:49], v[164:165]\n"
"v_pk_add_f32 v[50:51], v[50:51], v[166:167]\n"
"v_cvt_pk_bf16_f32 v48, v48, v49\n"
"v_cvt_pk_bf16_f32 v49, v50, v51\n"
"s_add_u32 s68, s60, s62\n"
"buffer_store_dwordx2 v[48:49], v231, s[84:87], s68 offen\n"
"LW2_%=:\n"
"s_branch LEND_%=\n"
"LZZ2_%=:\n"
".macro KB3 p0,p1,p2\n"
"s_barrier\n"
"s_cmp_lt_u32 s61, 0x36000\n"
"s_cbranch_scc0 7f\n"
"s_sleep 3\n"
"7:\n"
"s_sleep 1\n"
"s_setprio 1\n"
"ds_read_b128 v[232:235], v247 offset:4096\n"
"v_mfma_f32_16x16x32_bf16 v[16:19], v[160:163], v[224:227], v[16:19]\n"
"ds_read_b128 v[128:131], %[kb] offset:\\p0+2048\n"
"v_add_f32 v240, v240, v192\n"
"v_add_f32_e64 v241, v241, v196\n"
"v_mfma_f32_16x16x32_bf16 v[48:51], v[160:163], v[228:231], v[48:51]\n"
"ds_read_b128 v[132:135], %[kb] offset:\\p0+2304\n"
"v_add_f32 v240, v240, v193\n"
"v_add_f32 v241, v241, v197\n"
"v_mfma_f32_16x16x32_bf16 v[20:23], v[164:167], v[224:227], v[20:23]\n"
"ds_read_b128 v[136:139], %[kb] offset:\\p0+2560\n"
"v_add_f32 v240, v240, v194\n"
"v_add_f32 v241, v241, v198\n"
"v_mfma_f32_16x16x32_bf16 v[52:55], v[164:167], v[228:231], v[52:55]\n"
"ds_read_b128 v[140:143], %[kb] offset:\\p0+2816\n"
"v_add_f32 v240, v240, v195\n"
"v_add_f32 v241, v241, v199\n"
"v_mfma_f32_16x16x32_bf16 v[24:27], v[168:171], v[224:227], v[24:27]\n"
"ds_read_b128 v[144:147], %[kb] offset:\\p0+10240\n"
"v_add_f32 v240, v240, v200\n"
"v_add_f32 v241, v241, v204\n"
"v_mfma_f32_16x16x32_bf16 v[56:59], v[168:171], v[228:231], v[56:59]\n"
"ds_read_b128 v[148:151], %[kb] offset:\\p0+10496\n"
"v_add_f32 v240, v240, v201\n"
"v_add_f32 v241, v241, v205\n"
"v_mfma_f32_16x16x32_bf16 v[28:31], v[172:175], v[224:227], v[28:31]\n"
"ds_read_b128 v[152:155], %[kb] offset:\\p0+10752\n"
"v_add_f32 v240, v240, v202\n"
"v_add_f32 v241, v241, v206\n"
"v_mfma_f32_16x16x32_bf16 v[60:63], v[172:175], v[228:231], v[60:63]\n"
"ds_read_b128 v[156:159], %[kb] offset:\\p0+11008\n"
"v_add_f32 v240, v240, v203\n"
"v_add_f32 v241, v241, v207\n"
"v_mfma_f32_16x16x32_bf16 v[32:35], v[176:179], v[224:227], v[32:35]\n"
"ds_read_b64_tr_b16 v[160:161], %[vbe] offset:\\p0+0\n"
"v_mfma_f32_16x16x32_bf16 v[64:67], v[176:179], v[228:231], v[64:67]\n"
"ds_read_b64_tr_b16 v[162:163], %[vbe] offset:\\p0+8192\n"
"s_waitcnt lgkmcnt(10)\n"
"v_mfma_f32_16x16x32_bf16 v[80:83], v[176:179], v[232:235], v[80:83]\n"
"ds_read_b64_tr_b16 v[164:165], v243 offset:\\p0+0\n"
"v_mfma_f32_16x16x32_bf16 v[36:39], v[180:183], v[224:227], v[36:39]\n"
"ds_read_b64_tr_b16 v[166:167], v243 offset:\\p0+8192\n"
"v_mfma_f32_16x16x32_bf16 v[68:71], v[180:183], v[228:231], v[68:71]\n"
"ds_read_b64_tr_b16 v[168:169], %[vbe] offset:\\p0+256\n"
"v_mfma_f32_16x16x32_bf16 v[84:87], v[180:183], v[232:235], v[84:87]\n"
"ds_read_b64_tr_b16 v[170:171], %[vbe] offset:\\p0+8448\n"
"v_mfma_f32_16x16x32_bf16 v[40:43], v[184:187], v[224:227], v[40:43]\n"
"ds_read_b64_tr_b16 v[172:173], v243 offset:\\p0+256\n"
"v_mfma_f32_16x16x32_bf16 v[72:75], v[184:187], v[228:231], v[72:75]\n"
"ds_read_b64_tr_b16 v[174:175], v243 offset:\\p0+8448\n"
"v_mfma_f32_16x16x32_bf16 v[88:91], v[184:187], v[232:235], v[88:91]\n"
"ds_read_b64_tr_b16 v[176:177], %[vbe] offset:\\p0+512\n"
"v_mfma_f32_16x16x32_bf16 v[44:47], v[188:191], v[224:227], v[44:47]\n"
"ds_read_b64_tr_b16 v[178:179], %[vbe] offset:\\p0+8704\n"
"v_mfma_f32_16x16x32_bf16 v[76:79], v[188:191], v[228:231], v[76:79]\n"
"ds_read_b64_tr_b16 v[180:181], v243 offset:\\p0+512\n"
"v_mfma_f32_16x16x32_bf16 v[92:95], v[188:191], v[232:235], v[92:95]\n"
"ds_read_b64_tr_b16 v[182:183], v243 offset:\\p0+8704\n"
"s_waitcnt lgkmcnt(12)\n"
"v_mfma_f32_16x16x32_bf16 v[192:195], v[128:131], v[96:99], v[212:215]\n"
"ds_read_b64_tr_b16 v[184:185], %[vbe] offset:\\p0+768\n"
"v_mfma_f32_16x16x32_bf16 v[192:195], v[132:135], v[100:103], v[192:195]\n"
"ds_read_b64_tr_b16 v[186:187], %[vbe] offset:\\p0+8960\n"
"v_mfma_f32_16x16x32_bf16 v[192:195], v[136:139], v[104:107], v[192:195]\n"
"ds_read_b64_tr_b16 v[188:189], v243 offset:\\p0+768\n"
"v_mfma_f32_16x16x32_bf16 v[192:195], v[140:143], v[108:111], v[192:195]\n"
"ds_read_b64_tr_b16 v[190:191], v243 offset:\\p0+8960\n"
"v_mfma_f32_16x16x32_bf16 v[196:199], v[128:131], v[112:115], v[216:219]\n"
"v_mfma_f32_16x16x32_bf16 v[196:199], v[132:135], v[116:119], v[196:199]\n"
"v_mfma_f32_16x16x32_bf16 v[196:199], v[136:139], v[120:123], v[196:199]\n"
"v_mfma_f32_16x16x32_bf16 v[196:199], v[140:143], v[124:127], v[196:199]\n"
"v_mfma_f32_16x16x32_bf16 v[200:203], v[144:147], v[96:99], v[212:215]\n"
"v_mfma_f32_16x16x32_bf16 v[200:203], v[148:151], v[100:103], v[200:203]\n"
"v_mfma_f32_16x16x32_bf16 v[200:203], v[152:155], v[104:107], v[200:203]\n"
"v_mfma_f32_16x16x32_bf16 v[200:203], v[156:159], v[108:111], v[200:203]\n"
"v_mfma_f32_16x16x32_bf16 v[204:207], v[144:147], v[112:115], v[216:219]\n"
"v_mfma_f32_16x16x32_bf16 v[204:207], v[148:151], v[116:119], v[204:207]\n"
"v_mfma_f32_16x16x32_bf16 v[204:207], v[152:155], v[120:123], v[204:207]\n"
"v_mfma_f32_16x16x32_bf16 v[204:207], v[156:159], v[124:127], v[204:207]\n"
"s_waitcnt lgkmcnt(0)\n"
"s_barrier\n"
"s_mov_b32 m0, \\p1\n"
"s_setprio 0\n"
"buffer_load_dwordx4 %[g0], %[srdv], s61 offen nt lds\n"
"buffer_load_dwordx4 %[g1], %[srdv], s61 offen offset:1024 nt lds\n"
"buffer_load_dwordx4 %[g0], %[srdk], s61 offen offset:2048 nt lds\n"
"buffer_load_dwordx4 %[g1], %[srdk], s61 offen offset:3072 nt lds\n"
"s_mov_b32 m0, \\p2\n"
"s_nop 0\n"
"buffer_load_dwordx4 v248, %[srdk], s61 offen offset:3072 nt lds\n"
"s_add_u32 s61, s61, 0x2000\n"
"s_cmp_eq_u32 s61, 0x40000\n"
"v_exp_f32 v192, v192\n"
"v_exp_f32 v193, v193\n"
"v_exp_f32 v194, v194\n"
"v_exp_f32 v195, v195\n"
"v_cvt_pk_bf16_f32 v224, v192, v193\n"
"v_cvt_pk_bf16_f32 v225, v194, v195\n"
"v_exp_f32 v196, v196\n"
"v_exp_f32 v197, v197\n"
"v_exp_f32 v198, v198\n"
"v_exp_f32 v199, v199\n"
"v_cvt_pk_bf16_f32 v228, v196, v197\n"
"v_cvt_pk_bf16_f32 v229, v198, v199\n"
"v_exp_f32 v200, v200\n"
"v_exp_f32 v201, v201\n"
"v_exp_f32 v202, v202\n"
"v_exp_f32 v203, v203\n"
"v_cvt_pk_bf16_f32 v226, v200, v201\n"
"v_cvt_pk_bf16_f32 v227, v202, v203\n"
"v_exp_f32 v204, v204\n"
"v_exp_f32 v205, v205\n"
"v_exp_f32 v206, v206\n"
"v_exp_f32 v207, v207\n"
"v_cvt_pk_bf16_f32 v230, v204, v205\n"
"v_cvt_pk_bf16_f32 v231, v206, v207\n"
"s_waitcnt vmcnt(15)\n"
"s_cbranch_scc1 LQ3_%=\n"
".endm\n"
".macro KA3 p0,p1,p2,p3\n"
"s_barrier\n"
"s_sleep 3\n"
"s_setprio 1\n"
"ds_read_b128 v[232:235], v247 offset:4096\n"
"v_mfma_f32_16x16x32_bf16 v[16:19], v[160:163], v[224:227], v[16:19]\n"
"ds_read_b128 v[128:131], v244 offset:\\p0+2048\n"
"v_add_f32 v240, v240, v192\n"
"v_add_f32_e64 v241, v241, v196\n"
"v_mfma_f32_16x16x32_bf16 v[48:51], v[160:163], v[228:231], v[48:51]\n"
"ds_read_b128 v[132:135], v244 offset:\\p0+2304\n"
"v_add_f32 v240, v240, v193\n"
"v_add_f32 v241, v241, v197\n"
"v_mfma_f32_16x16x32_bf16 v[20:23], v[164:167], v[224:227], v[20:23]\n"
"ds_read_b128 v[136:139], v244 offset:\\p0+2560\n"
"v_add_f32 v240, v240, v194\n"
"v_add_f32 v241, v241, v198\n"
"v_mfma_f32_16x16x32_bf16 v[52:55], v[164:167], v[228:231], v[52:55]\n"
"ds_read_b128 v[140:143], v244 offset:\\p0+2816\n"
"v_add_f32 v240, v240, v195\n"
"v_add_f32 v241, v241, v199\n"
"v_mfma_f32_16x16x32_bf16 v[24:27], v[168:171], v[224:227], v[24:27]\n"
"ds_read_b128 v[144:147], v244 offset:\\p0+10240\n"
"v_add_f32 v240, v240, v200\n"
"v_add_f32 v241, v241, v204\n"
"v_mfma_f32_16x16x32_bf16 v[56:59], v[168:171], v[228:231], v[56:59]\n"
"ds_read_b128 v[148:151], v244 offset:\\p0+10496\n"
"v_add_f32 v240, v240, v201\n"
"v_add_f32 v241, v241, v205\n"
"v_mfma_f32_16x16x32_bf16 v[28:31], v[172:175], v[224:227], v[28:31]\n"
"ds_read_b128 v[152:155], v244 offset:\\p0+10752\n"
"v_add_f32 v240, v240, v202\n"
"v_add_f32 v241, v241, v206\n"
"v_mfma_f32_16x16x32_bf16 v[60:63], v[172:175], v[228:231], v[60:63]\n"
"ds_read_b128 v[156:159], v244 offset:\\p0+11008\n"
"v_add_f32 v240, v240, v203\n"
"v_add_f32 v241, v241, v207\n"
"v_mfma_f32_16x16x32_bf16 v[32:35], v[176:179], v[224:227], v[32:35]\n"
"ds_read_b64_tr_b16 v[160:161], v245 offset:\\p0+0\n"
"v_mfma_f32_16x16x32_bf16 v[64:67], v[176:179], v[228:231], v[64:67]\n"
"ds_read_b64_tr_b16 v[162:163], v245 offset:\\p0+8192\n"
"s_waitcnt lgkmcnt(10)\n"
"v_mfma_f32_16x16x32_bf16 v[80:83], v[176:179], v[232:235], v[80:83]\n"
"ds_read_b64_tr_b16 v[164:165], v246 offset:\\p0+0\n"
"v_mfma_f32_16x16x32_bf16 v[36:39], v[180:183], v[224:227], v[36:39]\n"
"ds_read_b64_tr_b16 v[166:167], v246 offset:\\p0+8192\n"
"v_mfma_f32_16x16x32_bf16 v[68:71], v[180:183], v[228:231], v[68:71]\n"
"ds_read_b64_tr_b16 v[168:169], v245 offset:\\p0+256\n"
"v_mfma_f32_16x16x32_bf16 v[84:87], v[180:183], v[232:235], v[84:87]\n"
"ds_read_b64_tr_b16 v[170:171], v245 offset:\\p0+8448\n"
"v_mfma_f32_16x16x32_bf16 v[40:43], v[184:187], v[224:227], v[40:43]\n"
"ds_read_b64_tr_b16 v[172:173], v246 offset:\\p0+256\n"
"v_mfma_f32_16x16x32_bf16 v[72:75], v[184:187], v[228:231], v[72:75]\n"
"ds_read_b64_tr_b16 v[174:175], v246 offset:\\p0+8448\n"
"v_mfma_f32_16x16x32_bf16 v[88:91], v[184:187], v[232:235], v[88:91]\n"
"ds_read_b64_tr_b16 v[176:177], v245 offset:\\p0+512\n"
"v_mfma_f32_16x16x32_bf16 v[44:47], v[188:191], v[224:227], v[44:47]\n"
"ds_read_b64_tr_b16 v[178:179], v245 offset:\\p0+8704\n"
"v_mfma_f32_16x16x32_bf16 v[76:79], v[188:191], v[228:231], v[76:79]\n"
"ds_read_b64_tr_b16 v[180:181], v246 offset:\\p0+512\n"
"v_mfma_f32_16x16x32_bf16 v[92:95], v[188:191], v[232:235], v[92:95]\n"
"ds_read_b64_tr_b16 v[182:183], v246 offset:\\p0+8704\n"
"s_waitcnt lgkmcnt(12)\n"
"v_mfma_f32_16x16x32_bf16 v[192:195], v[128:131], v[96:99], v[212:215]\n"
"ds_read_b64_tr_b16 v[184:185], v245 offset:\\p0+768\n"
"v_mfma_f32_16x16x32_bf16 v[192:195], v[132:135], v[100:103], v[192:195]\n"
"ds_read_b64_tr_b16 v[186:187], v245 offset:\\p0+8960\n"
"v_mfma_f32_16x16x32_bf16 v[192:195], v[136:139], v[104:107], v[192:195]\n"
"ds_read_b64_tr_b16 v[188:189], v246 offset:\\p0+768\n"
"v_mfma_f32_16x16x32_bf16 v[192:195], v[140:143], v[108:111], v[192:195]\n"
"ds_read_b64_tr_b16 v[190:191], v246 offset:\\p0+8960\n"
"v_mfma_f32_16x16x32_bf16 v[196:199], v[128:131], v[112:115], v[216:219]\n"
"v_mfma_f32_16x16x32_bf16 v[196:199], v[132:135], v[116:119], v[196:199]\n"
"v_mfma_f32_16x16x32_bf16 v[196:199], v[136:139], v[120:123], v[196:199]\n"
"v_mfma_f32_16x16x32_bf16 v[196:199], v[140:143], v[124:127], v[196:199]\n"
"v_mfma_f32_16x16x32_bf16 v[200:203], v[144:147], v[96:99], v[212:215]\n"
"v_mfma_f32_16x16x32_bf16 v[200:203], v[148:151], v[100:103], v[200:203]\n"
"v_mfma_f32_16x16x32_bf16 v[200:203], v[152:155], v[104:107], v[200:203]\n"
"v_mfma_f32_16x16x32_bf16 v[200:203], v[156:159], v[108:111], v[200:203]\n"
"v_mfma_f32_16x16x32_bf16 v[204:207], v[144:147], v[112:115], v[216:219]\n"
"v_mfma_f32_16x16x32_bf16 v[204:207], v[148:151], v[116:119], v[204:207]\n"
"v_mfma_f32_16x16x32_bf16 v[204:207], v[152:155], v[120:123], v[204:207]\n"
"v_mfma_f32_16x16x32_bf16 v[204:207], v[156:159], v[124:127], v[204:207]\n"
"s_waitcnt lgkmcnt(0)\n"
"s_barrier\n"
"s_mov_b32 m0, \\p1\n"
"s_setprio 0\n"
"buffer_load_dwordx4 %[g0], %[srdv], s61 offen nt lds\n"
"buffer_load_dwordx4 %[g1], %[srdv], s61 offen offset:1024 nt lds\n"
"v_exp_f32 v192, v192\n"
"v_exp_f32 v193, v193\n"
"v_exp_f32 v194, v194\n"
"v_exp_f32 v195, v195\n"
"v_cvt_pk_bf16_f32 v224, v192, v193\n"
"v_cvt_pk_bf16_f32 v225, v194, v195\n"
"v_exp_f32 v196, v196\n"
"v_exp_f32 v197, v197\n"
"v_exp_f32 v198, v198\n"
"v_exp_f32 v199, v199\n"
"v_cvt_pk_bf16_f32 v228, v196, v197\n"
"v_cvt_pk_bf16_f32 v229, v198, v199\n"
"v_exp_f32 v200, v200\n"
"v_exp_f32 v201, v201\n"
"v_exp_f32 v202, v202\n"
"v_exp_f32 v203, v203\n"
"v_cvt_pk_bf16_f32 v226, v200, v201\n"
"v_cvt_pk_bf16_f32 v227, v202, v203\n"
"v_exp_f32 v204, v204\n"
"v_exp_f32 v205, v205\n"
"v_exp_f32 v206, v206\n"
"v_exp_f32 v207, v207\n"
"v_cvt_pk_bf16_f32 v230, v204, v205\n"
"v_cvt_pk_bf16_f32 v231, v206, v207\n"
"s_waitcnt vmcnt(12)\n"
"s_barrier\n"
"s_sleep 3\n"
"s_setprio 1\n"
"ds_read_b128 v[232:235], v247 offset:4096\n"
"v_mfma_f32_16x16x32_bf16 v[16:19], v[160:163], v[224:227], v[16:19]\n"
"ds_read_b128 v[128:131], %[kb] offset:\\p2+2048\n"
"v_add_f32 v240, v240, v192\n"
"v_add_f32 v241, v241, v196\n"
"v_mfma_f32_16x16x32_bf16 v[48:51], v[160:163], v[228:231], v[48:51]\n"
"ds_read_b128 v[132:135], %[kb] offset:\\p2+2304\n"
"v_add_f32 v240, v240, v193\n"
"v_add_f32 v241, v241, v197\n"
"v_mfma_f32_16x16x32_bf16 v[20:23], v[164:167], v[224:227], v[20:23]\n"
"ds_read_b128 v[136:139], %[kb] offset:\\p2+2560\n"
"v_add_f32 v240, v240, v194\n"
"v_add_f32 v241, v241, v198\n"
"v_mfma_f32_16x16x32_bf16 v[52:55], v[164:167], v[228:231], v[52:55]\n"
"ds_read_b128 v[140:143], %[kb] offset:\\p2+2816\n"
"v_add_f32 v240, v240, v195\n"
"v_add_f32 v241, v241, v199\n"
"v_mfma_f32_16x16x32_bf16 v[24:27], v[168:171], v[224:227], v[24:27]\n"
"ds_read_b128 v[144:147], %[kb] offset:\\p2+10240\n"
"v_add_f32 v240, v240, v200\n"
"v_add_f32 v241, v241, v204\n"
"v_mfma_f32_16x16x32_bf16 v[56:59], v[168:171], v[228:231], v[56:59]\n"
"ds_read_b128 v[148:151], %[kb] offset:\\p2+10496\n"
"v_add_f32 v240, v240, v201\n"
"v_add_f32 v241, v241, v205\n"
"v_mfma_f32_16x16x32_bf16 v[28:31], v[172:175], v[224:227], v[28:31]\n"
"ds_read_b128 v[152:155], %[kb] offset:\\p2+10752\n"
"v_add_f32 v240, v240, v202\n"
"v_add_f32 v241, v241, v206\n"
"v_mfma_f32_16x16x32_bf16 v[60:63], v[172:175], v[228:231], v[60:63]\n"
"ds_read_b128 v[156:159], %[kb] offset:\\p2+11008\n"
"v_add_f32 v240, v240, v203\n"
"v_add_f32 v241, v241, v207\n"
"v_mfma_f32_16x16x32_bf16 v[32:35], v[176:179], v[224:227], v[32:35]\n"
"ds_read_b64_tr_b16 v[160:161], %[vbe] offset:\\p2+0\n"
"v_mfma_f32_16x16x32_bf16 v[64:67], v[176:179], v[228:231], v[64:67]\n"
"ds_read_b64_tr_b16 v[162:163], %[vbe] offset:\\p2+8192\n"
"s_waitcnt lgkmcnt(10)\n"
"v_mfma_f32_16x16x32_bf16 v[80:83], v[176:179], v[232:235], v[80:83]\n"
"ds_read_b64_tr_b16 v[164:165], v243 offset:\\p2+0\n"
"v_mfma_f32_16x16x32_bf16 v[36:39], v[180:183], v[224:227], v[36:39]\n"
"ds_read_b64_tr_b16 v[166:167], v243 offset:\\p2+8192\n"
"v_mfma_f32_16x16x32_bf16 v[68:71], v[180:183], v[228:231], v[68:71]\n"
"ds_read_b64_tr_b16 v[168:169], %[vbe] offset:\\p2+256\n"
"v_mfma_f32_16x16x32_bf16 v[84:87], v[180:183], v[232:235], v[84:87]\n"
"ds_read_b64_tr_b16 v[170:171], %[vbe] offset:\\p2+8448\n"
"v_mfma_f32_16x16x32_bf16 v[40:43], v[184:187], v[224:227], v[40:43]\n"
"ds_read_b64_tr_b16 v[172:173], v243 offset:\\p2+256\n"
"v_mfma_f32_16x16x32_bf16 v[72:75], v[184:187], v[228:231], v[72:75]\n"
"ds_read_b64_tr_b16 v[174:175], v243 offset:\\p2+8448\n"
"v_mfma_f32_16x16x32_bf16 v[88:91], v[184:187], v[232:235], v[88:91]\n"
"ds_read_b64_tr_b16 v[176:177], %[vbe] offset:\\p2+512\n"
"v_mfma_f32_16x16x32_bf16 v[44:47], v[188:191], v[224:227], v[44:47]\n"
"ds_read_b64_tr_b16 v[178:179], %[vbe] offset:\\p2+8704\n"
"v_mfma_f32_16x16x32_bf16 v[76:79], v[188:191], v[228:231], v[76:79]\n"
"ds_read_b64_tr_b16 v[180:181], v243 offset:\\p2+512\n"
"v_mfma_f32_16x16x32_bf16 v[92:95], v[188:191], v[232:235], v[92:95]\n"
"ds_read_b64_tr_b16 v[182:183], v243 offset:\\p2+8704\n"
"s_waitcnt lgkmcnt(12)\n"
"v_mfma_f32_16x16x32_bf16 v[192:195], v[128:131], v[96:99], v[212:215]\n"
"ds_read_b64_tr_b16 v[184:185], %[vbe] offset:\\p2+768\n"
"v_mfma_f32_16x16x32_bf16 v[192:195], v[132:135], v[100:103], v[192:195]\n"
"ds_read_b64_tr_b16 v[186:187], %[vbe] offset:\\p2+8960\n"
"v_mfma_f32_16x16x32_bf16 v[192:195], v[136:139], v[104:107], v[192:195]\n"
"ds_read_b64_tr_b16 v[188:189], v243 offset:\\p2+768\n"
"v_mfma_f32_16x16x32_bf16 v[192:195], v[140:143], v[108:111], v[192:195]\n"
"ds_read_b64_tr_b16 v[190:191], v243 offset:\\p2+8960\n"
"v_mfma_f32_16x16x32_bf16 v[196:199], v[128:131], v[112:115], v[216:219]\n"
"v_mfma_f32_16x16x32_bf16 v[196:199], v[132:135], v[116:119], v[196:199]\n"
"v_mfma_f32_16x16x32_bf16 v[196:199], v[136:139], v[120:123], v[196:199]\n"
"v_mfma_f32_16x16x32_bf16 v[196:199], v[140:143], v[124:127], v[196:199]\n"
"v_mfma_f32_16x16x32_bf16 v[200:203], v[144:147], v[96:99], v[212:215]\n"
"v_mfma_f32_16x16x32_bf16 v[200:203], v[148:151], v[100:103], v[200:203]\n"
"v_mfma_f32_16x16x32_bf16 v[200:203], v[152:155], v[104:107], v[200:203]\n"
"v_mfma_f32_16x16x32_bf16 v[200:203], v[156:159], v[108:111], v[200:203]\n"
"v_mfma_f32_16x16x32_bf16 v[204:207], v[144:147], v[112:115], v[216:219]\n"
"v_mfma_f32_16x16x32_bf16 v[204:207], v[148:151], v[116:119], v[204:207]\n"
"v_mfma_f32_16x16x32_bf16 v[204:207], v[152:155], v[120:123], v[204:207]\n"
"v_mfma_f32_16x16x32_bf16 v[204:207], v[156:159], v[124:127], v[204:207]\n"
"s_waitcnt lgkmcnt(0)\n"
"s_barrier\n"
"s_mov_b32 m0, \\p1\n"
"s_setprio 0\n"
"buffer_load_dwordx4 %[g0], %[srdk], s61 offen offset:2048 nt lds\n"
"buffer_load_dwordx4 %[g1], %[srdk], s61 offen offset:3072 nt lds\n"
"s_mov_b32 m0, \\p3\n"
"s_nop 0\n"
"buffer_load_dwordx4 v248, %[srdk], s61 offen offset:3072 nt lds\n"
"s_add_u32 s61, s61, 0x4000\n"
"s_cmp_eq_u32 s61, 0x40000\n"
"v_exp_f32 v192, v192\n"
"v_exp_f32 v193, v193\n"
"v_exp_f32 v194, v194\n"
"v_exp_f32 v195, v195\n"
"v_cvt_pk_bf16_f32 v224, v192, v193\n"
"v_cvt_pk_bf16_f32 v225, v194, v195\n"
"v_exp_f32 v196, v196\n"
"v_exp_f32 v197, v197\n"
"v_exp_f32 v198, v198\n"
"v_exp_f32 v199, v199\n"
"v_cvt_pk_bf16_f32 v228, v196, v197\n"
"v_cvt_pk_bf16_f32 v229, v198, v199\n"
"v_exp_f32 v200, v200\n"
"v_exp_f32 v201, v201\n"
"v_exp_f32 v202, v202\n"
"v_exp_f32 v203, v203\n"
"v_cvt_pk_bf16_f32 v226, v200, v201\n"
"v_cvt_pk_bf16_f32 v227, v202, v203\n"
"v_exp_f32 v204, v204\n"
"v_exp_f32 v205, v205\n"
"v_exp_f32 v206, v206\n"
"v_exp_f32 v207, v207\n"
"v_cvt_pk_bf16_f32 v230, v204, v205\n"
"v_cvt_pk_bf16_f32 v231, v206, v207\n"
".endm\n"
".p2align 3\n"
"LR3_%=:\n"
"s_mov_b32 s62, 0\n"
"s_mov_b32 s63, 0\n"
"s_mov_b32 s61, 0\n"
"s_lshr_b32 s60, %[hb], 5\n"
"s_lshr_b32 s64, %[hb], 6\n"
"v_lshrrev_b32 v198, 8, %[g0]\n"
"v_mul_u32_u24 v198, 7936, v198\n"
"v_add_u32 v198, %[g0], v198\n"
"v_xor_b32 v195, 32, v198\n"
"v_add_u32 v195, 31744, v195\n"
"v_xor_b32_e64 v193, 48, %[xoff]\n"
"s_add_u32 m0, %[hb], 38912\n"
"v_add_u32 v193, 8192, v193\n"
"s_add_u32 s62, s62, 0x40000\n"
"s_add_u32 m0, s60, 143360\n"
"s_nop 0\n"
"s_add_u32 s63, s63, 0x80\n"
"s_mul_i32 s91, %[xrot], 6\n"
"s_add_u32 m0, %[hb], 47104\n"
"s_nop 0\n"
"s_add_u32 s62, s62, 0x40000\n"
"s_add_u32 s52, %[hb], 12288\n"
"s_add_u32 s56, %[hb], 4096\n"
"s_add_u32 s53, %[hb], 28672\n"
"s_add_u32 s57, %[hb], 20480\n"
"s_add_u32 s54, %[hb], 45056\n"
"s_add_u32 s58, %[hb], 36864\n"
"s_add_u32 s55, %[hb], 61440\n"
"s_add_u32 s59, %[hb], 53248\n"
"s_lshr_b32 s90, %[hb], 3\n"
"s_add_u32 s90, s90, 131072\n"
"v_xor_b32 v192, 32, %[wbe]\n"
"v_xor_b32 v243, 32, %[vbe]\n"
"v_add_u32 v194, 143360, %[xb]\n"
"v_add_u32_e64 v194, s60, v194\n"
"v_xor_b32 v196, 2048, v194\n"
"v_add_u32 v244, %[d1], %[kb]\n"
"v_add_u32 v245, %[d1], %[vbe]\n"
"v_add_u32 v246, %[d1], v243\n"
"s_lshr_b32 s65, %[hb], 3\n"
"s_add_u32 s65, s65, 147456\n"
"v_add_u32_e64 v247, s65, %[l16]\n"
"v_subrev_u32 v248, 4096, %[g1]\n"
"s_mov_b32 s66, 0x3e0293ee\n"
"s_mov_b32 s67, s66\n"
"s_waitcnt vmcnt(2)\n"
"s_barrier\n"
"ds_read_b128 v[96:99], v194 offset:0\n"
"ds_read_b128 v[100:103], v196 offset:0\n"
"ds_read_b128 v[104:107], %[xb] offset:57344\n"
"ds_read_b64_tr_b16 v[128:129], %[wbe] offset:32768\n"
"ds_read_b64_tr_b16 v[130:131], v192 offset:33792\n"
"ds_read_b64_tr_b16 v[132:133], v192 offset:32768\n"
"ds_read_b64_tr_b16 v[134:135], %[wbe] offset:33792\n"
"ds_read_b64_tr_b16 v[136:137], %[wbe] offset:33024\n"
"ds_read_b64_tr_b16 v[138:139], v192 offset:34048\n"
"ds_read_b64_tr_b16 v[140:141], v192 offset:33024\n"
"ds_read_b64_tr_b16 v[142:143], %[wbe] offset:34048\n"
"ds_read_b64_tr_b16 v[144:145], %[wbe] offset:33280\n"
"ds_read_b64_tr_b16 v[146:147], v192 offset:34304\n"
"ds_read_b64_tr_b16 v[148:149], v192 offset:33280\n"
"ds_read_b64_tr_b16 v[150:151], %[wbe] offset:34304\n"
"ds_read_b64_tr_b16 v[152:153], %[wbe] offset:33536\n"
"ds_read_b64_tr_b16 v[154:155], v192 offset:34560\n"
"ds_read_b64_tr_b16 v[156:157], v192 offset:33536\n"
"s_add_u32 m0, s60, 159744\n"
"ds_read_b64_tr_b16 v[158:159], %[wbe] offset:34560\n"
"buffer_load_dwordx4 %[xoff], %[srdx], s63 offen lds\n"
"buffer_load_dwordx4 v193, %[srdx], s63 offen offset:1024 lds\n"
"s_add_u32 s63, s63, 0x80\n"
"s_add_u32 m0, %[hb], 55296\n"
"s_nop 0\n"
"buffer_load_dwordx4 v198, %[srdw], s62 offen lds\n"
"buffer_load_dwordx4 v195, %[srdw], s62 offen offset:1024 lds\n"
"s_add_u32 s62, s62, 0x40000\n"
"s_waitcnt vmcnt(4) lgkmcnt(0)\n"
"s_barrier\n"
"v_mfma_f32_16x16x32_bf16 v[16:19], v[128:131], v[96:99], 0\n"
"ds_read_b128 v[108:111], v194 offset:512\n"
"ds_read_b128 v[112:115], v196 offset:512\n"
"v_mfma_f32_16x16x32_bf16 v[48:51], v[128:131], v[100:103], 0\n"
"ds_read_b128 v[116:119], %[xb] offset:57856\n"
"v_mfma_f32_16x16x32_bf16 v[20:23], v[132:135], v[96:99], 0\n"
"ds_read_b64_tr_b16 v[160:161], %[wbe] offset:40960\n"
"v_mfma_f32_16x16x32_bf16 v[52:55], v[132:135], v[100:103], 0\n"
"ds_read_b64_tr_b16 v[162:163], v192 offset:41984\n"
"ds_read_b64_tr_b16 v[164:165], v192 offset:40960\n"
"s_add_u32 m0, %[hb], 38912\n"
"v_mfma_f32_16x16x32_bf16 v[24:27], v[136:139], v[96:99], 0\n"
"buffer_load_dwordx4 v198, %[srdw], s62 offen lds\n"
"v_mfma_f32_16x16x32_bf16 v[56:59], v[136:139], v[100:103], 0\n"
"ds_read_b64_tr_b16 v[166:167], %[wbe] offset:41984\n"
"v_mfma_f32_16x16x32_bf16 v[28:31], v[140:143], v[96:99], 0\n"
"ds_read_b64_tr_b16 v[168:169], %[wbe] offset:41216\n"
"v_mfma_f32_16x16x32_bf16 v[60:63], v[140:143], v[100:103], 0\n"
"ds_read_b64_tr_b16 v[170:171], v192 offset:42240\n"
"v_mfma_f32_16x16x32_bf16 v[32:35], v[144:147], v[96:99], 0\n"
"buffer_load_dwordx4 v195, %[srdw], s62 offen offset:1024 lds\n"
"s_add_u32 s62, s62, 0x40000\n"
"v_mfma_f32_16x16x32_bf16 v[64:67], v[144:147], v[100:103], 0\n"
"ds_read_b64_tr_b16 v[172:173], v192 offset:41216\n"
"ds_read_b64_tr_b16 v[174:175], %[wbe] offset:42240\n"
"v_mfma_f32_16x16x32_bf16 v[36:39], v[148:151], v[96:99], 0\n"
"ds_read_b64_tr_b16 v[176:177], %[wbe] offset:41472\n"
"v_mfma_f32_16x16x32_bf16 v[68:71], v[148:151], v[100:103], 0\n"
"ds_read_b64_tr_b16 v[178:179], v192 offset:42496\n"
"v_mfma_f32_16x16x32_bf16 v[40:43], v[152:155], v[96:99], 0\n"
"ds_read_b64_tr_b16 v[180:181], v192 offset:41472\n"
"v_mfma_f32_16x16x32_bf16 v[72:75], v[152:155], v[100:103], 0\n"
"ds_read_b64_tr_b16 v[182:183], %[wbe] offset:42496\n"
"ds_read_b64_tr_b16 v[184:185], %[wbe] offset:41728\n"
"v_mfma_f32_16x16x32_bf16 v[80:83], v[152:155], v[104:107], 0\n"
"ds_read_b64_tr_b16 v[186:187], v192 offset:42752\n"
"v_mfma_f32_16x16x32_bf16 v[44:47], v[156:159], v[96:99], 0\n"
"ds_read_b64_tr_b16 v[188:189], v192 offset:41728\n"
"v_mfma_f32_16x16x32_bf16 v[76:79], v[156:159], v[100:103], 0\n"
"ds_read_b64_tr_b16 v[190:191], %[wbe] offset:42752\n"
"v_mfma_f32_16x16x32_bf16 v[84:87], v[156:159], v[104:107], 0\n"
"s_waitcnt vmcnt(2) lgkmcnt(0)\n"
"s_barrier\n"
"v_mfma_f32_16x16x32_bf16 v[16:19], v[160:163], v[108:111], v[16:19]\n"
"ds_read_b128 v[96:99], v194 offset:16384\n"
"ds_read_b128 v[100:103], v196 offset:16384\n"
"v_mfma_f32_16x16x32_bf16 v[48:51], v[160:163], v[112:115], v[48:51]\n"
"ds_read_b128 v[104:107], %[xb] offset:59392\n"
"v_mfma_f32_16x16x32_bf16 v[20:23], v[164:167], v[108:111], v[20:23]\n"
"ds_read_b64_tr_b16 v[128:129], %[wbe] offset:49152\n"
"ds_read_b64_tr_b16 v[130:131], v192 offset:50176\n"
"v_mfma_f32_16x16x32_bf16 v[52:55], v[164:167], v[112:115], v[52:55]\n"
"ds_read_b64_tr_b16 v[132:133], v192 offset:49152\n"
"s_add_u32 m0, s60, 143360\n"
"v_mfma_f32_16x16x32_bf16 v[24:27], v[168:171], v[108:111], v[24:27]\n"
"buffer_load_dwordx4 %[xoff], %[srdx], s63 offen lds\n"
"v_mfma_f32_16x16x32_bf16 v[56:59], v[168:171], v[112:115], v[56:59]\n"
"ds_read_b64_tr_b16 v[134:135], %[wbe] offset:50176\n"
"ds_read_b64_tr_b16 v[136:137], %[wbe] offset:49408\n"
"v_mfma_f32_16x16x32_bf16 v[28:31], v[172:175], v[108:111], v[28:31]\n"
"ds_read_b64_tr_b16 v[138:139], v192 offset:50432\n"
"v_mfma_f32_16x16x32_bf16 v[60:63], v[172:175], v[112:115], v[60:63]\n"
"ds_read_b64_tr_b16 v[140:141], v192 offset:49408\n"
"ds_read_b64_tr_b16 v[142:143], %[wbe] offset:50432\n"
"v_mfma_f32_16x16x32_bf16 v[32:35], v[176:179], v[108:111], v[32:35]\n"
"buffer_load_dwordx4 v193, %[srdx], s63 offen offset:1024 lds\n"
"s_add_u32 s63, s63, 0x80\n"
"v_mfma_f32_16x16x32_bf16 v[64:67], v[176:179], v[112:115], v[64:67]\n"
"ds_read_b64_tr_b16 v[144:145], %[wbe] offset:49664\n"
"v_mfma_f32_16x16x32_bf16 v[36:39], v[180:183], v[108:111], v[36:39]\n"
"ds_read_b64_tr_b16 v[146:147], v192 offset:50688\n"
"ds_read_b64_tr_b16 v[148:149], v192 offset:49664\n"
"v_mfma_f32_16x16x32_bf16 v[68:71], v[180:183], v[112:115], v[68:71]\n"
"ds_read_b64_tr_b16 v[150:151], %[wbe] offset:50688\n"
"s_add_u32 m0, %[hb], 47104\n"
"v_mfma_f32_16x16x32_bf16 v[40:43], v[184:187], v[108:111], v[40:43]\n"
"buffer_load_dwordx4 v198, %[srdw], s62 offen lds\n"
"v_mfma_f32_16x16x32_bf16 v[72:75], v[184:187], v[112:115], v[72:75]\n"
"ds_read_b64_tr_b16 v[152:153], %[wbe] offset:49920\n"
"ds_read_b64_tr_b16 v[154:155], v192 offset:50944\n"
"v_mfma_f32_16x16x32_bf16 v[80:83], v[184:187], v[116:119], v[80:83]\n"
"ds_read_b64_tr_b16 v[156:157], v192 offset:49920\n"
"v_mfma_f32_16x16x32_bf16 v[44:47], v[188:191], v[108:111], v[44:47]\n"
"ds_read_b64_tr_b16 v[158:159], %[wbe] offset:50944\n"
"v_mfma_f32_16x16x32_bf16 v[76:79], v[188:191], v[112:115], v[76:79]\n"
"buffer_load_dwordx4 v195, %[srdw], s62 offen offset:1024 lds\n"
"s_add_u32 s62, s62, 0x40000\n"
"v_mfma_f32_16x16x32_bf16 v[84:87], v[188:191], v[116:119], v[84:87]\n"
"s_waitcnt vmcnt(4) lgkmcnt(0)\n"
"s_barrier\n"
"v_mfma_f32_16x16x32_bf16 v[16:19], v[128:131], v[96:99], v[16:19]\n"
"ds_read_b128 v[108:111], v194 offset:16896\n"
"ds_read_b128 v[112:115], v196 offset:16896\n"
"v_mfma_f32_16x16x32_bf16 v[48:51], v[128:131], v[100:103], v[48:51]\n"
"ds_read_b128 v[116:119], %[xb] offset:59904\n"
"v_mfma_f32_16x16x32_bf16 v[20:23], v[132:135], v[96:99], v[20:23]\n"
"ds_read_b64_tr_b16 v[160:161], %[wbe] offset:32768\n"
"v_mfma_f32_16x16x32_bf16 v[52:55], v[132:135], v[100:103], v[52:55]\n"
"ds_read_b64_tr_b16 v[162:163], v192 offset:33792\n"
"ds_read_b64_tr_b16 v[164:165], v192 offset:32768\n"
"s_add_u32 m0, %[hb], 55296\n"
"v_mfma_f32_16x16x32_bf16 v[24:27], v[136:139], v[96:99], v[24:27]\n"
"buffer_load_dwordx4 v198, %[srdw], s62 offen lds\n"
"v_mfma_f32_16x16x32_bf16 v[56:59], v[136:139], v[100:103], v[56:59]\n"
"ds_read_b64_tr_b16 v[166:167], %[wbe] offset:33792\n"
"v_mfma_f32_16x16x32_bf16 v[28:31], v[140:143], v[96:99], v[28:31]\n"
"ds_read_b64_tr_b16 v[168:169], %[wbe] offset:33024\n"
"v_mfma_f32_16x16x32_bf16 v[60:63], v[140:143], v[100:103], v[60:63]\n"
"ds_read_b64_tr_b16 v[170:171], v192 offset:34048\n"
"v_mfma_f32_16x16x32_bf16 v[32:35], v[144:147], v[96:99], v[32:35]\n"
"buffer_load_dwordx4 v195, %[srdw], s62 offen offset:1024 lds\n"
"s_add_u32 s62, s62, 0x40000\n"
"v_mfma_f32_16x16x32_bf16 v[64:67], v[144:147], v[100:103], v[64:67]\n"
"ds_read_b64_tr_b16 v[172:173], v192 offset:33024\n"
"ds_read_b64_tr_b16 v[174:175], %[wbe] offset:34048\n"
"v_mfma_f32_16x16x32_bf16 v[36:39], v[148:151], v[96:99], v[36:39]\n"
"ds_read_b64_tr_b16 v[176:177], %[wbe] offset:33280\n"
"v_mfma_f32_16x16x32_bf16 v[68:71], v[148:151], v[100:103], v[68:71]\n"
"ds_read_b64_tr_b16 v[178:179], v192 offset:34304\n"
"v_mfma_f32_16x16x32_bf16 v[40:43], v[152:155], v[96:99], v[40:43]\n"
"ds_read_b64_tr_b16 v[180:181], v192 offset:33280\n"
"v_mfma_f32_16x16x32_bf16 v[72:75], v[152:155], v[100:103], v[72:75]\n"
"ds_read_b64_tr_b16 v[182:183], %[wbe] offset:34304\n"
"ds_read_b64_tr_b16 v[184:185], %[wbe] offset:33536\n"
"v_mfma_f32_16x16x32_bf16 v[80:83], v[152:155], v[104:107], v[80:83]\n"
"ds_read_b64_tr_b16 v[186:187], v192 offset:34560\n"
"v_mfma_f32_16x16x32_bf16 v[44:47], v[156:159], v[96:99], v[44:47]\n"
"ds_read_b64_tr_b16 v[188:189], v192 offset:33536\n"
"v_mfma_f32_16x16x32_bf16 v[76:79], v[156:159], v[100:103], v[76:79]\n"
"ds_read_b64_tr_b16 v[190:191], %[wbe] offset:34560\n"
"v_mfma_f32_16x16x32_bf16 v[84:87], v[156:159], v[104:107], v[84:87]\n"
"s_waitcnt vmcnt(2) lgkmcnt(0)\n"
"s_barrier\n"
"v_mfma_f32_16x16x32_bf16 v[16:19], v[160:163], v[108:111], v[16:19]\n"
"ds_read_b128 v[96:99], v194 offset:0\n"
"ds_read_b128 v[100:103], v196 offset:0\n"
"v_mfma_f32_16x16x32_bf16 v[48:51], v[160:163], v[112:115], v[48:51]\n"
"ds_read_b128 v[104:107], %[xb] offset:57344\n"
"ds_read_b64_tr_b16 v[128:129], %[wbe] offset:40960\n"
"v_mfma_f32_16x16x32_bf16 v[20:23], v[164:167], v[108:111], v[20:23]\n"
"ds_read_b64_tr_b16 v[130:131], v192 offset:41984\n"
"s_add_u32 m0, s60, 159744\n"
"v_mfma_f32_16x16x32_bf16 v[52:55], v[164:167], v[112:115], v[52:55]\n"
"buffer_load_dwordx4 %[xoff], %[srdx], s63 offen lds\n"
"v_mfma_f32_16x16x32_bf16 v[24:27], v[168:171], v[108:111], v[24:27]\n"
"ds_read_b64_tr_b16 v[132:133], v192 offset:40960\n"
"ds_read_b64_tr_b16 v[134:135], %[wbe] offset:41984\n"
"v_mfma_f32_16x16x32_bf16 v[56:59], v[168:171], v[112:115], v[56:59]\n"
"ds_read_b64_tr_b16 v[136:137], %[wbe] offset:41216\n"
"v_mfma_f32_16x16x32_bf16 v[28:31], v[172:175], v[108:111], v[28:31]\n"
"buffer_load_dwordx4 v193, %[srdx], s63 offen offset:1024 lds\n"
"v_mfma_f32_16x16x32_bf16 v[60:63], v[172:175], v[112:115], v[60:63]\n"
"ds_read_b64_tr_b16 v[138:139], v192 offset:42240\n"
"ds_read_b64_tr_b16 v[140:141], v192 offset:41216\n"
"v_mfma_f32_16x16x32_bf16 v[32:35], v[176:179], v[108:111], v[32:35]\n"
"ds_read_b64_tr_b16 v[142:143], %[wbe] offset:42240\n"
"ds_read_b64_tr_b16 v[144:145], %[wbe] offset:41472\n"
"s_add_u32 m0, s64, 59392\n"
"v_mfma_f32_16x16x32_bf16 v[64:67], v[176:179], v[112:115], v[64:67]\n"
"buffer_load_dwordx4 %[xo9], %[srdx], s63 offen lds\n"
"s_add_u32 s63, s63, 0x80\n"
"v_mfma_f32_16x16x32_bf16 v[36:39], v[180:183], v[108:111], v[36:39]\n"
"ds_read_b64_tr_b16 v[146:147], v192 offset:42496\n"
"v_mfma_f32_16x16x32_bf16 v[68:71], v[180:183], v[112:115], v[68:71]\n"
"ds_read_b64_tr_b16 v[148:149], v192 offset:41472\n"
"ds_read_b64_tr_b16 v[150:151], %[wbe] offset:42496\n"
"v_mfma_f32_16x16x32_bf16 v[40:43], v[184:187], v[108:111], v[40:43]\n"
"ds_read_b64_tr_b16 v[152:153], %[wbe] offset:41728\n"
"s_add_u32 m0, %[hb], 38912\n"
"v_mfma_f32_16x16x32_bf16 v[72:75], v[184:187], v[112:115], v[72:75]\n"
"buffer_load_dwordx4 v198, %[srdw], s62 offen lds\n"
"v_mfma_f32_16x16x32_bf16 v[80:83], v[184:187], v[116:119], v[80:83]\n"
"ds_read_b64_tr_b16 v[154:155], v192 offset:42752\n"
"ds_read_b64_tr_b16 v[156:157], v192 offset:41728\n"
"v_mfma_f32_16x16x32_bf16 v[44:47], v[188:191], v[108:111], v[44:47]\n"
"ds_read_b64_tr_b16 v[158:159], %[wbe] offset:42752\n"
"v_mfma_f32_16x16x32_bf16 v[76:79], v[188:191], v[112:115], v[76:79]\n"
"buffer_load_dwordx4 v195, %[srdw], s62 offen offset:1024 lds\n"
"s_add_u32 s62, s62, 0x40000\n"
"v_mfma_f32_16x16x32_bf16 v[84:87], v[188:191], v[116:119], v[84:87]\n"
"s_waitcnt vmcnt(5) lgkmcnt(0)\n"
"s_barrier\n"
"v_mfma_f32_16x16x32_bf16 v[16:19], v[128:131], v[96:99], v[16:19]\n"
"ds_read_b128 v[108:111], v194 offset:512\n"
"ds_read_b128 v[112:115], v196 offset:512\n"
"v_mfma_f32_16x16x32_bf16 v[48:51], v[128:131], v[100:103], v[48:51]\n"
"ds_read_b128 v[116:119], %[xb] offset:57856\n"
"v_mfma_f32_16x16x32_bf16 v[20:23], v[132:135], v[96:99], v[20:23]\n"
"ds_read_b64_tr_b16 v[160:161], %[wbe] offset:49152\n"
"ds_read_b64_tr_b16 v[162:163], v192 offset:50176\n"
"v_mfma_f32_16x16x32_bf16 v[52:55], v[132:135], v[100:103], v[52:55]\n"
"ds_read_b64_tr_b16 v[164:165], v192 offset:49152\n"
"s_add_u32 m0, %[hb], 47104\n"
"v_mfma_f32_16x16x32_bf16 v[24:27], v[136:139], v[96:99], v[24:27]\n"
"buffer_load_dwordx4 v198, %[srdw], s62 offen lds\n"
"v_mfma_f32_16x16x32_bf16 v[56:59], v[136:139], v[100:103], v[56:59]\n"
"ds_read_b64_tr_b16 v[166:167], %[wbe] offset:50176\n"
"v_mfma_f32_16x16x32_bf16 v[28:31], v[140:143], v[96:99], v[28:31]\n"
"ds_read_b64_tr_b16 v[168:169], %[wbe] offset:49408\n"
"ds_read_b64_tr_b16 v[170:171], v192 offset:50432\n"
"v_mfma_f32_16x16x32_bf16 v[60:63], v[140:143], v[100:103], v[60:63]\n"
"ds_read_b64_tr_b16 v[172:173], v192 offset:49408\n"
"v_mfma_f32_16x16x32_bf16 v[32:35], v[144:147], v[96:99], v[32:35]\n"
"buffer_load_dwordx4 v195, %[srdw], s62 offen offset:1024 lds\n"
"s_add_u32 s62, s62, 0x40000\n"
"v_mfma_f32_16x16x32_bf16 v[64:67], v[144:147], v[100:103], v[64:67]\n"
"ds_read_b64_tr_b16 v[174:175], %[wbe] offset:50432\n"
"v_mfma_f32_16x16x32_bf16 v[36:39], v[148:151], v[96:99], v[36:39]\n"
"ds_read_b64_tr_b16 v[176:177], %[wbe] offset:49664\n"
"ds_read_b64_tr_b16 v[178:179], v192 offset:50688\n"
"v_mfma_f32_16x16x32_bf16 v[68:71], v[148:151], v[100:103], v[68:71]\n"
"ds_read_b64_tr_b16 v[180:181], v192 offset:49664\n"
"v_mfma_f32_16x16x32_bf16 v[40:43], v[152:155], v[96:99], v[40:43]\n"
"s_bitcmp0_b32 %[xrot], 4\n"
"s_cbranch_scc1 LK3_4_%=\n"
"s_mov_b32 m0, s52\n"
"s_nop 0\n"
"buffer_load_dwordx4 %[g0], %[srdv], s61 offen nt lds\n"
"buffer_load_dwordx4 %[g1], %[srdv], s61 offen offset:1024 nt lds\n"
"buffer_load_dwordx4 %[g0], %[srdk], s61 offen offset:2048 nt lds\n"
"buffer_load_dwordx4 %[g1], %[srdk], s61 offen offset:3072 nt lds\n"
"s_mov_b32 m0, s56\n"
"s_nop 0\n"
"buffer_load_dwordx4 v248, %[srdk], s61 offen offset:3072 nt lds\n"
"s_add_u32 s61, s61, %[kvs]\n"
"LK3_4_%=:\n"
"v_mfma_f32_16x16x32_bf16 v[72:75], v[152:155], v[100:103], v[72:75]\n"
"ds_read_b64_tr_b16 v[182:183], %[wbe] offset:50688\n"
"v_mfma_f32_16x16x32_bf16 v[80:83], v[152:155], v[104:107], v[80:83]\n"
"ds_read_b64_tr_b16 v[184:185], %[wbe] offset:49920\n"
"ds_read_b64_tr_b16 v[186:187], v192 offset:50944\n"
"v_mfma_f32_16x16x32_bf16 v[44:47], v[156:159], v[96:99], v[44:47]\n"
"ds_read_b64_tr_b16 v[188:189], v192 offset:49920\n"
"v_mfma_f32_16x16x32_bf16 v[76:79], v[156:159], v[100:103], v[76:79]\n"
"ds_read_b64_tr_b16 v[190:191], %[wbe] offset:50944\n"
"v_mfma_f32_16x16x32_bf16 v[84:87], v[156:159], v[104:107], v[84:87]\n"
"s_waitcnt vmcnt(7)\n"
"s_bitcmp1_b32 s91, 5\n"
"s_cbranch_scc1 LJ3_5_%=\n"
"s_waitcnt vmcnt(2)\n"
"LJ3_5_%=:\n"
"s_waitcnt lgkmcnt(0)\n"
"s_barrier\n"
"v_mfma_f32_16x16x32_bf16 v[16:19], v[160:163], v[108:111], v[16:19]\n"
"ds_read_b128 v[96:99], v194 offset:16384\n"
"ds_read_b128 v[100:103], v196 offset:16384\n"
"v_mfma_f32_16x16x32_bf16 v[48:51], v[160:163], v[112:115], v[48:51]\n"
"ds_read_b128 v[104:107], %[xb] offset:59392\n"
"ds_read_b64_tr_b16 v[128:129], %[wbe] offset:32768\n"
"v_mfma_f32_16x16x32_bf16 v[20:23], v[164:167], v[108:111], v[20:23]\n"
"ds_read_b64_tr_b16 v[130:131], v192 offset:33792\n"
"s_add_u32 m0, s60, 143360\n"
"v_mfma_f32_16x16x32_bf16 v[52:55], v[164:167], v[112:115], v[52:55]\n"
"buffer_load_dwordx4 %[xoff], %[srdx], s63 offen lds\n"
"v_mfma_f32_16x16x32_bf16 v[24:27], v[168:171], v[108:111], v[24:27]\n"
"ds_read_b64_tr_b16 v[132:133], v192 offset:32768\n"
"ds_read_b64_tr_b16 v[134:135], %[wbe] offset:33792\n"
"v_mfma_f32_16x16x32_bf16 v[56:59], v[168:171], v[112:115], v[56:59]\n"
"ds_read_b64_tr_b16 v[136:137], %[wbe] offset:33024\n"
"v_mfma_f32_16x16x32_bf16 v[28:31], v[172:175], v[108:111], v[28:31]\n"
"buffer_load_dwordx4 v193, %[srdx], s63 offen offset:1024 lds\n"
"s_add_u32 s63, s63, 0x80\n"
"v_mfma_f32_16x16x32_bf16 v[60:63], v[172:175], v[112:115], v[60:63]\n"
"ds_read_b64_tr_b16 v[138:139], v192 offset:34048\n"
"ds_read_b64_tr_b16 v[140:141], v192 offset:33024\n"
"v_mfma_f32_16x16x32_bf16 v[32:35], v[176:179], v[108:111], v[32:35]\n"
"ds_read_b64_tr_b16 v[142:143], %[wbe] offset:34048\n"
"ds_read_b64_tr_b16 v[144:145], %[wbe] offset:33280\n"
"s_add_u32 m0, %[hb], 55296\n"
"v_mfma_f32_16x16x32_bf16 v[64:67], v[176:179], v[112:115], v[64:67]\n"
"buffer_load_dwordx4 v198, %[srdw], s62 offen lds\n"
"v_mfma_f32_16x16x32_bf16 v[36:39], v[180:183], v[108:111], v[36:39]\n"
"ds_read_b64_tr_b16 v[146:147], v192 offset:34304\n"
"v_mfma_f32_16x16x32_bf16 v[68:71], v[180:183], v[112:115], v[68:71]\n"
"ds_read_b64_tr_b16 v[148:149], v192 offset:33280\n"
"ds_read_b64_tr_b16 v[150:151], %[wbe] offset:34304\n"
"v_mfma_f32_16x16x32_bf16 v[40:43], v[184:187], v[108:111], v[40:43]\n"
"ds_read_b64_tr_b16 v[152:153], %[wbe] offset:33536\n"
"v_mfma_f32_16x16x32_bf16 v[72:75], v[184:187], v[112:115], v[72:75]\n"
"buffer_load_dwordx4 v195, %[srdw], s62 offen offset:1024 lds\n"
"s_add_u32 s62, s62, 0x40000\n"
"v_mfma_f32_16x16x32_bf16 v[80:83], v[184:187], v[116:119], v[80:83]\n"
"ds_read_b64_tr_b16 v[154:155], v192 offset:34560\n"
"ds_read_b64_tr_b16 v[156:157], v192 offset:33536\n"
"v_mfma_f32_16x16x32_bf16 v[44:47], v[188:191], v[108:111], v[44:47]\n"
"ds_read_b64_tr_b16 v[158:159], %[wbe] offset:34560\n"
"v_mfma_f32_16x16x32_bf16 v[76:79], v[188:191], v[112:115], v[76:79]\n"
"s_bitcmp0_b32 %[xrot], 5\n"
"s_cbranch_scc1 LK3_5_%=\n"
"s_mov_b32 m0, s52\n"
"s_nop 0\n"
"buffer_load_dwordx4 %[g0], %[srdv], s61 offen nt lds\n"
"buffer_load_dwordx4 %[g1], %[srdv], s61 offen offset:1024 nt lds\n"
"buffer_load_dwordx4 %[g0], %[srdk], s61 offen offset:2048 nt lds\n"
"buffer_load_dwordx4 %[g1], %[srdk], s61 offen offset:3072 nt lds\n"
"s_mov_b32 m0, s56\n"
"s_nop 0\n"
"buffer_load_dwordx4 v248, %[srdk], s61 offen offset:3072 nt lds\n"
"s_add_u32 s61, s61, %[kvs]\n"
"LK3_5_%=:\n"
"v_mfma_f32_16x16x32_bf16 v[84:87], v[188:191], v[116:119], v[84:87]\n"
"s_waitcnt vmcnt(9)\n"
"s_bitcmp1_b32 s91, 6\n"
"s_cbranch_scc1 LJ3_6_%=\n"
"s_waitcnt vmcnt(4)\n"
"LJ3_6_%=:\n"
"s_waitcnt lgkmcnt(0)\n"
"s_barrier\n"
"v_mfma_f32_16x16x32_bf16 v[16:19], v[128:131], v[96:99], v[16:19]\n"
"ds_read_b128 v[108:111], v194 offset:16896\n"
"ds_read_b128 v[112:115], v196 offset:16896\n"
"v_mfma_f32_16x16x32_bf16 v[48:51], v[128:131], v[100:103], v[48:51]\n"
"ds_read_b128 v[116:119], %[xb] offset:59904\n"
"v_mfma_f32_16x16x32_bf16 v[20:23], v[132:135], v[96:99], v[20:23]\n"
"ds_read_b64_tr_b16 v[160:161], %[wbe] offset:40960\n"
"ds_read_b64_tr_b16 v[162:163], v192 offset:41984\n"
"v_mfma_f32_16x16x32_bf16 v[52:55], v[132:135], v[100:103], v[52:55]\n"
"ds_read_b64_tr_b16 v[164:165], v192 offset:40960\n"
"s_add_u32 m0, %[hb], 38912\n"
"v_mfma_f32_16x16x32_bf16 v[24:27], v[136:139], v[96:99], v[24:27]\n"
"buffer_load_dwordx4 v198, %[srdw], s62 offen lds\n"
"v_mfma_f32_16x16x32_bf16 v[56:59], v[136:139], v[100:103], v[56:59]\n"
"ds_read_b64_tr_b16 v[166:167], %[wbe] offset:41984\n"
"v_mfma_f32_16x16x32_bf16 v[28:31], v[140:143], v[96:99], v[28:31]\n"
"ds_read_b64_tr_b16 v[168:169], %[wbe] offset:41216\n"
"ds_read_b64_tr_b16 v[170:171], v192 offset:42240\n"
"v_mfma_f32_16x16x32_bf16 v[60:63], v[140:143], v[100:103], v[60:63]\n"
"ds_read_b64_tr_b16 v[172:173], v192 offset:41216\n"
"v_mfma_f32_16x16x32_bf16 v[32:35], v[144:147], v[96:99], v[32:35]\n"
"buffer_load_dwordx4 v195, %[srdw], s62 offen offset:1024 lds\n"
"s_add_u32 s62, s62, 0x40000\n"
"v_mfma_f32_16x16x32_bf16 v[64:67], v[144:147], v[100:103], v[64:67]\n"
"ds_read_b64_tr_b16 v[174:175], %[wbe] offset:42240\n"
"v_mfma_f32_16x16x32_bf16 v[36:39], v[148:151], v[96:99], v[36:39]\n"
"ds_read_b64_tr_b16 v[176:177], %[wbe] offset:41472\n"
"ds_read_b64_tr_b16 v[178:179], v192 offset:42496\n"
"v_mfma_f32_16x16x32_bf16 v[68:71], v[148:151], v[100:103], v[68:71]\n"
"ds_read_b64_tr_b16 v[180:181], v192 offset:41472\n"
"v_mfma_f32_16x16x32_bf16 v[40:43], v[152:155], v[96:99], v[40:43]\n"
"s_bitcmp0_b32 %[xrot], 6\n"
"s_cbranch_scc1 LK3_6_%=\n"
"s_mov_b32 m0, s52\n"
"s_nop 0\n"
"buffer_load_dwordx4 %[g0], %[srdv], s61 offen nt lds\n"
"buffer_load_dwordx4 %[g1], %[srdv], s61 offen offset:1024 nt lds\n"
"buffer_load_dwordx4 %[g0], %[srdk], s61 offen offset:2048 nt lds\n"
"buffer_load_dwordx4 %[g1], %[srdk], s61 offen offset:3072 nt lds\n"
"s_mov_b32 m0, s56\n"
"s_nop 0\n"
"buffer_load_dwordx4 v248, %[srdk], s61 offen offset:3072 nt lds\n"
"s_add_u32 s61, s61, %[kvs]\n"
"LK3_6_%=:\n"
"v_mfma_f32_16x16x32_bf16 v[72:75], v[152:155], v[100:103], v[72:75]\n"
"ds_read_b64_tr_b16 v[182:183], %[wbe] offset:42496\n"
"v_mfma_f32_16x16x32_bf16 v[80:83], v[152:155], v[104:107], v[80:83]\n"
"ds_read_b64_tr_b16 v[184:185], %[wbe] offset:41728\n"
"ds_read_b64_tr_b16 v[186:187], v192 offset:42752\n"
"v_mfma_f32_16x16x32_bf16 v[44:47], v[156:159], v[96:99], v[44:47]\n"
"ds_read_b64_tr_b16 v[188:189], v192 offset:41728\n"
"v_mfma_f32_16x16x32_bf16 v[76:79], v[156:159], v[100:103], v[76:79]\n"
"ds_read_b64_tr_b16 v[190:191], %[wbe] offset:42752\n"
"v_mfma_f32_16x16x32_bf16 v[84:87], v[156:159], v[104:107], v[84:87]\n"
"s_waitcnt vmcnt(7)\n"
"s_bitcmp1_b32 s91, 7\n"
"s_cbranch_scc1 LJ3_7_%=\n"
"s_waitcnt vmcnt(2)\n"
"LJ3_7_%=:\n"
"s_waitcnt lgkmcnt(0)\n"
"s_barrier\n"
"v_mfma_f32_16x16x32_bf16 v[16:19], v[160:163], v[108:111], v[16:19]\n"
"ds_read_b128 v[96:99], v194 offset:0\n"
"ds_read_b128 v[100:103], v196 offset:0\n"
"v_mfma_f32_16x16x32_bf16 v[48:51], v[160:163], v[112:115], v[48:51]\n"
"ds_read_b128 v[104:107], %[xb] offset:57344\n"
"ds_read_b64_tr_b16 v[128:129], %[wbe] offset:49152\n"
"v_mfma_f32_16x16x32_bf16 v[20:23], v[164:167], v[108:111], v[20:23]\n"
"ds_read_b64_tr_b16 v[130:131], v192 offset:50176\n"
"s_add_u32 m0, s60, 159744\n"
"v_mfma_f32_16x16x32_bf16 v[52:55], v[164:167], v[112:115], v[52:55]\n"
"buffer_load_dwordx4 %[xoff], %[srdx], s63 offen lds\n"
"v_mfma_f32_16x16x32_bf16 v[24:27], v[168:171], v[108:111], v[24:27]\n"
"ds_read_b64_tr_b16 v[132:133], v192 offset:49152\n"
"ds_read_b64_tr_b16 v[134:135], %[wbe] offset:50176\n"
"v_mfma_f32_16x16x32_bf16 v[56:59], v[168:171], v[112:115], v[56:59]\n"
"ds_read_b64_tr_b16 v[136:137], %[wbe] offset:49408\n"
"v_mfma_f32_16x16x32_bf16 v[28:31], v[172:175], v[108:111], v[28:31]\n"
"buffer_load_dwordx4 v193, %[srdx], s63 offen offset:1024 lds\n"
"s_add_u32 s63, s63, 0x80\n"
"v_mfma_f32_16x16x32_bf16 v[60:63], v[172:175], v[112:115], v[60:63]\n"
"ds_read_b64_tr_b16 v[138:139], v192 offset:50432\n"
"ds_read_b64_tr_b16 v[140:141], v192 offset:49408\n"
"v_mfma_f32_16x16x32_bf16 v[32:35], v[176:179], v[108:111], v[32:35]\n"
"ds_read_b64_tr_b16 v[142:143], %[wbe] offset:50432\n"
"ds_read_b64_tr_b16 v[144:145], %[wbe] offset:49664\n"
"s_add_u32 m0, %[hb], 47104\n"
"v_mfma_f32_16x16x32_bf16 v[64:67], v[176:179], v[112:115], v[64:67]\n"
"buffer_load_dwordx4 v198, %[srdw], s62 offen lds\n"
"v_mfma_f32_16x16x32_bf16 v[36:39], v[180:183], v[108:111], v[36:39]\n"
"ds_read_b64_tr_b16 v[146:147], v192 offset:50688\n"
"v_mfma_f32_16x16x32_bf16 v[68:71], v[180:183], v[112:115], v[68:71]\n"
"ds_read_b64_tr_b16 v[148:149], v192 offset:49664\n"
"ds_read_b64_tr_b16 v[150:151], %[wbe] offset:50688\n"
"v_mfma_f32_16x16x32_bf16 v[40:43], v[184:187], v[108:111], v[40:43]\n"
"ds_read_b64_tr_b16 v[152:153], %[wbe] offset:49920\n"
"v_mfma_f32_16x16x32_bf16 v[72:75], v[184:187], v[112:115], v[72:75]\n"
"buffer_load_dwordx4 v195, %[srdw], s62 offen offset:1024 lds\n"
"s_add_u32 s62, s62, 0x40000\n"
"v_mfma_f32_16x16x32_bf16 v[80:83], v[184:187], v[116:119], v[80:83]\n"
"ds_read_b64_tr_b16 v[154:155], v192 offset:50944\n"
"ds_read_b64_tr_b16 v[156:157], v192 offset:49920\n"
"v_mfma_f32_16x16x32_bf16 v[44:47], v[188:191], v[108:111], v[44:47]\n"
"ds_read_b64_tr_b16 v[158:159], %[wbe] offset:50944\n"
"v_mfma_f32_16x16x32_bf16 v[76:79], v[188:191], v[112:115], v[76:79]\n"
"s_bitcmp0_b32 %[xrot], 7\n"
"s_cbranch_scc1 LK3_7_%=\n"
"s_mov_b32 m0, s52\n"
"s_nop 0\n"
"buffer_load_dwordx4 %[g0], %[srdv], s61 offen nt lds\n"
"buffer_load_dwordx4 %[g1], %[srdv], s61 offen offset:1024 nt lds\n"
"buffer_load_dwordx4 %[g0], %[srdk], s61 offen offset:2048 nt lds\n"
"buffer_load_dwordx4 %[g1], %[srdk], s61 offen offset:3072 nt lds\n"
"s_mov_b32 m0, s56\n"
"s_nop 0\n"
"buffer_load_dwordx4 v248, %[srdk], s61 offen offset:3072 nt lds\n"
"s_add_u32 s61, s61, %[kvs]\n"
"LK3_7_%=:\n"
"v_mfma_f32_16x16x32_bf16 v[84:87], v[188:191], v[116:119], v[84:87]\n"
"s_waitcnt vmcnt(9)\n"
"s_bitcmp1_b32 s91, 8\n"
"s_cbranch_scc1 LJ3_8_%=\n"
"s_waitcnt vmcnt(4)\n"
"LJ3_8_%=:\n"
"s_waitcnt lgkmcnt(0)\n"
"s_barrier\n"
"v_mfma_f32_16x16x32_bf16 v[16:19], v[128:131], v[96:99], v[16:19]\n"
"ds_read_b128 v[108:111], v194 offset:512\n"
"ds_read_b128 v[112:115], v196 offset:512\n"
"v_mfma_f32_16x16x32_bf16 v[48:51], v[128:131], v[100:103], v[48:51]\n"
"ds_read_b128 v[116:119], %[xb] offset:57856\n"
"v_mfma_f32_16x16x32_bf16 v[20:23], v[132:135], v[96:99], v[20:23]\n"
"ds_read_b64_tr_b16 v[160:161], %[wbe] offset:32768\n"
"v_mfma_f32_16x16x32_bf16 v[52:55], v[132:135], v[100:103], v[52:55]\n"
"ds_read_b64_tr_b16 v[162:163], v192 offset:33792\n"
"ds_read_b64_tr_b16 v[164:165], v192 offset:32768\n"
"s_add_u32 m0, %[hb], 55296\n"
"v_mfma_f32_16x16x32_bf16 v[24:27], v[136:139], v[96:99], v[24:27]\n"
"buffer_load_dwordx4 v198, %[srdw], s62 offen lds\n"
"v_mfma_f32_16x16x32_bf16 v[56:59], v[136:139], v[100:103], v[56:59]\n"
"ds_read_b64_tr_b16 v[166:167], %[wbe] offset:33792\n"
"v_mfma_f32_16x16x32_bf16 v[28:31], v[140:143], v[96:99], v[28:31]\n"
"ds_read_b64_tr_b16 v[168:169], %[wbe] offset:33024\n"
"v_mfma_f32_16x16x32_bf16 v[60:63], v[140:143], v[100:103], v[60:63]\n"
"ds_read_b64_tr_b16 v[170:171], v192 offset:34048\n"
"v_mfma_f32_16x16x32_bf16 v[32:35], v[144:147], v[96:99], v[32:35]\n"
"buffer_load_dwordx4 v195, %[srdw], s62 offen offset:1024 lds\n"
"s_add_u32 s62, s62, 0x40000\n"
"v_mfma_f32_16x16x32_bf16 v[64:67], v[144:147], v[100:103], v[64:67]\n"
"ds_read_b64_tr_b16 v[172:173], v192 offset:33024\n"
"ds_read_b64_tr_b16 v[174:175], %[wbe] offset:34048\n"
"v_mfma_f32_16x16x32_bf16 v[36:39], v[148:151], v[96:99], v[36:39]\n"
"ds_read_b64_tr_b16 v[176:177], %[wbe] offset:33280\n"
"v_mfma_f32_16x16x32_bf16 v[68:71], v[148:151], v[100:103], v[68:71]\n"
"ds_read_b64_tr_b16 v[178:179], v192 offset:34304\n"
"v_mfma_f32_16x16x32_bf16 v[40:43], v[152:155], v[96:99], v[40:43]\n"
"ds_read_b64_tr_b16 v[180:181], v192 offset:33280\n"
"v_mfma_f32_16x16x32_bf16 v[72:75], v[152:155], v[100:103], v[72:75]\n"
"ds_read_b64_tr_b16 v[182:183], %[wbe] offset:34304\n"
"ds_read_b64_tr_b16 v[184:185], %[wbe] offset:33536\n"
"v_mfma_f32_16x16x32_bf16 v[80:83], v[152:155], v[104:107], v[80:83]\n"
"ds_read_b64_tr_b16 v[186:187], v192 offset:34560\n"
"v_mfma_f32_16x16x32_bf16 v[44:47], v[156:159], v[96:99], v[44:47]\n"
"ds_read_b64_tr_b16 v[188:189], v192 offset:33536\n"
"v_mfma_f32_16x16x32_bf16 v[76:79], v[156:159], v[100:103], v[76:79]\n"
"ds_read_b64_tr_b16 v[190:191], %[wbe] offset:34560\n"
"v_mfma_f32_16x16x32_bf16 v[84:87], v[156:159], v[104:107], v[84:87]\n"
"s_waitcnt vmcnt(7)\n"
"s_bitcmp1_b32 s91, 9\n"
"s_cbranch_scc1 LJ3_9_%=\n"
"s_waitcnt vmcnt(2)\n"
"LJ3_9_%=:\n"
"s_waitcnt lgkmcnt(0)\n"
"s_barrier\n"
"v_mfma_f32_16x16x32_bf16 v[16:19], v[160:163], v[108:111], v[16:19]\n"
"ds_read_b128 v[96:99], v194 offset:16384\n"
"ds_read_b128 v[100:103], v196 offset:16384\n"
"v_mfma_f32_16x16x32_bf16 v[48:51], v[160:163], v[112:115], v[48:51]\n"
"ds_read_b128 v[104:107], %[xb] offset:59392\n"
"v_mfma_f32_16x16x32_bf16 v[20:23], v[164:167], v[108:111], v[20:23]\n"
"ds_read_b64_tr_b16 v[128:129], %[wbe] offset:40960\n"
"ds_read_b64_tr_b16 v[130:131], v192 offset:41984\n"
"v_mfma_f32_16x16x32_bf16 v[52:55], v[164:167], v[112:115], v[52:55]\n"
"ds_read_b64_tr_b16 v[132:133], v192 offset:40960\n"
"s_add_u32 m0, s60, 143360\n"
"v_mfma_f32_16x16x32_bf16 v[24:27], v[168:171], v[108:111], v[24:27]\n"
"buffer_load_dwordx4 %[xoff], %[srdx], s63 offen lds\n"
"v_mfma_f32_16x16x32_bf16 v[56:59], v[168:171], v[112:115], v[56:59]\n"
"ds_read_b64_tr_b16 v[134:135], %[wbe] offset:41984\n"
"ds_read_b64_tr_b16 v[136:137], %[wbe] offset:41216\n"
"v_mfma_f32_16x16x32_bf16 v[28:31], v[172:175], v[108:111], v[28:31]\n"
"ds_read_b64_tr_b16 v[138:139], v192 offset:42240\n"
"v_mfma_f32_16x16x32_bf16 v[60:63], v[172:175], v[112:115], v[60:63]\n"
"ds_read_b64_tr_b16 v[140:141], v192 offset:41216\n"
"ds_read_b64_tr_b16 v[142:143], %[wbe] offset:42240\n"
"v_mfma_f32_16x16x32_bf16 v[32:35], v[176:179], v[108:111], v[32:35]\n"
"buffer_load_dwordx4 v193, %[srdx], s63 offen offset:1024 lds\n"
"s_add_u32 s63, s63, 0x80\n"
"v_mfma_f32_16x16x32_bf16 v[64:67], v[176:179], v[112:115], v[64:67]\n"
"ds_read_b64_tr_b16 v[144:145], %[wbe] offset:41472\n"
"v_mfma_f32_16x16x32_bf16 v[36:39], v[180:183], v[108:111], v[36:39]\n"
"ds_read_b64_tr_b16 v[146:147], v192 offset:42496\n"
"ds_read_b64_tr_b16 v[148:149], v192 offset:41472\n"
"v_mfma_f32_16x16x32_bf16 v[68:71], v[180:183], v[112:115], v[68:71]\n"
"ds_read_b64_tr_b16 v[150:151], %[wbe] offset:42496\n"
"s_add_u32 m0, %[hb], 38912\n"
"v_mfma_f32_16x16x32_bf16 v[40:43], v[184:187], v[108:111], v[40:43]\n"
"buffer_load_dwordx4 v198, %[srdw], s62 offen lds\n"
"v_mfma_f32_16x16x32_bf16 v[72:75], v[184:187], v[112:115], v[72:75]\n"
"ds_read_b64_tr_b16 v[152:153], %[wbe] offset:41728\n"
"ds_read_b64_tr_b16 v[154:155], v192 offset:42752\n"
"v_mfma_f32_16x16x32_bf16 v[80:83], v[184:187], v[116:119], v[80:83]\n"
"ds_read_b64_tr_b16 v[156:157], v192 offset:41728\n"
"v_mfma_f32_16x16x32_bf16 v[44:47], v[188:191], v[108:111], v[44:47]\n"
"ds_read_b64_tr_b16 v[158:159], %[wbe] offset:42752\n"
"v_mfma_f32_16x16x32_bf16 v[76:79], v[188:191], v[112:115], v[76:79]\n"
"buffer_load_dwordx4 v195, %[srdw], s62 offen offset:1024 lds\n"
"s_add_u32 s62, s62, 0x40000\n"
"v_mfma_f32_16x16x32_bf16 v[84:87], v[188:191], v[116:119], v[84:87]\n"
"s_waitcnt vmcnt(4) lgkmcnt(0)\n"
"s_barrier\n"
"v_mfma_f32_16x16x32_bf16 v[16:19], v[128:131], v[96:99], v[16:19]\n"
"ds_read_b128 v[108:111], v194 offset:16896\n"
"ds_read_b128 v[112:115], v196 offset:16896\n"
"v_mfma_f32_16x16x32_bf16 v[48:51], v[128:131], v[100:103], v[48:51]\n"
"ds_read_b128 v[116:119], %[xb] offset:59904\n"
"v_mfma_f32_16x16x32_bf16 v[20:23], v[132:135], v[96:99], v[20:23]\n"
"ds_read_b64_tr_b16 v[160:161], %[wbe] offset:49152\n"
"ds_read_b64_tr_b16 v[162:163], v192 offset:50176\n"
"v_mfma_f32_16x16x32_bf16 v[52:55], v[132:135], v[100:103], v[52:55]\n"
"ds_read_b64_tr_b16 v[164:165], v192 offset:49152\n"
"s_add_u32 m0, %[hb], 47104\n"
"v_mfma_f32_16x16x32_bf16 v[24:27], v[136:139], v[96:99], v[24:27]\n"
"buffer_load_dwordx4 v198, %[srdw], s62 offen lds\n"
"v_mfma_f32_16x16x32_bf16 v[56:59], v[136:139], v[100:103], v[56:59]\n"
"ds_read_b64_tr_b16 v[166:167], %[wbe] offset:50176\n"
"v_mfma_f32_16x16x32_bf16 v[28:31], v[140:143], v[96:99], v[28:31]\n"
"ds_read_b64_tr_b16 v[168:169], %[wbe] offset:49408\n"
"ds_read_b64_tr_b16 v[170:171], v192 offset:50432\n"
"v_mfma_f32_16x16x32_bf16 v[60:63], v[140:143], v[100:103], v[60:63]\n"
"ds_read_b64_tr_b16 v[172:173], v192 offset:49408\n"
"v_mfma_f32_16x16x32_bf16 v[32:35], v[144:147], v[96:99], v[32:35]\n"
"buffer_load_dwordx4 v195, %[srdw], s62 offen offset:1024 lds\n"
"s_add_u32 s62, s62, 0x40000\n"
"v_mfma_f32_16x16x32_bf16 v[64:67], v[144:147], v[100:103], v[64:67]\n"
"ds_read_b64_tr_b16 v[174:175], %[wbe] offset:50432\n"
"v_mfma_f32_16x16x32_bf16 v[36:39], v[148:151], v[96:99], v[36:39]\n"
"ds_read_b64_tr_b16 v[176:177], %[wbe] offset:49664\n"
"ds_read_b64_tr_b16 v[178:179], v192 offset:50688\n"
"v_mfma_f32_16x16x32_bf16 v[68:71], v[148:151], v[100:103], v[68:71]\n"
"ds_read_b64_tr_b16 v[180:181], v192 offset:49664\n"
"v_mfma_f32_16x16x32_bf16 v[40:43], v[152:155], v[96:99], v[40:43]\n"
"s_bitcmp0_b32 %[xrot], 10\n"
"s_cbranch_scc1 LK3_10_%=\n"
"s_mov_b32 m0, s53\n"
"s_nop 0\n"
"buffer_load_dwordx4 %[g0], %[srdv], s61 offen nt lds\n"
"buffer_load_dwordx4 %[g1], %[srdv], s61 offen offset:1024 nt lds\n"
"buffer_load_dwordx4 %[g0], %[srdk], s61 offen offset:2048 nt lds\n"
"buffer_load_dwordx4 %[g1], %[srdk], s61 offen offset:3072 nt lds\n"
"s_mov_b32 m0, s57\n"
"s_nop 0\n"
"buffer_load_dwordx4 v248, %[srdk], s61 offen offset:3072 nt lds\n"
"s_add_u32 s61, s61, %[kvs]\n"
"LK3_10_%=:\n"
"v_mfma_f32_16x16x32_bf16 v[72:75], v[152:155], v[100:103], v[72:75]\n"
"ds_read_b64_tr_b16 v[182:183], %[wbe] offset:50688\n"
"v_mfma_f32_16x16x32_bf16 v[80:83], v[152:155], v[104:107], v[80:83]\n"
"ds_read_b64_tr_b16 v[184:185], %[wbe] offset:49920\n"
"ds_read_b64_tr_b16 v[186:187], v192 offset:50944\n"
"v_mfma_f32_16x16x32_bf16 v[44:47], v[156:159], v[96:99], v[44:47]\n"
"ds_read_b64_tr_b16 v[188:189], v192 offset:49920\n"
"v_mfma_f32_16x16x32_bf16 v[76:79], v[156:159], v[100:103], v[76:79]\n"
"ds_read_b64_tr_b16 v[190:191], %[wbe] offset:50944\n"
"v_mfma_f32_16x16x32_bf16 v[84:87], v[156:159], v[104:107], v[84:87]\n"
"s_waitcnt vmcnt(7)\n"
"s_bitcmp1_b32 s91, 11\n"
"s_cbranch_scc1 LJ3_11_%=\n"
"s_waitcnt vmcnt(2)\n"
"LJ3_11_%=:\n"
"s_waitcnt lgkmcnt(0)\n"
"s_barrier\n"
"v_mfma_f32_16x16x32_bf16 v[16:19], v[160:163], v[108:111], v[16:19]\n"
"ds_read_b128 v[96:99], v194 offset:0\n"
"ds_read_b128 v[100:103], v196 offset:0\n"
"v_mfma_f32_16x16x32_bf16 v[48:51], v[160:163], v[112:115], v[48:51]\n"
"ds_read_b128 v[104:107], %[xb] offset:57344\n"
"ds_read_b64_tr_b16 v[128:129], %[wbe] offset:32768\n"
"v_mfma_f32_16x16x32_bf16 v[20:23], v[164:167], v[108:111], v[20:23]\n"
"ds_read_b64_tr_b16 v[130:131], v192 offset:33792\n"
"ds_read_b64_tr_b16 v[132:133], v192 offset:32768\n"
"s_add_u32 m0, s60, 159744\n"
"v_mfma_f32_16x16x32_bf16 v[52:55], v[164:167], v[112:115], v[52:55]\n"
"buffer_load_dwordx4 %[xoff], %[srdx], s63 offen lds\n"
"v_mfma_f32_16x16x32_bf16 v[24:27], v[168:171], v[108:111], v[24:27]\n"
"ds_read_b64_tr_b16 v[134:135], %[wbe] offset:33792\n"
"v_mfma_f32_16x16x32_bf16 v[56:59], v[168:171], v[112:115], v[56:59]\n"
"buffer_load_dwordx4 v193, %[srdx], s63 offen offset:1024 lds\n"
"v_mfma_f32_16x16x32_bf16 v[28:31], v[172:175], v[108:111], v[28:31]\n"
"ds_read_b64_tr_b16 v[136:137], %[wbe] offset:33024\n"
"ds_read_b64_tr_b16 v[138:139], v192 offset:34048\n"
"v_mfma_f32_16x16x32_bf16 v[60:63], v[172:175], v[112:115], v[60:63]\n"
"ds_read_b64_tr_b16 v[140:141], v192 offset:33024\n"
"ds_read_b64_tr_b16 v[142:143], %[wbe] offset:34048\n"
"s_add_u32 m0, s64, 59392\n"
"v_mfma_f32_16x16x32_bf16 v[32:35], v[176:179], v[108:111], v[32:35]\n"
"buffer_load_dwordx4 %[xo9], %[srdx], s63 offen lds\n"
"s_add_u32 s63, s63, 0x80\n"
"v_mfma_f32_16x16x32_bf16 v[64:67], v[176:179], v[112:115], v[64:67]\n"
"ds_read_b64_tr_b16 v[144:145], %[wbe] offset:33280\n"
"ds_read_b64_tr_b16 v[146:147], v192 offset:34304\n"
"v_mfma_f32_16x16x32_bf16 v[36:39], v[180:183], v[108:111], v[36:39]\n"
"ds_read_b64_tr_b16 v[148:149], v192 offset:33280\n"
"s_add_u32 m0, %[hb], 55296\n"
"v_mfma_f32_16x16x32_bf16 v[68:71], v[180:183], v[112:115], v[68:71]\n"
"buffer_load_dwordx4 v198, %[srdw], s62 offen lds\n"
"v_mfma_f32_16x16x32_bf16 v[40:43], v[184:187], v[108:111], v[40:43]\n"
"ds_read_b64_tr_b16 v[150:151], %[wbe] offset:34304\n"
"ds_read_b64_tr_b16 v[152:153], %[wbe] offset:33536\n"
"v_mfma_f32_16x16x32_bf16 v[72:75], v[184:187], v[112:115], v[72:75]\n"
"buffer_load_dwordx4 v195, %[srdw], s62 offen offset:1024 lds\n"
"s_add_u32 s62, s62, 0x40000\n"
"v_mfma_f32_16x16x32_bf16 v[80:83], v[184:187], v[116:119], v[80:83]\n"
"ds_read_b64_tr_b16 v[154:155], v192 offset:34560\n"
"ds_read_b64_tr_b16 v[156:157], v192 offset:33536\n"
"v_mfma_f32_16x16x32_bf16 v[44:47], v[188:191], v[108:111], v[44:47]\n"
"ds_read_b64_tr_b16 v[158:159], %[wbe] offset:34560\n"
"v_mfma_f32_16x16x32_bf16 v[76:79], v[188:191], v[112:115], v[76:79]\n"
"s_bitcmp0_b32 %[xrot], 11\n"
"s_cbranch_scc1 LK3_11_%=\n"
"s_mov_b32 m0, s53\n"
"s_nop 0\n"
"buffer_load_dwordx4 %[g0], %[srdv], s61 offen nt lds\n"
"buffer_load_dwordx4 %[g1], %[srdv], s61 offen offset:1024 nt lds\n"
"buffer_load_dwordx4 %[g0], %[srdk], s61 offen offset:2048 nt lds\n"
"buffer_load_dwordx4 %[g1], %[srdk], s61 offen offset:3072 nt lds\n"
"s_mov_b32 m0, s57\n"
"s_nop 0\n"
"buffer_load_dwordx4 v248, %[srdk], s61 offen offset:3072 nt lds\n"
"s_add_u32 s61, s61, %[kvs]\n"
"LK3_11_%=:\n"
"v_mfma_f32_16x16x32_bf16 v[84:87], v[188:191], v[116:119], v[84:87]\n"
"s_waitcnt vmcnt(10)\n"
"s_bitcmp1_b32 s91, 12\n"
"s_cbranch_scc1 LJ3_12_%=\n"
"s_waitcnt vmcnt(5)\n"
"LJ3_12_%=:\n"
"s_waitcnt lgkmcnt(0)\n"
"s_barrier\n"
"v_mfma_f32_16x16x32_bf16 v[16:19], v[128:131], v[96:99], v[16:19]\n"
"ds_read_b128 v[108:111], v194 offset:512\n"
"ds_read_b128 v[112:115], v196 offset:512\n"
"v_mfma_f32_16x16x32_bf16 v[48:51], v[128:131], v[100:103], v[48:51]\n"
"ds_read_b128 v[116:119], %[xb] offset:57856\n"
"v_mfma_f32_16x16x32_bf16 v[20:23], v[132:135], v[96:99], v[20:23]\n"
"ds_read_b64_tr_b16 v[160:161], %[wbe] offset:40960\n"
"ds_read_b64_tr_b16 v[162:163], v192 offset:41984\n"
"v_mfma_f32_16x16x32_bf16 v[52:55], v[132:135], v[100:103], v[52:55]\n"
"ds_read_b64_tr_b16 v[164:165], v192 offset:40960\n"
"s_add_u32 m0, %[hb], 38912\n"
"v_mfma_f32_16x16x32_bf16 v[24:27], v[136:139], v[96:99], v[24:27]\n"
"buffer_load_dwordx4 v198, %[srdw], s62 offen lds\n"
"v_mfma_f32_16x16x32_bf16 v[56:59], v[136:139], v[100:103], v[56:59]\n"
"ds_read_b64_tr_b16 v[166:167], %[wbe] offset:41984\n"
"v_mfma_f32_16x16x32_bf16 v[28:31], v[140:143], v[96:99], v[28:31]\n"
"ds_read_b64_tr_b16 v[168:169], %[wbe] offset:41216\n"
"ds_read_b64_tr_b16 v[170:171], v192 offset:42240\n"
"v_mfma_f32_16x16x32_bf16 v[60:63], v[140:143], v[100:103], v[60:63]\n"
"ds_read_b64_tr_b16 v[172:173], v192 offset:41216\n"
"v_mfma_f32_16x16x32_bf16 v[32:35], v[144:147], v[96:99], v[32:35]\n"
"buffer_load_dwordx4 v195, %[srdw], s62 offen offset:1024 lds\n"
"s_add_u32 s62, s62, 0x40000\n"
"v_mfma_f32_16x16x32_bf16 v[64:67], v[144:147], v[100:103], v[64:67]\n"
"ds_read_b64_tr_b16 v[174:175], %[wbe] offset:42240\n"
"v_mfma_f32_16x16x32_bf16 v[36:39], v[148:151], v[96:99], v[36:39]\n"
"ds_read_b64_tr_b16 v[176:177], %[wbe] offset:41472\n"
"ds_read_b64_tr_b16 v[178:179], v192 offset:42496\n"
"v_mfma_f32_16x16x32_bf16 v[68:71], v[148:151], v[100:103], v[68:71]\n"
"ds_read_b64_tr_b16 v[180:181], v192 offset:41472\n"
"v_mfma_f32_16x16x32_bf16 v[40:43], v[152:155], v[96:99], v[40:43]\n"
"s_bitcmp0_b32 %[xrot], 12\n"
"s_cbranch_scc1 LK3_12_%=\n"
"s_mov_b32 m0, s53\n"
"s_nop 0\n"
"buffer_load_dwordx4 %[g0], %[srdv], s61 offen nt lds\n"
"buffer_load_dwordx4 %[g1], %[srdv], s61 offen offset:1024 nt lds\n"
"buffer_load_dwordx4 %[g0], %[srdk], s61 offen offset:2048 nt lds\n"
"buffer_load_dwordx4 %[g1], %[srdk], s61 offen offset:3072 nt lds\n"
"s_mov_b32 m0, s57\n"
"s_nop 0\n"
"buffer_load_dwordx4 v248, %[srdk], s61 offen offset:3072 nt lds\n"
"s_add_u32 s61, s61, %[kvs]\n"
"LK3_12_%=:\n"
"v_mfma_f32_16x16x32_bf16 v[72:75], v[152:155], v[100:103], v[72:75]\n"
"ds_read_b64_tr_b16 v[182:183], %[wbe] offset:42496\n"
"v_mfma_f32_16x16x32_bf16 v[80:83], v[152:155], v[104:107], v[80:83]\n"
"ds_read_b64_tr_b16 v[184:185], %[wbe] offset:41728\n"
"ds_read_b64_tr_b16 v[186:187], v192 offset:42752\n"
"v_mfma_f32_16x16x32_bf16 v[44:47], v[156:159], v[96:99], v[44:47]\n"
"ds_read_b64_tr_b16 v[188:189], v192 offset:41728\n"
"v_mfma_f32_16x16x32_bf16 v[76:79], v[156:159], v[100:103], v[76:79]\n"
"ds_read_b64_tr_b16 v[190:191], %[wbe] offset:42752\n"
"v_mfma_f32_16x16x32_bf16 v[84:87], v[156:159], v[104:107], v[84:87]\n"
"s_waitcnt vmcnt(7)\n"
"s_bitcmp1_b32 s91, 13\n"
"s_cbranch_scc1 LJ3_13_%=\n"
"s_waitcnt vmcnt(2)\n"
"LJ3_13_%=:\n"
"s_waitcnt lgkmcnt(0)\n"
"s_barrier\n"
"v_mfma_f32_16x16x32_bf16 v[16:19], v[160:163], v[108:111], v[16:19]\n"
"ds_read_b128 v[96:99], v194 offset:16384\n"
"ds_read_b128 v[100:103], v196 offset:16384\n"
"v_mfma_f32_16x16x32_bf16 v[48:51], v[160:163], v[112:115], v[48:51]\n"
"ds_read_b128 v[104:107], %[xb] offset:59392\n"
"ds_read_b64_tr_b16 v[128:129], %[wbe] offset:49152\n"
"v_mfma_f32_16x16x32_bf16 v[20:23], v[164:167], v[108:111], v[20:23]\n"
"ds_read_b64_tr_b16 v[130:131], v192 offset:50176\n"
"s_add_u32 m0, s60, 143360\n"
"v_mfma_f32_16x16x32_bf16 v[52:55], v[164:167], v[112:115], v[52:55]\n"
"buffer_load_dwordx4 %[xoff], %[srdx], s63 offen lds\n"
"v_mfma_f32_16x16x32_bf16 v[24:27], v[168:171], v[108:111], v[24:27]\n"
"ds_read_b64_tr_b16 v[132:133], v192 offset:49152\n"
"ds_read_b64_tr_b16 v[134:135], %[wbe] offset:50176\n"
"v_mfma_f32_16x16x32_bf16 v[56:59], v[168:171], v[112:115], v[56:59]\n"
"ds_read_b64_tr_b16 v[136:137], %[wbe] offset:49408\n"
"v_mfma_f32_16x16x32_bf16 v[28:31], v[172:175], v[108:111], v[28:31]\n"
"buffer_load_dwordx4 v193, %[srdx], s63 offen offset:1024 lds\n"
"s_add_u32 s63, s63, 0x80\n"
"v_mfma_f32_16x16x32_bf16 v[60:63], v[172:175], v[112:115], v[60:63]\n"
"ds_read_b64_tr_b16 v[138:139], v192 offset:50432\n"
"ds_read_b64_tr_b16 v[140:141], v192 offset:49408\n"
"v_mfma_f32_16x16x32_bf16 v[32:35], v[176:179], v[108:111], v[32:35]\n"
"ds_read_b64_tr_b16 v[142:143], %[wbe] offset:50432\n"
"ds_read_b64_tr_b16 v[144:145], %[wbe] offset:49664\n"
"s_add_u32 m0, %[hb], 47104\n"
"v_mfma_f32_16x16x32_bf16 v[64:67], v[176:179], v[112:115], v[64:67]\n"
"buffer_load_dwordx4 v198, %[srdw], s62 offen lds\n"
"v_mfma_f32_16x16x32_bf16 v[36:39], v[180:183], v[108:111], v[36:39]\n"
"ds_read_b64_tr_b16 v[146:147], v192 offset:50688\n"
"v_mfma_f32_16x16x32_bf16 v[68:71], v[180:183], v[112:115], v[68:71]\n"
"ds_read_b64_tr_b16 v[148:149], v192 offset:49664\n"
"ds_read_b64_tr_b16 v[150:151], %[wbe] offset:50688\n"
"v_mfma_f32_16x16x32_bf16 v[40:43], v[184:187], v[108:111], v[40:43]\n"
"ds_read_b64_tr_b16 v[152:153], %[wbe] offset:49920\n"
"v_mfma_f32_16x16x32_bf16 v[72:75], v[184:187], v[112:115], v[72:75]\n"
"buffer_load_dwordx4 v195, %[srdw], s62 offen offset:1024 lds\n"
"s_add_u32 s62, s62, 0x40000\n"
"v_mfma_f32_16x16x32_bf16 v[80:83], v[184:187], v[116:119], v[80:83]\n"
"ds_read_b64_tr_b16 v[154:155], v192 offset:50944\n"
"ds_read_b64_tr_b16 v[156:157], v192 offset:49920\n"
"v_mfma_f32_16x16x32_bf16 v[44:47], v[188:191], v[108:111], v[44:47]\n"
"ds_read_b64_tr_b16 v[158:159], %[wbe] offset:50944\n"
"v_mfma_f32_16x16x32_bf16 v[76:79], v[188:191], v[112:115], v[76:79]\n"
"s_bitcmp0_b32 %[xrot], 13\n"
"s_cbranch_scc1 LK3_13_%=\n"
"s_mov_b32 m0, s53\n"
"s_nop 0\n"
"buffer_load_dwordx4 %[g0], %[srdv], s61 offen nt lds\n"
"buffer_load_dwordx4 %[g1], %[srdv], s61 offen offset:1024 nt lds\n"
"buffer_load_dwordx4 %[g0], %[srdk], s61 offen offset:2048 nt lds\n"
"buffer_load_dwordx4 %[g1], %[srdk], s61 offen offset:3072 nt lds\n"
"s_mov_b32 m0, s57\n"
"s_nop 0\n"
"buffer_load_dwordx4 v248, %[srdk], s61 offen offset:3072 nt lds\n"
"s_add_u32 s61, s61, %[kvs]\n"
"LK3_13_%=:\n"
"v_mfma_f32_16x16x32_bf16 v[84:87], v[188:191], v[116:119], v[84:87]\n"
"s_waitcnt vmcnt(9)\n"
"s_bitcmp1_b32 s91, 14\n"
"s_cbranch_scc1 LJ3_14_%=\n"
"s_waitcnt vmcnt(4)\n"
"LJ3_14_%=:\n"
"s_waitcnt lgkmcnt(0)\n"
"s_barrier\n"
"v_mfma_f32_16x16x32_bf16 v[16:19], v[128:131], v[96:99], v[16:19]\n"
"ds_read_b128 v[108:111], v194 offset:16896\n"
"ds_read_b128 v[112:115], v196 offset:16896\n"
"v_mfma_f32_16x16x32_bf16 v[48:51], v[128:131], v[100:103], v[48:51]\n"
"ds_read_b128 v[116:119], %[xb] offset:59904\n"
"v_mfma_f32_16x16x32_bf16 v[20:23], v[132:135], v[96:99], v[20:23]\n"
"ds_read_b64_tr_b16 v[160:161], %[wbe] offset:32768\n"
"v_mfma_f32_16x16x32_bf16 v[52:55], v[132:135], v[100:103], v[52:55]\n"
"ds_read_b64_tr_b16 v[162:163], v192 offset:33792\n"
"ds_read_b64_tr_b16 v[164:165], v192 offset:32768\n"
"s_add_u32 m0, %[hb], 55296\n"
"v_mfma_f32_16x16x32_bf16 v[24:27], v[136:139], v[96:99], v[24:27]\n"
"buffer_load_dwordx4 v198, %[srdw], s62 offen lds\n"
"v_mfma_f32_16x16x32_bf16 v[56:59], v[136:139], v[100:103], v[56:59]\n"
"ds_read_b64_tr_b16 v[166:167], %[wbe] offset:33792\n"
"v_mfma_f32_16x16x32_bf16 v[28:31], v[140:143], v[96:99], v[28:31]\n"
"ds_read_b64_tr_b16 v[168:169], %[wbe] offset:33024\n"
"v_mfma_f32_16x16x32_bf16 v[60:63], v[140:143], v[100:103], v[60:63]\n"
"ds_read_b64_tr_b16 v[170:171], v192 offset:34048\n"
"v_mfma_f32_16x16x32_bf16 v[32:35], v[144:147], v[96:99], v[32:35]\n"
"buffer_load_dwordx4 v195, %[srdw], s62 offen offset:1024 lds\n"
"s_add_u32 s62, s62, 0x40000\n"
"v_mfma_f32_16x16x32_bf16 v[64:67], v[144:147], v[100:103], v[64:67]\n"
"ds_read_b64_tr_b16 v[172:173], v192 offset:33024\n"
"ds_read_b64_tr_b16 v[174:175], %[wbe] offset:34048\n"
"v_mfma_f32_16x16x32_bf16 v[36:39], v[148:151], v[96:99], v[36:39]\n"
"ds_read_b64_tr_b16 v[176:177], %[wbe] offset:33280\n"
"v_mfma_f32_16x16x32_bf16 v[68:71], v[148:151], v[100:103], v[68:71]\n"
"ds_read_b64_tr_b16 v[178:179], v192 offset:34304\n"
"v_mfma_f32_16x16x32_bf16 v[40:43], v[152:155], v[96:99], v[40:43]\n"
"ds_read_b64_tr_b16 v[180:181], v192 offset:33280\n"
"v_mfma_f32_16x16x32_bf16 v[72:75], v[152:155], v[100:103], v[72:75]\n"
"ds_read_b64_tr_b16 v[182:183], %[wbe] offset:34304\n"
"ds_read_b64_tr_b16 v[184:185], %[wbe] offset:33536\n"
"v_mfma_f32_16x16x32_bf16 v[80:83], v[152:155], v[104:107], v[80:83]\n"
"ds_read_b64_tr_b16 v[186:187], v192 offset:34560\n"
"v_mfma_f32_16x16x32_bf16 v[44:47], v[156:159], v[96:99], v[44:47]\n"
"ds_read_b64_tr_b16 v[188:189], v192 offset:33536\n"
"v_mfma_f32_16x16x32_bf16 v[76:79], v[156:159], v[100:103], v[76:79]\n"
"ds_read_b64_tr_b16 v[190:191], %[wbe] offset:34560\n"
"v_mfma_f32_16x16x32_bf16 v[84:87], v[156:159], v[104:107], v[84:87]\n"
"s_waitcnt vmcnt(7)\n"
"s_bitcmp1_b32 s91, 15\n"
"s_cbranch_scc1 LJ3_15_%=\n"
"s_waitcnt vmcnt(2)\n"
"LJ3_15_%=:\n"
"s_waitcnt lgkmcnt(0)\n"
"s_barrier\n"
"v_mfma_f32_16x16x32_bf16 v[16:19], v[160:163], v[108:111], v[16:19]\n"
"ds_read_b128 v[96:99], v194 offset:0\n"
"ds_read_b128 v[100:103], v196 offset:0\n"
"v_mfma_f32_16x16x32_bf16 v[48:51], v[160:163], v[112:115], v[48:51]\n"
"ds_read_b128 v[104:107], %[xb] offset:57344\n"
"v_mfma_f32_16x16x32_bf16 v[20:23], v[164:167], v[108:111], v[20:23]\n"
"ds_read_b64_tr_b16 v[128:129], %[wbe] offset:40960\n"
"v_mfma_f32_16x16x32_bf16 v[52:55], v[164:167], v[112:115], v[52:55]\n"
"ds_read_b64_tr_b16 v[130:131], v192 offset:41984\n"
"v_mfma_f32_16x16x32_bf16 v[24:27], v[168:171], v[108:111], v[24:27]\n"
"ds_read_b64_tr_b16 v[132:133], v192 offset:40960\n"
"v_mfma_f32_16x16x32_bf16 v[56:59], v[168:171], v[112:115], v[56:59]\n"
"ds_read_b64_tr_b16 v[134:135], %[wbe] offset:41984\n"
"v_mfma_f32_16x16x32_bf16 v[28:31], v[172:175], v[108:111], v[28:31]\n"
"ds_read_b64_tr_b16 v[136:137], %[wbe] offset:41216\n"
"v_mfma_f32_16x16x32_bf16 v[60:63], v[172:175], v[112:115], v[60:63]\n"
"ds_read_b64_tr_b16 v[138:139], v192 offset:42240\n"
"v_mfma_f32_16x16x32_bf16 v[32:35], v[176:179], v[108:111], v[32:35]\n"
"ds_read_b64_tr_b16 v[140:141], v192 offset:41216\n"
"ds_read_b64_tr_b16 v[142:143], %[wbe] offset:42240\n"
"v_mfma_f32_16x16x32_bf16 v[64:67], v[176:179], v[112:115], v[64:67]\n"
"ds_read_b64_tr_b16 v[144:145], %[wbe] offset:41472\n"
"v_mfma_f32_16x16x32_bf16 v[36:39], v[180:183], v[108:111], v[36:39]\n"
"ds_read_b64_tr_b16 v[146:147], v192 offset:42496\n"
"v_mfma_f32_16x16x32_bf16 v[68:71], v[180:183], v[112:115], v[68:71]\n"
"ds_read_b64_tr_b16 v[148:149], v192 offset:41472\n"
"v_mfma_f32_16x16x32_bf16 v[40:43], v[184:187], v[108:111], v[40:43]\n"
"ds_read_b64_tr_b16 v[150:151], %[wbe] offset:42496\n"
"v_mfma_f32_16x16x32_bf16 v[72:75], v[184:187], v[112:115], v[72:75]\n"
"ds_read_b64_tr_b16 v[152:153], %[wbe] offset:41728\n"
"v_mfma_f32_16x16x32_bf16 v[80:83], v[184:187], v[116:119], v[80:83]\n"
"ds_read_b64_tr_b16 v[154:155], v192 offset:42752\n"
"v_mfma_f32_16x16x32_bf16 v[44:47], v[188:191], v[108:111], v[44:47]\n"
"ds_read_b64_tr_b16 v[156:157], v192 offset:41728\n"
"v_mfma_f32_16x16x32_bf16 v[76:79], v[188:191], v[112:115], v[76:79]\n"
"ds_read_b64_tr_b16 v[158:159], %[wbe] offset:42752\n"
"v_mfma_f32_16x16x32_bf16 v[84:87], v[188:191], v[116:119], v[84:87]\n"
"s_waitcnt vmcnt(0) lgkmcnt(0)\n"
"s_barrier\n"
"v_mfma_f32_16x16x32_bf16 v[16:19], v[128:131], v[96:99], v[16:19]\n"
"ds_read_b128 v[108:111], v194 offset:512\n"
"ds_read_b128 v[112:115], v196 offset:512\n"
"v_mfma_f32_16x16x32_bf16 v[48:51], v[128:131], v[100:103], v[48:51]\n"
"ds_read_b128 v[116:119], %[xb] offset:57856\n"
"ds_read_b64_tr_b16 v[160:161], %[wbe] offset:49152\n"
"v_mfma_f32_16x16x32_bf16 v[20:23], v[132:135], v[96:99], v[20:23]\n"
"ds_read_b64_tr_b16 v[162:163], v192 offset:50176\n"
"s_mov_b32 m0, s54\n"
"v_mfma_f32_16x16x32_bf16 v[52:55], v[132:135], v[100:103], v[52:55]\n"
"buffer_load_dwordx4 %[g0], %[srdv], s61 offen nt lds\n"
"v_mfma_f32_16x16x32_bf16 v[24:27], v[136:139], v[96:99], v[24:27]\n"
"ds_read_b64_tr_b16 v[164:165], v192 offset:49152\n"
"ds_read_b64_tr_b16 v[166:167], %[wbe] offset:50176\n"
"v_mfma_f32_16x16x32_bf16 v[56:59], v[136:139], v[100:103], v[56:59]\n"
"ds_read_b64_tr_b16 v[168:169], %[wbe] offset:49408\n"
"v_mfma_f32_16x16x32_bf16 v[28:31], v[140:143], v[96:99], v[28:31]\n"
"buffer_load_dwordx4 %[g1], %[srdv], s61 offen offset:1024 nt lds\n"
"v_mfma_f32_16x16x32_bf16 v[60:63], v[140:143], v[100:103], v[60:63]\n"
"ds_read_b64_tr_b16 v[170:171], v192 offset:50432\n"
"ds_read_b64_tr_b16 v[172:173], v192 offset:49408\n"
"v_mfma_f32_16x16x32_bf16 v[32:35], v[144:147], v[96:99], v[32:35]\n"
"ds_read_b64_tr_b16 v[174:175], %[wbe] offset:50432\n"
"ds_read_b64_tr_b16 v[176:177], %[wbe] offset:49664\n"
"v_mfma_f32_16x16x32_bf16 v[64:67], v[144:147], v[100:103], v[64:67]\n"
"buffer_load_dwordx4 %[g0], %[srdk], s61 offen offset:2048 nt lds\n"
"v_mfma_f32_16x16x32_bf16 v[36:39], v[148:151], v[96:99], v[36:39]\n"
"ds_read_b64_tr_b16 v[178:179], v192 offset:50688\n"
"v_mfma_f32_16x16x32_bf16 v[68:71], v[148:151], v[100:103], v[68:71]\n"
"ds_read_b64_tr_b16 v[180:181], v192 offset:49664\n"
"ds_read_b64_tr_b16 v[182:183], %[wbe] offset:50688\n"
"v_mfma_f32_16x16x32_bf16 v[40:43], v[152:155], v[96:99], v[40:43]\n"
"ds_read_b64_tr_b16 v[184:185], %[wbe] offset:49920\n"
"v_mfma_f32_16x16x32_bf16 v[72:75], v[152:155], v[100:103], v[72:75]\n"
"buffer_load_dwordx4 %[g1], %[srdk], s61 offen offset:3072 nt lds\n"
"v_mfma_f32_16x16x32_bf16 v[80:83], v[152:155], v[104:107], v[80:83]\n"
"ds_read_b64_tr_b16 v[186:187], v192 offset:50944\n"
"ds_read_b64_tr_b16 v[188:189], v192 offset:49920\n"
"v_mfma_f32_16x16x32_bf16 v[44:47], v[156:159], v[96:99], v[44:47]\n"
"ds_read_b64_tr_b16 v[190:191], %[wbe] offset:50944\n"
"s_mov_b32 m0, s58\n"
"v_mfma_f32_16x16x32_bf16 v[76:79], v[156:159], v[100:103], v[76:79]\n"
"buffer_load_dwordx4 v248, %[srdk], s61 offen offset:3072 nt lds\n"
"s_add_u32 s61, s61, %[kvs]\n"
"v_mfma_f32_16x16x32_bf16 v[84:87], v[156:159], v[104:107], v[84:87]\n"
"s_waitcnt lgkmcnt(0)\n"
"s_barrier\n"
"v_mfma_f32_16x16x32_bf16 v[16:19], v[160:163], v[108:111], v[16:19]\n"
"ds_read_b128 v[128:131], %[kb] offset:2048\n"
"v_mfma_f32_16x16x32_bf16 v[48:51], v[160:163], v[112:115], v[48:51]\n"
"ds_read_b128 v[132:135], %[kb] offset:2304\n"
"v_mfma_f32_16x16x32_bf16 v[20:23], v[164:167], v[108:111], v[20:23]\n"
"s_mov_b32 m0, s55\n"
"v_mfma_f32_16x16x32_bf16 v[52:55], v[164:167], v[112:115], v[52:55]\n"
"buffer_load_dwordx4 %[g0], %[srdv], s61 offen nt lds\n"
"v_mfma_f32_16x16x32_bf16 v[24:27], v[168:171], v[108:111], v[24:27]\n"
"ds_read_b128 v[136:139], %[kb] offset:2560\n"
"v_mfma_f32_16x16x32_bf16 v[56:59], v[168:171], v[112:115], v[56:59]\n"
"ds_read_b128 v[140:143], %[kb] offset:2816\n"
"v_mfma_f32_16x16x32_bf16 v[28:31], v[172:175], v[108:111], v[28:31]\n"
"buffer_load_dwordx4 %[g1], %[srdv], s61 offen offset:1024 nt lds\n"
"v_mfma_f32_16x16x32_bf16 v[60:63], v[172:175], v[112:115], v[60:63]\n"
"v_mfma_f32_16x16x32_bf16 v[32:35], v[176:179], v[108:111], v[32:35]\n"
"ds_read_b128 v[144:147], %[kb] offset:10240\n"
"v_mfma_f32_16x16x32_bf16 v[64:67], v[176:179], v[112:115], v[64:67]\n"
"buffer_load_dwordx4 %[g0], %[srdk], s61 offen offset:2048 nt lds\n"
"v_mfma_f32_16x16x32_bf16 v[36:39], v[180:183], v[108:111], v[36:39]\n"
"ds_read_b128 v[148:151], %[kb] offset:10496\n"
"v_mfma_f32_16x16x32_bf16 v[68:71], v[180:183], v[112:115], v[68:71]\n"
"v_mfma_f32_16x16x32_bf16 v[40:43], v[184:187], v[108:111], v[40:43]\n"
"ds_read_b128 v[152:155], %[kb] offset:10752\n"
"v_mfma_f32_16x16x32_bf16 v[72:75], v[184:187], v[112:115], v[72:75]\n"
"buffer_load_dwordx4 %[g1], %[srdk], s61 offen offset:3072 nt lds\n"
"v_mfma_f32_16x16x32_bf16 v[80:83], v[184:187], v[116:119], v[80:83]\n"
"ds_read_b128 v[156:159], %[kb] offset:11008\n"
"v_mfma_f32_16x16x32_bf16 v[44:47], v[188:191], v[108:111], v[44:47]\n"
"s_mov_b32 m0, s59\n"
"v_mfma_f32_16x16x32_bf16 v[76:79], v[188:191], v[112:115], v[76:79]\n"
"buffer_load_dwordx4 v248, %[srdk], s61 offen offset:3072 nt lds\n"
"s_add_u32 s61, s61, %[kvs]\n"
"v_mfma_f32_16x16x32_bf16 v[84:87], v[188:191], v[116:119], v[84:87]\n"
"ds_read_b64_tr_b16 v[160:161], %[vbe] offset:0\n"
"ds_read_b64_tr_b16 v[162:163], %[vbe] offset:8192\n"
"ds_read_b64_tr_b16 v[164:165], v243 offset:0\n"
"ds_read_b64_tr_b16 v[166:167], v243 offset:8192\n"
"ds_read_b64_tr_b16 v[168:169], %[vbe] offset:256\n"
"ds_read_b64_tr_b16 v[170:171], %[vbe] offset:8448\n"
"ds_read_b64_tr_b16 v[172:173], v243 offset:256\n"
"ds_read_b64_tr_b16 v[174:175], v243 offset:8448\n"
"ds_read_b64_tr_b16 v[176:177], %[vbe] offset:512\n"
"ds_read_b64_tr_b16 v[178:179], %[vbe] offset:8704\n"
"ds_read_b64_tr_b16 v[180:181], v243 offset:512\n"
"ds_read_b64_tr_b16 v[182:183], v243 offset:8704\n"
"ds_read_b64_tr_b16 v[184:185], %[vbe] offset:768\n"
"ds_read_b64_tr_b16 v[186:187], %[vbe] offset:8960\n"
"ds_read_b64_tr_b16 v[188:189], v243 offset:768\n"
"ds_read_b64_tr_b16 v[190:191], v243 offset:8960\n"
"v_pk_mul_f32 v[80:81], v[80:81], s[66:67]\n"
"v_pk_mul_f32 v[82:83], v[82:83], s[66:67]\n"
"v_pk_mul_f32 v[84:85], v[84:85], s[66:67]\n"
"v_pk_mul_f32 v[86:87], v[86:87], s[66:67]\n"
"v_cvt_pk_bf16_f32 v232, v80, v81\n"
"v_cvt_pk_bf16_f32 v233, v82, v83\n"
"v_cvt_pk_bf16_f32 v234, v84, v85\n"
"v_cvt_pk_bf16_f32 v235, v86, v87\n"
"s_nop 0\n"
"v_permlane32_swap_b32 v232, v234\n"
"v_permlane32_swap_b32 v233, v235\n"
"s_nop 0\n"
"v_permlane16_swap_b32 v232, v234\n"
"v_permlane16_swap_b32 v233, v235\n"
"ds_write_b128 v247, v[232:235] offset:3072\n"
"v_pk_mul_f32 v[16:17], v[16:17], s[66:67]\n"
"v_pk_mul_f32 v[18:19], v[18:19], s[66:67]\n"
"v_pk_mul_f32 v[20:21], v[20:21], s[66:67]\n"
"v_pk_mul_f32 v[22:23], v[22:23], s[66:67]\n"
"v_cvt_pk_bf16_f32 v96, v16, v17\n"
"v_cvt_pk_bf16_f32 v97, v18, v19\n"
"v_cvt_pk_bf16_f32 v98, v20, v21\n"
"v_cvt_pk_bf16_f32 v99, v22, v23\n"
"v_pk_mul_f32 v[24:25], v[24:25], s[66:67]\n"
"v_pk_mul_f32 v[26:27], v[26:27], s[66:67]\n"
"v_pk_mul_f32 v[28:29], v[28:29], s[66:67]\n"
"v_pk_mul_f32 v[30:31], v[30:31], s[66:67]\n"
"v_cvt_pk_bf16_f32 v100, v24, v25\n"
"v_cvt_pk_bf16_f32 v101, v26, v27\n"
"v_cvt_pk_bf16_f32 v102, v28, v29\n"
"v_cvt_pk_bf16_f32 v103, v30, v31\n"
"v_pk_mul_f32 v[32:33], v[32:33], s[66:67]\n"
"v_pk_mul_f32 v[34:35], v[34:35], s[66:67]\n"
"v_pk_mul_f32 v[36:37], v[36:37], s[66:67]\n"
"v_pk_mul_f32 v[38:39], v[38:39], s[66:67]\n"
"v_cvt_pk_bf16_f32 v104, v32, v33\n"
"v_cvt_pk_bf16_f32 v105, v34, v35\n"
"v_cvt_pk_bf16_f32 v106, v36, v37\n"
"v_cvt_pk_bf16_f32 v107, v38, v39\n"
"v_pk_mul_f32 v[40:41], v[40:41], s[66:67]\n"
"v_pk_mul_f32 v[42:43], v[42:43], s[66:67]\n"
"v_pk_mul_f32 v[44:45], v[44:45], s[66:67]\n"
"v_pk_mul_f32 v[46:47], v[46:47], s[66:67]\n"
"v_cvt_pk_bf16_f32 v108, v40, v41\n"
"v_cvt_pk_bf16_f32 v109, v42, v43\n"
"v_cvt_pk_bf16_f32 v110, v44, v45\n"
"v_cvt_pk_bf16_f32 v111, v46, v47\n"
"v_pk_mul_f32 v[48:49], v[48:49], s[66:67]\n"
"v_pk_mul_f32 v[50:51], v[50:51], s[66:67]\n"
"v_pk_mul_f32 v[52:53], v[52:53], s[66:67]\n"
"v_pk_mul_f32 v[54:55], v[54:55], s[66:67]\n"
"v_cvt_pk_bf16_f32 v112, v48, v49\n"
"v_cvt_pk_bf16_f32 v113, v50, v51\n"
"v_cvt_pk_bf16_f32 v114, v52, v53\n"
"v_cvt_pk_bf16_f32 v115, v54, v55\n"
"v_pk_mul_f32 v[56:57], v[56:57], s[66:67]\n"
"v_pk_mul_f32 v[58:59], v[58:59], s[66:67]\n"
"v_pk_mul_f32 v[60:61], v[60:61], s[66:67]\n"
"v_pk_mul_f32 v[62:63], v[62:63], s[66:67]\n"
"v_cvt_pk_bf16_f32 v116, v56, v57\n"
"v_cvt_pk_bf16_f32 v117, v58, v59\n"
"v_cvt_pk_bf16_f32 v118, v60, v61\n"
"v_cvt_pk_bf16_f32 v119, v62, v63\n"
"v_pk_mul_f32 v[64:65], v[64:65], s[66:67]\n"
"v_pk_mul_f32 v[66:67], v[66:67], s[66:67]\n"
"v_pk_mul_f32 v[68:69], v[68:69], s[66:67]\n"
"v_pk_mul_f32 v[70:71], v[70:71], s[66:67]\n"
"v_cvt_pk_bf16_f32 v120, v64, v65\n"
"v_cvt_pk_bf16_f32 v121, v66, v67\n"
"v_cvt_pk_bf16_f32 v122, v68, v69\n"
"v_cvt_pk_bf16_f32 v123, v70, v71\n"
"v_pk_mul_f32 v[72:73], v[72:73], s[66:67]\n"
"v_pk_mul_f32 v[74:75], v[74:75], s[66:67]\n"
"v_pk_mul_f32 v[76:77], v[76:77], s[66:67]\n"
"v_pk_mul_f32 v[78:79], v[78:79], s[66:67]\n"
"v_cvt_pk_bf16_f32 v124, v72, v73\n"
"v_cvt_pk_bf16_f32 v125, v74, v75\n"
"v_cvt_pk_bf16_f32 v126, v76, v77\n"
"v_cvt_pk_bf16_f32 v127, v78, v79\n"
"v_permlane32_swap_b32 v96, v98\n"
"v_permlane32_swap_b32 v97, v99\n"
"v_permlane32_swap_b32 v100, v102\n"
"v_permlane32_swap_b32 v101, v103\n"
"v_permlane32_swap_b32 v104, v106\n"
"v_permlane32_swap_b32 v105, v107\n"
"v_permlane32_swap_b32 v108, v110\n"
"v_permlane32_swap_b32 v109, v111\n"
"v_permlane32_swap_b32 v112, v114\n"
"v_permlane32_swap_b32 v113, v115\n"
"v_permlane32_swap_b32 v116, v118\n"
"v_permlane32_swap_b32 v117, v119\n"
"v_permlane32_swap_b32 v120, v122\n"
"v_permlane32_swap_b32 v121, v123\n"
"v_permlane32_swap_b32 v124, v126\n"
"v_permlane32_swap_b32 v125, v127\n"
"v_permlane16_swap_b32 v96, v98\n"
"v_permlane16_swap_b32 v97, v99\n"
"v_permlane16_swap_b32 v100, v102\n"
"v_permlane16_swap_b32 v101, v103\n"
"v_permlane16_swap_b32 v104, v106\n"
"v_permlane16_swap_b32 v105, v107\n"
"v_permlane16_swap_b32 v108, v110\n"
"v_permlane16_swap_b32 v109, v111\n"
"v_permlane16_swap_b32 v112, v114\n"
"v_permlane16_swap_b32 v113, v115\n"
"v_permlane16_swap_b32 v116, v118\n"
"v_permlane16_swap_b32 v117, v119\n"
"v_permlane16_swap_b32 v120, v122\n"
"v_permlane16_swap_b32 v121, v123\n"
"v_permlane16_swap_b32 v124, v126\n"
"v_permlane16_swap_b32 v125, v127\n"
"s_waitcnt lgkmcnt(0)\n"
"s_barrier\n"
"v_mfma_f32_16x16x32_bf16 v[192:195], v[128:131], v[96:99], 0\n"
"v_mov_b32 v16, 0\n"
"v_mov_b32 v17, 0\n"
"v_mov_b32 v18, 0\n"
"v_mov_b32 v19, 0\n"
"v_mov_b32 v20, 0\n"
"v_mov_b32 v21, 0\n"
"v_mfma_f32_16x16x32_bf16 v[192:195], v[132:135], v[100:103], v[192:195]\n"
"v_mov_b32 v22, 0\n"
"v_mov_b32 v23, 0\n"
"v_mov_b32 v24, 0\n"
"v_mov_b32 v25, 0\n"
"v_mov_b32 v26, 0\n"
"v_mov_b32 v27, 0\n"
"v_mfma_f32_16x16x32_bf16 v[192:195], v[136:139], v[104:107], v[192:195]\n"
"v_mov_b32 v28, 0\n"
"v_mov_b32 v29, 0\n"
"v_mov_b32 v30, 0\n"
"v_mov_b32 v31, 0\n"
"v_mov_b32 v32, 0\n"
"v_mov_b32 v33, 0\n"
"v_mfma_f32_16x16x32_bf16 v[192:195], v[140:143], v[108:111], v[192:195]\n"
"v_mov_b32 v34, 0\n"
"v_mov_b32 v35, 0\n"
"v_mov_b32 v36, 0\n"
"v_mov_b32 v37, 0\n"
"v_mov_b32 v38, 0\n"
"v_mov_b32 v39, 0\n"
"v_mfma_f32_16x16x32_bf16 v[196:199], v[128:131], v[112:115], 0\n"
"v_mov_b32 v40, 0\n"
"v_mov_b32 v41, 0\n"
"v_mov_b32 v42, 0\n"
"v_mov_b32 v43, 0\n"
"v_mov_b32 v44, 0\n"
"v_mov_b32 v45, 0\n"
"v_mfma_f32_16x16x32_bf16 v[196:199], v[132:135], v[116:119], v[196:199]\n"
"v_mov_b32 v46, 0\n"
"v_mov_b32 v47, 0\n"
"v_mov_b32 v48, 0\n"
"v_mov_b32 v49, 0\n"
"v_mov_b32 v50, 0\n"
"v_mov_b32 v51, 0\n"
"v_mfma_f32_16x16x32_bf16 v[196:199], v[136:139], v[120:123], v[196:199]\n"
"v_mov_b32 v52, 0\n"
"v_mov_b32 v53, 0\n"
"v_mov_b32 v54, 0\n"
"v_mov_b32 v55, 0\n"
"v_mov_b32 v56, 0\n"
"v_mov_b32 v57, 0\n"
"v_mfma_f32_16x16x32_bf16 v[196:199], v[140:143], v[124:127], v[196:199]\n"
"v_mov_b32 v58, 0\n"
"v_mov_b32 v59, 0\n"
"v_mov_b32 v60, 0\n"
"v_mov_b32 v61, 0\n"
"v_mov_b32 v62, 0\n"
"v_mov_b32 v63, 0\n"
"v_mfma_f32_16x16x32_bf16 v[200:203], v[144:147], v[96:99], 0\n"
"v_mov_b32 v64, 0\n"
"v_mov_b32 v65, 0\n"
"v_mov_b32 v66, 0\n"
"v_mov_b32 v67, 0\n"
"v_mov_b32 v68, 0\n"
"v_mov_b32 v69, 0\n"
"v_mfma_f32_16x16x32_bf16 v[200:203], v[148:151], v[100:103], v[200:203]\n"
"v_mov_b32 v70, 0\n"
"v_mov_b32 v71, 0\n"
"v_mov_b32 v72, 0\n"
"v_mov_b32 v73, 0\n"
"v_mov_b32 v74, 0\n"
"v_mov_b32 v75, 0\n"
"v_mfma_f32_16x16x32_bf16 v[200:203], v[152:155], v[104:107], v[200:203]\n"
"v_mov_b32 v76, 0\n"
"v_mov_b32 v77, 0\n"
"v_mov_b32 v78, 0\n"
"v_mov_b32 v79, 0\n"
"v_mov_b32 v240, 0\n"
"v_mov_b32 v241, 0\n"
"v_mfma_f32_16x16x32_bf16 v[200:203], v[156:159], v[108:111], v[200:203]\n"
"v_mov_b32 v242, 0\n"
"v_mov_b32 v80, 0\n"
"v_mov_b32 v81, 0\n"
"v_mov_b32 v82, 0\n"
"v_mov_b32 v83, 0\n"
"v_mov_b32 v84, 0\n"
"v_mfma_f32_16x16x32_bf16 v[204:207], v[144:147], v[112:115], 0\n"
"v_mov_b32 v85, 0\n"
"v_mov_b32 v86, 0\n"
"v_mov_b32 v87, 0\n"
"v_mov_b32 v88, 0\n"
"v_mov_b32 v89, 0\n"
"v_mov_b32 v90, 0\n"
"v_mfma_f32_16x16x32_bf16 v[204:207], v[148:151], v[116:119], v[204:207]\n"
"v_mov_b32 v91, 0\n"
"v_mov_b32 v92, 0\n"
"v_mov_b32 v93, 0\n"
"v_mov_b32 v94, 0\n"
"v_mov_b32 v95, 0\n"
"v_mfma_f32_16x16x32_bf16 v[204:207], v[152:155], v[120:123], v[204:207]\n"
"v_mfma_f32_16x16x32_bf16 v[204:207], v[156:159], v[124:127], v[204:207]\n"
"v_max3_f32 v249, v192, v193, v194\n"
"v_max3_f32 v249, v249, v195, v200\n"
"v_max3_f32 v249, v249, v201, v202\n"
"v_max_f32 v249, v249, v203\n"
"v_max3_f32 v251, v196, v197, v198\n"
"s_nop 2\n"
"v_max3_f32 v251, v251, v199, v204\n"
"v_max3_f32 v251, v251, v205, v206\n"
"v_max_f32 v251, v251, v207\n"
"v_mov_b32 v250, v249\n"
"v_mov_b32 v252, v251\n"
"s_nop 0\n"
"v_permlane32_swap_b32 v249, v250\n"
"v_permlane32_swap_b32 v251, v252\n"
"v_max_f32 v249, v249, v250\n"
"v_max_f32 v251, v251, v252\n"
"v_mov_b32 v250, v249\n"
"v_mov_b32 v252, v251\n"
"s_nop 0\n"
"v_permlane16_swap_b32 v249, v250\n"
"v_permlane16_swap_b32 v251, v252\n"
"v_max_f32 v249, v249, v250\n"
"v_max_f32 v251, v251, v252\n"
"v_sub_f32 v212, 0, v249\n"
"v_sub_f32 v213, 0, v249\n"
"v_sub_f32 v214, 0, v249\n"
"v_sub_f32 v215, 0, v249\n"
"v_sub_f32 v192, v192, v249\n"
"v_sub_f32 v193, v193, v249\n"
"v_sub_f32 v194, v194, v249\n"
"v_sub_f32 v195, v195, v249\n"
"v_sub_f32 v200, v200, v249\n"
"v_sub_f32 v201, v201, v249\n"
"v_sub_f32 v202, v202, v249\n"
"v_sub_f32 v203, v203, v249\n"
"v_sub_f32 v216, 0, v251\n"
"v_sub_f32 v217, 0, v251\n"
"v_sub_f32 v218, 0, v251\n"
"v_sub_f32 v219, 0, v251\n"
"v_sub_f32 v196, v196, v251\n"
"v_sub_f32 v197, v197, v251\n"
"v_sub_f32 v198, v198, v251\n"
"v_sub_f32 v199, v199, v251\n"
"v_sub_f32 v204, v204, v251\n"
"v_sub_f32 v205, v205, v251\n"
"v_sub_f32 v206, v206, v251\n"
"v_sub_f32 v207, v207, v251\n"
"s_movk_i32 s68, 1152\n"
"s_lshr_b32 s69, %[nb256], 3\n"
"s_add_u32 s69, s69, 5632\n"
"v_and_b32 v254, 255, %[g0]\n"
"v_add_u32_e64 v254, %[kq64], v254\n"
"v_and_b32 v254, 255, v254\n"
"v_lshrrev_b32_e64 v255, 8, %[g0]\n"
"v_mul_u32_u24 v255, s68, v255\n"
"v_add_u32 v255, %[nb256], v255\n"
"v_add_u32 v249, v254, v255\n"
"v_xor_b32 v254, 32, v254\n"
"v_add_u32 v250, v254, v255\n"
"v_add_u32 v250, 3584, v250\n"
"v_subrev_u32 v251, 18432, v250\n"
"v_lshrrev_b32_e64 v254, 5, %[l16]\n"
"v_mul_u32_u24 v254, s68, v254\n"
"v_and_b32 v255, 16, %[l16]\n"
"v_add_u32 v254, v254, v255\n"
"v_add_u32 v252, s69, v254\n"
"s_cmp_eq_u32 %[hb], 0\n"
"s_cbranch_scc1 LH3_%=\n"
"s_barrier\n"
"LH3_%=:\n"
"v_exp_f32 v192, v192\n"
"v_exp_f32 v193, v193\n"
"v_exp_f32 v194, v194\n"
"v_exp_f32 v195, v195\n"
"v_cvt_pk_bf16_f32 v224, v192, v193\n"
"v_cvt_pk_bf16_f32 v225, v194, v195\n"
"v_exp_f32 v196, v196\n"
"v_exp_f32 v197, v197\n"
"v_exp_f32 v198, v198\n"
"v_exp_f32 v199, v199\n"
"v_cvt_pk_bf16_f32 v228, v196, v197\n"
"v_cvt_pk_bf16_f32 v229, v198, v199\n"
"v_exp_f32 v200, v200\n"
"v_exp_f32 v201, v201\n"
"v_exp_f32 v202, v202\n"
"v_exp_f32 v203, v203\n"
"v_cvt_pk_bf16_f32 v226, v200, v201\n"
"v_cvt_pk_bf16_f32 v227, v202, v203\n"
"v_exp_f32 v204, v204\n"
"v_exp_f32 v205, v205\n"
"v_exp_f32 v206, v206\n"
"v_exp_f32 v207, v207\n"
"v_cvt_pk_bf16_f32 v230, v204, v205\n"
"v_cvt_pk_bf16_f32 v231, v206, v207\n"
"s_cmp_lg_u32 %[isa], 0\n"
"s_cbranch_scc1 LA3_%=\n"
"s_mov_b32 m0, s52\n"
"s_nop 0\n"
"buffer_load_dwordx4 %[g0], %[srdv], s61 offen nt lds\n"
"buffer_load_dwordx4 %[g1], %[srdv], s61 offen offset:1024 nt lds\n"
"buffer_load_dwordx4 %[g0], %[srdk], s61 offen offset:2048 nt lds\n"
"buffer_load_dwordx4 %[g1], %[srdk], s61 offen offset:3072 nt lds\n"
"s_mov_b32 m0, s56\n"
"s_nop 0\n"
"buffer_load_dwordx4 v248, %[srdk], s61 offen offset:3072 nt lds\n"
"s_add_u32 s61, s61, 0x2000\n"
"s_waitcnt vmcnt(15)\n"
".p2align 6\n"
"LB3_%=:\n"
"KB3 16384,s53,s57\n"
"KB3 32768,s54,s58\n"
"KB3 49152,s55,s59\n"
"KB3 0,s52,s56\n"
"s_branch LB3_%=\n"
".p2align 3\n"
"LQ3_%=:\n"
"s_barrier\n"
"s_setprio 1\n"
"ds_read_b128 v[232:235], v247 offset:4096\n"
"v_mfma_f32_16x16x32_bf16 v[16:19], v[160:163], v[224:227], v[16:19]\n"
"ds_read_b128 v[128:131], %[kb] offset:2048\n"
"v_add_f32 v240, v240, v192\n"
"v_add_f32 v241, v241, v196\n"
"v_mfma_f32_16x16x32_bf16 v[48:51], v[160:163], v[228:231], v[48:51]\n"
"ds_read_b128 v[132:135], %[kb] offset:2304\n"
"v_add_f32 v240, v240, v193\n"
"v_add_f32 v241, v241, v197\n"
"v_mfma_f32_16x16x32_bf16 v[20:23], v[164:167], v[224:227], v[20:23]\n"
"ds_read_b128 v[136:139], %[kb] offset:2560\n"
"v_add_f32 v240, v240, v194\n"
"v_add_f32 v241, v241, v198\n"
"v_mfma_f32_16x16x32_bf16 v[52:55], v[164:167], v[228:231], v[52:55]\n"
"ds_read_b128 v[140:143], %[kb] offset:2816\n"
"v_add_f32 v240, v240, v195\n"
"v_add_f32 v241, v241, v199\n"
"v_mfma_f32_16x16x32_bf16 v[24:27], v[168:171], v[224:227], v[24:27]\n"
"ds_read_b128 v[144:147], %[kb] offset:10240\n"
"v_add_f32 v240, v240, v200\n"
"v_add_f32 v241, v241, v204\n"
"v_mfma_f32_16x16x32_bf16 v[56:59], v[168:171], v[228:231], v[56:59]\n"
"ds_read_b128 v[148:151], %[kb] offset:10496\n"
"v_add_f32 v240, v240, v201\n"
"v_add_f32 v241, v241, v205\n"
"v_mfma_f32_16x16x32_bf16 v[28:31], v[172:175], v[224:227], v[28:31]\n"
"ds_read_b128 v[152:155], %[kb] offset:10752\n"
"v_add_f32 v240, v240, v202\n"
"v_add_f32 v241, v241, v206\n"
"v_mfma_f32_16x16x32_bf16 v[60:63], v[172:175], v[228:231], v[60:63]\n"
"ds_read_b128 v[156:159], %[kb] offset:11008\n"
"v_add_f32 v240, v240, v203\n"
"v_add_f32 v241, v241, v207\n"
"v_mfma_f32_16x16x32_bf16 v[32:35], v[176:179], v[224:227], v[32:35]\n"
"ds_read_b64_tr_b16 v[160:161], %[vbe] offset:0\n"
"v_mfma_f32_16x16x32_bf16 v[64:67], v[176:179], v[228:231], v[64:67]\n"
"ds_read_b64_tr_b16 v[162:163], %[vbe] offset:8192\n"
"s_waitcnt lgkmcnt(10)\n"
"v_mfma_f32_16x16x32_bf16 v[80:83], v[176:179], v[232:235], v[80:83]\n"
"ds_read_b64_tr_b16 v[164:165], v243 offset:0\n"
"v_mfma_f32_16x16x32_bf16 v[36:39], v[180:183], v[224:227], v[36:39]\n"
"ds_read_b64_tr_b16 v[166:167], v243 offset:8192\n"
"v_mfma_f32_16x16x32_bf16 v[68:71], v[180:183], v[228:231], v[68:71]\n"
"ds_read_b64_tr_b16 v[168:169], %[vbe] offset:256\n"
"v_mfma_f32_16x16x32_bf16 v[84:87], v[180:183], v[232:235], v[84:87]\n"
"ds_read_b64_tr_b16 v[170:171], %[vbe] offset:8448\n"
"v_mfma_f32_16x16x32_bf16 v[40:43], v[184:187], v[224:227], v[40:43]\n"
"ds_read_b64_tr_b16 v[172:173], v243 offset:256\n"
"v_mfma_f32_16x16x32_bf16 v[72:75], v[184:187], v[228:231], v[72:75]\n"
"ds_read_b64_tr_b16 v[174:175], v243 offset:8448\n"
"v_mfma_f32_16x16x32_bf16 v[88:91], v[184:187], v[232:235], v[88:91]\n"
"ds_read_b64_tr_b16 v[176:177], %[vbe] offset:512\n"
"v_mfma_f32_16x16x32_bf16 v[44:47], v[188:191], v[224:227], v[44:47]\n"
"ds_read_b64_tr_b16 v[178:179], %[vbe] offset:8704\n"
"v_mfma_f32_16x16x32_bf16 v[76:79], v[188:191], v[228:231], v[76:79]\n"
"ds_read_b64_tr_b16 v[180:181], v243 offset:512\n"
"v_mfma_f32_16x16x32_bf16 v[92:95], v[188:191], v[232:235], v[92:95]\n"
"ds_read_b64_tr_b16 v[182:183], v243 offset:8704\n"
"s_waitcnt lgkmcnt(12)\n"
"v_mfma_f32_16x16x32_bf16 v[192:195], v[128:131], v[96:99], v[212:215]\n"
"ds_read_b64_tr_b16 v[184:185], %[vbe] offset:768\n"
"v_mfma_f32_16x16x32_bf16 v[192:195], v[132:135], v[100:103], v[192:195]\n"
"ds_read_b64_tr_b16 v[186:187], %[vbe] offset:8960\n"
"v_mfma_f32_16x16x32_bf16 v[192:195], v[136:139], v[104:107], v[192:195]\n"
"ds_read_b64_tr_b16 v[188:189], v243 offset:768\n"
"v_mfma_f32_16x16x32_bf16 v[192:195], v[140:143], v[108:111], v[192:195]\n"
"ds_read_b64_tr_b16 v[190:191], v243 offset:8960\n"
"v_mfma_f32_16x16x32_bf16 v[196:199], v[128:131], v[112:115], v[216:219]\n"
"v_mfma_f32_16x16x32_bf16 v[196:199], v[132:135], v[116:119], v[196:199]\n"
"v_mfma_f32_16x16x32_bf16 v[196:199], v[136:139], v[120:123], v[196:199]\n"
"v_mfma_f32_16x16x32_bf16 v[196:199], v[140:143], v[124:127], v[196:199]\n"
"v_mfma_f32_16x16x32_bf16 v[200:203], v[144:147], v[96:99], v[212:215]\n"
"v_mfma_f32_16x16x32_bf16 v[200:203], v[148:151], v[100:103], v[200:203]\n"
"v_mfma_f32_16x16x32_bf16 v[200:203], v[152:155], v[104:107], v[200:203]\n"
"v_mfma_f32_16x16x32_bf16 v[200:203], v[156:159], v[108:111], v[200:203]\n"
"v_mfma_f32_16x16x32_bf16 v[204:207], v[144:147], v[112:115], v[216:219]\n"
"v_mfma_f32_16x16x32_bf16 v[204:207], v[148:151], v[116:119], v[204:207]\n"
"v_mfma_f32_16x16x32_bf16 v[204:207], v[152:155], v[120:123], v[204:207]\n"
"v_mfma_f32_16x16x32_bf16 v[204:207], v[156:159], v[124:127], v[204:207]\n"
"s_waitcnt lgkmcnt(0)\n"
"s_barrier\n"
"s_setprio 0\n"
"s_getpc_b64 s[94:95]\n"
"LMb3_%=:\n"
"s_add_u32 s94, s94, (LZZ3_%=-16384-LMb3_%=)&0xffffffff\n"
"s_addc_u32 s95, s95, (LZZ3_%=-16384-LMb3_%=)>>32\n"
"v_lshlrev_b32 v236, 4, %[l16]\n"
"s_lshr_b32 s68, %[hb], 9\n"
"v_add_u32 v236, s68, v236\n"
"s_lshl_b32 exec_lo, 0x11111111, %[wrot]\n"
"s_mov_b32 exec_hi, exec_lo\n"
"global_load_dword v253, v236, s[94:95]\n"
"s_mov_b64 exec, -1\n"
"s_lshl_b32 s88, %[osoff], 2\n"
"s_mov_b32 m0, s52\n"
"s_nop 0\n"
"buffer_load_dwordx4 v249, s[76:79], s88 offen lds\n"
"buffer_load_dwordx4 v250, s[76:79], s88 offen offset:1024 lds\n"
"s_lshl_b32 s89, %[osoff], 2\n"
"s_add_u32 s89, s89, 34816\n"
"buffer_load_dwordx4 v249, s[76:79], s89 offen offset:2048 lds\n"
"buffer_load_dwordx4 v250, s[76:79], s89 offen offset:3072 lds\n"
"s_mov_b32 m0, s56\n"
"s_nop 0\n"
"buffer_load_dwordx4 v251, s[76:79], s89 offen offset:3072 lds\n"
"v_exp_f32 v192, v192\n"
"v_exp_f32 v193, v193\n"
"v_exp_f32 v194, v194\n"
"v_exp_f32_e64 v195, v195\n"
"v_cvt_pk_bf16_f32 v224, v192, v193\n"
"v_cvt_pk_bf16_f32 v225, v194, v195\n"
"v_exp_f32 v196, v196\n"
"v_exp_f32 v197, v197\n"
"v_exp_f32 v198, v198\n"
"v_exp_f32 v199, v199\n"
"v_cvt_pk_bf16_f32 v228, v196, v197\n"
"v_cvt_pk_bf16_f32 v229, v198, v199\n"
"v_exp_f32 v200, v200\n"
"v_exp_f32 v201, v201\n"
"v_exp_f32 v202, v202\n"
"v_exp_f32 v203, v203\n"
"v_cvt_pk_bf16_f32 v226, v200, v201\n"
"v_cvt_pk_bf16_f32 v227, v202, v203\n"
"v_exp_f32 v204, v204\n"
"v_exp_f32 v205, v205\n"
"v_exp_f32 v206, v206\n"
"v_exp_f32 v207, v207\n"
"v_cvt_pk_bf16_f32 v230, v204, v205\n"
"v_cvt_pk_bf16_f32 v231, v206, v207\n"
"s_waitcnt vmcnt(15)\n"
"s_barrier\n"
"s_setprio 1\n"
"ds_read_b128 v[232:235], v247 offset:4096\n"
"v_mfma_f32_16x16x32_bf16 v[16:19], v[160:163], v[224:227], v[16:19]\n"
"ds_read_b128 v[128:131], %[kb] offset:18432\n"
"v_add_f32 v240, v240, v192\n"
"v_add_f32_e64 v241, v241, v196\n"
"v_mfma_f32_16x16x32_bf16 v[48:51], v[160:163], v[228:231], v[48:51]\n"
"ds_read_b128 v[132:135], %[kb] offset:18688\n"
"v_add_f32 v240, v240, v193\n"
"v_add_f32 v241, v241, v197\n"
"v_mfma_f32_16x16x32_bf16 v[20:23], v[164:167], v[224:227], v[20:23]\n"
"ds_read_b128 v[136:139], %[kb] offset:18944\n"
"v_add_f32 v240, v240, v194\n"
"v_add_f32 v241, v241, v198\n"
"v_mfma_f32_16x16x32_bf16 v[52:55], v[164:167], v[228:231], v[52:55]\n"
"ds_read_b128 v[140:143], %[kb] offset:19200\n"
"v_add_f32 v240, v240, v195\n"
"v_add_f32 v241, v241, v199\n"
"v_mfma_f32_16x16x32_bf16 v[24:27], v[168:171], v[224:227], v[24:27]\n"
"ds_read_b128 v[144:147], %[kb] offset:26624\n"
"v_add_f32 v240, v240, v200\n"
"v_add_f32 v241, v241, v204\n"
"v_mfma_f32_16x16x32_bf16 v[56:59], v[168:171], v[228:231], v[56:59]\n"
"ds_read_b128 v[148:151], %[kb] offset:26880\n"
"v_add_f32 v240, v240, v201\n"
"v_add_f32 v241, v241, v205\n"
"v_mfma_f32_16x16x32_bf16 v[28:31], v[172:175], v[224:227], v[28:31]\n"
"ds_read_b128 v[152:155], %[kb] offset:27136\n"
"v_add_f32 v240, v240, v202\n"
"v_add_f32 v241, v241, v206\n"
"v_mfma_f32_16x16x32_bf16 v[60:63], v[172:175], v[228:231], v[60:63]\n"
"ds_read_b128 v[156:159], %[kb] offset:27392\n"
"v_add_f32 v240, v240, v203\n"
"v_add_f32 v241, v241, v207\n"
"v_mfma_f32_16x16x32_bf16 v[32:35], v[176:179], v[224:227], v[32:35]\n"
"ds_read_b64_tr_b16 v[160:161], %[vbe] offset:16384\n"
"v_mfma_f32_16x16x32_bf16 v[64:67], v[176:179], v[228:231], v[64:67]\n"
"ds_read_b64_tr_b16 v[162:163], %[vbe] offset:24576\n"
"s_waitcnt lgkmcnt(10)\n"
"v_mfma_f32_16x16x32_bf16 v[80:83], v[176:179], v[232:235], v[80:83]\n"
"ds_read_b64_tr_b16 v[164:165], v243 offset:16384\n"
"v_mfma_f32_16x16x32_bf16 v[36:39], v[180:183], v[224:227], v[36:39]\n"
"ds_read_b64_tr_b16 v[166:167], v243 offset:24576\n"
"v_mfma_f32_16x16x32_bf16 v[68:71], v[180:183], v[228:231], v[68:71]\n"
"ds_read_b64_tr_b16 v[168:169], %[vbe] offset:16640\n"
"v_mfma_f32_16x16x32_bf16 v[84:87], v[180:183], v[232:235], v[84:87]\n"
"ds_read_b64_tr_b16 v[170:171], %[vbe] offset:24832\n"
"v_mfma_f32_16x16x32_bf16 v[40:43], v[184:187], v[224:227], v[40:43]\n"
"ds_read_b64_tr_b16 v[172:173], v243 offset:16640\n"
"v_mfma_f32_16x16x32_bf16 v[72:75], v[184:187], v[228:231], v[72:75]\n"
"ds_read_b64_tr_b16 v[174:175], v243 offset:24832\n"
"v_mfma_f32_16x16x32_bf16 v[88:91], v[184:187], v[232:235], v[88:91]\n"
"ds_read_b64_tr_b16 v[176:177], %[vbe] offset:16896\n"
"v_mfma_f32_16x16x32_bf16 v[44:47], v[188:191], v[224:227], v[44:47]\n"
"ds_read_b64_tr_b16 v[178:179], %[vbe] offset:25088\n"
"v_mfma_f32_16x16x32_bf16 v[76:79], v[188:191], v[228:231], v[76:79]\n"
"ds_read_b64_tr_b16 v[180:181], v243 offset:16896\n"
"v_mfma_f32_16x16x32_bf16 v[92:95], v[188:191], v[232:235], v[92:95]\n"
"ds_read_b64_tr_b16 v[182:183], v243 offset:25088\n"
"s_waitcnt lgkmcnt(12)\n"
"v_mfma_f32_16x16x32_bf16 v[192:195], v[128:131], v[96:99], v[212:215]\n"
"ds_read_b64_tr_b16 v[184:185], %[vbe] offset:17152\n"
"v_mfma_f32_16x16x32_bf16 v[192:195], v[132:135], v[100:103], v[192:195]\n"
"ds_read_b64_tr_b16 v[186:187], %[vbe] offset:25344\n"
"v_mfma_f32_16x16x32_bf16 v[192:195], v[136:139], v[104:107], v[192:195]\n"
"ds_read_b64_tr_b16 v[188:189], v243 offset:17152\n"
"v_mfma_f32_16x16x32_bf16 v[192:195], v[140:143], v[108:111], v[192:195]\n"
"ds_read_b64_tr_b16 v[190:191], v243 offset:25344\n"
"v_mfma_f32_16x16x32_bf16 v[196:199], v[128:131], v[112:115], v[216:219]\n"
"v_mfma_f32_16x16x32_bf16 v[196:199], v[132:135], v[116:119], v[196:199]\n"
"v_mfma_f32_16x16x32_bf16 v[196:199], v[136:139], v[120:123], v[196:199]\n"
"v_mfma_f32_16x16x32_bf16 v[196:199], v[140:143], v[124:127], v[196:199]\n"
"v_mfma_f32_16x16x32_bf16 v[200:203], v[144:147], v[96:99], v[212:215]\n"
"v_mfma_f32_16x16x32_bf16 v[200:203], v[148:151], v[100:103], v[200:203]\n"
"v_mfma_f32_16x16x32_bf16 v[200:203], v[152:155], v[104:107], v[200:203]\n"
"v_mfma_f32_16x16x32_bf16 v[200:203], v[156:159], v[108:111], v[200:203]\n"
"v_mfma_f32_16x16x32_bf16 v[204:207], v[144:147], v[112:115], v[216:219]\n"
"v_mfma_f32_16x16x32_bf16 v[204:207], v[148:151], v[116:119], v[204:207]\n"
"v_mfma_f32_16x16x32_bf16 v[204:207], v[152:155], v[120:123], v[204:207]\n"
"v_mfma_f32_16x16x32_bf16 v[204:207], v[156:159], v[124:127], v[204:207]\n"
"s_waitcnt lgkmcnt(0)\n"
"s_barrier\n"
"s_setprio 0\n"
"s_lshl_b32 s88, %[osoff], 2\n"
"s_add_u32 s88, s88, 73728\n"
"s_mov_b32 m0, s53\n"
"s_nop 0\n"
"buffer_load_dwordx4 v249, s[76:79], s88 offen lds\n"
"buffer_load_dwordx4 v250, s[76:79], s88 offen offset:1024 lds\n"
"s_lshl_b32 s89, %[osoff], 2\n"
"s_add_u32 s89, s89, 108544\n"
"buffer_load_dwordx4 v249, s[76:79], s89 offen offset:2048 lds\n"
"buffer_load_dwordx4 v250, s[76:79], s89 offen offset:3072 lds\n"
"s_mov_b32 m0, s57\n"
"s_nop 0\n"
"buffer_load_dwordx4 v251, s[76:79], s89 offen offset:3072 lds\n"
"s_add_u32 m0, s90, 1024\n"
"s_nop 0\n"
"buffer_load_dwordx4 v252, s[76:79], s89 offen offset:2048 lds\n"
"v_exp_f32 v192, v192\n"
"v_exp_f32 v193, v193\n"
"v_exp_f32 v194, v194\n"
"v_exp_f32 v195, v195\n"
"v_cvt_pk_bf16_f32 v224, v192, v193\n"
"v_cvt_pk_bf16_f32 v225, v194, v195\n"
"v_exp_f32 v196, v196\n"
"v_exp_f32 v197, v197\n"
"v_exp_f32 v198, v198\n"
"v_exp_f32 v199, v199\n"
"v_cvt_pk_bf16_f32 v228, v196, v197\n"
"v_cvt_pk_bf16_f32 v229, v198, v199\n"
"v_exp_f32 v200, v200\n"
"v_exp_f32 v201, v201\n"
"v_exp_f32 v202, v202\n"
"v_exp_f32 v203, v203\n"
"v_cvt_pk_bf16_f32 v226, v200, v201\n"
"v_cvt_pk_bf16_f32 v227, v202, v203\n"
"v_exp_f32 v204, v204\n"
"v_exp_f32 v205, v205\n"
"v_exp_f32 v206, v206\n"
"v_exp_f32 v207, v207\n"
"v_cvt_pk_bf16_f32 v230, v204, v205\n"
"v_cvt_pk_bf16_f32 v231, v206, v207\n"
"s_waitcnt vmcnt(16)\n"
"s_barrier\n"
"s_setprio 1\n"
"ds_read_b128 v[232:235], v247 offset:4096\n"
"v_mfma_f32_16x16x32_bf16 v[16:19], v[160:163], v[224:227], v[16:19]\n"
"ds_read_b128 v[128:131], %[kb] offset:34816\n"
"v_add_f32 v240, v240, v192\n"
"v_add_f32_e64 v241, v241, v196\n"
"v_mfma_f32_16x16x32_bf16 v[48:51], v[160:163], v[228:231], v[48:51]\n"
"ds_read_b128 v[132:135], %[kb] offset:35072\n"
"v_add_f32 v240, v240, v193\n"
"v_add_f32 v241, v241, v197\n"
"v_mfma_f32_16x16x32_bf16 v[20:23], v[164:167], v[224:227], v[20:23]\n"
"ds_read_b128 v[136:139], %[kb] offset:35328\n"
"v_add_f32 v240, v240, v194\n"
"v_add_f32 v241, v241, v198\n"
"v_mfma_f32_16x16x32_bf16 v[52:55], v[164:167], v[228:231], v[52:55]\n"
"ds_read_b128 v[140:143], %[kb] offset:35584\n"
"v_add_f32 v240, v240, v195\n"
"v_add_f32 v241, v241, v199\n"
"v_mfma_f32_16x16x32_bf16 v[24:27], v[168:171], v[224:227], v[24:27]\n"
"ds_read_b128 v[144:147], %[kb] offset:43008\n"
"v_add_f32 v240, v240, v200\n"
"v_add_f32 v241, v241, v204\n"
"v_mfma_f32_16x16x32_bf16 v[56:59], v[168:171], v[228:231], v[56:59]\n"
"ds_read_b128 v[148:151], %[kb] offset:43264\n"
"v_add_f32 v240, v240, v201\n"
"v_add_f32 v241, v241, v205\n"
"v_mfma_f32_16x16x32_bf16 v[28:31], v[172:175], v[224:227], v[28:31]\n"
"ds_read_b128 v[152:155], %[kb] offset:43520\n"
"v_add_f32 v240, v240, v202\n"
"v_add_f32 v241, v241, v206\n"
"v_mfma_f32_16x16x32_bf16 v[60:63], v[172:175], v[228:231], v[60:63]\n"
"ds_read_b128 v[156:159], %[kb] offset:43776\n"
"v_add_f32 v240, v240, v203\n"
"v_add_f32 v241, v241, v207\n"
"v_mfma_f32_16x16x32_bf16 v[32:35], v[176:179], v[224:227], v[32:35]\n"
"ds_read_b64_tr_b16 v[160:161], %[vbe] offset:32768\n"
"v_mfma_f32_16x16x32_bf16 v[64:67], v[176:179], v[228:231], v[64:67]\n"
"ds_read_b64_tr_b16 v[162:163], %[vbe] offset:40960\n"
"s_waitcnt lgkmcnt(10)\n"
"v_mfma_f32_16x16x32_bf16 v[80:83], v[176:179], v[232:235], v[80:83]\n"
"ds_read_b64_tr_b16 v[164:165], v243 offset:32768\n"
"v_mfma_f32_16x16x32_bf16 v[36:39], v[180:183], v[224:227], v[36:39]\n"
"ds_read_b64_tr_b16 v[166:167], v243 offset:40960\n"
"v_mfma_f32_16x16x32_bf16 v[68:71], v[180:183], v[228:231], v[68:71]\n"
"ds_read_b64_tr_b16 v[168:169], %[vbe] offset:33024\n"
"v_mfma_f32_16x16x32_bf16 v[84:87], v[180:183], v[232:235], v[84:87]\n"
"ds_read_b64_tr_b16 v[170:171], %[vbe] offset:41216\n"
"v_mfma_f32_16x16x32_bf16 v[40:43], v[184:187], v[224:227], v[40:43]\n"
"ds_read_b64_tr_b16 v[172:173], v243 offset:33024\n"
"v_mfma_f32_16x16x32_bf16 v[72:75], v[184:187], v[228:231], v[72:75]\n"
"ds_read_b64_tr_b16 v[174:175], v243 offset:41216\n"
"v_mfma_f32_16x16x32_bf16 v[88:91], v[184:187], v[232:235], v[88:91]\n"
"ds_read_b64_tr_b16 v[176:177], %[vbe] offset:33280\n"
"v_mfma_f32_16x16x32_bf16 v[44:47], v[188:191], v[224:227], v[44:47]\n"
"ds_read_b64_tr_b16 v[178:179], %[vbe] offset:41472\n"
"v_mfma_f32_16x16x32_bf16 v[76:79], v[188:191], v[228:231], v[76:79]\n"
"ds_read_b64_tr_b16 v[180:181], v243 offset:33280\n"
"v_mfma_f32_16x16x32_bf16 v[92:95], v[188:191], v[232:235], v[92:95]\n"
"ds_read_b64_tr_b16 v[182:183], v243 offset:41472\n"
"s_waitcnt lgkmcnt(12)\n"
"v_mfma_f32_16x16x32_bf16 v[192:195], v[128:131], v[96:99], v[212:215]\n"
"ds_read_b64_tr_b16 v[184:185], %[vbe] offset:33536\n"
"v_mfma_f32_16x16x32_bf16 v[192:195], v[132:135], v[100:103], v[192:195]\n"
"ds_read_b64_tr_b16 v[186:187], %[vbe] offset:41728\n"
"v_mfma_f32_16x16x32_bf16 v[192:195], v[136:139], v[104:107], v[192:195]\n"
"ds_read_b64_tr_b16 v[188:189], v243 offset:33536\n"
"v_mfma_f32_16x16x32_bf16 v[192:195], v[140:143], v[108:111], v[192:195]\n"
"ds_read_b64_tr_b16 v[190:191], v243 offset:41728\n"
"v_mfma_f32_16x16x32_bf16 v[196:199], v[128:131], v[112:115], v[216:219]\n"
"v_mfma_f32_16x16x32_bf16 v[196:199], v[132:135], v[116:119], v[196:199]\n"
"v_mfma_f32_16x16x32_bf16 v[196:199], v[136:139], v[120:123], v[196:199]\n"
"v_mfma_f32_16x16x32_bf16 v[196:199], v[140:143], v[124:127], v[196:199]\n"
"v_mfma_f32_16x16x32_bf16 v[200:203], v[144:147], v[96:99], v[212:215]\n"
"v_mfma_f32_16x16x32_bf16 v[200:203], v[148:151], v[100:103], v[200:203]\n"
"v_mfma_f32_16x16x32_bf16 v[200:203], v[152:155], v[104:107], v[200:203]\n"
"v_mfma_f32_16x16x32_bf16 v[200:203], v[156:159], v[108:111], v[200:203]\n"
"v_mfma_f32_16x16x32_bf16 v[204:207], v[144:147], v[112:115], v[216:219]\n"
"v_mfma_f32_16x16x32_bf16 v[204:207], v[148:151], v[116:119], v[204:207]\n"
"v_mfma_f32_16x16x32_bf16 v[204:207], v[152:155], v[120:123], v[204:207]\n"
"v_mfma_f32_16x16x32_bf16 v[204:207], v[156:159], v[124:127], v[204:207]\n"
"s_waitcnt lgkmcnt(0)\n"
"s_barrier\n"
"s_setprio 0\n"
"s_lshl_b32 s88, %[os1], 2\n"
"s_mov_b32 m0, s54\n"
"s_nop 0\n"
"buffer_load_dwordx4 v249, s[76:79], s88 offen lds\n"
"buffer_load_dwordx4 v250, s[76:79], s88 offen offset:1024 lds\n"
"s_lshl_b32 s89, %[os1], 2\n"
"s_add_u32 s89, s89, 34816\n"
"buffer_load_dwordx4 v249, s[76:79], s89 offen offset:2048 lds\n"
"buffer_load_dwordx4 v250, s[76:79], s89 offen offset:3072 lds\n"
"s_mov_b32 m0, s58\n"
"s_nop 0\n"
"buffer_load_dwordx4 v251, s[76:79], s89 offen offset:3072 lds\n"
"v_exp_f32 v192, v192\n"
"v_exp_f32 v193, v193\n"
"v_exp_f32 v194, v194\n"
"v_exp_f32_e64 v195, v195\n"
"v_cvt_pk_bf16_f32 v224, v192, v193\n"
"v_cvt_pk_bf16_f32 v225, v194, v195\n"
"v_exp_f32 v196, v196\n"
"v_exp_f32 v197, v197\n"
"v_exp_f32 v198, v198\n"
"v_exp_f32 v199, v199\n"
"v_cvt_pk_bf16_f32 v228, v196, v197\n"
"v_cvt_pk_bf16_f32 v229, v198, v199\n"
"v_exp_f32 v200, v200\n"
"v_exp_f32 v201, v201\n"
"v_exp_f32 v202, v202\n"
"v_exp_f32 v203, v203\n"
"v_cvt_pk_bf16_f32 v226, v200, v201\n"
"v_cvt_pk_bf16_f32 v227, v202, v203\n"
"v_exp_f32 v204, v204\n"
"v_exp_f32 v205, v205\n"
"v_exp_f32 v206, v206\n"
"v_exp_f32 v207, v207\n"
"v_cvt_pk_bf16_f32 v230, v204, v205\n"
"v_cvt_pk_bf16_f32 v231, v206, v207\n"
"s_waitcnt vmcnt(16)\n"
"s_barrier\n"
"s_setprio 1\n"
"ds_read_b128 v[232:235], v247 offset:4096\n"
"v_mfma_f32_16x16x32_bf16 v[16:19], v[160:163], v[224:227], v[16:19]\n"
"ds_read_b128 v[128:131], %[kb] offset:51200\n"
"v_add_f32 v240, v240, v192\n"
"v_add_f32_e64 v241, v241, v196\n"
"v_mfma_f32_16x16x32_bf16 v[48:51], v[160:163], v[228:231], v[48:51]\n"
"ds_read_b128 v[132:135], %[kb] offset:51456\n"
"v_add_f32 v240, v240, v193\n"
"v_add_f32 v241, v241, v197\n"
"v_mfma_f32_16x16x32_bf16 v[20:23], v[164:167], v[224:227], v[20:23]\n"
"ds_read_b128 v[136:139], %[kb] offset:51712\n"
"v_add_f32 v240, v240, v194\n"
"v_add_f32 v241, v241, v198\n"
"v_mfma_f32_16x16x32_bf16 v[52:55], v[164:167], v[228:231], v[52:55]\n"
"ds_read_b128 v[140:143], %[kb] offset:51968\n"
"v_add_f32 v240, v240, v195\n"
"v_add_f32 v241, v241, v199\n"
"v_mfma_f32_16x16x32_bf16 v[24:27], v[168:171], v[224:227], v[24:27]\n"
"ds_read_b128 v[144:147], %[kb] offset:59392\n"
"v_add_f32 v240, v240, v200\n"
"v_add_f32 v241, v241, v204\n"
"v_mfma_f32_16x16x32_bf16 v[56:59], v[168:171], v[228:231], v[56:59]\n"
"ds_read_b128 v[148:151], %[kb] offset:59648\n"
"v_add_f32 v240, v240, v201\n"
"v_add_f32 v241, v241, v205\n"
"v_mfma_f32_16x16x32_bf16 v[28:31], v[172:175], v[224:227], v[28:31]\n"
"ds_read_b128 v[152:155], %[kb] offset:59904\n"
"v_add_f32 v240, v240, v202\n"
"v_add_f32 v241, v241, v206\n"
"v_mfma_f32_16x16x32_bf16 v[60:63], v[172:175], v[228:231], v[60:63]\n"
"ds_read_b128 v[156:159], %[kb] offset:60160\n"
"v_add_f32 v240, v240, v203\n"
"v_add_f32 v241, v241, v207\n"
"v_mfma_f32_16x16x32_bf16 v[32:35], v[176:179], v[224:227], v[32:35]\n"
"ds_read_b64_tr_b16 v[160:161], %[vbe] offset:49152\n"
"v_mfma_f32_16x16x32_bf16 v[64:67], v[176:179], v[228:231], v[64:67]\n"
"ds_read_b64_tr_b16 v[162:163], %[vbe] offset:57344\n"
"s_waitcnt lgkmcnt(10)\n"
"v_mfma_f32_16x16x32_bf16 v[80:83], v[176:179], v[232:235], v[80:83]\n"
"ds_read_b64_tr_b16 v[164:165], v243 offset:49152\n"
"v_mfma_f32_16x16x32_bf16 v[36:39], v[180:183], v[224:227], v[36:39]\n"
"ds_read_b64_tr_b16 v[166:167], v243 offset:57344\n"
"v_mfma_f32_16x16x32_bf16 v[68:71], v[180:183], v[228:231], v[68:71]\n"
"ds_read_b64_tr_b16 v[168:169], %[vbe] offset:49408\n"
"v_mfma_f32_16x16x32_bf16 v[84:87], v[180:183], v[232:235], v[84:87]\n"
"ds_read_b64_tr_b16 v[170:171], %[vbe] offset:57600\n"
"v_mfma_f32_16x16x32_bf16 v[40:43], v[184:187], v[224:227], v[40:43]\n"
"ds_read_b64_tr_b16 v[172:173], v243 offset:49408\n"
"v_mfma_f32_16x16x32_bf16 v[72:75], v[184:187], v[228:231], v[72:75]\n"
"ds_read_b64_tr_b16 v[174:175], v243 offset:57600\n"
"v_mfma_f32_16x16x32_bf16 v[88:91], v[184:187], v[232:235], v[88:91]\n"
"ds_read_b64_tr_b16 v[176:177], %[vbe] offset:49664\n"
"v_mfma_f32_16x16x32_bf16 v[44:47], v[188:191], v[224:227], v[44:47]\n"
"ds_read_b64_tr_b16 v[178:179], %[vbe] offset:57856\n"
"v_mfma_f32_16x16x32_bf16 v[76:79], v[188:191], v[228:231], v[76:79]\n"
"ds_read_b64_tr_b16 v[180:181], v243 offset:49664\n"
"v_mfma_f32_16x16x32_bf16 v[92:95], v[188:191], v[232:235], v[92:95]\n"
"ds_read_b64_tr_b16 v[182:183], v243 offset:57856\n"
"s_waitcnt lgkmcnt(12)\n"
"v_mfma_f32_16x16x32_bf16 v[192:195], v[128:131], v[96:99], v[212:215]\n"
"ds_read_b64_tr_b16 v[184:185], %[vbe] offset:49920\n"
"v_mfma_f32_16x16x32_bf16 v[192:195], v[132:135], v[100:103], v[192:195]\n"
"ds_read_b64_tr_b16 v[186:187], %[vbe] offset:58112\n"
"v_mfma_f32_16x16x32_bf16 v[192:195], v[136:139], v[104:107], v[192:195]\n"
"ds_read_b64_tr_b16 v[188:189], v243 offset:49920\n"
"v_mfma_f32_16x16x32_bf16 v[192:195], v[140:143], v[108:111], v[192:195]\n"
"ds_read_b64_tr_b16 v[190:191], v243 offset:58112\n"
"v_mfma_f32_16x16x32_bf16 v[196:199], v[128:131], v[112:115], v[216:219]\n"
"v_mfma_f32_16x16x32_bf16 v[196:199], v[132:135], v[116:119], v[196:199]\n"
"v_mfma_f32_16x16x32_bf16 v[196:199], v[136:139], v[120:123], v[196:199]\n"
"v_mfma_f32_16x16x32_bf16 v[196:199], v[140:143], v[124:127], v[196:199]\n"
"v_mfma_f32_16x16x32_bf16 v[200:203], v[144:147], v[96:99], v[212:215]\n"
"v_mfma_f32_16x16x32_bf16 v[200:203], v[148:151], v[100:103], v[200:203]\n"
"v_mfma_f32_16x16x32_bf16 v[200:203], v[152:155], v[104:107], v[200:203]\n"
"v_mfma_f32_16x16x32_bf16 v[200:203], v[156:159], v[108:111], v[200:203]\n"
"v_mfma_f32_16x16x32_bf16 v[204:207], v[144:147], v[112:115], v[216:219]\n"
"v_mfma_f32_16x16x32_bf16 v[204:207], v[148:151], v[116:119], v[204:207]\n"
"v_mfma_f32_16x16x32_bf16 v[204:207], v[152:155], v[120:123], v[204:207]\n"
"v_mfma_f32_16x16x32_bf16 v[204:207], v[156:159], v[124:127], v[204:207]\n"
"s_waitcnt lgkmcnt(0)\n"
"s_barrier\n"
"s_setprio 0\n"
"s_branch LT3_%=\n"
".p2align 3\n"
"LF3_%=:\n"
"s_add_u32 s52, %[hb], 12288\n"
"s_add_u32 s56, %[hb], 4096\n"
"s_add_u32 s53, %[hb], 28672\n"
"s_add_u32 s57, %[hb], 20480\n"
"s_add_u32 s54, %[hb], 45056\n"
"s_add_u32 s58, %[hb], 36864\n"
"s_add_u32 s55, %[hb], 61440\n"
"s_add_u32 s59, %[hb], 53248\n"
"s_lshr_b32 s90, %[hb], 3\n"
"s_add_u32 s90, s90, 131072\n"
"s_movk_i32 s68, 1152\n"
"s_lshr_b32 s69, %[nb256], 3\n"
"s_add_u32 s69, s69, 5632\n"
"v_and_b32 v254, 255, %[g0]\n"
"v_add_u32 v254, %[kq64], v254\n"
"v_and_b32 v254, 255, v254\n"
"v_lshrrev_b32 v255, 8, %[g0]\n"
"v_mul_u32_u24 v255, s68, v255\n"
"v_add_u32 v255, %[nb256], v255\n"
"v_add_u32 v249, v254, v255\n"
"v_xor_b32 v254, 32, v254\n"
"v_add_u32 v250, v254, v255\n"
"v_add_u32 v250, 3584, v250\n"
"v_subrev_u32 v251, 18432, v250\n"
"v_lshrrev_b32 v254, 5, %[l16]\n"
"v_mul_u32_u24 v254, s68, v254\n"
"v_and_b32 v255, 16, %[l16]\n"
"v_add_u32 v254, v254, v255\n"
"v_add_u32 v252, s69, v254\n"
"s_lshr_b32 s68, %[hb], 11\n"
"s_or_b32 s68, s68, %[hb]\n"
"v_and_b32 v244, 0xffff, %[vbe]\n"
"v_xor_b32 v244, s68, v244\n"
"v_xor_b32 v245, 32, v244\n"
"v_lshrrev_b32 v246, 1, %[l16]\n"
"v_add_u32 v246, s90, v246\n"
"v_add_u32 v228, 2048, %[l16]\n"
"s_add_u32 s68, s90, 16384\n"
"v_add_u32 v247, s68, %[l16]\n"
"s_lshl_b32 s88, %[osoff], 2\n"
"s_mov_b32 m0, s52\n"
"s_nop 0\n"
"buffer_load_dwordx4 v249, s[76:79], s88 offen lds\n"
"buffer_load_dwordx4 v250, s[76:79], s88 offen offset:1024 lds\n"
"s_lshl_b32 s89, %[osoff], 2\n"
"s_add_u32 s89, s89, 34816\n"
"buffer_load_dwordx4 v249, s[76:79], s89 offen offset:2048 lds\n"
"buffer_load_dwordx4 v250, s[76:79], s89 offen offset:3072 lds\n"
"s_mov_b32 m0, s56\n"
"s_nop 0\n"
"buffer_load_dwordx4 v251, s[76:79], s89 offen offset:3072 lds\n"
"s_lshl_b32 s88, %[osoff], 2\n"
"s_add_u32 s88, s88, 73728\n"
"s_mov_b32 m0, s53\n"
"s_nop 0\n"
"buffer_load_dwordx4 v249, s[76:79], s88 offen lds\n"
"buffer_load_dwordx4 v250, s[76:79], s88 offen offset:1024 lds\n"
"s_lshl_b32 s89, %[osoff], 2\n"
"s_add_u32 s89, s89, 108544\n"
"buffer_load_dwordx4 v249, s[76:79], s89 offen offset:2048 lds\n"
"buffer_load_dwordx4 v250, s[76:79], s89 offen offset:3072 lds\n"
"s_mov_b32 m0, s57\n"
"s_nop 0\n"
"buffer_load_dwordx4 v251, s[76:79], s89 offen offset:3072 lds\n"
"s_add_u32 m0, s90, 1024\n"
"s_nop 0\n"
"buffer_load_dwordx4 v252, s[76:79], s89 offen offset:2048 lds\n"
"s_lshl_b32 s88, %[os1], 2\n"
"s_mov_b32 m0, s54\n"
"s_nop 0\n"
"buffer_load_dwordx4 v249, s[76:79], s88 offen lds\n"
"buffer_load_dwordx4 v250, s[76:79], s88 offen offset:1024 lds\n"
"s_lshl_b32 s89, %[os1], 2\n"
"s_add_u32 s89, s89, 34816\n"
"buffer_load_dwordx4 v249, s[76:79], s89 offen offset:2048 lds\n"
"buffer_load_dwordx4 v250, s[76:79], s89 offen offset:3072 lds\n"
"s_mov_b32 m0, s58\n"
"s_nop 0\n"
"buffer_load_dwordx4 v251, s[76:79], s89 offen offset:3072 lds\n"
"s_lshl_b32 s88, %[os1], 2\n"
"s_add_u32 s88, s88, 73728\n"
"s_mov_b32 m0, s55\n"
"s_nop 0\n"
"buffer_load_dwordx4 v249, s[76:79], s88 offen lds\n"
"buffer_load_dwordx4 v250, s[76:79], s88 offen offset:1024 lds\n"
"s_lshl_b32 s89, %[os1], 2\n"
"s_add_u32 s89, s89, 108544\n"
"buffer_load_dwordx4 v249, s[76:79], s89 offen offset:2048 lds\n"
"buffer_load_dwordx4 v250, s[76:79], s89 offen offset:3072 lds\n"
"s_mov_b32 m0, s59\n"
"s_nop 0\n"
"buffer_load_dwordx4 v251, s[76:79], s89 offen offset:3072 lds\n"
"s_add_u32 m0, s90, 5120\n"
"s_nop 0\n"
"buffer_load_dwordx4 v252, s[76:79], s89 offen offset:2048 lds\n"
"s_add_u32 s60, %[osoff], 6144\n"
"buffer_load_dwordx4 v[144:147], %[l16], %[srdo], s60 offen offset:1024 sc1\n"
"buffer_load_dwordx4 v[128:131], %[l16], %[srdo], s60 offen sc1\n"
"buffer_load_dwordx4 v[160:163], v228, %[srdo], s60 offen sc1\n"
"s_add_u32 s61, %[osoff], 15360\n"
"buffer_load_dwordx4 v[148:151], %[l16], %[srdo], s61 offen offset:1024 sc1\n"
"buffer_load_dwordx4 v[132:135], %[l16], %[srdo], s61 offen sc1\n"
"buffer_load_dwordx4 v[164:167], v228, %[srdo], s61 offen sc1\n"
"s_add_u32 s62, %[osoff], 24576\n"
"buffer_load_dwordx4 v[152:155], %[l16], %[srdo], s62 offen offset:1024 sc1\n"
"buffer_load_dwordx4 v[136:139], %[l16], %[srdo], s62 offen sc1\n"
"buffer_load_dwordx4 v[168:171], v228, %[srdo], s62 offen sc1\n"
"s_add_u32 s63, %[osoff], 33792\n"
"buffer_load_dwordx4 v[156:159], %[l16], %[srdo], s63 offen offset:1024 sc1\n"
"buffer_load_dwordx4 v[140:143], %[l16], %[srdo], s63 offen sc1\n"
"buffer_load_dwordx4 v[172:175], v228, %[srdo], s63 offen sc1\n"
"v_mov_b32 v176, 0\n"
"v_mov_b32 v177, 0\n"
"v_mov_b32 v178, 0\n"
"v_mov_b32 v179, 0\n"
"v_mov_b32 v180, 0\n"
"v_mov_b32 v181, 0\n"
"v_mov_b32 v182, 0\n"
"v_mov_b32 v183, 0\n"
"s_waitcnt vmcnt(0)\n"
"s_barrier\n"
"ds_read_b64_tr_b16 v[100:101], v244 offset:0\n"
"ds_read_b64_tr_b16 v[102:103], v244 offset:8192\n"
"ds_read_b64_tr_b16 v[104:105], v245 offset:0\n"
"ds_read_b64_tr_b16 v[106:107], v245 offset:8192\n"
"ds_read_b64_tr_b16 v[108:109], v244 offset:256\n"
"ds_read_b64_tr_b16 v[110:111], v244 offset:8448\n"
"ds_read_b64_tr_b16 v[112:113], v245 offset:256\n"
"ds_read_b64_tr_b16 v[114:115], v245 offset:8448\n"
"ds_read_b64_tr_b16 v[116:117], v244 offset:512\n"
"ds_read_b64_tr_b16 v[118:119], v244 offset:8704\n"
"ds_read_b64_tr_b16 v[120:121], v245 offset:512\n"
"ds_read_b64_tr_b16 v[122:123], v245 offset:8704\n"
"ds_read_b64_tr_b16 v[124:125], v244 offset:768\n"
"ds_read_b64_tr_b16 v[126:127], v244 offset:8960\n"
"s_waitcnt lgkmcnt(0)\n"
"s_nop 7\n"
"s_branch LO3_%=\n"
".p2align 6\n"
"LA3_%=:\n"
"s_cmp_eq_u32 s61, 0x30000\n"
"s_cbranch_scc0 LNd3_%=\n"
"s_getpc_b64 s[94:95]\n"
"LMd3_%=:\n"
"s_add_u32 s94, s94, (LZZ3_%=-16384-LMd3_%=)&0xffffffff\n"
"s_addc_u32 s95, s95, (LZZ3_%=-16384-LMd3_%=)>>32\n"
"v_lshlrev_b32 v236, 4, %[l16]\n"
"s_lshr_b32 s68, %[hb], 9\n"
"v_add_u32 v236, s68, v236\n"
"s_lshl_b32 exec_lo, 0x11111111, %[wrot]\n"
"s_mov_b32 exec_hi, exec_lo\n"
"global_load_dword v253, v236, s[94:95]\n"
"s_mov_b64 exec, -1\n"
"LNd3_%=:\n"
"s_nop 0\n"
"KA3 0,s52,16384,s56\n"
"KA3 16384,s53,32768,s57\n"
"KA3 32768,s54,49152,s58\n"
"KA3 49152,s55,0,s59\n"
"s_cbranch_scc0 LA3_%=\n"
"s_nop 0\n"
"s_barrier\n"
"s_setprio 1\n"
"ds_read_b128 v[232:235], v247 offset:4096\n"
"v_mfma_f32_16x16x32_bf16 v[16:19], v[160:163], v[224:227], v[16:19]\n"
"ds_read_b128 v[128:131], v244 offset:2048\n"
"v_add_f32 v240, v240, v192\n"
"v_add_f32 v241, v241, v196\n"
"v_mfma_f32_16x16x32_bf16 v[48:51], v[160:163], v[228:231], v[48:51]\n"
"ds_read_b128 v[132:135], v244 offset:2304\n"
"v_add_f32 v240, v240, v193\n"
"v_add_f32 v241, v241, v197\n"
"v_mfma_f32_16x16x32_bf16 v[20:23], v[164:167], v[224:227], v[20:23]\n"
"ds_read_b128 v[136:139], v244 offset:2560\n"
"v_add_f32 v240, v240, v194\n"
"v_add_f32 v241, v241, v198\n"
"v_mfma_f32_16x16x32_bf16 v[52:55], v[164:167], v[228:231], v[52:55]\n"
"ds_read_b128 v[140:143], v244 offset:2816\n"
"v_add_f32 v240, v240, v195\n"
"v_add_f32 v241, v241, v199\n"
"v_mfma_f32_16x16x32_bf16 v[24:27], v[168:171], v[224:227], v[24:27]\n"
"ds_read_b128 v[144:147], v244 offset:10240\n"
"v_add_f32 v240, v240, v200\n"
"v_add_f32 v241, v241, v204\n"
"v_mfma_f32_16x16x32_bf16 v[56:59], v[168:171], v[228:231], v[56:59]\n"
"ds_read_b128 v[148:151], v244 offset:10496\n"
"v_add_f32 v240, v240, v201\n"
"v_add_f32 v241, v241, v205\n"
"v_mfma_f32_16x16x32_bf16 v[28:31], v[172:175], v[224:227], v[28:31]\n"
"ds_read_b128 v[152:155], v244 offset:10752\n"
"v_add_f32 v240, v240, v202\n"
"v_add_f32 v241, v241, v206\n"
"v_mfma_f32_16x16x32_bf16 v[60:63], v[172:175], v[228:231], v[60:63]\n"
"ds_read_b128 v[156:159], v244 offset:11008\n"
"v_add_f32 v240, v240, v203\n"
"v_add_f32 v241, v241, v207\n"
"v_mfma_f32_16x16x32_bf16 v[32:35], v[176:179], v[224:227], v[32:35]\n"
"ds_read_b64_tr_b16 v[160:161], v245 offset:0\n"
"v_mfma_f32_16x16x32_bf16 v[64:67], v[176:179], v[228:231], v[64:67]\n"
"ds_read_b64_tr_b16 v[162:163], v245 offset:8192\n"
"s_waitcnt lgkmcnt(10)\n"
"v_mfma_f32_16x16x32_bf16 v[80:83], v[176:179], v[232:235], v[80:83]\n"
"ds_read_b64_tr_b16 v[164:165], v246 offset:0\n"
"v_mfma_f32_16x16x32_bf16 v[36:39], v[180:183], v[224:227], v[36:39]\n"
"ds_read_b64_tr_b16 v[166:167], v246 offset:8192\n"
"v_mfma_f32_16x16x32_bf16 v[68:71], v[180:183], v[228:231], v[68:71]\n"
"ds_read_b64_tr_b16 v[168:169], v245 offset:256\n"
"v_mfma_f32_16x16x32_bf16 v[84:87], v[180:183], v[232:235], v[84:87]\n"
"ds_read_b64_tr_b16 v[170:171], v245 offset:8448\n"
"v_mfma_f32_16x16x32_bf16 v[40:43], v[184:187], v[224:227], v[40:43]\n"
"ds_read_b64_tr_b16 v[172:173], v246 offset:256\n"
"v_mfma_f32_16x16x32_bf16 v[72:75], v[184:187], v[228:231], v[72:75]\n"
"ds_read_b64_tr_b16 v[174:175], v246 offset:8448\n"
"v_mfma_f32_16x16x32_bf16 v[88:91], v[184:187], v[232:235], v[88:91]\n"
"ds_read_b64_tr_b16 v[176:177], v245 offset:512\n"
"v_mfma_f32_16x16x32_bf16 v[44:47], v[188:191], v[224:227], v[44:47]\n"
"ds_read_b64_tr_b16 v[178:179], v245 offset:8704\n"
"v_mfma_f32_16x16x32_bf16 v[76:79], v[188:191], v[228:231], v[76:79]\n"
"ds_read_b64_tr_b16 v[180:181], v246 offset:512\n"
"v_mfma_f32_16x16x32_bf16 v[92:95], v[188:191], v[232:235], v[92:95]\n"
"ds_read_b64_tr_b16 v[182:183], v246 offset:8704\n"
"s_waitcnt lgkmcnt(12)\n"
"v_mfma_f32_16x16x32_bf16 v[192:195], v[128:131], v[96:99], v[212:215]\n"
"ds_read_b64_tr_b16 v[184:185], v245 offset:768\n"
"v_mfma_f32_16x16x32_bf16 v[192:195], v[132:135], v[100:103], v[192:195]\n"
"ds_read_b64_tr_b16 v[186:187], v245 offset:8960\n"
"v_mfma_f32_16x16x32_bf16 v[192:195], v[136:139], v[104:107], v[192:195]\n"
"ds_read_b64_tr_b16 v[188:189], v246 offset:768\n"
"v_mfma_f32_16x16x32_bf16 v[192:195], v[140:143], v[108:111], v[192:195]\n"
"ds_read_b64_tr_b16 v[190:191], v246 offset:8960\n"
"v_mfma_f32_16x16x32_bf16 v[196:199], v[128:131], v[112:115], v[216:219]\n"
"v_mfma_f32_16x16x32_bf16 v[196:199], v[132:135], v[116:119], v[196:199]\n"
"v_mfma_f32_16x16x32_bf16 v[196:199], v[136:139], v[120:123], v[196:199]\n"
"v_mfma_f32_16x16x32_bf16 v[196:199], v[140:143], v[124:127], v[196:199]\n"
"v_mfma_f32_16x16x32_bf16 v[200:203], v[144:147], v[96:99], v[212:215]\n"
"v_mfma_f32_16x16x32_bf16 v[200:203], v[148:151], v[100:103], v[200:203]\n"
"v_mfma_f32_16x16x32_bf16 v[200:203], v[152:155], v[104:107], v[200:203]\n"
"v_mfma_f32_16x16x32_bf16 v[200:203], v[156:159], v[108:111], v[200:203]\n"
"v_mfma_f32_16x16x32_bf16 v[204:207], v[144:147], v[112:115], v[216:219]\n"
"v_mfma_f32_16x16x32_bf16 v[204:207], v[148:151], v[116:119], v[204:207]\n"
"v_mfma_f32_16x16x32_bf16 v[204:207], v[152:155], v[120:123], v[204:207]\n"
"v_mfma_f32_16x16x32_bf16 v[204:207], v[156:159], v[124:127], v[204:207]\n"
"s_waitcnt lgkmcnt(0)\n"
"s_barrier\n"
"s_setprio 0\n"
"s_lshl_b32 s88, %[osoff], 2\n"
"s_mov_b32 m0, s52\n"
"s_nop 0\n"
"buffer_load_dwordx4 v249, s[76:79], s88 offen lds\n"
"buffer_load_dwordx4 v250, s[76:79], s88 offen offset:1024 lds\n"
"v_exp_f32 v192, v192\n"
"v_exp_f32 v193, v193\n"
"v_exp_f32 v194, v194\n"
"v_exp_f32 v195, v195\n"
"v_cvt_pk_bf16_f32 v224, v192, v193\n"
"v_cvt_pk_bf16_f32 v225, v194, v195\n"
"v_exp_f32 v196, v196\n"
"v_exp_f32 v197, v197\n"
"v_exp_f32 v198, v198\n"
"v_exp_f32 v199, v199\n"
"v_cvt_pk_bf16_f32 v228, v196, v197\n"
"v_cvt_pk_bf16_f32 v229, v198, v199\n"
"v_exp_f32 v200, v200\n"
"v_exp_f32 v201, v201\n"
"v_exp_f32 v202, v202\n"
"v_exp_f32 v203, v203\n"
"v_cvt_pk_bf16_f32 v226, v200, v201\n"
"v_cvt_pk_bf16_f32 v227, v202, v203\n"
"v_exp_f32 v204, v204\n"
"v_exp_f32 v205, v205\n"
"v_exp_f32 v206, v206\n"
"v_exp_f32 v207, v207\n"
"v_cvt_pk_bf16_f32 v230, v204, v205\n"
"v_cvt_pk_bf16_f32 v231, v206, v207\n"
"s_waitcnt vmcnt(12)\n"
"s_barrier\n"
"s_setprio 1\n"
"ds_read_b128 v[232:235], v247 offset:4096\n"
"v_mfma_f32_16x16x32_bf16 v[16:19], v[160:163], v[224:227], v[16:19]\n"
"ds_read_b128 v[128:131], %[kb] offset:18432\n"
"v_add_f32 v240, v240, v192\n"
"v_add_f32_e64 v241, v241, v196\n"
"v_mfma_f32_16x16x32_bf16 v[48:51], v[160:163], v[228:231], v[48:51]\n"
"ds_read_b128 v[132:135], %[kb] offset:18688\n"
"v_add_f32 v240, v240, v193\n"
"v_add_f32 v241, v241, v197\n"
"v_mfma_f32_16x16x32_bf16 v[20:23], v[164:167], v[224:227], v[20:23]\n"
"ds_read_b128 v[136:139], %[kb] offset:18944\n"
"v_add_f32 v240, v240, v194\n"
"v_add_f32 v241, v241, v198\n"
"v_mfma_f32_16x16x32_bf16 v[52:55], v[164:167], v[228:231], v[52:55]\n"
"ds_read_b128 v[140:143], %[kb] offset:19200\n"
"v_add_f32 v240, v240, v195\n"
"v_add_f32 v241, v241, v199\n"
"v_mfma_f32_16x16x32_bf16 v[24:27], v[168:171], v[224:227], v[24:27]\n"
"ds_read_b128 v[144:147], %[kb] offset:26624\n"
"v_add_f32 v240, v240, v200\n"
"v_add_f32 v241, v241, v204\n"
"v_mfma_f32_16x16x32_bf16 v[56:59], v[168:171], v[228:231], v[56:59]\n"
"ds_read_b128 v[148:151], %[kb] offset:26880\n"
"v_add_f32 v240, v240, v201\n"
"v_add_f32 v241, v241, v205\n"
"v_mfma_f32_16x16x32_bf16 v[28:31], v[172:175], v[224:227], v[28:31]\n"
"ds_read_b128 v[152:155], %[kb] offset:27136\n"
"v_add_f32 v240, v240, v202\n"
"v_add_f32 v241, v241, v206\n"
"v_mfma_f32_16x16x32_bf16 v[60:63], v[172:175], v[228:231], v[60:63]\n"
"ds_read_b128 v[156:159], %[kb] offset:27392\n"
"v_add_f32 v240, v240, v203\n"
"v_add_f32 v241, v241, v207\n"
"v_mfma_f32_16x16x32_bf16 v[32:35], v[176:179], v[224:227], v[32:35]\n"
"ds_read_b64_tr_b16 v[160:161], %[vbe] offset:16384\n"
"v_mfma_f32_16x16x32_bf16 v[64:67], v[176:179], v[228:231], v[64:67]\n"
"ds_read_b64_tr_b16 v[162:163], %[vbe] offset:24576\n"
"s_waitcnt lgkmcnt(10)\n"
"v_mfma_f32_16x16x32_bf16 v[80:83], v[176:179], v[232:235], v[80:83]\n"
"ds_read_b64_tr_b16 v[164:165], v243 offset:16384\n"
"v_mfma_f32_16x16x32_bf16 v[36:39], v[180:183], v[224:227], v[36:39]\n"
"ds_read_b64_tr_b16 v[166:167], v243 offset:24576\n"
"v_mfma_f32_16x16x32_bf16 v[68:71], v[180:183], v[228:231], v[68:71]\n"
"ds_read_b64_tr_b16 v[168:169], %[vbe] offset:16640\n"
"v_mfma_f32_16x16x32_bf16 v[84:87], v[180:183], v[232:235], v[84:87]\n"
"ds_read_b64_tr_b16 v[170:171], %[vbe] offset:24832\n"
"v_mfma_f32_16x16x32_bf16 v[40:43], v[184:187], v[224:227], v[40:43]\n"
"ds_read_b64_tr_b16 v[172:173], v243 offset:16640\n"
"v_mfma_f32_16x16x32_bf16 v[72:75], v[184:187], v[228:231], v[72:75]\n"
"ds_read_b64_tr_b16 v[174:175], v243 offset:24832\n"
"v_mfma_f32_16x16x32_bf16 v[88:91], v[184:187], v[232:235], v[88:91]\n"
"ds_read_b64_tr_b16 v[176:177], %[vbe] offset:16896\n"
"v_mfma_f32_16x16x32_bf16 v[44:47], v[188:191], v[224:227], v[44:47]\n"
"ds_read_b64_tr_b16 v[178:179], %[vbe] offset:25088\n"
"v_mfma_f32_16x16x32_bf16 v[76:79], v[188:191], v[228:231], v[76:79]\n"
"ds_read_b64_tr_b16 v[180:181], v243 offset:16896\n"
"v_mfma_f32_16x16x32_bf16 v[92:95], v[188:191], v[232:235], v[92:95]\n"
"ds_read_b64_tr_b16 v[182:183], v243 offset:25088\n"
"s_waitcnt lgkmcnt(12)\n"
"v_mfma_f32_16x16x32_bf16 v[192:195], v[128:131], v[96:99], v[212:215]\n"
"ds_read_b64_tr_b16 v[184:185], %[vbe] offset:17152\n"
"v_mfma_f32_16x16x32_bf16 v[192:195], v[132:135], v[100:103], v[192:195]\n"
"ds_read_b64_tr_b16 v[186:187], %[vbe] offset:25344\n"
"v_mfma_f32_16x16x32_bf16 v[192:195], v[136:139], v[104:107], v[192:195]\n"
"ds_read_b64_tr_b16 v[188:189], v243 offset:17152\n"
"v_mfma_f32_16x16x32_bf16 v[192:195], v[140:143], v[108:111], v[192:195]\n"
"ds_read_b64_tr_b16 v[190:191], v243 offset:25344\n"
"v_mfma_f32_16x16x32_bf16 v[196:199], v[128:131], v[112:115], v[216:219]\n"
"v_mfma_f32_16x16x32_bf16 v[196:199], v[132:135], v[116:119], v[196:199]\n"
"v_mfma_f32_16x16x32_bf16 v[196:199], v[136:139], v[120:123], v[196:199]\n"
"v_mfma_f32_16x16x32_bf16 v[196:199], v[140:143], v[124:127], v[196:199]\n"
"v_mfma_f32_16x16x32_bf16 v[200:203], v[144:147], v[96:99], v[212:215]\n"
"v_mfma_f32_16x16x32_bf16 v[200:203], v[148:151], v[100:103], v[200:203]\n"
"v_mfma_f32_16x16x32_bf16 v[200:203], v[152:155], v[104:107], v[200:203]\n"
"v_mfma_f32_16x16x32_bf16 v[200:203], v[156:159], v[108:111], v[200:203]\n"
"v_mfma_f32_16x16x32_bf16 v[204:207], v[144:147], v[112:115], v[216:219]\n"
"v_mfma_f32_16x16x32_bf16 v[204:207], v[148:151], v[116:119], v[204:207]\n"
"v_mfma_f32_16x16x32_bf16 v[204:207], v[152:155], v[120:123], v[204:207]\n"
"v_mfma_f32_16x16x32_bf16 v[204:207], v[156:159], v[124:127], v[204:207]\n"
"s_waitcnt lgkmcnt(0)\n"
"s_barrier\n"
"s_setprio 0\n"
"s_lshl_b32 s89, %[osoff], 2\n"
"s_add_u32 s89, s89, 34816\n"
"buffer_load_dwordx4 v249, s[76:79], s89 offen offset:2048 lds\n"
"buffer_load_dwordx4 v250, s[76:79], s89 offen offset:3072 lds\n"
"s_mov_b32 m0, s56\n"
"s_nop 0\n"
"buffer_load_dwordx4 v251, s[76:79], s89 offen offset:3072 lds\n"
"v_exp_f32 v192, v192\n"
"v_exp_f32 v193, v193\n"
"v_exp_f32 v194, v194\n"
"v_exp_f32 v195, v195\n"
"v_cvt_pk_bf16_f32 v224, v192, v193\n"
"v_cvt_pk_bf16_f32 v225, v194, v195\n"
"v_exp_f32 v196, v196\n"
"v_exp_f32 v197, v197\n"
"v_exp_f32 v198, v198\n"
"v_exp_f32 v199, v199\n"
"v_cvt_pk_bf16_f32 v228, v196, v197\n"
"v_cvt_pk_bf16_f32 v229, v198, v199\n"
"v_exp_f32 v200, v200\n"
"v_exp_f32 v201, v201\n"
"v_exp_f32 v202, v202\n"
"v_exp_f32 v203, v203\n"
"v_cvt_pk_bf16_f32 v226, v200, v201\n"
"v_cvt_pk_bf16_f32 v227, v202, v203\n"
"v_exp_f32 v204, v204\n"
"v_exp_f32 v205, v205\n"
"v_exp_f32 v206, v206\n"
"v_exp_f32 v207, v207\n"
"v_cvt_pk_bf16_f32 v230, v204, v205\n"
"v_cvt_pk_bf16_f32 v231, v206, v207\n"
"s_barrier\n"
"s_setprio 1\n"
"ds_read_b128 v[232:235], v247 offset:4096\n"
"v_mfma_f32_16x16x32_bf16 v[16:19], v[160:163], v[224:227], v[16:19]\n"
"ds_read_b128 v[128:131], v244 offset:18432\n"
"v_add_f32 v240, v240, v192\n"
"v_add_f32 v241, v241, v196\n"
"v_mfma_f32_16x16x32_bf16 v[48:51], v[160:163], v[228:231], v[48:51]\n"
"ds_read_b128 v[132:135], v244 offset:18688\n"
"v_add_f32 v240, v240, v193\n"
"v_add_f32 v241, v241, v197\n"
"v_mfma_f32_16x16x32_bf16 v[20:23], v[164:167], v[224:227], v[20:23]\n"
"ds_read_b128 v[136:139], v244 offset:18944\n"
"v_add_f32 v240, v240, v194\n"
"v_add_f32 v241, v241, v198\n"
"v_mfma_f32_16x16x32_bf16 v[52:55], v[164:167], v[228:231], v[52:55]\n"
"ds_read_b128 v[140:143], v244 offset:19200\n"
"v_add_f32 v240, v240, v195\n"
"v_add_f32 v241, v241, v199\n"
"v_mfma_f32_16x16x32_bf16 v[24:27], v[168:171], v[224:227], v[24:27]\n"
"ds_read_b128 v[144:147], v244 offset:26624\n"
"v_add_f32 v240, v240, v200\n"
"v_add_f32 v241, v241, v204\n"
"v_mfma_f32_16x16x32_bf16 v[56:59], v[168:171], v[228:231], v[56:59]\n"
"ds_read_b128 v[148:151], v244 offset:26880\n"
"v_add_f32 v240, v240, v201\n"
"v_add_f32 v241, v241, v205\n"
"v_mfma_f32_16x16x32_bf16 v[28:31], v[172:175], v[224:227], v[28:31]\n"
"ds_read_b128 v[152:155], v244 offset:27136\n"
"v_add_f32 v240, v240, v202\n"
"v_add_f32 v241, v241, v206\n"
"v_mfma_f32_16x16x32_bf16 v[60:63], v[172:175], v[228:231], v[60:63]\n"
"ds_read_b128 v[156:159], v244 offset:27392\n"
"v_add_f32 v240, v240, v203\n"
"v_add_f32 v241, v241, v207\n"
"v_mfma_f32_16x16x32_bf16 v[32:35], v[176:179], v[224:227], v[32:35]\n"
"ds_read_b64_tr_b16 v[160:161], v245 offset:16384\n"
"v_mfma_f32_16x16x32_bf16 v[64:67], v[176:179], v[228:231], v[64:67]\n"
"ds_read_b64_tr_b16 v[162:163], v245 offset:24576\n"
"s_waitcnt lgkmcnt(10)\n"
"v_mfma_f32_16x16x32_bf16 v[80:83], v[176:179], v[232:235], v[80:83]\n"
"ds_read_b64_tr_b16 v[164:165], v246 offset:16384\n"
"v_mfma_f32_16x16x32_bf16 v[36:39], v[180:183], v[224:227], v[36:39]\n"
"ds_read_b64_tr_b16 v[166:167], v246 offset:24576\n"
"v_mfma_f32_16x16x32_bf16 v[68:71], v[180:183], v[228:231], v[68:71]\n"
"ds_read_b64_tr_b16 v[168:169], v245 offset:16640\n"
"v_mfma_f32_16x16x32_bf16 v[84:87], v[180:183], v[232:235], v[84:87]\n"
"ds_read_b64_tr_b16 v[170:171], v245 offset:24832\n"
"v_mfma_f32_16x16x32_bf16 v[40:43], v[184:187], v[224:227], v[40:43]\n"
"ds_read_b64_tr_b16 v[172:173], v246 offset:16640\n"
"v_mfma_f32_16x16x32_bf16 v[72:75], v[184:187], v[228:231], v[72:75]\n"
"ds_read_b64_tr_b16 v[174:175], v246 offset:24832\n"
"v_mfma_f32_16x16x32_bf16 v[88:91], v[184:187], v[232:235], v[88:91]\n"
"ds_read_b64_tr_b16 v[176:177], v245 offset:16896\n"
"v_mfma_f32_16x16x32_bf16 v[44:47], v[188:191], v[224:227], v[44:47]\n"
"ds_read_b64_tr_b16 v[178:179], v245 offset:25088\n"
"v_mfma_f32_16x16x32_bf16 v[76:79], v[188:191], v[228:231], v[76:79]\n"
"ds_read_b64_tr_b16 v[180:181], v246 offset:16896\n"
"v_mfma_f32_16x16x32_bf16 v[92:95], v[188:191], v[232:235], v[92:95]\n"
"ds_read_b64_tr_b16 v[182:183], v246 offset:25088\n"
"s_waitcnt lgkmcnt(12)\n"
"v_mfma_f32_16x16x32_bf16 v[192:195], v[128:131], v[96:99], v[212:215]\n"
"ds_read_b64_tr_b16 v[184:185], v245 offset:17152\n"
"v_mfma_f32_16x16x32_bf16 v[192:195], v[132:135], v[100:103], v[192:195]\n"
"ds_read_b64_tr_b16 v[186:187], v245 offset:25344\n"
"v_mfma_f32_16x16x32_bf16 v[192:195], v[136:139], v[104:107], v[192:195]\n"
"ds_read_b64_tr_b16 v[188:189], v246 offset:17152\n"
"v_mfma_f32_16x16x32_bf16 v[192:195], v[140:143], v[108:111], v[192:195]\n"
"ds_read_b64_tr_b16 v[190:191], v246 offset:25344\n"
"v_mfma_f32_16x16x32_bf16 v[196:199], v[128:131], v[112:115], v[216:219]\n"
"v_mfma_f32_16x16x32_bf16 v[196:199], v[132:135], v[116:119], v[196:199]\n"
"v_mfma_f32_16x16x32_bf16 v[196:199], v[136:139], v[120:123], v[196:199]\n"
"v_mfma_f32_16x16x32_bf16 v[196:199], v[140:143], v[124:127], v[196:199]\n"
"v_mfma_f32_16x16x32_bf16 v[200:203], v[144:147], v[96:99], v[212:215]\n"
"v_mfma_f32_16x16x32_bf16 v[200:203], v[148:151], v[100:103], v[200:203]\n"
"v_mfma_f32_16x16x32_bf16 v[200:203], v[152:155], v[104:107], v[200:203]\n"
"v_mfma_f32_16x16x32_bf16 v[200:203], v[156:159], v[108:111], v[200:203]\n"
"v_mfma_f32_16x16x32_bf16 v[204:207], v[144:147], v[112:115], v[216:219]\n"
"v_mfma_f32_16x16x32_bf16 v[204:207], v[148:151], v[116:119], v[204:207]\n"
"v_mfma_f32_16x16x32_bf16 v[204:207], v[152:155], v[120:123], v[204:207]\n"
"v_mfma_f32_16x16x32_bf16 v[204:207], v[156:159], v[124:127], v[204:207]\n"
"s_waitcnt lgkmcnt(0)\n"
"s_barrier\n"
"s_setprio 0\n"
"s_lshl_b32 s88, %[osoff], 2\n"
"s_add_u32 s88, s88, 73728\n"
"s_mov_b32 m0, s53\n"
"s_nop 0\n"
"buffer_load_dwordx4 v249, s[76:79], s88 offen lds\n"
"buffer_load_dwordx4 v250, s[76:79], s88 offen offset:1024 lds\n"
"v_exp_f32 v192, v192\n"
"v_exp_f32 v193, v193\n"
"v_exp_f32 v194, v194\n"
"v_exp_f32 v195, v195\n"
"v_cvt_pk_bf16_f32 v224, v192, v193\n"
"v_cvt_pk_bf16_f32 v225, v194, v195\n"
"v_exp_f32 v196, v196\n"
"v_exp_f32 v197, v197\n"
"v_exp_f32 v198, v198\n"
"v_exp_f32 v199, v199\n"
"v_cvt_pk_bf16_f32 v228, v196, v197\n"
"v_cvt_pk_bf16_f32 v229, v198, v199\n"
"v_exp_f32 v200, v200\n"
"v_exp_f32 v201, v201\n"
"v_exp_f32 v202, v202\n"
"v_exp_f32 v203, v203\n"
"v_cvt_pk_bf16_f32 v226, v200, v201\n"
"v_cvt_pk_bf16_f32 v227, v202, v203\n"
"v_exp_f32 v204, v204\n"
"v_exp_f32 v205, v205\n"
"v_exp_f32 v206, v206\n"
"v_exp_f32 v207, v207\n"
"v_cvt_pk_bf16_f32 v230, v204, v205\n"
"v_cvt_pk_bf16_f32 v231, v206, v207\n"
"s_waitcnt vmcnt(12)\n"
"s_barrier\n"
"s_setprio 1\n"
"ds_read_b128 v[232:235], v247 offset:4096\n"
"v_mfma_f32_16x16x32_bf16 v[16:19], v[160:163], v[224:227], v[16:19]\n"
"ds_read_b128 v[128:131], %[kb] offset:34816\n"
"v_add_f32 v240, v240, v192\n"
"v_add_f32_e64 v241, v241, v196\n"
"v_mfma_f32_16x16x32_bf16 v[48:51], v[160:163], v[228:231], v[48:51]\n"
"ds_read_b128 v[132:135], %[kb] offset:35072\n"
"v_add_f32 v240, v240, v193\n"
"v_add_f32 v241, v241, v197\n"
"v_mfma_f32_16x16x32_bf16 v[20:23], v[164:167], v[224:227], v[20:23]\n"
"ds_read_b128 v[136:139], %[kb] offset:35328\n"
"v_add_f32 v240, v240, v194\n"
"v_add_f32 v241, v241, v198\n"
"v_mfma_f32_16x16x32_bf16 v[52:55], v[164:167], v[228:231], v[52:55]\n"
"ds_read_b128 v[140:143], %[kb] offset:35584\n"
"v_add_f32 v240, v240, v195\n"
"v_add_f32 v241, v241, v199\n"
"v_mfma_f32_16x16x32_bf16 v[24:27], v[168:171], v[224:227], v[24:27]\n"
"ds_read_b128 v[144:147], %[kb] offset:43008\n"
"v_add_f32 v240, v240, v200\n"
"v_add_f32 v241, v241, v204\n"
"v_mfma_f32_16x16x32_bf16 v[56:59], v[168:171], v[228:231], v[56:59]\n"
"ds_read_b128 v[148:151], %[kb] offset:43264\n"
"v_add_f32 v240, v240, v201\n"
"v_add_f32 v241, v241, v205\n"
"v_mfma_f32_16x16x32_bf16 v[28:31], v[172:175], v[224:227], v[28:31]\n"
"ds_read_b128 v[152:155], %[kb] offset:43520\n"
"v_add_f32 v240, v240, v202\n"
"v_add_f32 v241, v241, v206\n"
"v_mfma_f32_16x16x32_bf16 v[60:63], v[172:175], v[228:231], v[60:63]\n"
"ds_read_b128 v[156:159], %[kb] offset:43776\n"
"v_add_f32 v240, v240, v203\n"
"v_add_f32 v241, v241, v207\n"
"v_mfma_f32_16x16x32_bf16 v[32:35], v[176:179], v[224:227], v[32:35]\n"
"ds_read_b64_tr_b16 v[160:161], %[vbe] offset:32768\n"
"v_mfma_f32_16x16x32_bf16 v[64:67], v[176:179], v[228:231], v[64:67]\n"
"ds_read_b64_tr_b16 v[162:163], %[vbe] offset:40960\n"
"s_waitcnt lgkmcnt(10)\n"
"v_mfma_f32_16x16x32_bf16 v[80:83], v[176:179], v[232:235], v[80:83]\n"
"ds_read_b64_tr_b16 v[164:165], v243 offset:32768\n"
"v_mfma_f32_16x16x32_bf16 v[36:39], v[180:183], v[224:227], v[36:39]\n"
"ds_read_b64_tr_b16 v[166:167], v243 offset:40960\n"
"v_mfma_f32_16x16x32_bf16 v[68:71], v[180:183], v[228:231], v[68:71]\n"
"ds_read_b64_tr_b16 v[168:169], %[vbe] offset:33024\n"
"v_mfma_f32_16x16x32_bf16 v[84:87], v[180:183], v[232:235], v[84:87]\n"
"ds_read_b64_tr_b16 v[170:171], %[vbe] offset:41216\n"
"v_mfma_f32_16x16x32_bf16 v[40:43], v[184:187], v[224:227], v[40:43]\n"
"ds_read_b64_tr_b16 v[172:173], v243 offset:33024\n"
"v_mfma_f32_16x16x32_bf16 v[72:75], v[184:187], v[228:231], v[72:75]\n"
"ds_read_b64_tr_b16 v[174:175], v243 offset:41216\n"
"v_mfma_f32_16x16x32_bf16 v[88:91], v[184:187], v[232:235], v[88:91]\n"
"ds_read_b64_tr_b16 v[176:177], %[vbe] offset:33280\n"
"v_mfma_f32_16x16x32_bf16 v[44:47], v[188:191], v[224:227], v[44:47]\n"
"ds_read_b64_tr_b16 v[178:179], %[vbe] offset:41472\n"
"v_mfma_f32_16x16x32_bf16 v[76:79], v[188:191], v[228:231], v[76:79]\n"
"ds_read_b64_tr_b16 v[180:181], v243 offset:33280\n"
"v_mfma_f32_16x16x32_bf16 v[92:95], v[188:191], v[232:235], v[92:95]\n"
"ds_read_b64_tr_b16 v[182:183], v243 offset:41472\n"
"s_waitcnt lgkmcnt(12)\n"
"v_mfma_f32_16x16x32_bf16 v[192:195], v[128:131], v[96:99], v[212:215]\n"
"ds_read_b64_tr_b16 v[184:185], %[vbe] offset:33536\n"
"v_mfma_f32_16x16x32_bf16 v[192:195], v[132:135], v[100:103], v[192:195]\n"
"ds_read_b64_tr_b16 v[186:187], %[vbe] offset:41728\n"
"v_mfma_f32_16x16x32_bf16 v[192:195], v[136:139], v[104:107], v[192:195]\n"
"ds_read_b64_tr_b16 v[188:189], v243 offset:33536\n"
"v_mfma_f32_16x16x32_bf16 v[192:195], v[140:143], v[108:111], v[192:195]\n"
"ds_read_b64_tr_b16 v[190:191], v243 offset:41728\n"
"v_mfma_f32_16x16x32_bf16 v[196:199], v[128:131], v[112:115], v[216:219]\n"
"v_mfma_f32_16x16x32_bf16 v[196:199], v[132:135], v[116:119], v[196:199]\n"
"v_mfma_f32_16x16x32_bf16 v[196:199], v[136:139], v[120:123], v[196:199]\n"
"v_mfma_f32_16x16x32_bf16 v[196:199], v[140:143], v[124:127], v[196:199]\n"
"v_mfma_f32_16x16x32_bf16 v[200:203], v[144:147], v[96:99], v[212:215]\n"
"v_mfma_f32_16x16x32_bf16 v[200:203], v[148:151], v[100:103], v[200:203]\n"
"v_mfma_f32_16x16x32_bf16 v[200:203], v[152:155], v[104:107], v[200:203]\n"
"v_mfma_f32_16x16x32_bf16 v[200:203], v[156:159], v[108:111], v[200:203]\n"
"v_mfma_f32_16x16x32_bf16 v[204:207], v[144:147], v[112:115], v[216:219]\n"
"v_mfma_f32_16x16x32_bf16 v[204:207], v[148:151], v[116:119], v[204:207]\n"
"v_mfma_f32_16x16x32_bf16 v[204:207], v[152:155], v[120:123], v[204:207]\n"
"v_mfma_f32_16x16x32_bf16 v[204:207], v[156:159], v[124:127], v[204:207]\n"
"s_waitcnt lgkmcnt(0)\n"
"s_barrier\n"
"s_setprio 0\n"
"s_lshl_b32 s89, %[osoff], 2\n"
"s_add_u32 s89, s89, 108544\n"
"buffer_load_dwordx4 v249, s[76:79], s89 offen offset:2048 lds\n"
"buffer_load_dwordx4 v250, s[76:79], s89 offen offset:3072 lds\n"
"s_mov_b32 m0, s57\n"
"s_nop 0\n"
"buffer_load_dwordx4 v251, s[76:79], s89 offen offset:3072 lds\n"
"s_add_u32 m0, s90, 1024\n"
"s_nop 0\n"
"buffer_load_dwordx4 v252, s[76:79], s89 offen offset:2048 lds\n"
"v_exp_f32 v192, v192\n"
"v_exp_f32 v193, v193\n"
"v_exp_f32 v194, v194\n"
"v_exp_f32_e64 v195, v195\n"
"v_cvt_pk_bf16_f32 v224, v192, v193\n"
"v_cvt_pk_bf16_f32 v225, v194, v195\n"
"v_exp_f32 v196, v196\n"
"v_exp_f32 v197, v197\n"
"v_exp_f32 v198, v198\n"
"v_exp_f32 v199, v199\n"
"v_cvt_pk_bf16_f32 v228, v196, v197\n"
"v_cvt_pk_bf16_f32 v229, v198, v199\n"
"v_exp_f32 v200, v200\n"
"v_exp_f32 v201, v201\n"
"v_exp_f32 v202, v202\n"
"v_exp_f32 v203, v203\n"
"v_cvt_pk_bf16_f32 v226, v200, v201\n"
"v_cvt_pk_bf16_f32 v227, v202, v203\n"
"v_exp_f32 v204, v204\n"
"v_exp_f32 v205, v205\n"
"v_exp_f32 v206, v206\n"
"v_exp_f32 v207, v207\n"
"v_cvt_pk_bf16_f32 v230, v204, v205\n"
"v_cvt_pk_bf16_f32 v231, v206, v207\n"
"s_barrier\n"
"s_setprio 1\n"
"ds_read_b128 v[232:235], v247 offset:4096\n"
"v_mfma_f32_16x16x32_bf16 v[16:19], v[160:163], v[224:227], v[16:19]\n"
"ds_read_b128 v[128:131], v244 offset:34816\n"
"v_add_f32 v240, v240, v192\n"
"v_add_f32 v241, v241, v196\n"
"v_mfma_f32_16x16x32_bf16 v[48:51], v[160:163], v[228:231], v[48:51]\n"
"ds_read_b128 v[132:135], v244 offset:35072\n"
"v_add_f32 v240, v240, v193\n"
"v_add_f32 v241, v241, v197\n"
"v_mfma_f32_16x16x32_bf16 v[20:23], v[164:167], v[224:227], v[20:23]\n"
"ds_read_b128 v[136:139], v244 offset:35328\n"
"v_add_f32 v240, v240, v194\n"
"v_add_f32 v241, v241, v198\n"
"v_mfma_f32_16x16x32_bf16 v[52:55], v[164:167], v[228:231], v[52:55]\n"
"ds_read_b128 v[140:143], v244 offset:35584\n"
"v_add_f32 v240, v240, v195\n"
"v_add_f32 v241, v241, v199\n"
"v_mfma_f32_16x16x32_bf16 v[24:27], v[168:171], v[224:227], v[24:27]\n"
"ds_read_b128 v[144:147], v244 offset:43008\n"
"v_add_f32 v240, v240, v200\n"
"v_add_f32 v241, v241, v204\n"
"v_mfma_f32_16x16x32_bf16 v[56:59], v[168:171], v[228:231], v[56:59]\n"
"ds_read_b128 v[148:151], v244 offset:43264\n"
"v_add_f32 v240, v240, v201\n"
"v_add_f32 v241, v241, v205\n"
"v_mfma_f32_16x16x32_bf16 v[28:31], v[172:175], v[224:227], v[28:31]\n"
"ds_read_b128 v[152:155], v244 offset:43520\n"
"v_add_f32 v240, v240, v202\n"
"v_add_f32 v241, v241, v206\n"
"v_mfma_f32_16x16x32_bf16 v[60:63], v[172:175], v[228:231], v[60:63]\n"
"ds_read_b128 v[156:159], v244 offset:43776\n"
"v_add_f32 v240, v240, v203\n"
"v_add_f32 v241, v241, v207\n"
"v_mfma_f32_16x16x32_bf16 v[32:35], v[176:179], v[224:227], v[32:35]\n"
"ds_read_b64_tr_b16 v[160:161], v245 offset:32768\n"
"v_mfma_f32_16x16x32_bf16 v[64:67], v[176:179], v[228:231], v[64:67]\n"
"ds_read_b64_tr_b16 v[162:163], v245 offset:40960\n"
"s_waitcnt lgkmcnt(10)\n"
"v_mfma_f32_16x16x32_bf16 v[80:83], v[176:179], v[232:235], v[80:83]\n"
"ds_read_b64_tr_b16 v[164:165], v246 offset:32768\n"
"v_mfma_f32_16x16x32_bf16 v[36:39], v[180:183], v[224:227], v[36:39]\n"
"ds_read_b64_tr_b16 v[166:167], v246 offset:40960\n"
"v_mfma_f32_16x16x32_bf16 v[68:71], v[180:183], v[228:231], v[68:71]\n"
"ds_read_b64_tr_b16 v[168:169], v245 offset:33024\n"
"v_mfma_f32_16x16x32_bf16 v[84:87], v[180:183], v[232:235], v[84:87]\n"
"ds_read_b64_tr_b16 v[170:171], v245 offset:41216\n"
"v_mfma_f32_16x16x32_bf16 v[40:43], v[184:187], v[224:227], v[40:43]\n"
"ds_read_b64_tr_b16 v[172:173], v246 offset:33024\n"
"v_mfma_f32_16x16x32_bf16 v[72:75], v[184:187], v[228:231], v[72:75]\n"
"ds_read_b64_tr_b16 v[174:175], v246 offset:41216\n"
"v_mfma_f32_16x16x32_bf16 v[88:91], v[184:187], v[232:235], v[88:91]\n"
"ds_read_b64_tr_b16 v[176:177], v245 offset:33280\n"
"v_mfma_f32_16x16x32_bf16 v[44:47], v[188:191], v[224:227], v[44:47]\n"
"ds_read_b64_tr_b16 v[178:179], v245 offset:41472\n"
"v_mfma_f32_16x16x32_bf16 v[76:79], v[188:191], v[228:231], v[76:79]\n"
"ds_read_b64_tr_b16 v[180:181], v246 offset:33280\n"
"v_mfma_f32_16x16x32_bf16 v[92:95], v[188:191], v[232:235], v[92:95]\n"
"ds_read_b64_tr_b16 v[182:183], v246 offset:41472\n"
"s_waitcnt lgkmcnt(12)\n"
"v_mfma_f32_16x16x32_bf16 v[192:195], v[128:131], v[96:99], v[212:215]\n"
"ds_read_b64_tr_b16 v[184:185], v245 offset:33536\n"
"v_mfma_f32_16x16x32_bf16 v[192:195], v[132:135], v[100:103], v[192:195]\n"
"ds_read_b64_tr_b16 v[186:187], v245 offset:41728\n"
"v_mfma_f32_16x16x32_bf16 v[192:195], v[136:139], v[104:107], v[192:195]\n"
"ds_read_b64_tr_b16 v[188:189], v246 offset:33536\n"
"v_mfma_f32_16x16x32_bf16 v[192:195], v[140:143], v[108:111], v[192:195]\n"
"ds_read_b64_tr_b16 v[190:191], v246 offset:41728\n"
"v_mfma_f32_16x16x32_bf16 v[196:199], v[128:131], v[112:115], v[216:219]\n"
"v_mfma_f32_16x16x32_bf16 v[196:199], v[132:135], v[116:119], v[196:199]\n"
"v_mfma_f32_16x16x32_bf16 v[196:199], v[136:139], v[120:123], v[196:199]\n"
"v_mfma_f32_16x16x32_bf16 v[196:199], v[140:143], v[124:127], v[196:199]\n"
"v_mfma_f32_16x16x32_bf16 v[200:203], v[144:147], v[96:99], v[212:215]\n"
"v_mfma_f32_16x16x32_bf16 v[200:203], v[148:151], v[100:103], v[200:203]\n"
"v_mfma_f32_16x16x32_bf16 v[200:203], v[152:155], v[104:107], v[200:203]\n"
"v_mfma_f32_16x16x32_bf16 v[200:203], v[156:159], v[108:111], v[200:203]\n"
"v_mfma_f32_16x16x32_bf16 v[204:207], v[144:147], v[112:115], v[216:219]\n"
"v_mfma_f32_16x16x32_bf16 v[204:207], v[148:151], v[116:119], v[204:207]\n"
"v_mfma_f32_16x16x32_bf16 v[204:207], v[152:155], v[120:123], v[204:207]\n"
"v_mfma_f32_16x16x32_bf16 v[204:207], v[156:159], v[124:127], v[204:207]\n"
"s_waitcnt lgkmcnt(0)\n"
"s_barrier\n"
"s_setprio 0\n"
"s_getpc_b64 s[94:95]\n"
"LMa3_%=:\n"
"s_add_u32 s94, s94, (LZZ3_%=-16384-LMa3_%=)&0xffffffff\n"
"s_addc_u32 s95, s95, (LZZ3_%=-16384-LMa3_%=)>>32\n"
"v_lshlrev_b32 v236, 4, %[l16]\n"
"s_lshr_b32 s68, %[hb], 9\n"
"v_add_u32 v236, s68, v236\n"
"s_lshl_b32 exec_lo, 0x11111111, %[wrot]\n"
"s_mov_b32 exec_hi, exec_lo\n"
"global_load_dword v253, v236, s[94:95]\n"
"s_mov_b64 exec, -1\n"
"s_lshl_b32 s88, %[os1], 2\n"
"s_mov_b32 m0, s54\n"
"s_nop 0\n"
"buffer_load_dwordx4 v249, s[76:79], s88 offen lds\n"
"buffer_load_dwordx4 v250, s[76:79], s88 offen offset:1024 lds\n"
"v_exp_f32 v192, v192\n"
"v_exp_f32 v193, v193\n"
"v_exp_f32 v194, v194\n"
"v_exp_f32 v195, v195\n"
"v_cvt_pk_bf16_f32 v224, v192, v193\n"
"v_cvt_pk_bf16_f32 v225, v194, v195\n"
"v_exp_f32 v196, v196\n"
"v_exp_f32 v197, v197\n"
"v_exp_f32 v198, v198\n"
"v_exp_f32 v199, v199\n"
"v_cvt_pk_bf16_f32 v228, v196, v197\n"
"v_cvt_pk_bf16_f32 v229, v198, v199\n"
"v_exp_f32 v200, v200\n"
"v_exp_f32 v201, v201\n"
"v_exp_f32 v202, v202\n"
"v_exp_f32 v203, v203\n"
"v_cvt_pk_bf16_f32 v226, v200, v201\n"
"v_cvt_pk_bf16_f32 v227, v202, v203\n"
"v_exp_f32 v204, v204\n"
"v_exp_f32 v205, v205\n"
"v_exp_f32 v206, v206\n"
"v_exp_f32 v207, v207\n"
"v_cvt_pk_bf16_f32 v230, v204, v205\n"
"v_cvt_pk_bf16_f32 v231, v206, v207\n"
"s_waitcnt vmcnt(13)\n"
"s_barrier\n"
"s_setprio 1\n"
"ds_read_b128 v[232:235], v247 offset:4096\n"
"v_mfma_f32_16x16x32_bf16 v[16:19], v[160:163], v[224:227], v[16:19]\n"
"ds_read_b128 v[128:131], %[kb] offset:51200\n"
"v_add_f32 v240, v240, v192\n"
"v_add_f32_e64 v241, v241, v196\n"
"v_mfma_f32_16x16x32_bf16 v[48:51], v[160:163], v[228:231], v[48:51]\n"
"ds_read_b128 v[132:135], %[kb] offset:51456\n"
"v_add_f32 v240, v240, v193\n"
"v_add_f32 v241, v241, v197\n"
"v_mfma_f32_16x16x32_bf16 v[20:23], v[164:167], v[224:227], v[20:23]\n"
"ds_read_b128 v[136:139], %[kb] offset:51712\n"
"v_add_f32 v240, v240, v194\n"
"v_add_f32 v241, v241, v198\n"
"v_mfma_f32_16x16x32_bf16 v[52:55], v[164:167], v[228:231], v[52:55]\n"
"ds_read_b128 v[140:143], %[kb] offset:51968\n"
"v_add_f32 v240, v240, v195\n"
"v_add_f32 v241, v241, v199\n"
"v_mfma_f32_16x16x32_bf16 v[24:27], v[168:171], v[224:227], v[24:27]\n"
"ds_read_b128 v[144:147], %[kb] offset:59392\n"
"v_add_f32 v240, v240, v200\n"
"v_add_f32 v241, v241, v204\n"
"v_mfma_f32_16x16x32_bf16 v[56:59], v[168:171], v[228:231], v[56:59]\n"
"ds_read_b128 v[148:151], %[kb] offset:59648\n"
"v_add_f32 v240, v240, v201\n"
"v_add_f32 v241, v241, v205\n"
"v_mfma_f32_16x16x32_bf16 v[28:31], v[172:175], v[224:227], v[28:31]\n"
"ds_read_b128 v[152:155], %[kb] offset:59904\n"
"v_add_f32 v240, v240, v202\n"
"v_add_f32 v241, v241, v206\n"
"v_mfma_f32_16x16x32_bf16 v[60:63], v[172:175], v[228:231], v[60:63]\n"
"ds_read_b128 v[156:159], %[kb] offset:60160\n"
"v_add_f32 v240, v240, v203\n"
"v_add_f32 v241, v241, v207\n"
"v_mfma_f32_16x16x32_bf16 v[32:35], v[176:179], v[224:227], v[32:35]\n"
"ds_read_b64_tr_b16 v[160:161], %[vbe] offset:49152\n"
"v_mfma_f32_16x16x32_bf16 v[64:67], v[176:179], v[228:231], v[64:67]\n"
"ds_read_b64_tr_b16 v[162:163], %[vbe] offset:57344\n"
"s_waitcnt lgkmcnt(10)\n"
"v_mfma_f32_16x16x32_bf16 v[80:83], v[176:179], v[232:235], v[80:83]\n"
"ds_read_b64_tr_b16 v[164:165], v243 offset:49152\n"
"v_mfma_f32_16x16x32_bf16 v[36:39], v[180:183], v[224:227], v[36:39]\n"
"ds_read_b64_tr_b16 v[166:167], v243 offset:57344\n"
"v_mfma_f32_16x16x32_bf16 v[68:71], v[180:183], v[228:231], v[68:71]\n"
"ds_read_b64_tr_b16 v[168:169], %[vbe] offset:49408\n"
"v_mfma_f32_16x16x32_bf16 v[84:87], v[180:183], v[232:235], v[84:87]\n"
"ds_read_b64_tr_b16 v[170:171], %[vbe] offset:57600\n"
"v_mfma_f32_16x16x32_bf16 v[40:43], v[184:187], v[224:227], v[40:43]\n"
"ds_read_b64_tr_b16 v[172:173], v243 offset:49408\n"
"v_mfma_f32_16x16x32_bf16 v[72:75], v[184:187], v[228:231], v[72:75]\n"
"ds_read_b64_tr_b16 v[174:175], v243 offset:57600\n"
"v_mfma_f32_16x16x32_bf16 v[88:91], v[184:187], v[232:235], v[88:91]\n"
"ds_read_b64_tr_b16 v[176:177], %[vbe] offset:49664\n"
"v_mfma_f32_16x16x32_bf16 v[44:47], v[188:191], v[224:227], v[44:47]\n"
"ds_read_b64_tr_b16 v[178:179], %[vbe] offset:57856\n"
"v_mfma_f32_16x16x32_bf16 v[76:79], v[188:191], v[228:231], v[76:79]\n"
"ds_read_b64_tr_b16 v[180:181], v243 offset:49664\n"
"v_mfma_f32_16x16x32_bf16 v[92:95], v[188:191], v[232:235], v[92:95]\n"
"ds_read_b64_tr_b16 v[182:183], v243 offset:57856\n"
"s_waitcnt lgkmcnt(12)\n"
"v_mfma_f32_16x16x32_bf16 v[192:195], v[128:131], v[96:99], v[212:215]\n"
"ds_read_b64_tr_b16 v[184:185], %[vbe] offset:49920\n"
"v_mfma_f32_16x16x32_bf16 v[192:195], v[132:135], v[100:103], v[192:195]\n"
"ds_read_b64_tr_b16 v[186:187], %[vbe] offset:58112\n"
"v_mfma_f32_16x16x32_bf16 v[192:195], v[136:139], v[104:107], v[192:195]\n"
"ds_read_b64_tr_b16 v[188:189], v243 offset:49920\n"
"v_mfma_f32_16x16x32_bf16 v[192:195], v[140:143], v[108:111], v[192:195]\n"
"ds_read_b64_tr_b16 v[190:191], v243 offset:58112\n"
"v_mfma_f32_16x16x32_bf16 v[196:199], v[128:131], v[112:115], v[216:219]\n"
"v_mfma_f32_16x16x32_bf16 v[196:199], v[132:135], v[116:119], v[196:199]\n"
"v_mfma_f32_16x16x32_bf16 v[196:199], v[136:139], v[120:123], v[196:199]\n"
"v_mfma_f32_16x16x32_bf16 v[196:199], v[140:143], v[124:127], v[196:199]\n"
"v_mfma_f32_16x16x32_bf16 v[200:203], v[144:147], v[96:99], v[212:215]\n"
"v_mfma_f32_16x16x32_bf16 v[200:203], v[148:151], v[100:103], v[200:203]\n"
"v_mfma_f32_16x16x32_bf16 v[200:203], v[152:155], v[104:107], v[200:203]\n"
"v_mfma_f32_16x16x32_bf16 v[200:203], v[156:159], v[108:111], v[200:203]\n"
"v_mfma_f32_16x16x32_bf16 v[204:207], v[144:147], v[112:115], v[216:219]\n"
"v_mfma_f32_16x16x32_bf16 v[204:207], v[148:151], v[116:119], v[204:207]\n"
"v_mfma_f32_16x16x32_bf16 v[204:207], v[152:155], v[120:123], v[204:207]\n"
"v_mfma_f32_16x16x32_bf16 v[204:207], v[156:159], v[124:127], v[204:207]\n"
"s_waitcnt lgkmcnt(0)\n"
"s_barrier\n"
"s_setprio 0\n"
"s_lshl_b32 s89, %[os1], 2\n"
"s_add_u32 s89, s89, 34816\n"
"buffer_load_dwordx4 v249, s[76:79], s89 offen offset:2048 lds\n"
"buffer_load_dwordx4 v250, s[76:79], s89 offen offset:3072 lds\n"
"s_mov_b32 m0, s58\n"
"s_nop 0\n"
"buffer_load_dwordx4 v251, s[76:79], s89 offen offset:3072 lds\n"
"v_exp_f32 v192, v192\n"
"v_exp_f32 v193, v193\n"
"v_exp_f32 v194, v194\n"
"v_exp_f32 v195, v195\n"
"v_cvt_pk_bf16_f32 v224, v192, v193\n"
"v_cvt_pk_bf16_f32 v225, v194, v195\n"
"v_exp_f32 v196, v196\n"
"v_exp_f32 v197, v197\n"
"v_exp_f32 v198, v198\n"
"v_exp_f32 v199, v199\n"
"v_cvt_pk_bf16_f32 v228, v196, v197\n"
"v_cvt_pk_bf16_f32 v229, v198, v199\n"
"v_exp_f32 v200, v200\n"
"v_exp_f32 v201, v201\n"
"v_exp_f32 v202, v202\n"
"v_exp_f32 v203, v203\n"
"v_cvt_pk_bf16_f32 v226, v200, v201\n"
"v_cvt_pk_bf16_f32 v227, v202, v203\n"
"v_exp_f32 v204, v204\n"
"v_exp_f32 v205, v205\n"
"v_exp_f32 v206, v206\n"
"v_exp_f32 v207, v207\n"
"v_cvt_pk_bf16_f32 v230, v204, v205\n"
"v_cvt_pk_bf16_f32 v231, v206, v207\n"
"s_barrier\n"
"s_setprio 1\n"
"ds_read_b128 v[232:235], v247 offset:4096\n"
"v_mfma_f32_16x16x32_bf16 v[16:19], v[160:163], v[224:227], v[16:19]\n"
"ds_read_b128 v[128:131], v244 offset:51200\n"
"v_add_f32 v240, v240, v192\n"
"v_add_f32 v241, v241, v196\n"
"v_mfma_f32_16x16x32_bf16 v[48:51], v[160:163], v[228:231], v[48:51]\n"
"ds_read_b128 v[132:135], v244 offset:51456\n"
"v_add_f32 v240, v240, v193\n"
"v_add_f32 v241, v241, v197\n"
"v_mfma_f32_16x16x32_bf16 v[20:23], v[164:167], v[224:227], v[20:23]\n"
"ds_read_b128 v[136:139], v244 offset:51712\n"
"v_add_f32 v240, v240, v194\n"
"v_add_f32 v241, v241, v198\n"
"v_mfma_f32_16x16x32_bf16 v[52:55], v[164:167], v[228:231], v[52:55]\n"
"ds_read_b128 v[140:143], v244 offset:51968\n"
"v_add_f32 v240, v240, v195\n"
"v_add_f32 v241, v241, v199\n"
"v_mfma_f32_16x16x32_bf16 v[24:27], v[168:171], v[224:227], v[24:27]\n"
"ds_read_b128 v[144:147], v244 offset:59392\n"
"v_add_f32 v240, v240, v200\n"
"v_add_f32 v241, v241, v204\n"
"v_mfma_f32_16x16x32_bf16 v[56:59], v[168:171], v[228:231], v[56:59]\n"
"ds_read_b128 v[148:151], v244 offset:59648\n"
"v_add_f32 v240, v240, v201\n"
"v_add_f32 v241, v241, v205\n"
"v_mfma_f32_16x16x32_bf16 v[28:31], v[172:175], v[224:227], v[28:31]\n"
"ds_read_b128 v[152:155], v244 offset:59904\n"
"v_add_f32 v240, v240, v202\n"
"v_add_f32 v241, v241, v206\n"
"v_mfma_f32_16x16x32_bf16 v[60:63], v[172:175], v[228:231], v[60:63]\n"
"ds_read_b128 v[156:159], v244 offset:60160\n"
"v_add_f32 v240, v240, v203\n"
"v_add_f32 v241, v241, v207\n"
"v_mfma_f32_16x16x32_bf16 v[32:35], v[176:179], v[224:227], v[32:35]\n"
"ds_read_b64_tr_b16 v[160:161], v245 offset:49152\n"
"v_mfma_f32_16x16x32_bf16 v[64:67], v[176:179], v[228:231], v[64:67]\n"
"ds_read_b64_tr_b16 v[162:163], v245 offset:57344\n"
"s_waitcnt lgkmcnt(10)\n"
"v_mfma_f32_16x16x32_bf16 v[80:83], v[176:179], v[232:235], v[80:83]\n"
"ds_read_b64_tr_b16 v[164:165], v246 offset:49152\n"
"v_mfma_f32_16x16x32_bf16 v[36:39], v[180:183], v[224:227], v[36:39]\n"
"ds_read_b64_tr_b16 v[166:167], v246 offset:57344\n"
"v_mfma_f32_16x16x32_bf16 v[68:71], v[180:183], v[228:231], v[68:71]\n"
"ds_read_b64_tr_b16 v[168:169], v245 offset:49408\n"
"v_mfma_f32_16x16x32_bf16 v[84:87], v[180:183], v[232:235], v[84:87]\n"
"ds_read_b64_tr_b16 v[170:171], v245 offset:57600\n"
"v_mfma_f32_16x16x32_bf16 v[40:43], v[184:187], v[224:227], v[40:43]\n"
"ds_read_b64_tr_b16 v[172:173], v246 offset:49408\n"
"v_mfma_f32_16x16x32_bf16 v[72:75], v[184:187], v[228:231], v[72:75]\n"
"ds_read_b64_tr_b16 v[174:175], v246 offset:57600\n"
"v_mfma_f32_16x16x32_bf16 v[88:91], v[184:187], v[232:235], v[88:91]\n"
"ds_read_b64_tr_b16 v[176:177], v245 offset:49664\n"
"v_mfma_f32_16x16x32_bf16 v[44:47], v[188:191], v[224:227], v[44:47]\n"
"ds_read_b64_tr_b16 v[178:179], v245 offset:57856\n"
"v_mfma_f32_16x16x32_bf16 v[76:79], v[188:191], v[228:231], v[76:79]\n"
"ds_read_b64_tr_b16 v[180:181], v246 offset:49664\n"
"v_mfma_f32_16x16x32_bf16 v[92:95], v[188:191], v[232:235], v[92:95]\n"
"ds_read_b64_tr_b16 v[182:183], v246 offset:57856\n"
"s_waitcnt lgkmcnt(12)\n"
"v_mfma_f32_16x16x32_bf16 v[192:195], v[128:131], v[96:99], v[212:215]\n"
"ds_read_b64_tr_b16 v[184:185], v245 offset:49920\n"
"v_mfma_f32_16x16x32_bf16 v[192:195], v[132:135], v[100:103], v[192:195]\n"
"ds_read_b64_tr_b16 v[186:187], v245 offset:58112\n"
"v_mfma_f32_16x16x32_bf16 v[192:195], v[136:139], v[104:107], v[192:195]\n"
"ds_read_b64_tr_b16 v[188:189], v246 offset:49920\n"
"v_mfma_f32_16x16x32_bf16 v[192:195], v[140:143], v[108:111], v[192:195]\n"
"ds_read_b64_tr_b16 v[190:191], v246 offset:58112\n"
"v_mfma_f32_16x16x32_bf16 v[196:199], v[128:131], v[112:115], v[216:219]\n"
"v_mfma_f32_16x16x32_bf16 v[196:199], v[132:135], v[116:119], v[196:199]\n"
"v_mfma_f32_16x16x32_bf16 v[196:199], v[136:139], v[120:123], v[196:199]\n"
"v_mfma_f32_16x16x32_bf16 v[196:199], v[140:143], v[124:127], v[196:199]\n"
"v_mfma_f32_16x16x32_bf16 v[200:203], v[144:147], v[96:99], v[212:215]\n"
"v_mfma_f32_16x16x32_bf16 v[200:203], v[148:151], v[100:103], v[200:203]\n"
"v_mfma_f32_16x16x32_bf16 v[200:203], v[152:155], v[104:107], v[200:203]\n"
"v_mfma_f32_16x16x32_bf16 v[200:203], v[156:159], v[108:111], v[200:203]\n"
"v_mfma_f32_16x16x32_bf16 v[204:207], v[144:147], v[112:115], v[216:219]\n"
"v_mfma_f32_16x16x32_bf16 v[204:207], v[148:151], v[116:119], v[204:207]\n"
"v_mfma_f32_16x16x32_bf16 v[204:207], v[152:155], v[120:123], v[204:207]\n"
"v_mfma_f32_16x16x32_bf16 v[204:207], v[156:159], v[124:127], v[204:207]\n"
"s_waitcnt lgkmcnt(0)\n"
"s_barrier\n"
"s_setprio 0\n"
"s_nop 0\n"
"LT3_%=:\n"
"s_lshl_b32 s88, %[os1], 2\n"
"s_add_u32 s88, s88, 73728\n"
"s_mov_b32 m0, s55\n"
"s_nop 0\n"
"buffer_load_dwordx4 v249, s[76:79], s88 offen lds\n"
"buffer_load_dwordx4 v250, s[76:79], s88 offen offset:1024 lds\n"
"s_lshl_b32 s89, %[os1], 2\n"
"s_add_u32 s89, s89, 108544\n"
"buffer_load_dwordx4 v249, s[76:79], s89 offen offset:2048 lds\n"
"buffer_load_dwordx4 v250, s[76:79], s89 offen offset:3072 lds\n"
"s_mov_b32 m0, s59\n"
"s_nop 0\n"
"buffer_load_dwordx4 v251, s[76:79], s89 offen offset:3072 lds\n"
"s_add_u32 m0, s90, 5120\n"
"s_nop 0\n"
"buffer_load_dwordx4 v252, s[76:79], s89 offen offset:2048 lds\n"
"v_exp_f32 v192, v192\n"
"v_exp_f32 v193, v193\n"
"v_exp_f32 v194, v194\n"
"v_exp_f32_e64 v195, v195\n"
"v_cvt_pk_bf16_f32 v224, v192, v193\n"
"v_cvt_pk_bf16_f32 v225, v194, v195\n"
"v_exp_f32 v196, v196\n"
"v_exp_f32 v197, v197\n"
"v_exp_f32 v198, v198\n"
"v_exp_f32 v199, v199\n"
"v_cvt_pk_bf16_f32 v228, v196, v197\n"
"v_cvt_pk_bf16_f32 v229, v198, v199\n"
"v_exp_f32 v200, v200\n"
"v_exp_f32 v201, v201\n"
"v_exp_f32 v202, v202\n"
"v_exp_f32 v203, v203\n"
"v_cvt_pk_bf16_f32 v226, v200, v201\n"
"v_cvt_pk_bf16_f32 v227, v202, v203\n"
"v_exp_f32 v204, v204\n"
"v_exp_f32 v205, v205\n"
"v_exp_f32 v206, v206\n"
"v_exp_f32 v207, v207\n"
"v_cvt_pk_bf16_f32 v230, v204, v205\n"
"v_cvt_pk_bf16_f32 v231, v206, v207\n"
"s_waitcnt vmcnt(11)\n"
"s_barrier\n"
"s_setprio 1\n"
"ds_read_b128 v[232:235], v247 offset:4096\n"
"v_mfma_f32_16x16x32_bf16 v[16:19], v[160:163], v[224:227], v[16:19]\n"
"ds_read_b64 v[96:97], v247 offset:5120\n"
"v_add_f32 v240, v240, v192\n"
"v_add_f32_e64 v241, v241, v196\n"
"v_mfma_f32_16x16x32_bf16 v[48:51], v[160:163], v[228:231], v[48:51]\n"
"v_add_f32 v240, v240, v193\n"
"v_add_f32 v241, v241, v197\n"
"v_mfma_f32_16x16x32_bf16 v[20:23], v[164:167], v[224:227], v[20:23]\n"
"v_add_f32 v240, v240, v194\n"
"v_add_f32 v241, v241, v198\n"
"v_mfma_f32_16x16x32_bf16 v[52:55], v[164:167], v[228:231], v[52:55]\n"
"v_add_f32 v240, v240, v195\n"
"v_add_f32 v241, v241, v199\n"
"v_mfma_f32_16x16x32_bf16 v[24:27], v[168:171], v[224:227], v[24:27]\n"
"v_add_f32 v240, v240, v200\n"
"v_add_f32 v241, v241, v204\n"
"v_mfma_f32_16x16x32_bf16 v[56:59], v[168:171], v[228:231], v[56:59]\n"
"v_add_f32 v240, v240, v201\n"
"v_add_f32 v241, v241, v205\n"
"v_mfma_f32_16x16x32_bf16 v[28:31], v[172:175], v[224:227], v[28:31]\n"
"v_add_f32 v240, v240, v202\n"
"v_add_f32 v241, v241, v206\n"
"v_mfma_f32_16x16x32_bf16 v[60:63], v[172:175], v[228:231], v[60:63]\n"
"v_add_f32 v240, v240, v203\n"
"v_add_f32 v241, v241, v207\n"
"v_mfma_f32_16x16x32_bf16 v[32:35], v[176:179], v[224:227], v[32:35]\n"
"v_mfma_f32_16x16x32_bf16 v[64:67], v[176:179], v[228:231], v[64:67]\n"
"s_waitcnt lgkmcnt(1)\n"
"v_mfma_f32_16x16x32_bf16 v[80:83], v[176:179], v[232:235], v[80:83]\n"
"v_mfma_f32_16x16x32_bf16 v[36:39], v[180:183], v[224:227], v[36:39]\n"
"v_mfma_f32_16x16x32_bf16 v[68:71], v[180:183], v[228:231], v[68:71]\n"
"v_mfma_f32_16x16x32_bf16 v[84:87], v[180:183], v[232:235], v[84:87]\n"
"v_mfma_f32_16x16x32_bf16 v[40:43], v[184:187], v[224:227], v[40:43]\n"
"v_mfma_f32_16x16x32_bf16 v[72:75], v[184:187], v[228:231], v[72:75]\n"
"v_mfma_f32_16x16x32_bf16 v[88:91], v[184:187], v[232:235], v[88:91]\n"
"v_mfma_f32_16x16x32_bf16 v[44:47], v[188:191], v[224:227], v[44:47]\n"
"v_mfma_f32_16x16x32_bf16 v[76:79], v[188:191], v[228:231], v[76:79]\n"
"v_mfma_f32_16x16x32_bf16 v[92:95], v[188:191], v[232:235], v[92:95]\n"
"s_setprio 0\n"
"s_cmp_lg_u32 %[hb], 0\n"
"s_cbranch_scc1 LG3_%=\n"
"s_barrier\n"
"LG3_%=:\n"
"v_mov_b32 v206, v240\n"
"v_mov_b32 v207, v241\n"
"s_nop 0\n"
"v_permlane32_swap_b32 v240, v206\n"
"v_permlane32_swap_b32 v241, v207\n"
"v_add_f32 v240, v240, v206\n"
"v_add_f32 v241, v241, v207\n"
"v_mov_b32 v206, v240\n"
"v_mov_b32 v207, v241\n"
"s_nop 0\n"
"v_permlane16_swap_b32 v240, v206\n"
"v_permlane16_swap_b32 v241, v207\n"
"v_add_f32 v240, v240, v206\n"
"v_add_f32 v241, v241, v207\n"
"v_rcp_f32 v192, v240\n"
"v_rcp_f32 v194, v241\n"
"s_waitcnt lgkmcnt(0)\n"
"v_add_f32 v96, v96, v97\n"
"v_rcp_f32 v196, v96\n"
"s_lshr_b32 s68, %[hb], 11\n"
"s_or_b32 s68, s68, %[hb]\n"
"v_and_b32 v244, 0xffff, %[vbe]\n"
"v_xor_b32 v244, s68, v244\n"
"v_xor_b32 v245, 32, v244\n"
"v_lshrrev_b32 v246, 1, %[l16]\n"
"v_add_u32 v246, s90, v246\n"
"v_add_u32 v228, 2048, %[l16]\n"
"s_add_u32 s60, %[osoff], 6144\n"
"s_add_u32 s61, %[osoff], 15360\n"
"s_add_u32 s62, %[osoff], 24576\n"
"s_add_u32 s63, %[osoff], 33792\n"
"v_pk_mul_f32 v[80:81], v[80:81], v[196:197] op_sel_hi:[1,0]\n"
"v_pk_mul_f32 v[82:83], v[82:83], v[196:197] op_sel_hi:[1,0]\n"
"v_pk_mul_f32 v[84:85], v[84:85], v[196:197] op_sel_hi:[1,0]\n"
"v_pk_mul_f32 v[86:87], v[86:87], v[196:197] op_sel_hi:[1,0]\n"
"v_cvt_pk_bf16_f32 v168, v80, v81\n"
"v_cvt_pk_bf16_f32 v169, v82, v83\n"
"v_cvt_pk_bf16_f32 v170, v84, v85\n"
"v_cvt_pk_bf16_f32 v171, v86, v87\n"
"buffer_store_dwordx4 v[168:171], v228, %[srdo], s62 offen sc1\n"
"ds_write_b128 v247, v[168:171] offset:2048\n"
"v_pk_mul_f32 v[88:89], v[88:89], v[196:197] op_sel_hi:[1,0]\n"
"v_pk_mul_f32 v[90:91], v[90:91], v[196:197] op_sel_hi:[1,0]\n"
"v_pk_mul_f32 v[92:93], v[92:93], v[196:197] op_sel_hi:[1,0]\n"
"v_pk_mul_f32 v[94:95], v[94:95], v[196:197] op_sel_hi:[1,0]\n"
"v_cvt_pk_bf16_f32 v172, v88, v89\n"
"v_cvt_pk_bf16_f32 v173, v90, v91\n"
"v_cvt_pk_bf16_f32 v174, v92, v93\n"
"v_cvt_pk_bf16_f32 v175, v94, v95\n"
"buffer_store_dwordx4 v[172:175], v228, %[srdo], s63 offen sc1\n"
"ds_write_b128 v247, v[172:175] offset:3072\n"
"v_pk_mul_f32 v[16:17], v[16:17], v[192:193] op_sel_hi:[1,0]\n"
"v_pk_mul_f32 v[18:19], v[18:19], v[192:193] op_sel_hi:[1,0]\n"
"v_pk_mul_f32 v[20:21], v[20:21], v[192:193] op_sel_hi:[1,0]\n"
"v_pk_mul_f32 v[22:23], v[22:23], v[192:193] op_sel_hi:[1,0]\n"
"v_cvt_pk_bf16_f32 v128, v16, v17\n"
"v_cvt_pk_bf16_f32 v129, v18, v19\n"
"v_cvt_pk_bf16_f32 v130, v20, v21\n"
"v_cvt_pk_bf16_f32 v131, v22, v23\n"
"buffer_store_dwordx4 v[128:131], %[l16], %[srdo], s60 offen sc1\n"
"v_pk_mul_f32 v[24:25], v[24:25], v[192:193] op_sel_hi:[1,0]\n"
"v_pk_mul_f32 v[26:27], v[26:27], v[192:193] op_sel_hi:[1,0]\n"
"v_pk_mul_f32 v[28:29], v[28:29], v[192:193] op_sel_hi:[1,0]\n"
"v_pk_mul_f32 v[30:31], v[30:31], v[192:193] op_sel_hi:[1,0]\n"
"v_cvt_pk_bf16_f32 v132, v24, v25\n"
"v_cvt_pk_bf16_f32 v133, v26, v27\n"
"v_cvt_pk_bf16_f32 v134, v28, v29\n"
"v_cvt_pk_bf16_f32 v135, v30, v31\n"
"buffer_store_dwordx4 v[132:135], %[l16], %[srdo], s61 offen sc1\n"
"v_pk_mul_f32 v[32:33], v[32:33], v[192:193] op_sel_hi:[1,0]\n"
"v_pk_mul_f32 v[34:35], v[34:35], v[192:193] op_sel_hi:[1,0]\n"
"v_pk_mul_f32 v[36:37], v[36:37], v[192:193] op_sel_hi:[1,0]\n"
"v_pk_mul_f32 v[38:39], v[38:39], v[192:193] op_sel_hi:[1,0]\n"
"v_cvt_pk_bf16_f32 v136, v32, v33\n"
"v_cvt_pk_bf16_f32 v137, v34, v35\n"
"v_cvt_pk_bf16_f32 v138, v36, v37\n"
"v_cvt_pk_bf16_f32 v139, v38, v39\n"
"buffer_store_dwordx4 v[136:139], %[l16], %[srdo], s62 offen sc1\n"
"v_pk_mul_f32 v[40:41], v[40:41], v[192:193] op_sel_hi:[1,0]\n"
"v_pk_mul_f32 v[42:43], v[42:43], v[192:193] op_sel_hi:[1,0]\n"
"v_pk_mul_f32 v[44:45], v[44:45], v[192:193] op_sel_hi:[1,0]\n"
"v_pk_mul_f32 v[46:47], v[46:47], v[192:193] op_sel_hi:[1,0]\n"
"v_cvt_pk_bf16_f32 v140, v40, v41\n"
"v_cvt_pk_bf16_f32 v141, v42, v43\n"
"v_cvt_pk_bf16_f32 v142, v44, v45\n"
"v_cvt_pk_bf16_f32 v143, v46, v47\n"
"buffer_store_dwordx4 v[140:143], %[l16], %[srdo], s63 offen sc1\n"
"v_pk_mul_f32 v[48:49], v[48:49], v[194:195] op_sel_hi:[1,0]\n"
"v_pk_mul_f32 v[50:51], v[50:51], v[194:195] op_sel_hi:[1,0]\n"
"v_pk_mul_f32 v[52:53], v[52:53], v[194:195] op_sel_hi:[1,0]\n"
"v_pk_mul_f32 v[54:55], v[54:55], v[194:195] op_sel_hi:[1,0]\n"
"v_cvt_pk_bf16_f32 v144, v48, v49\n"
"v_cvt_pk_bf16_f32 v145, v50, v51\n"
"v_cvt_pk_bf16_f32 v146, v52, v53\n"
"v_cvt_pk_bf16_f32 v147, v54, v55\n"
"buffer_store_dwordx4 v[144:147], %[l16], %[srdo], s60 offen offset:1024 sc1\n"
"v_pk_mul_f32 v[56:57], v[56:57], v[194:195] op_sel_hi:[1,0]\n"
"v_pk_mul_f32 v[58:59], v[58:59], v[194:195] op_sel_hi:[1,0]\n"
"v_pk_mul_f32 v[60:61], v[60:61], v[194:195] op_sel_hi:[1,0]\n"
"v_pk_mul_f32 v[62:63], v[62:63], v[194:195] op_sel_hi:[1,0]\n"
"v_cvt_pk_bf16_f32 v148, v56, v57\n"
"v_cvt_pk_bf16_f32 v149, v58, v59\n"
"v_cvt_pk_bf16_f32 v150, v60, v61\n"
"v_cvt_pk_bf16_f32 v151, v62, v63\n"
"buffer_store_dwordx4 v[148:151], %[l16], %[srdo], s61 offen offset:1024 sc1\n"
"v_pk_mul_f32 v[64:65], v[64:65], v[194:195] op_sel_hi:[1,0]\n"
"v_pk_mul_f32 v[66:67], v[66:67], v[194:195] op_sel_hi:[1,0]\n"
"v_pk_mul_f32 v[68:69], v[68:69], v[194:195] op_sel_hi:[1,0]\n"
"v_pk_mul_f32 v[70:71], v[70:71], v[194:195] op_sel_hi:[1,0]\n"
"v_cvt_pk_bf16_f32 v152, v64, v65\n"
"v_cvt_pk_bf16_f32 v153, v66, v67\n"
"v_cvt_pk_bf16_f32 v154, v68, v69\n"
"v_cvt_pk_bf16_f32 v155, v70, v71\n"
"buffer_store_dwordx4 v[152:155], %[l16], %[srdo], s62 offen offset:1024 sc1\n"
"v_pk_mul_f32 v[72:73], v[72:73], v[194:195] op_sel_hi:[1,0]\n"
"v_pk_mul_f32 v[74:75], v[74:75], v[194:195] op_sel_hi:[1,0]\n"
"v_pk_mul_f32 v[76:77], v[76:77], v[194:195] op_sel_hi:[1,0]\n"
"v_pk_mul_f32 v[78:79], v[78:79], v[194:195] op_sel_hi:[1,0]\n"
"v_cvt_pk_bf16_f32 v156, v72, v73\n"
"v_cvt_pk_bf16_f32 v157, v74, v75\n"
"v_cvt_pk_bf16_f32 v158, v76, v77\n"
"v_cvt_pk_bf16_f32 v159, v78, v79\n"
"buffer_store_dwordx4 v[156:159], %[l16], %[srdo], s63 offen offset:1024 sc1\n"
"v_max_f32 v202, v240, v241\n"
"v_add_f32_e64 v200, v240, v241\n"
"v_mov_b32 v201, 0\n"
"v_pk_add_f32 v[200:201], v[200:201], v[16:17]\n"
"ds_read_b64_tr_b16 v[100:101], v244 offset:0\n"
"v_pk_add_f32 v[200:201], v[200:201], v[18:19]\n"
"v_pk_add_f32 v[200:201], v[200:201], v[20:21]\n"
"ds_read_b64_tr_b16 v[102:103], v244 offset:8192\n"
"v_pk_add_f32 v[200:201], v[200:201], v[22:23]\n"
"v_pk_add_f32 v[200:201], v[200:201], v[24:25]\n"
"ds_read_b64_tr_b16 v[104:105], v245 offset:0\n"
"v_pk_add_f32 v[200:201], v[200:201], v[26:27]\n"
"v_pk_add_f32 v[200:201], v[200:201], v[28:29]\n"
"ds_read_b64_tr_b16 v[106:107], v245 offset:8192\n"
"v_pk_add_f32 v[200:201], v[200:201], v[30:31]\n"
"v_pk_add_f32 v[200:201], v[200:201], v[32:33]\n"
"ds_read_b64_tr_b16 v[108:109], v244 offset:256\n"
"v_pk_add_f32 v[200:201], v[200:201], v[34:35]\n"
"v_pk_add_f32 v[200:201], v[200:201], v[36:37]\n"
"ds_read_b64_tr_b16 v[110:111], v244 offset:8448\n"
"v_pk_add_f32 v[200:201], v[200:201], v[38:39]\n"
"v_pk_add_f32 v[200:201], v[200:201], v[40:41]\n"
"ds_read_b64_tr_b16 v[112:113], v245 offset:256\n"
"v_pk_add_f32 v[200:201], v[200:201], v[42:43]\n"
"v_pk_add_f32 v[200:201], v[200:201], v[44:45]\n"
"ds_read_b64_tr_b16 v[114:115], v245 offset:8448\n"
"v_pk_add_f32 v[200:201], v[200:201], v[46:47]\n"
"v_pk_add_f32 v[200:201], v[200:201], v[48:49]\n"
"ds_read_b64_tr_b16 v[116:117], v244 offset:512\n"
"v_pk_add_f32 v[200:201], v[200:201], v[50:51]\n"
"v_pk_add_f32 v[200:201], v[200:201], v[52:53]\n"
"ds_read_b64_tr_b16 v[118:119], v244 offset:8704\n"
"v_pk_add_f32 v[200:201], v[200:201], v[54:55]\n"
"v_pk_add_f32 v[200:201], v[200:201], v[56:57]\n"
"ds_read_b64_tr_b16 v[120:121], v245 offset:512\n"
"v_pk_add_f32 v[200:201], v[200:201], v[58:59]\n"
"v_pk_add_f32 v[200:201], v[200:201], v[60:61]\n"
"ds_read_b64_tr_b16 v[122:123], v245 offset:8704\n"
"v_pk_add_f32 v[200:201], v[200:201], v[62:63]\n"
"v_pk_add_f32 v[200:201], v[200:201], v[64:65]\n"
"ds_read_b64_tr_b16 v[124:125], v244 offset:768\n"
"v_pk_add_f32 v[200:201], v[200:201], v[66:67]\n"
"v_pk_add_f32 v[200:201], v[200:201], v[68:69]\n"
"ds_read_b64_tr_b16 v[126:127], v244 offset:8960\n"
"v_pk_add_f32 v[200:201], v[200:201], v[70:71]\n"
"v_pk_add_f32 v[200:201], v[200:201], v[72:73]\n"
"v_pk_add_f32 v[200:201], v[200:201], v[74:75]\n"
"v_pk_add_f32 v[200:201], v[200:201], v[76:77]\n"
"v_pk_add_f32 v[200:201], v[200:201], v[78:79]\n"
"v_pk_add_f32 v[200:201], v[200:201], v[80:81]\n"
"v_pk_add_f32 v[200:201], v[200:201], v[82:83]\n"
"v_pk_add_f32 v[200:201], v[200:201], v[84:85]\n"
"v_pk_add_f32 v[200:201], v[200:201], v[86:87]\n"
"v_pk_add_f32 v[200:201], v[200:201], v[88:89]\n"
"v_pk_add_f32 v[200:201], v[200:201], v[90:91]\n"
"v_pk_add_f32 v[200:201], v[200:201], v[92:93]\n"
"v_pk_add_f32 v[200:201], v[200:201], v[94:95]\n"
"v_add_f32_e64 v200, v200, v201\n"
"v_mov_b32 v203, 0x207\n"
"v_mov_b32 v205, 0x71800000\n"
"v_xor_b32 v204, 8192, v247\n"
"v_cmp_class_f32 s[68:69], v200, v203\n"
"v_cmp_nlt_f32 vcc, v202, v205\n"
"s_nop 4\n"
"s_or_b64 vcc, vcc, s[68:69]\n"
"s_nop 1\n"
"v_cndmask_b32 v232, 0, 1, vcc\n"
"ds_write_b32 v247, v232 offset:6156\n"
"s_waitcnt lgkmcnt(0)\n"
"s_barrier\n"
"ds_read_b128 v[160:163], v247 offset:0\n"
"ds_read_b128 v[164:167], v247 offset:1024\n"
"ds_read_b128 v[176:179], v247 offset:6144\n"
"ds_read_b128 v[180:183], v204 offset:6144\n"
"LO3_%=:\n"
"v_mfma_f32_16x16x32_bf16 v[52:55], v[100:103], v[144:147], 0\n"
"ds_read_b64_tr_b16 v[224:225], v245 offset:768\n"
"v_mfma_f32_16x16x32_bf16 v[16:19], v[100:103], v[128:131], 0\n"
"ds_read_b64_tr_b16 v[226:227], v245 offset:8960\n"
"v_mfma_f32_16x16x32_bf16 v[56:59], v[104:107], v[144:147], 0\n"
"ds_read_b64_tr_b16 v[100:101], v246 offset:0\n"
"v_mfma_f32_16x16x32_bf16 v[20:23], v[104:107], v[128:131], 0\n"
"ds_read_b64_tr_b16 v[102:103], v246 offset:512\n"
"v_mfma_f32_16x16x32_bf16 v[60:63], v[108:111], v[144:147], 0\n"
"ds_read_b64_tr_b16 v[104:105], v244 offset:2048\n"
"v_mfma_f32_16x16x32_bf16 v[24:27], v[108:111], v[128:131], 0\n"
"ds_read_b64_tr_b16 v[106:107], v244 offset:10240\n"
"v_mfma_f32_16x16x32_bf16 v[64:67], v[112:115], v[144:147], 0\n"
"s_waitcnt lgkmcnt(7)\n"
"v_or3_b32 v176, v176, v177, v178\n"
"ds_read_b64_tr_b16 v[108:109], v245 offset:2048\n"
"v_mfma_f32_16x16x32_bf16 v[28:31], v[112:115], v[128:131], 0\n"
"s_waitcnt lgkmcnt(7)\n"
"v_or3_b32 v180, v180, v181, v182\n"
"ds_read_b64_tr_b16 v[110:111], v245 offset:10240\n"
"v_mfma_f32_16x16x32_bf16 v[68:71], v[116:119], v[144:147], 0\n"
"v_or3_b32 v176, v176, v179, v183\n"
"ds_read_b64_tr_b16 v[112:113], v244 offset:2304\n"
"v_mfma_f32_16x16x32_bf16 v[32:35], v[116:119], v[128:131], 0\n"
"v_or_b32_e64 v176, v176, v180\n"
"ds_read_b64_tr_b16 v[114:115], v244 offset:10496\n"
"v_mfma_f32_16x16x32_bf16 v[72:75], v[120:123], v[144:147], 0\n"
"v_cmp_ne_u32 vcc, 0, v176\n"
"ds_read_b64_tr_b16 v[116:117], v245 offset:2304\n"
"v_mfma_f32_16x16x32_bf16 v[36:39], v[120:123], v[128:131], 0\n"
"ds_read_b64_tr_b16 v[118:119], v245 offset:10496\n"
"v_mfma_f32_16x16x32_bf16 v[76:79], v[124:127], v[144:147], 0\n"
"ds_read_b64_tr_b16 v[120:121], v244 offset:2560\n"
"v_mfma_f32_16x16x32_bf16 v[40:43], v[124:127], v[128:131], 0\n"
"ds_read_b64_tr_b16 v[122:123], v244 offset:10752\n"
"v_mfma_f32_16x16x32_bf16 v[88:91], v[124:127], v[160:163], 0\n"
"s_waitcnt lgkmcnt(12)\n"
"v_mfma_f32_16x16x32_bf16 v[80:83], v[224:227], v[144:147], 0\n"
"ds_read_b64_tr_b16 v[124:125], v245 offset:2560\n"
"v_mfma_f32_16x16x32_bf16 v[44:47], v[224:227], v[128:131], 0\n"
"ds_read_b64_tr_b16 v[126:127], v245 offset:10752\n"
"v_mfma_f32_16x16x32_bf16 v[92:95], v[224:227], v[160:163], 0\n"
"s_waitcnt lgkmcnt(12)\n"
"v_mfma_f32_16x16x32_bf16 v[84:87], v[100:103], v[144:147], 0\n"
"ds_read_b64_tr_b16 v[224:225], v244 offset:2816\n"
"v_mfma_f32_16x16x32_bf16 v[48:51], v[100:103], v[128:131], 0\n"
"ds_read_b64_tr_b16 v[226:227], v244 offset:11008\n"
"s_waitcnt lgkmcnt(12)\n"
"v_mfma_f32_16x16x32_bf16 v[52:55], v[104:107], v[148:151], v[52:55]\n"
"ds_read_b64_tr_b16 v[100:101], v245 offset:2816\n"
"v_mfma_f32_16x16x32_bf16 v[16:19], v[104:107], v[132:135], v[16:19]\n"
"ds_read_b64_tr_b16 v[102:103], v245 offset:11008\n"
"s_waitcnt lgkmcnt(12)\n"
"v_mfma_f32_16x16x32_bf16 v[56:59], v[108:111], v[148:151], v[56:59]\n"
"ds_read_b64_tr_b16 v[104:105], v246 offset:1024\n"
"v_mfma_f32_16x16x32_bf16 v[20:23], v[108:111], v[132:135], v[20:23]\n"
"ds_read_b64_tr_b16 v[106:107], v246 offset:1536\n"
"s_waitcnt lgkmcnt(12)\n"
"v_mfma_f32_16x16x32_bf16 v[60:63], v[112:115], v[148:151], v[60:63]\n"
"v_mfma_f32_16x16x32_bf16 v[24:27], v[112:115], v[132:135], v[24:27]\n"
"s_waitcnt lgkmcnt(10)\n"
"v_mfma_f32_16x16x32_bf16 v[64:67], v[116:119], v[148:151], v[64:67]\n"
"v_mfma_f32_16x16x32_bf16 v[28:31], v[116:119], v[132:135], v[28:31]\n"
"s_waitcnt lgkmcnt(8)\n"
"v_mfma_f32_16x16x32_bf16 v[68:71], v[120:123], v[148:151], v[68:71]\n"
"s_waitcnt vmcnt(0)\n"
"s_cmp_lg_u64 vcc, 0\n"
"s_cbranch_scc1 LBAD_%=\n"
"s_waitcnt lgkmcnt(0)\n"
"s_barrier\n"
"ds_read_b64_tr_b16 v[108:109], v244 offset:16384\n"
"ds_read_b64_tr_b16 v[112:113], v245 offset:16384\n"
"ds_read_b64_tr_b16 v[116:117], v244 offset:16640\n"
"v_mfma_f32_16x16x32_bf16 v[32:35], v[120:123], v[132:135], v[32:35]\n"
"s_lshl_b32 s88, %[os2], 2\n"
"s_mov_b32 m0, s52\n"
"s_nop 0\n"
"buffer_load_dwordx4 v249, s[76:79], s88 offen lds\n"
"ds_read_b64_tr_b16 v[110:111], v244 offset:24576\n"
"ds_read_b64_tr_b16 v[114:115], v245 offset:24576\n"
"ds_read_b64_tr_b16 v[118:119], v244 offset:24832\n"
"v_mfma_f32_16x16x32_bf16 v[72:75], v[124:127], v[148:151], v[72:75]\n"
"buffer_load_dwordx4 v250, s[76:79], s88 offen offset:1024 lds\n"
"ds_read_b64_tr_b16 v[120:121], v245 offset:16640\n"
"v_mfma_f32_16x16x32_bf16 v[36:39], v[124:127], v[132:135], v[36:39]\n"
"s_lshl_b32 s89, %[os2], 2\n"
"s_add_u32 s89, s89, 34816\n"
"buffer_load_dwordx4 v249, s[76:79], s89 offen offset:2048 lds\n"
"ds_read_b64_tr_b16 v[122:123], v245 offset:24832\n"
"v_mfma_f32_16x16x32_bf16 v[76:79], v[224:227], v[148:151], v[76:79]\n"
"buffer_load_dwordx4 v250, s[76:79], s89 offen offset:3072 lds\n"
"ds_read_b64_tr_b16 v[124:125], v244 offset:16896\n"
"v_mfma_f32_16x16x32_bf16 v[40:43], v[224:227], v[132:135], v[40:43]\n"
"ds_read_b64_tr_b16 v[126:127], v244 offset:25088\n"
"v_mfma_f32_16x16x32_bf16 v[88:91], v[224:227], v[164:167], v[88:91]\n"
"v_mfma_f32_16x16x32_bf16 v[80:83], v[100:103], v[148:151], v[80:83]\n"
"ds_read_b64_tr_b16 v[224:225], v245 offset:16896\n"
"v_mfma_f32_16x16x32_bf16 v[44:47], v[100:103], v[132:135], v[44:47]\n"
"ds_read_b64_tr_b16 v[226:227], v245 offset:25088\n"
"v_mfma_f32_16x16x32_bf16 v[92:95], v[100:103], v[164:167], v[92:95]\n"
"v_mfma_f32_16x16x32_bf16 v[84:87], v[104:107], v[148:151], v[84:87]\n"
"ds_read_b64_tr_b16 v[100:101], v244 offset:17152\n"
"v_mfma_f32_16x16x32_bf16 v[48:51], v[104:107], v[132:135], v[48:51]\n"
"ds_read_b64_tr_b16 v[102:103], v244 offset:25344\n"
"s_waitcnt lgkmcnt(10)\n"
"v_mfma_f32_16x16x32_bf16 v[52:55], v[108:111], v[152:155], v[52:55]\n"
"ds_read_b64_tr_b16 v[104:105], v245 offset:17152\n"
"v_mfma_f32_16x16x32_bf16 v[16:19], v[108:111], v[136:139], v[16:19]\n"
"ds_read_b64_tr_b16 v[106:107], v245 offset:25344\n"
"s_waitcnt lgkmcnt(11)\n"
"v_mfma_f32_16x16x32_bf16 v[56:59], v[112:115], v[152:155], v[56:59]\n"
"ds_read_b64_tr_b16 v[108:109], v246 offset:2048\n"
"v_mfma_f32_16x16x32_bf16 v[20:23], v[112:115], v[136:139], v[20:23]\n"
"ds_read_b64_tr_b16 v[110:111], v246 offset:2560\n"
"s_waitcnt lgkmcnt(12)\n"
"v_mfma_f32_16x16x32_bf16 v[60:63], v[116:119], v[152:155], v[60:63]\n"
"ds_read_b64_tr_b16 v[112:113], v244 offset:18432\n"
"v_mfma_f32_16x16x32_bf16 v[24:27], v[116:119], v[136:139], v[24:27]\n"
"ds_read_b64_tr_b16 v[114:115], v244 offset:26624\n"
"s_waitcnt lgkmcnt(12)\n"
"v_mfma_f32_16x16x32_bf16 v[64:67], v[120:123], v[152:155], v[64:67]\n"
"ds_read_b64_tr_b16 v[116:117], v245 offset:18432\n"
"v_mfma_f32_16x16x32_bf16 v[28:31], v[120:123], v[136:139], v[28:31]\n"
"ds_read_b64_tr_b16 v[118:119], v245 offset:26624\n"
"s_waitcnt lgkmcnt(12)\n"
"v_mfma_f32_16x16x32_bf16 v[68:71], v[124:127], v[152:155], v[68:71]\n"
"ds_read_b64_tr_b16 v[120:121], v244 offset:18688\n"
"v_mfma_f32_16x16x32_bf16 v[32:35], v[124:127], v[136:139], v[32:35]\n"
"ds_read_b64_tr_b16 v[122:123], v244 offset:26880\n"
"s_waitcnt lgkmcnt(12)\n"
"v_mfma_f32_16x16x32_bf16 v[72:75], v[224:227], v[152:155], v[72:75]\n"
"ds_read_b64_tr_b16 v[124:125], v245 offset:18688\n"
"v_mfma_f32_16x16x32_bf16 v[36:39], v[224:227], v[136:139], v[36:39]\n"
"ds_read_b64_tr_b16 v[126:127], v245 offset:26880\n"
"s_waitcnt lgkmcnt(12)\n"
"v_mfma_f32_16x16x32_bf16 v[76:79], v[100:103], v[152:155], v[76:79]\n"
"ds_read_b64_tr_b16 v[224:225], v244 offset:18944\n"
"v_mfma_f32_16x16x32_bf16 v[40:43], v[100:103], v[136:139], v[40:43]\n"
"ds_read_b64_tr_b16 v[226:227], v244 offset:27136\n"
"v_mfma_f32_16x16x32_bf16 v[88:91], v[100:103], v[168:171], v[88:91]\n"
"s_waitcnt lgkmcnt(12)\n"
"v_mfma_f32_16x16x32_bf16 v[80:83], v[104:107], v[152:155], v[80:83]\n"
"ds_read_b64_tr_b16 v[100:101], v245 offset:18944\n"
"v_mfma_f32_16x16x32_bf16 v[44:47], v[104:107], v[136:139], v[44:47]\n"
"ds_read_b64_tr_b16 v[102:103], v245 offset:27136\n"
"v_mfma_f32_16x16x32_bf16 v[92:95], v[104:107], v[168:171], v[92:95]\n"
"s_waitcnt lgkmcnt(12)\n"
"v_mfma_f32_16x16x32_bf16 v[84:87], v[108:111], v[152:155], v[84:87]\n"
"ds_read_b64_tr_b16 v[104:105], v244 offset:19200\n"
"v_mfma_f32_16x16x32_bf16 v[48:51], v[108:111], v[136:139], v[48:51]\n"
"ds_read_b64_tr_b16 v[106:107], v244 offset:27392\n"
"s_waitcnt lgkmcnt(12)\n"
"v_mfma_f32_16x16x32_bf16 v[52:55], v[112:115], v[156:159], v[52:55]\n"
"ds_read_b64_tr_b16 v[108:109], v245 offset:19200\n"
"v_mfma_f32_16x16x32_bf16 v[16:19], v[112:115], v[140:143], v[16:19]\n"
"ds_read_b64_tr_b16 v[110:111], v245 offset:27392\n"
"s_waitcnt lgkmcnt(12)\n"
"v_mfma_f32_16x16x32_bf16 v[56:59], v[116:119], v[156:159], v[56:59]\n"
"ds_read_b64_tr_b16 v[112:113], v246 offset:3072\n"
"v_mfma_f32_16x16x32_bf16 v[20:23], v[116:119], v[140:143], v[20:23]\n"
"ds_read_b64_tr_b16 v[114:115], v246 offset:3584\n"
"s_waitcnt lgkmcnt(12)\n"
"v_mfma_f32_16x16x32_bf16 v[60:63], v[120:123], v[156:159], v[60:63]\n"
"v_mfma_f32_16x16x32_bf16 v[24:27], v[120:123], v[140:143], v[24:27]\n"
"s_waitcnt lgkmcnt(10)\n"
"v_mfma_f32_16x16x32_bf16 v[64:67], v[124:127], v[156:159], v[64:67]\n"
"v_mfma_f32_16x16x32_bf16 v[28:31], v[124:127], v[140:143], v[28:31]\n"
"s_waitcnt lgkmcnt(8)\n"
"v_mfma_f32_16x16x32_bf16 v[68:71], v[224:227], v[156:159], v[68:71]\n"
"s_waitcnt lgkmcnt(0)\n"
"s_barrier\n"
"ds_read_b64_tr_b16 v[116:117], v244 offset:32768\n"
"ds_read_b64_tr_b16 v[120:121], v245 offset:32768\n"
"ds_read_b64_tr_b16 v[124:125], v244 offset:33024\n"
"v_mfma_f32_16x16x32_bf16 v[32:35], v[224:227], v[140:143], v[32:35]\n"
"s_lshl_b32 s88, %[os2], 2\n"
"s_add_u32 s88, s88, 73728\n"
"s_mov_b32 m0, s53\n"
"s_nop 0\n"
"buffer_load_dwordx4 v249, s[76:79], s88 offen lds\n"
"ds_read_b64_tr_b16 v[118:119], v244 offset:40960\n"
"ds_read_b64_tr_b16 v[122:123], v245 offset:40960\n"
"ds_read_b64_tr_b16 v[126:127], v244 offset:41216\n"
"v_mfma_f32_16x16x32_bf16 v[72:75], v[100:103], v[156:159], v[72:75]\n"
"buffer_load_dwordx4 v250, s[76:79], s88 offen offset:1024 lds\n"
"ds_read_b64_tr_b16 v[224:225], v245 offset:33024\n"
"v_mfma_f32_16x16x32_bf16 v[36:39], v[100:103], v[140:143], v[36:39]\n"
"s_lshl_b32 s89, %[os2], 2\n"
"s_add_u32 s89, s89, 108544\n"
"buffer_load_dwordx4 v249, s[76:79], s89 offen offset:2048 lds\n"
"ds_read_b64_tr_b16 v[226:227], v245 offset:41216\n"
"v_mfma_f32_16x16x32_bf16 v[76:79], v[104:107], v[156:159], v[76:79]\n"
"buffer_load_dwordx4 v250, s[76:79], s89 offen offset:3072 lds\n"
"ds_read_b64_tr_b16 v[100:101], v244 offset:33280\n"
"s_add_u32 m0, s90, 1024\n"
"v_mfma_f32_16x16x32_bf16 v[40:43], v[104:107], v[140:143], v[40:43]\n"
"buffer_load_dwordx4 v252, s[76:79], s89 offen offset:2048 lds\n"
"ds_read_b64_tr_b16 v[102:103], v244 offset:41472\n"
"v_mfma_f32_16x16x32_bf16 v[88:91], v[104:107], v[172:175], v[88:91]\n"
"v_mfma_f32_16x16x32_bf16 v[80:83], v[108:111], v[156:159], v[80:83]\n"
"ds_read_b64_tr_b16 v[104:105], v245 offset:33280\n"
"v_mfma_f32_16x16x32_bf16 v[44:47], v[108:111], v[140:143], v[44:47]\n"
"ds_read_b64_tr_b16 v[106:107], v245 offset:41472\n"
"v_mfma_f32_16x16x32_bf16 v[92:95], v[108:111], v[172:175], v[92:95]\n"
"v_mfma_f32_16x16x32_bf16 v[84:87], v[112:115], v[156:159], v[84:87]\n"
"ds_read_b64_tr_b16 v[160:161], v244 offset:33536\n"
"v_mfma_f32_16x16x32_bf16 v[48:51], v[112:115], v[140:143], v[48:51]\n"
"s_barrier\n"
"s_add_u32 s60, %[os1], 6144\n"
"buffer_load_dwordx4 v[192:195], %[l16], %[srdo], s60 offen offset:1024 sc1\n"
"buffer_load_dwordx4 v[176:179], %[l16], %[srdo], s60 offen sc1\n"
"s_add_u32 s64, %[os1], 35840\n"
"buffer_load_dwordx4 v[220:223], %[l16], %[srdo], s64 offen sc1\n"
"s_add_u32 s61, %[os1], 15360\n"
"buffer_load_dwordx4 v[196:199], %[l16], %[srdo], s61 offen offset:1024 sc1\n"
"buffer_load_dwordx4 v[180:183], %[l16], %[srdo], s61 offen sc1\n"
"s_add_u32 s62, %[os1], 24576\n"
"buffer_load_dwordx4 v[200:203], %[l16], %[srdo], s62 offen offset:1024 sc1\n"
"buffer_load_dwordx4 v[184:187], %[l16], %[srdo], s62 offen sc1\n"
"s_add_u32 s63, %[os1], 33792\n"
"buffer_load_dwordx4 v[204:207], %[l16], %[srdo], s63 offen offset:1024 sc1\n"
"buffer_load_dwordx4 v[188:191], %[l16], %[srdo], s63 offen sc1\n"
"s_add_u32 s64, %[os2], 35840\n"
"buffer_load_dwordx4 v[240:243], %[l16], %[srdo], s64 offen sc1\n"
"ds_read_b64_tr_b16 v[162:163], v244 offset:41728\n"
"s_waitcnt vmcnt(9) lgkmcnt(10)\n"
"v_mfma_f32_16x16x32_bf16 v[52:55], v[116:119], v[192:195], v[52:55]\n"
"ds_read_b64_tr_b16 v[164:165], v245 offset:33536\n"
"s_waitcnt vmcnt(8)\n"
"v_mfma_f32_16x16x32_bf16 v[16:19], v[116:119], v[176:179], v[16:19]\n"
"s_add_u32 s60, %[os2], 6144\n"
"buffer_load_dwordx4 v[144:147], %[l16], %[srdo], s60 offen offset:1024 sc1\n"
"ds_read_b64_tr_b16 v[166:167], v245 offset:41728\n"
"s_waitcnt lgkmcnt(11)\n"
"v_mfma_f32_16x16x32_bf16 v[56:59], v[120:123], v[192:195], v[56:59]\n"
"ds_read_b64_tr_b16 v[116:117], v246 offset:4096\n"
"v_mfma_f32_16x16x32_bf16 v[20:23], v[120:123], v[176:179], v[20:23]\n"
"buffer_load_dwordx4 v[128:131], %[l16], %[srdo], s60 offen sc1\n"
"ds_read_b64_tr_b16 v[118:119], v246 offset:4608\n"
"s_waitcnt lgkmcnt(12)\n"
"v_mfma_f32_16x16x32_bf16 v[60:63], v[124:127], v[192:195], v[60:63]\n"
"ds_read_b64_tr_b16 v[120:121], v244 offset:34816\n"
"v_mfma_f32_16x16x32_bf16 v[24:27], v[124:127], v[176:179], v[24:27]\n"
"ds_read_b64_tr_b16 v[122:123], v244 offset:43008\n"
"s_waitcnt lgkmcnt(12)\n"
"v_mfma_f32_16x16x32_bf16 v[64:67], v[224:227], v[192:195], v[64:67]\n"
"ds_read_b64_tr_b16 v[124:125], v245 offset:34816\n"
"v_mfma_f32_16x16x32_bf16 v[28:31], v[224:227], v[176:179], v[28:31]\n"
"s_add_u32 s61, %[os2], 15360\n"
"buffer_load_dwordx4 v[148:151], %[l16], %[srdo], s61 offen offset:1024 sc1\n"
"ds_read_b64_tr_b16 v[126:127], v245 offset:43008\n"
"s_waitcnt lgkmcnt(12)\n"
"v_mfma_f32_16x16x32_bf16 v[68:71], v[100:103], v[192:195], v[68:71]\n"
"ds_read_b64_tr_b16 v[224:225], v244 offset:35072\n"
"v_mfma_f32_16x16x32_bf16 v[32:35], v[100:103], v[176:179], v[32:35]\n"
"buffer_load_dwordx4 v[132:135], %[l16], %[srdo], s61 offen sc1\n"
"ds_read_b64_tr_b16 v[226:227], v244 offset:43264\n"
"s_waitcnt lgkmcnt(12)\n"
"v_mfma_f32_16x16x32_bf16 v[72:75], v[104:107], v[192:195], v[72:75]\n"
"ds_read_b64_tr_b16 v[100:101], v245 offset:35072\n"
"v_mfma_f32_16x16x32_bf16 v[36:39], v[104:107], v[176:179], v[36:39]\n"
"ds_read_b64_tr_b16 v[102:103], v245 offset:43264\n"
"s_waitcnt lgkmcnt(12)\n"
"v_mfma_f32_16x16x32_bf16 v[76:79], v[160:163], v[192:195], v[76:79]\n"
"ds_read_b64_tr_b16 v[104:105], v244 offset:35328\n"
"v_mfma_f32_16x16x32_bf16 v[40:43], v[160:163], v[176:179], v[40:43]\n"
"s_add_u32 s62, %[os2], 24576\n"
"buffer_load_dwordx4 v[152:155], %[l16], %[srdo], s62 offen offset:1024 sc1\n"
"ds_read_b64_tr_b16 v[106:107], v244 offset:43520\n"
"s_waitcnt lgkmcnt(12)\n"
"v_mfma_f32_16x16x32_bf16 v[80:83], v[164:167], v[192:195], v[80:83]\n"
"ds_read_b64_tr_b16 v[108:109], v245 offset:35328\n"
"v_mfma_f32_16x16x32_bf16 v[44:47], v[164:167], v[176:179], v[44:47]\n"
"buffer_load_dwordx4 v[136:139], %[l16], %[srdo], s62 offen sc1\n"
"ds_read_b64_tr_b16 v[110:111], v245 offset:43520\n"
"s_waitcnt lgkmcnt(12)\n"
"v_mfma_f32_16x16x32_bf16 v[84:87], v[116:119], v[192:195], v[84:87]\n"
"ds_read_b64_tr_b16 v[168:169], v244 offset:35584\n"
"v_mfma_f32_16x16x32_bf16 v[48:51], v[116:119], v[176:179], v[48:51]\n"
"ds_read_b64_tr_b16 v[170:171], v244 offset:43776\n"
"s_waitcnt vmcnt(12) lgkmcnt(12)\n"
"v_mfma_f32_16x16x32_bf16 v[52:55], v[120:123], v[196:199], v[52:55]\n"
"ds_read_b64_tr_b16 v[172:173], v245 offset:35584\n"
"s_waitcnt vmcnt(11)\n"
"v_mfma_f32_16x16x32_bf16 v[16:19], v[120:123], v[180:183], v[16:19]\n"
"s_add_u32 s63, %[os2], 33792\n"
"buffer_load_dwordx4 v[156:159], %[l16], %[srdo], s63 offen offset:1024 sc1\n"
"ds_read_b64_tr_b16 v[174:175], v245 offset:43776\n"
"s_waitcnt lgkmcnt(12)\n"
"v_mfma_f32_16x16x32_bf16 v[56:59], v[124:127], v[196:199], v[56:59]\n"
"ds_read_b64_tr_b16 v[120:121], v246 offset:5120\n"
"v_mfma_f32_16x16x32_bf16 v[20:23], v[124:127], v[180:183], v[20:23]\n"
"buffer_load_dwordx4 v[140:143], %[l16], %[srdo], s63 offen sc1\n"
"ds_read_b64_tr_b16 v[122:123], v246 offset:5632\n"
"s_waitcnt lgkmcnt(12)\n"
"v_mfma_f32_16x16x32_bf16 v[60:63], v[224:227], v[196:199], v[60:63]\n"
"v_mfma_f32_16x16x32_bf16 v[24:27], v[224:227], v[180:183], v[24:27]\n"
"ds_write_b128 v247, v[220:223] offset:3072\n"
"s_waitcnt lgkmcnt(11)\n"
"v_mfma_f32_16x16x32_bf16 v[64:67], v[100:103], v[196:199], v[64:67]\n"
"v_mfma_f32_16x16x32_bf16 v[28:31], v[100:103], v[180:183], v[28:31]\n"
"s_add_u32 s60, %[os3], 6144\n"
"buffer_load_dwordx4 v[192:195], %[l16], %[srdo], s60 offen offset:1024 sc1\n"
"s_waitcnt lgkmcnt(9)\n"
"v_mfma_f32_16x16x32_bf16 v[68:71], v[104:107], v[196:199], v[68:71]\n"
"s_waitcnt lgkmcnt(0)\n"
"s_barrier\n"
"ds_read_b64_tr_b16 v[124:125], v244 offset:49152\n"
"ds_read_b64_tr_b16 v[224:225], v245 offset:49152\n"
"ds_read_b64_tr_b16 v[100:101], v244 offset:49408\n"
"v_mfma_f32_16x16x32_bf16 v[32:35], v[104:107], v[180:183], v[32:35]\n"
"ds_read_b128 v[208:211], v247 offset:0\n"
"s_lshl_b32 s88, %[os3], 2\n"
"s_mov_b32 m0, s54\n"
"s_nop 0\n"
"buffer_load_dwordx4 v249, s[76:79], s88 offen lds\n"
"buffer_load_dwordx4 v[176:179], %[l16], %[srdo], s60 offen sc1\n"
"ds_read_b64_tr_b16 v[126:127], v244 offset:57344\n"
"ds_read_b64_tr_b16 v[226:227], v245 offset:57344\n"
"ds_read_b64_tr_b16 v[102:103], v244 offset:57600\n"
"v_mfma_f32_16x16x32_bf16 v[72:75], v[108:111], v[196:199], v[72:75]\n"
"ds_read_b128 v[212:215], v247 offset:1024\n"
"buffer_load_dwordx4 v250, s[76:79], s88 offen offset:1024 lds\n"
"ds_read_b64_tr_b16 v[104:105], v245 offset:49408\n"
"v_mfma_f32_16x16x32_bf16 v[36:39], v[108:111], v[180:183], v[36:39]\n"
"ds_read_b128 v[216:219], v247 offset:2048\n"
"s_lshl_b32 s89, %[os3], 2\n"
"s_add_u32 s89, s89, 34816\n"
"buffer_load_dwordx4 v249, s[76:79], s89 offen offset:2048 lds\n"
"ds_read_b64_tr_b16 v[106:107], v245 offset:57600\n"
"v_mfma_f32_16x16x32_bf16 v[76:79], v[168:171], v[196:199], v[76:79]\n"
"buffer_load_dwordx4 v250, s[76:79], s89 offen offset:3072 lds\n"
"ds_read_b64_tr_b16 v[108:109], v244 offset:49664\n"
"v_mfma_f32_16x16x32_bf16 v[40:43], v[168:171], v[180:183], v[40:43]\n"
"ds_read_b64_tr_b16 v[110:111], v244 offset:57856\n"
"v_mfma_f32_16x16x32_bf16 v[80:83], v[172:175], v[196:199], v[80:83]\n"
"ds_read_b64_tr_b16 v[112:113], v245 offset:49664\n"
"v_mfma_f32_16x16x32_bf16 v[44:47], v[172:175], v[180:183], v[44:47]\n"
"ds_read_b64_tr_b16 v[114:115], v245 offset:57856\n"
"v_mfma_f32_16x16x32_bf16 v[84:87], v[120:123], v[196:199], v[84:87]\n"
"ds_read_b64_tr_b16 v[116:117], v244 offset:49920\n"
"v_mfma_f32_16x16x32_bf16 v[48:51], v[120:123], v[180:183], v[48:51]\n"
"ds_read_b64_tr_b16 v[118:119], v244 offset:58112\n"
"s_waitcnt lgkmcnt(13)\n"
"v_mfma_f32_16x16x32_bf16 v[88:91], v[160:163], v[208:211], v[88:91]\n"
"s_waitcnt vmcnt(18) lgkmcnt(12)\n"
"v_mfma_f32_16x16x32_bf16 v[52:55], v[124:127], v[200:203], v[52:55]\n"
"ds_read_b64_tr_b16 v[120:121], v245 offset:49920\n"
"s_waitcnt vmcnt(17)\n"
"v_mfma_f32_16x16x32_bf16 v[16:19], v[124:127], v[184:187], v[16:19]\n"
"s_add_u32 s61, %[os3], 15360\n"
"buffer_load_dwordx4 v[196:199], %[l16], %[srdo], s61 offen offset:1024 sc1\n"
"ds_read_b64_tr_b16 v[122:123], v245 offset:58112\n"
"v_mfma_f32_16x16x32_bf16 v[92:95], v[164:167], v[208:211], v[92:95]\n"
"buffer_load_dwordx4 v[180:183], %[l16], %[srdo], s61 offen sc1\n"
"s_waitcnt lgkmcnt(13)\n"
"v_mfma_f32_16x16x32_bf16 v[56:59], v[224:227], v[200:203], v[56:59]\n"
"ds_read_b64_tr_b16 v[124:125], v246 offset:6144\n"
"v_mfma_f32_16x16x32_bf16 v[20:23], v[224:227], v[184:187], v[20:23]\n"
"ds_read_b64_tr_b16 v[126:127], v246 offset:6656\n"
"s_waitcnt lgkmcnt(13)\n"
"v_mfma_f32_16x16x32_bf16 v[88:91], v[168:171], v[212:215], v[88:91]\n"
"v_mfma_f32_16x16x32_bf16 v[60:63], v[100:103], v[200:203], v[60:63]\n"
"ds_read_b64_tr_b16 v[224:225], v244 offset:51200\n"
"v_mfma_f32_16x16x32_bf16 v[24:27], v[100:103], v[184:187], v[24:27]\n"
"ds_read_b64_tr_b16 v[226:227], v244 offset:59392\n"
"v_mfma_f32_16x16x32_bf16 v[92:95], v[172:175], v[212:215], v[92:95]\n"
"s_waitcnt lgkmcnt(12)\n"
"v_mfma_f32_16x16x32_bf16 v[64:67], v[104:107], v[200:203], v[64:67]\n"
"ds_read_b64_tr_b16 v[100:101], v245 offset:51200\n"
"v_mfma_f32_16x16x32_bf16 v[28:31], v[104:107], v[184:187], v[28:31]\n"
"ds_read_b64_tr_b16 v[102:103], v245 offset:59392\n"
"s_waitcnt lgkmcnt(12)\n"
"v_mfma_f32_16x16x32_bf16 v[68:71], v[108:111], v[200:203], v[68:71]\n"
"ds_read_b64_tr_b16 v[104:105], v244 offset:51456\n"
"v_mfma_f32_16x16x32_bf16 v[32:35], v[108:111], v[184:187], v[32:35]\n"
"ds_read_b64_tr_b16 v[106:107], v244 offset:59648\n"
"s_waitcnt lgkmcnt(12)\n"
"v_mfma_f32_16x16x32_bf16 v[72:75], v[112:115], v[200:203], v[72:75]\n"
"ds_read_b64_tr_b16 v[108:109], v245 offset:51456\n"
"v_mfma_f32_16x16x32_bf16 v[36:39], v[112:115], v[184:187], v[36:39]\n"
"ds_read_b64_tr_b16 v[110:111], v245 offset:59648\n"
"s_waitcnt lgkmcnt(12)\n"
"v_mfma_f32_16x16x32_bf16 v[76:79], v[116:119], v[200:203], v[76:79]\n"
"ds_read_b64_tr_b16 v[112:113], v244 offset:51712\n"
"v_mfma_f32_16x16x32_bf16 v[40:43], v[116:119], v[184:187], v[40:43]\n"
"ds_read_b64_tr_b16 v[114:115], v244 offset:59904\n"
"v_mfma_f32_16x16x32_bf16 v[88:91], v[116:119], v[216:219], v[88:91]\n"
"s_waitcnt lgkmcnt(12)\n"
"v_mfma_f32_16x16x32_bf16 v[80:83], v[120:123], v[200:203], v[80:83]\n"
"ds_read_b64_tr_b16 v[116:117], v245 offset:51712\n"
"v_mfma_f32_16x16x32_bf16 v[44:47], v[120:123], v[184:187], v[44:47]\n"
"ds_read_b64_tr_b16 v[118:119], v245 offset:59904\n"
"v_mfma_f32_16x16x32_bf16 v[92:95], v[120:123], v[216:219], v[92:95]\n"
"s_waitcnt lgkmcnt(12)\n"
"v_mfma_f32_16x16x32_bf16 v[84:87], v[124:127], v[200:203], v[84:87]\n"
"ds_read_b64_tr_b16 v[120:121], v244 offset:51968\n"
"v_mfma_f32_16x16x32_bf16 v[48:51], v[124:127], v[184:187], v[48:51]\n"
"ds_read_b64_tr_b16 v[122:123], v244 offset:60160\n"
"s_waitcnt vmcnt(18) lgkmcnt(12)\n"
"v_mfma_f32_16x16x32_bf16 v[52:55], v[224:227], v[204:207], v[52:55]\n"
"ds_read_b64_tr_b16 v[124:125], v245 offset:51968\n"
"s_waitcnt vmcnt(17)\n"
"v_mfma_f32_16x16x32_bf16 v[16:19], v[224:227], v[188:191], v[16:19]\n"
"s_add_u32 s62, %[os3], 24576\n"
"buffer_load_dwordx4 v[200:203], %[l16], %[srdo], s62 offen offset:1024 sc1\n"
"ds_read_b64_tr_b16 v[126:127], v245 offset:60160\n"
"s_waitcnt lgkmcnt(12)\n"
"v_mfma_f32_16x16x32_bf16 v[56:59], v[100:103], v[204:207], v[56:59]\n"
"buffer_load_dwordx4 v[184:187], %[l16], %[srdo], s62 offen sc1\n"
"ds_read_b64_tr_b16 v[224:225], v246 offset:7168\n"
"v_mfma_f32_16x16x32_bf16 v[20:23], v[100:103], v[188:191], v[20:23]\n"
"ds_read_b64_tr_b16 v[226:227], v246 offset:7680\n"
"s_waitcnt lgkmcnt(12)\n"
"v_mfma_f32_16x16x32_bf16 v[60:63], v[104:107], v[204:207], v[60:63]\n"
"v_mfma_f32_16x16x32_bf16 v[24:27], v[104:107], v[188:191], v[24:27]\n"
"s_waitcnt vmcnt(18)\n"
"ds_write_b128 v247, v[240:243] offset:7168\n"
"s_waitcnt lgkmcnt(11)\n"
"v_mfma_f32_16x16x32_bf16 v[64:67], v[108:111], v[204:207], v[64:67]\n"
"v_mfma_f32_16x16x32_bf16 v[28:31], v[108:111], v[188:191], v[28:31]\n"
"s_waitcnt lgkmcnt(9)\n"
"v_mfma_f32_16x16x32_bf16 v[68:71], v[112:115], v[204:207], v[68:71]\n"
"s_waitcnt lgkmcnt(0)\n"
"s_barrier\n"
"s_add_u32 s64, %[os3], 35840\n"
"buffer_load_dwordx4 v[240:243], %[l16], %[srdo], s64 offen sc1\n"
"ds_read_b64_tr_b16 v[100:101], v244 offset:0\n"
"ds_read_b64_tr_b16 v[104:105], v245 offset:0\n"
"ds_read_b64_tr_b16 v[108:109], v244 offset:256\n"
"v_mfma_f32_16x16x32_bf16 v[32:35], v[112:115], v[188:191], v[32:35]\n"
"ds_read_b128 v[160:163], v247 offset:4096\n"
"s_lshl_b32 s88, %[os3], 2\n"
"s_add_u32 s88, s88, 73728\n"
"s_mov_b32 m0, s55\n"
"s_nop 0\n"
"buffer_load_dwordx4 v249, s[76:79], s88 offen lds\n"
"ds_read_b64_tr_b16 v[102:103], v244 offset:8192\n"
"ds_read_b64_tr_b16 v[106:107], v245 offset:8192\n"
"ds_read_b64_tr_b16 v[110:111], v244 offset:8448\n"
"v_mfma_f32_16x16x32_bf16 v[72:75], v[116:119], v[204:207], v[72:75]\n"
"ds_read_b128 v[164:167], v247 offset:5120\n"
"buffer_load_dwordx4 v250, s[76:79], s88 offen offset:1024 lds\n"
"ds_read_b64_tr_b16 v[112:113], v245 offset:256\n"
"v_mfma_f32_16x16x32_bf16 v[36:39], v[116:119], v[188:191], v[36:39]\n"
"ds_read_b128 v[168:171], v247 offset:6144\n"
"s_lshl_b32 s89, %[os3], 2\n"
"s_add_u32 s89, s89, 108544\n"
"buffer_load_dwordx4 v249, s[76:79], s89 offen offset:2048 lds\n"
"ds_read_b64_tr_b16 v[114:115], v245 offset:8448\n"
"v_mfma_f32_16x16x32_bf16 v[76:79], v[120:123], v[204:207], v[76:79]\n"
"ds_read_b128 v[172:175], v247 offset:7168\n"
"buffer_load_dwordx4 v250, s[76:79], s89 offen offset:3072 lds\n"
"ds_read_b64_tr_b16 v[116:117], v244 offset:512\n"
"s_add_u32 m0, s90, 5120\n"
"v_mfma_f32_16x16x32_bf16 v[40:43], v[120:123], v[188:191], v[40:43]\n"
"buffer_load_dwordx4 v252, s[76:79], s89 offen offset:2048 lds\n"
"ds_read_b64_tr_b16 v[118:119], v244 offset:8704\n"
"v_mfma_f32_16x16x32_bf16 v[88:91], v[120:123], v[220:223], v[88:91]\n"
"v_mfma_f32_16x16x32_bf16 v[80:83], v[124:127], v[204:207], v[80:83]\n"
"ds_read_b64_tr_b16 v[120:121], v245 offset:512\n"
"v_mfma_f32_16x16x32_bf16 v[44:47], v[124:127], v[188:191], v[44:47]\n"
"ds_read_b64_tr_b16 v[122:123], v245 offset:8704\n"
"v_mfma_f32_16x16x32_bf16 v[92:95], v[124:127], v[220:223], v[92:95]\n"
"v_mfma_f32_16x16x32_bf16 v[84:87], v[224:227], v[204:207], v[84:87]\n"
"ds_read_b64_tr_b16 v[124:125], v244 offset:768\n"
"v_mfma_f32_16x16x32_bf16 v[48:51], v[224:227], v[188:191], v[48:51]\n"
"ds_read_b64_tr_b16 v[126:127], v244 offset:8960\n"
"s_waitcnt vmcnt(23) lgkmcnt(13)\n"
"v_mfma_f32_16x16x32_bf16 v[52:55], v[100:103], v[144:147], v[52:55]\n"
"ds_read_b64_tr_b16 v[224:225], v245 offset:768\n"
"s_waitcnt vmcnt(22)\n"
"v_mfma_f32_16x16x32_bf16 v[16:19], v[100:103], v[128:131], v[16:19]\n"
"s_add_u32 s63, %[os3], 33792\n"
"buffer_load_dwordx4 v[204:207], %[l16], %[srdo], s63 offen offset:1024 sc1\n"
"ds_read_b64_tr_b16 v[226:227], v245 offset:8960\n"
"s_waitcnt lgkmcnt(14)\n"
"v_mfma_f32_16x16x32_bf16 v[56:59], v[104:107], v[144:147], v[56:59]\n"
"buffer_load_dwordx4 v[188:191], %[l16], %[srdo], s63 offen sc1\n"
"ds_read_b64_tr_b16 v[100:101], v246 offset:0\n"
"v_mfma_f32_16x16x32_bf16 v[20:23], v[104:107], v[128:131], v[20:23]\n"
"ds_read_b64_tr_b16 v[102:103], v246 offset:512\n"
"s_waitcnt lgkmcnt(15)\n"
"v_mfma_f32_16x16x32_bf16 v[60:63], v[108:111], v[144:147], v[60:63]\n"
"ds_read_b64_tr_b16 v[104:105], v244 offset:2048\n"
"v_mfma_f32_16x16x32_bf16 v[24:27], v[108:111], v[128:131], v[24:27]\n"
"ds_read_b64_tr_b16 v[106:107], v244 offset:10240\n"
"s_waitcnt lgkmcnt(13)\n"
"v_mfma_f32_16x16x32_bf16 v[64:67], v[112:115], v[144:147], v[64:67]\n"
"ds_read_b64_tr_b16 v[108:109], v245 offset:2048\n"
"v_mfma_f32_16x16x32_bf16 v[28:31], v[112:115], v[128:131], v[28:31]\n"
"ds_read_b64_tr_b16 v[110:111], v245 offset:10240\n"
"s_waitcnt lgkmcnt(12)\n"
"v_mfma_f32_16x16x32_bf16 v[68:71], v[116:119], v[144:147], v[68:71]\n"
"ds_read_b64_tr_b16 v[112:113], v244 offset:2304\n"
"v_mfma_f32_16x16x32_bf16 v[32:35], v[116:119], v[128:131], v[32:35]\n"
"ds_read_b64_tr_b16 v[114:115], v244 offset:10496\n"
"s_waitcnt lgkmcnt(12)\n"
"v_mfma_f32_16x16x32_bf16 v[72:75], v[120:123], v[144:147], v[72:75]\n"
"ds_read_b64_tr_b16 v[116:117], v245 offset:2304\n"
"v_mfma_f32_16x16x32_bf16 v[36:39], v[120:123], v[128:131], v[36:39]\n"
"ds_read_b64_tr_b16 v[118:119], v245 offset:10496\n"
"s_waitcnt lgkmcnt(12)\n"
"v_mfma_f32_16x16x32_bf16 v[76:79], v[124:127], v[144:147], v[76:79]\n"
"ds_read_b64_tr_b16 v[120:121], v244 offset:2560\n"
"v_mfma_f32_16x16x32_bf16 v[40:43], v[124:127], v[128:131], v[40:43]\n"
"ds_read_b64_tr_b16 v[122:123], v244 offset:10752\n"
"v_mfma_f32_16x16x32_bf16 v[88:91], v[124:127], v[160:163], v[88:91]\n"
"s_waitcnt lgkmcnt(12)\n"
"v_mfma_f32_16x16x32_bf16 v[80:83], v[224:227], v[144:147], v[80:83]\n"
"ds_read_b64_tr_b16 v[124:125], v245 offset:2560\n"
"v_mfma_f32_16x16x32_bf16 v[44:47], v[224:227], v[128:131], v[44:47]\n"
"ds_read_b64_tr_b16 v[126:127], v245 offset:10752\n"
"v_mfma_f32_16x16x32_bf16 v[92:95], v[224:227], v[160:163], v[92:95]\n"
"s_waitcnt lgkmcnt(12)\n"
"v_mfma_f32_16x16x32_bf16 v[84:87], v[100:103], v[144:147], v[84:87]\n"
"ds_read_b64_tr_b16 v[224:225], v244 offset:2816\n"
"v_mfma_f32_16x16x32_bf16 v[48:51], v[100:103], v[128:131], v[48:51]\n"
"ds_read_b64_tr_b16 v[226:227], v244 offset:11008\n"
"s_waitcnt vmcnt(23) lgkmcnt(12)\n"
"v_mfma_f32_16x16x32_bf16 v[52:55], v[104:107], v[148:151], v[52:55]\n"
"ds_read_b64_tr_b16 v[100:101], v245 offset:2816\n"
"s_waitcnt vmcnt(22)\n"
"v_mfma_f32_16x16x32_bf16 v[16:19], v[104:107], v[132:135], v[16:19]\n"
"ds_read_b64_tr_b16 v[102:103], v245 offset:11008\n"
"s_waitcnt lgkmcnt(12)\n"
"v_mfma_f32_16x16x32_bf16 v[56:59], v[108:111], v[148:151], v[56:59]\n"
"ds_read_b64_tr_b16 v[104:105], v246 offset:1024\n"
"v_mfma_f32_16x16x32_bf16 v[20:23], v[108:111], v[132:135], v[20:23]\n"
"ds_read_b64_tr_b16 v[106:107], v246 offset:1536\n"
"s_waitcnt lgkmcnt(12)\n"
"v_mfma_f32_16x16x32_bf16 v[60:63], v[112:115], v[148:151], v[60:63]\n"
"v_mfma_f32_16x16x32_bf16 v[24:27], v[112:115], v[132:135], v[24:27]\n"
"s_waitcnt lgkmcnt(10)\n"
"v_mfma_f32_16x16x32_bf16 v[64:67], v[116:119], v[148:151], v[64:67]\n"
"v_mfma_f32_16x16x32_bf16 v[28:31], v[116:119], v[132:135], v[28:31]\n"
"s_waitcnt lgkmcnt(8)\n"
"v_mfma_f32_16x16x32_bf16 v[68:71], v[120:123], v[148:151], v[68:71]\n"
"s_barrier\n"
"ds_read_b64_tr_b16 v[108:109], v244 offset:16384\n"
"ds_read_b64_tr_b16 v[112:113], v245 offset:16384\n"
"ds_read_b64_tr_b16 v[116:117], v244 offset:16640\n"
"v_mfma_f32_16x16x32_bf16 v[32:35], v[120:123], v[132:135], v[32:35]\n"
"ds_read_b64_tr_b16 v[110:111], v244 offset:24576\n"
"ds_read_b64_tr_b16 v[114:115], v245 offset:24576\n"
"ds_read_b64_tr_b16 v[118:119], v244 offset:24832\n"
"s_waitcnt lgkmcnt(12)\n"
"v_mfma_f32_16x16x32_bf16 v[72:75], v[124:127], v[148:151], v[72:75]\n"
"ds_read_b64_tr_b16 v[120:121], v245 offset:16640\n"
"v_mfma_f32_16x16x32_bf16 v[36:39], v[124:127], v[132:135], v[36:39]\n"
"ds_read_b64_tr_b16 v[122:123], v245 offset:24832\n"
"s_waitcnt lgkmcnt(12)\n"
"v_mfma_f32_16x16x32_bf16 v[76:79], v[224:227], v[148:151], v[76:79]\n"
"ds_read_b64_tr_b16 v[124:125], v244 offset:16896\n"
"v_mfma_f32_16x16x32_bf16 v[40:43], v[224:227], v[132:135], v[40:43]\n"
"ds_read_b64_tr_b16 v[126:127], v244 offset:25088\n"
"v_mfma_f32_16x16x32_bf16 v[88:91], v[224:227], v[164:167], v[88:91]\n"
"s_waitcnt lgkmcnt(12)\n"
"v_mfma_f32_16x16x32_bf16 v[80:83], v[100:103], v[148:151], v[80:83]\n"
"ds_read_b64_tr_b16 v[224:225], v245 offset:16896\n"
"v_mfma_f32_16x16x32_bf16 v[44:47], v[100:103], v[132:135], v[44:47]\n"
"ds_read_b64_tr_b16 v[226:227], v245 offset:25088\n"
"v_mfma_f32_16x16x32_bf16 v[92:95], v[100:103], v[164:167], v[92:95]\n"
"s_waitcnt lgkmcnt(12)\n"
"v_mfma_f32_16x16x32_bf16 v[84:87], v[104:107], v[148:151], v[84:87]\n"
"ds_read_b64_tr_b16 v[100:101], v244 offset:17152\n"
"v_mfma_f32_16x16x32_bf16 v[48:51], v[104:107], v[132:135], v[48:51]\n"
"ds_read_b64_tr_b16 v[102:103], v244 offset:25344\n"
"s_waitcnt vmcnt(21) lgkmcnt(10)\n"
"v_mfma_f32_16x16x32_bf16 v[52:55], v[108:111], v[152:155], v[52:55]\n"
"ds_read_b64_tr_b16 v[104:105], v245 offset:17152\n"
"s_waitcnt vmcnt(20)\n"
"v_mfma_f32_16x16x32_bf16 v[16:19], v[108:111], v[136:139], v[16:19]\n"
"ds_read_b64_tr_b16 v[106:107], v245 offset:25344\n"
"s_waitcnt lgkmcnt(11)\n"
"v_mfma_f32_16x16x32_bf16 v[56:59], v[112:115], v[152:155], v[56:59]\n"
"ds_read_b64_tr_b16 v[108:109], v246 offset:2048\n"
"v_mfma_f32_16x16x32_bf16 v[20:23], v[112:115], v[136:139], v[20:23]\n"
"ds_read_b64_tr_b16 v[110:111], v246 offset:2560\n"
"s_waitcnt lgkmcnt(12)\n"
"v_mfma_f32_16x16x32_bf16 v[60:63], v[116:119], v[152:155], v[60:63]\n"
"ds_read_b64_tr_b16 v[112:113], v244 offset:18432\n"
"v_mfma_f32_16x16x32_bf16 v[24:27], v[116:119], v[136:139], v[24:27]\n"
"ds_read_b64_tr_b16 v[114:115], v244 offset:26624\n"
"s_waitcnt lgkmcnt(12)\n"
"v_mfma_f32_16x16x32_bf16 v[64:67], v[120:123], v[152:155], v[64:67]\n"
"ds_read_b64_tr_b16 v[116:117], v245 offset:18432\n"
"v_mfma_f32_16x16x32_bf16 v[28:31], v[120:123], v[136:139], v[28:31]\n"
"ds_read_b64_tr_b16 v[118:119], v245 offset:26624\n"
"s_waitcnt lgkmcnt(12)\n"
"v_mfma_f32_16x16x32_bf16 v[68:71], v[124:127], v[152:155], v[68:71]\n"
"ds_read_b64_tr_b16 v[120:121], v244 offset:18688\n"
"v_mfma_f32_16x16x32_bf16 v[32:35], v[124:127], v[136:139], v[32:35]\n"
"ds_read_b64_tr_b16 v[122:123], v244 offset:26880\n"
"s_waitcnt lgkmcnt(12)\n"
"v_mfma_f32_16x16x32_bf16 v[72:75], v[224:227], v[152:155], v[72:75]\n"
"ds_read_b64_tr_b16 v[124:125], v245 offset:18688\n"
"v_mfma_f32_16x16x32_bf16 v[36:39], v[224:227], v[136:139], v[36:39]\n"
"ds_read_b64_tr_b16 v[126:127], v245 offset:26880\n"
"s_waitcnt lgkmcnt(12)\n"
"v_mfma_f32_16x16x32_bf16 v[76:79], v[100:103], v[152:155], v[76:79]\n"
"ds_read_b64_tr_b16 v[224:225], v244 offset:18944\n"
"v_mfma_f32_16x16x32_bf16 v[40:43], v[100:103], v[136:139], v[40:43]\n"
"ds_read_b64_tr_b16 v[226:227], v244 offset:27136\n"
"v_mfma_f32_16x16x32_bf16 v[88:91], v[100:103], v[168:171], v[88:91]\n"
"s_waitcnt lgkmcnt(12)\n"
"v_mfma_f32_16x16x32_bf16 v[80:83], v[104:107], v[152:155], v[80:83]\n"
"ds_read_b64_tr_b16 v[100:101], v245 offset:18944\n"
"v_mfma_f32_16x16x32_bf16 v[44:47], v[104:107], v[136:139], v[44:47]\n"
"ds_read_b64_tr_b16 v[102:103], v245 offset:27136\n"
"v_mfma_f32_16x16x32_bf16 v[92:95], v[104:107], v[168:171], v[92:95]\n"
"s_waitcnt lgkmcnt(12)\n"
"v_mfma_f32_16x16x32_bf16 v[84:87], v[108:111], v[152:155], v[84:87]\n"
"ds_read_b64_tr_b16 v[104:105], v244 offset:19200\n"
"v_mfma_f32_16x16x32_bf16 v[48:51], v[108:111], v[136:139], v[48:51]\n"
"ds_read_b64_tr_b16 v[106:107], v244 offset:27392\n"
"s_waitcnt vmcnt(19) lgkmcnt(12)\n"
"v_mfma_f32_16x16x32_bf16 v[52:55], v[112:115], v[156:159], v[52:55]\n"
"ds_read_b64_tr_b16 v[108:109], v245 offset:19200\n"
"s_waitcnt vmcnt(18)\n"
"v_mfma_f32_16x16x32_bf16 v[16:19], v[112:115], v[140:143], v[16:19]\n"
"ds_read_b64_tr_b16 v[110:111], v245 offset:27392\n"
"s_waitcnt lgkmcnt(12)\n"
"v_mfma_f32_16x16x32_bf16 v[56:59], v[116:119], v[156:159], v[56:59]\n"
"ds_read_b64_tr_b16 v[112:113], v246 offset:3072\n"
"v_mfma_f32_16x16x32_bf16 v[20:23], v[116:119], v[140:143], v[20:23]\n"
"ds_read_b64_tr_b16 v[114:115], v246 offset:3584\n"
"s_waitcnt lgkmcnt(12)\n"
"v_mfma_f32_16x16x32_bf16 v[60:63], v[120:123], v[156:159], v[60:63]\n"
"v_mfma_f32_16x16x32_bf16 v[24:27], v[120:123], v[140:143], v[24:27]\n"
"s_waitcnt vmcnt(7)\n"
"ds_write_b128 v247, v[240:243] offset:3072\n"
"s_waitcnt lgkmcnt(11)\n"
"v_mfma_f32_16x16x32_bf16 v[64:67], v[124:127], v[156:159], v[64:67]\n"
"v_mfma_f32_16x16x32_bf16 v[28:31], v[124:127], v[140:143], v[28:31]\n"
"s_waitcnt lgkmcnt(9)\n"
"v_mfma_f32_16x16x32_bf16 v[68:71], v[224:227], v[156:159], v[68:71]\n"
"s_waitcnt lgkmcnt(0)\n"
"s_barrier\n"
"ds_read_b64_tr_b16 v[116:117], v244 offset:33024\n"
"ds_read_b64_tr_b16 v[120:121], v245 offset:33024\n"
"ds_read_b64_tr_b16 v[124:125], v244 offset:33280\n"
"v_mfma_f32_16x16x32_bf16 v[32:35], v[224:227], v[140:143], v[32:35]\n"
"ds_read_b128 v[208:211], v247 offset:0\n"
"ds_read_b64_tr_b16 v[118:119], v244 offset:41216\n"
"ds_read_b64_tr_b16 v[122:123], v245 offset:41216\n"
"ds_read_b64_tr_b16 v[126:127], v244 offset:41472\n"
"v_mfma_f32_16x16x32_bf16 v[72:75], v[100:103], v[156:159], v[72:75]\n"
"ds_read_b128 v[212:215], v247 offset:1024\n"
"ds_read_b64_tr_b16 v[224:225], v245 offset:33280\n"
"v_mfma_f32_16x16x32_bf16 v[36:39], v[100:103], v[140:143], v[36:39]\n"
"ds_read_b128 v[216:219], v247 offset:2048\n"
"ds_read_b64_tr_b16 v[226:227], v245 offset:41472\n"
"v_mfma_f32_16x16x32_bf16 v[76:79], v[104:107], v[156:159], v[76:79]\n"
"ds_read_b128 v[220:223], v247 offset:3072\n"
"ds_read_b64_tr_b16 v[100:101], v244 offset:33536\n"
"v_mfma_f32_16x16x32_bf16 v[40:43], v[104:107], v[140:143], v[40:43]\n"
"ds_read_b64_tr_b16 v[102:103], v244 offset:41728\n"
"v_mfma_f32_16x16x32_bf16 v[88:91], v[104:107], v[172:175], v[88:91]\n"
"v_mfma_f32_16x16x32_bf16 v[80:83], v[108:111], v[156:159], v[80:83]\n"
"ds_read_b64_tr_b16 v[104:105], v245 offset:33536\n"
"v_mfma_f32_16x16x32_bf16 v[44:47], v[108:111], v[140:143], v[44:47]\n"
"ds_read_b64_tr_b16 v[106:107], v245 offset:41728\n"
"v_mfma_f32_16x16x32_bf16 v[92:95], v[108:111], v[172:175], v[92:95]\n"
"v_mfma_f32_16x16x32_bf16 v[84:87], v[112:115], v[156:159], v[84:87]\n"
"ds_read_b64_tr_b16 v[108:109], v246 offset:4096\n"
"v_mfma_f32_16x16x32_bf16 v[48:51], v[112:115], v[140:143], v[48:51]\n"
"ds_read_b64_tr_b16 v[110:111], v246 offset:4608\n"
"s_waitcnt lgkmcnt(13)\n"
"v_mfma_f32_16x16x32_bf16 v[60:63], v[116:119], v[192:195], v[60:63]\n"
"ds_read_b64_tr_b16 v[112:113], v244 offset:35072\n"
"v_mfma_f32_16x16x32_bf16 v[24:27], v[116:119], v[176:179], v[24:27]\n"
"ds_read_b64_tr_b16 v[114:115], v244 offset:43264\n"
"s_waitcnt lgkmcnt(14)\n"
"v_mfma_f32_16x16x32_bf16 v[64:67], v[120:123], v[192:195], v[64:67]\n"
"ds_read_b64_tr_b16 v[116:117], v245 offset:35072\n"
"s_add_u32 s68, %[hb], 24576\n"
"v_add_u32_e64 v229, s68, %[l16]\n"
"v_xor_b32 v230, 65536, v229\n"
"v_mfma_f32_16x16x32_bf16 v[28:31], v[120:123], v[176:179], v[28:31]\n"
"ds_read_b64_tr_b16 v[118:119], v245 offset:43264\n"
"v_xor_b32 v234, 8192, v247\n"
"s_lshr_b32 s64, %[kq64], 6\n"
"s_lshr_b32 s65, %[hb], 6\n"
"s_waitcnt lgkmcnt(15)\n"
"v_mfma_f32_16x16x32_bf16 v[68:71], v[124:127], v[192:195], v[68:71]\n"
"ds_read_b64_tr_b16 v[120:121], v244 offset:35328\n"
"s_add_u32 s60, s64, 1\n"
"s_and_b32 s60, s60, 3\n"
"s_lshl_b32 s60, s60, 16\n"
"v_mfma_f32_16x16x32_bf16 v[32:35], v[124:127], v[176:179], v[32:35]\n"
"ds_read_b64_tr_b16 v[122:123], v244 offset:43520\n"
"s_add_u32 s60, s60, %[pbase]\n"
"s_add_u32 s60, s60, 0\n"
"s_add_u32 s61, s64, 2\n"
"s_waitcnt lgkmcnt(13)\n"
"v_mfma_f32_16x16x32_bf16 v[72:75], v[224:227], v[192:195], v[72:75]\n"
"ds_read_b64_tr_b16 v[124:125], v245 offset:35328\n"
"s_and_b32 s61, s61, 3\n"
"s_lshl_b32 s61, s61, 16\n"
"s_add_u32 s61, s61, %[pbase]\n"
"v_mfma_f32_16x16x32_bf16 v[36:39], v[224:227], v[176:179], v[36:39]\n"
"ds_read_b64_tr_b16 v[126:127], v245 offset:43520\n"
"s_add_u32 s61, s61, 21504\n"
"s_add_u32 s62, s64, 3\n"
"s_and_b32 s62, s62, 3\n"
"s_waitcnt lgkmcnt(12)\n"
"v_mfma_f32_16x16x32_bf16 v[76:79], v[100:103], v[192:195], v[76:79]\n"
"ds_read_b64_tr_b16 v[224:225], v244 offset:35584\n"
"s_lshl_b32 s62, s62, 16\n"
"s_add_u32 s62, s62, %[pbase]\n"
"s_add_u32 s62, s62, 43008\n"
"v_mfma_f32_16x16x32_bf16 v[40:43], v[100:103], v[176:179], v[40:43]\n"
"ds_read_b64_tr_b16 v[226:227], v244 offset:43776\n"
"s_lshl_b32 s63, s64, 16\n"
"s_add_u32 s63, s63, %[pbase]\n"
"s_lshl_b32 s66, s65, 1\n"
"v_mfma_f32_16x16x32_bf16 v[88:91], v[100:103], v[208:211], v[88:91]\n"
"s_add_u32 s66, s66, 12288\n"
"v_add_u32_e64 v232, s66, %[l16]\n"
"s_add_u32 s66, s65, 16384\n"
"s_waitcnt lgkmcnt(12)\n"
"v_mfma_f32_16x16x32_bf16 v[80:83], v[104:107], v[192:195], v[80:83]\n"
"ds_read_b64_tr_b16 v[100:101], v245 offset:35584\n"
"v_add_u32 v228, s66, %[l16]\n"
"s_sub_u32 s67, 6, s64\n"
"s_and_b32 s67, s67, 3\n"
"v_mfma_f32_16x16x32_bf16 v[44:47], v[104:107], v[176:179], v[44:47]\n"
"ds_read_b64_tr_b16 v[102:103], v245 offset:43776\n"
"s_mul_i32 s67, s67, 21504\n"
"s_add_u32 s67, s67, %[pbase]\n"
"s_add_u32 s67, s67, 196608\n"
"v_mfma_f32_16x16x32_bf16 v[92:95], v[104:107], v[208:211], v[92:95]\n"
"v_lshrrev_b32 v231, 4, %[l16]\n"
"v_and_b32_e64 v231, 15, v231\n"
"s_movk_i32 s68, 2304\n"
"s_waitcnt lgkmcnt(12)\n"
"v_mfma_f32_16x16x32_bf16 v[84:87], v[108:111], v[192:195], v[84:87]\n"
"ds_read_b64_tr_b16 v[104:105], v246 offset:5120\n"
"v_mul_u32_u24 v231, s68, v231\n"
"v_lshrrev_b32_e64 v233, 8, %[l16]\n"
"v_lshl_add_u32 v231, v233, 3, v231\n"
"v_mfma_f32_16x16x32_bf16 v[48:51], v[108:111], v[176:179], v[48:51]\n"
"ds_read_b64_tr_b16 v[106:107], v246 offset:5632\n"
"s_waitcnt lgkmcnt(12)\n"
"v_mfma_f32_16x16x32_bf16 v[60:63], v[112:115], v[196:199], v[60:63]\n"
"v_mfma_f32_16x16x32_bf16 v[24:27], v[112:115], v[180:183], v[24:27]\n"
"s_waitcnt lgkmcnt(10)\n"
"v_mfma_f32_16x16x32_bf16 v[64:67], v[116:119], v[196:199], v[64:67]\n"
"v_mfma_f32_16x16x32_bf16 v[28:31], v[116:119], v[180:183], v[28:31]\n"
"s_waitcnt lgkmcnt(8)\n"
"v_mfma_f32_16x16x32_bf16 v[68:71], v[120:123], v[196:199], v[68:71]\n"
"s_waitcnt vmcnt(2) lgkmcnt(0)\n"
"s_barrier\n"
"ds_read_b64_tr_b16 v[108:109], v244 offset:49408\n"
"ds_read_b64_tr_b16 v[112:113], v245 offset:49408\n"
"ds_read_b64_tr_b16 v[116:117], v244 offset:51456\n"
"v_mfma_f32_16x16x32_bf16 v[32:35], v[120:123], v[180:183], v[32:35]\n"
"ds_read_b64_tr_b16 v[110:111], v244 offset:57600\n"
"ds_read_b64_tr_b16 v[114:115], v245 offset:57600\n"
"ds_read_b64_tr_b16 v[118:119], v244 offset:59648\n"
"v_mfma_f32_16x16x32_bf16 v[72:75], v[124:127], v[196:199], v[72:75]\n"
"ds_read_b64_tr_b16 v[120:121], v245 offset:51456\n"
"v_mfma_f32_16x16x32_bf16 v[36:39], v[124:127], v[180:183], v[36:39]\n"
"ds_read_b64_tr_b16 v[122:123], v245 offset:59648\n"
"v_mfma_f32_16x16x32_bf16 v[76:79], v[224:227], v[196:199], v[76:79]\n"
"ds_read_b64_tr_b16 v[124:125], v244 offset:49664\n"
"v_mfma_f32_16x16x32_bf16 v[40:43], v[224:227], v[180:183], v[40:43]\n"
"ds_read_b64_tr_b16 v[126:127], v244 offset:57856\n"
"v_mfma_f32_16x16x32_bf16 v[88:91], v[224:227], v[212:215], v[88:91]\n"
"v_mfma_f32_16x16x32_bf16 v[80:83], v[100:103], v[196:199], v[80:83]\n"
"ds_read_b64_tr_b16 v[224:225], v245 offset:49664\n"
"v_mfma_f32_16x16x32_bf16 v[44:47], v[100:103], v[180:183], v[44:47]\n"
"ds_read_b64_tr_b16 v[226:227], v245 offset:57856\n"
"v_mfma_f32_16x16x32_bf16 v[92:95], v[100:103], v[212:215], v[92:95]\n"
"v_mfma_f32_16x16x32_bf16 v[84:87], v[104:107], v[196:199], v[84:87]\n"
"ds_read_b64_tr_b16 v[100:101], v244 offset:51712\n"
"v_mfma_f32_16x16x32_bf16 v[48:51], v[104:107], v[180:183], v[48:51]\n"
"ds_read_b64_tr_b16 v[102:103], v244 offset:59904\n"
"s_waitcnt lgkmcnt(10)\n"
"v_mfma_f32_16x16x32_bf16 v[60:63], v[108:111], v[200:203], v[60:63]\n"
"ds_read_b64_tr_b16 v[104:105], v245 offset:51712\n"
"v_mfma_f32_16x16x32_bf16 v[24:27], v[108:111], v[184:187], v[24:27]\n"
"ds_read_b64_tr_b16 v[106:107], v245 offset:59904\n"
"s_waitcnt lgkmcnt(11)\n"
"v_mfma_f32_16x16x32_bf16 v[64:67], v[112:115], v[200:203], v[64:67]\n"
"ds_read_b64_tr_b16 v[108:109], v244 offset:49920\n"
"v_mfma_f32_16x16x32_bf16 v[28:31], v[112:115], v[184:187], v[28:31]\n"
"ds_read_b64_tr_b16 v[110:111], v244 offset:58112\n"
"s_waitcnt vmcnt(1) lgkmcnt(12)\n"
"v_mfma_f32_16x16x32_bf16 v[60:63], v[116:119], v[204:207], v[60:63]\n"
"ds_read_b64_tr_b16 v[112:113], v245 offset:49920\n"
"s_waitcnt vmcnt(0)\n"
"v_mfma_f32_16x16x32_bf16 v[24:27], v[116:119], v[188:191], v[24:27]\n"
"ds_read_b64_tr_b16 v[114:115], v245 offset:58112\n"
"s_waitcnt lgkmcnt(12)\n"
"v_mfma_f32_16x16x32_bf16 v[64:67], v[120:123], v[204:207], v[64:67]\n"
"ds_read_b64_tr_b16 v[116:117], v246 offset:6144\n"
"v_mfma_f32_16x16x32_bf16 v[28:31], v[120:123], v[188:191], v[28:31]\n"
"ds_read_b64_tr_b16 v[118:119], v246 offset:6656\n"
"s_waitcnt lgkmcnt(12)\n"
"v_mfma_f32_16x16x32_bf16 v[68:71], v[124:127], v[200:203], v[68:71]\n"
"ds_read_b64_tr_b16 v[120:121], v244 offset:51968\n"
"ds_write_b128 v229, v[60:63] offset:0\n"
"v_mfma_f32_16x16x32_bf16 v[32:35], v[124:127], v[184:187], v[32:35]\n"
"ds_read_b64_tr_b16 v[122:123], v244 offset:60160\n"
"s_waitcnt lgkmcnt(13)\n"
"v_mfma_f32_16x16x32_bf16 v[72:75], v[224:227], v[200:203], v[72:75]\n"
"ds_read_b64_tr_b16 v[124:125], v245 offset:51968\n"
"ds_write_b128 v229, v[64:67] offset:1024\n"
"v_mfma_f32_16x16x32_bf16 v[36:39], v[224:227], v[184:187], v[36:39]\n"
"ds_read_b64_tr_b16 v[126:127], v245 offset:60160\n"
"s_waitcnt lgkmcnt(14)\n"
"v_mfma_f32_16x16x32_bf16 v[68:71], v[100:103], v[204:207], v[68:71]\n"
"ds_read_b64_tr_b16 v[224:225], v246 offset:7168\n"
"v_mfma_f32_16x16x32_bf16 v[32:35], v[100:103], v[188:191], v[32:35]\n"
"ds_read_b64_tr_b16 v[226:227], v246 offset:7680\n"
"s_waitcnt lgkmcnt(14)\n"
"v_mfma_f32_16x16x32_bf16 v[72:75], v[104:107], v[204:207], v[72:75]\n"
"ds_read_b64_tr_b16 v[100:101], v244 offset:32768\n"
"v_mfma_f32_16x16x32_bf16 v[36:39], v[104:107], v[188:191], v[36:39]\n"
"ds_read_b64_tr_b16 v[102:103], v244 offset:40960\n"
"s_waitcnt lgkmcnt(14)\n"
"v_mfma_f32_16x16x32_bf16 v[76:79], v[108:111], v[200:203], v[76:79]\n"
"ds_read_b64_tr_b16 v[104:105], v245 offset:32768\n"
"ds_write_b128 v229, v[68:71] offset:2048\n"
"v_mfma_f32_16x16x32_bf16 v[40:43], v[108:111], v[184:187], v[40:43]\n"
"ds_read_b64_tr_b16 v[106:107], v245 offset:40960\n"
"v_mfma_f32_16x16x32_bf16 v[88:91], v[108:111], v[216:219], v[88:91]\n"
"ds_write_b128 v229, v[72:75] offset:3072\n"
"s_waitcnt lgkmcnt(15)\n"
"v_mfma_f32_16x16x32_bf16 v[80:83], v[112:115], v[200:203], v[80:83]\n"
"ds_read_b64_tr_b16 v[108:109], v244 offset:34816\n"
"v_mfma_f32_16x16x32_bf16 v[44:47], v[112:115], v[184:187], v[44:47]\n"
"ds_read_b64_tr_b16 v[110:111], v244 offset:43008\n"
"v_mfma_f32_16x16x32_bf16 v[92:95], v[112:115], v[216:219], v[92:95]\n"
"s_waitcnt lgkmcnt(15)\n"
"v_mfma_f32_16x16x32_bf16 v[84:87], v[116:119], v[200:203], v[84:87]\n"
"ds_read_b64_tr_b16 v[112:113], v245 offset:34816\n"
"v_mfma_f32_16x16x32_bf16 v[48:51], v[116:119], v[184:187], v[48:51]\n"
"ds_read_b64_tr_b16 v[114:115], v245 offset:43008\n"
"s_waitcnt lgkmcnt(15)\n"
"v_mfma_f32_16x16x32_bf16 v[76:79], v[120:123], v[204:207], v[76:79]\n"
"ds_read_b64_tr_b16 v[116:117], v244 offset:49152\n"
"s_waitcnt lgkmcnt(5)\n"
"s_barrier\n"
"ds_read_b128 v[128:131], v230 offset:1024\n"
"ds_read_b128 v[132:135], v230 offset:0\n"
"v_mfma_f32_16x16x32_bf16 v[40:43], v[120:123], v[188:191], v[40:43]\n"
"ds_read_b64_tr_b16 v[118:119], v244 offset:57344\n"
"ds_read_b128 v[136:139], v230 offset:3072\n"
"ds_read_b128 v[140:143], v230 offset:2048\n"
"v_mfma_f32_16x16x32_bf16 v[88:91], v[120:123], v[220:223], v[88:91]\n"
"v_mfma_f32_16x16x32_bf16 v[80:83], v[124:127], v[204:207], v[80:83]\n"
"ds_read_b64_tr_b16 v[120:121], v245 offset:49152\n"
"s_waitcnt lgkmcnt(5)\n"
"v_pk_add_f32 v[24:25], v[24:25], v[128:129]\n"
"v_pk_add_f32 v[26:27], v[26:27], v[130:131]\n"
"buffer_store_dwordx4 v[24:27], v232, s[80:83], s60 offen sc1\n"
"v_mfma_f32_16x16x32_bf16 v[44:47], v[124:127], v[188:191], v[44:47]\n"
"ds_read_b64_tr_b16 v[122:123], v245 offset:57344\n"
"s_waitcnt lgkmcnt(5)\n"
"v_pk_add_f32 v[28:29], v[28:29], v[132:133]\n"
"v_pk_add_f32 v[30:31], v[30:31], v[134:135]\n"
"buffer_store_dwordx4 v[28:31], v232, s[80:83], s60 offen offset:1024 sc1\n"
"ds_write_b128 v229, v[76:79] offset:4096\n"
"v_mfma_f32_16x16x32_bf16 v[92:95], v[124:127], v[220:223], v[92:95]\n"
"s_waitcnt lgkmcnt(4)\n"
"v_pk_add_f32 v[32:33], v[32:33], v[136:137]\n"
"v_pk_add_f32 v[34:35], v[34:35], v[138:139]\n"
"buffer_store_dwordx4 v[32:35], v232, s[80:83], s61 offen sc1\n"
"v_mfma_f32_16x16x32_bf16 v[84:87], v[224:227], v[204:207], v[84:87]\n"
"ds_read_b64_tr_b16 v[124:125], v244 offset:51200\n"
"s_waitcnt lgkmcnt(4)\n"
"v_pk_add_f32 v[36:37], v[36:37], v[140:141]\n"
"v_pk_add_f32 v[38:39], v[38:39], v[142:143]\n"
"buffer_store_dwordx4 v[36:39], v232, s[80:83], s61 offen offset:1024 sc1\n"
"v_mfma_f32_16x16x32_bf16 v[48:51], v[224:227], v[188:191], v[48:51]\n"
"ds_read_b64_tr_b16 v[126:127], v244 offset:59392\n"
"ds_write_b128 v229, v[80:83] offset:5120\n"
"v_mfma_f32_16x16x32_bf16 v[52:55], v[100:103], v[192:195], v[52:55]\n"
"ds_read_b64_tr_b16 v[224:225], v245 offset:51200\n"
"v_mfma_f32_16x16x32_bf16 v[16:19], v[100:103], v[176:179], v[16:19]\n"
"ds_read_b64_tr_b16 v[226:227], v245 offset:59392\n"
"ds_write_b128 v229, v[92:95] offset:7168\n"
"v_mfma_f32_16x16x32_bf16 v[56:59], v[104:107], v[192:195], v[56:59]\n"
"ds_write_b128 v229, v[84:87] offset:6144\n"
"v_mfma_f32_16x16x32_bf16 v[20:23], v[104:107], v[176:179], v[20:23]\n"
"v_mfma_f32_16x16x32_bf16 v[52:55], v[108:111], v[196:199], v[52:55]\n"
"v_mfma_f32_16x16x32_bf16 v[16:19], v[108:111], v[180:183], v[16:19]\n"
"v_mfma_f32_16x16x32_bf16 v[56:59], v[112:115], v[196:199], v[56:59]\n"
"s_waitcnt lgkmcnt(0)\n"
"s_barrier\n"
"ds_read_b128 v[144:147], v230 offset:5120\n"
"ds_read_b128 v[148:151], v230 offset:4096\n"
"v_mfma_f32_16x16x32_bf16 v[20:23], v[112:115], v[180:183], v[20:23]\n"
"ds_read_b128 v[152:155], v230 offset:7168\n"
"ds_read_b128 v[156:159], v230 offset:6144\n"
"v_mfma_f32_16x16x32_bf16 v[52:55], v[116:119], v[200:203], v[52:55]\n"
"v_mfma_f32_16x16x32_bf16 v[16:19], v[116:119], v[184:187], v[16:19]\n"
"s_waitcnt lgkmcnt(3)\n"
"v_pk_add_f32 v[40:41], v[40:41], v[144:145]\n"
"v_pk_add_f32 v[42:43], v[42:43], v[146:147]\n"
"buffer_store_dwordx4 v[40:43], v232, s[80:83], s62 offen sc1\n"
"v_mfma_f32_16x16x32_bf16 v[56:59], v[120:123], v[200:203], v[56:59]\n"
"s_waitcnt lgkmcnt(2)\n"
"v_pk_add_f32 v[44:45], v[44:45], v[148:149]\n"
"v_pk_add_f32 v[46:47], v[46:47], v[150:151]\n"
"buffer_store_dwordx4 v[44:47], v232, s[80:83], s62 offen offset:1024 sc1\n"
"v_mfma_f32_16x16x32_bf16 v[20:23], v[120:123], v[184:187], v[20:23]\n"
"s_waitcnt lgkmcnt(1)\n"
"v_pk_add_f32 v[88:89], v[88:89], v[152:153]\n"
"v_pk_add_f32 v[90:91], v[90:91], v[154:155]\n"
"buffer_store_dwordx4 v[88:91], v228, s[80:83], s62 offen offset:2048 sc1\n"
"v_mfma_f32_16x16x32_bf16 v[52:55], v[124:127], v[204:207], v[52:55]\n"
"s_waitcnt lgkmcnt(0)\n"
"v_pk_add_f32 v[48:49], v[48:49], v[156:157]\n"
"v_pk_add_f32 v[50:51], v[50:51], v[158:159]\n"
"s_cmp_eq_u32 s64, 3\n"
"s_cbranch_scc1 LX3_%=\n"
"buffer_store_dwordx4 v[48:51], v228, s[80:83], s67 offen sc1\n"
"LX3_%=:\n"
"v_mfma_f32_16x16x32_bf16 v[16:19], v[124:127], v[188:191], v[16:19]\n"
"v_mfma_f32_16x16x32_bf16 v[56:59], v[224:227], v[204:207], v[56:59]\n"
"v_mfma_f32_16x16x32_bf16 v[20:23], v[224:227], v[188:191], v[20:23]\n"
"ds_write_b128 v229, v[52:55] offset:0\n"
"s_nop 5\n"
"ds_write_b128 v229, v[56:59] offset:1024\n"
"s_mul_i32 s60, s65, 36\n"
"s_add_u32 s60, s60, 221184\n"
"s_add_u32 s61, %[nb256], %[kq64]\n"
"s_lshr_b32 s62, %[nb256], 3\n"
"s_add_u32 s62, s62, 1024\n"
"s_lshr_b32 s65, s65, 5\n"
"s_waitcnt vmcnt(0) lgkmcnt(0)\n"
"s_barrier\n"
"ds_read_b128 v[192:195], v230 offset:1024\n"
"ds_read_b128 v[196:199], v230 offset:0\n"
"s_waitcnt lgkmcnt(1)\n"
"v_pk_add_f32 v[16:17], v[16:17], v[192:193]\n"
"v_pk_add_f32 v[18:19], v[18:19], v[194:195]\n"
"s_waitcnt lgkmcnt(0)\n"
"v_pk_add_f32 v[20:21], v[20:21], v[196:197]\n"
"v_pk_add_f32 v[22:23], v[22:23], v[198:199]\n"
"s_barrier\n"
"s_add_u32 s68, s63, 0\n"
"buffer_load_dwordx4 v[128:131], v232, s[80:83], s68 offen sc1\n"
"s_add_u32 s68, s63, 21504\n"
"buffer_load_dwordx4 v[132:135], v232, s[80:83], s68 offen sc1\n"
"s_add_u32 s68, s63, 43008\n"
"buffer_load_dwordx4 v[136:139], v232, s[80:83], s68 offen sc1\n"
"s_add_u32 s68, s63, 0\n"
"buffer_load_dwordx4 v[140:143], v232, s[80:83], s68 offen offset:1024 sc1\n"
"s_add_u32 s68, s63, 21504\n"
"buffer_load_dwordx4 v[144:147], v232, s[80:83], s68 offen offset:1024 sc1\n"
"s_add_u32 s68, s63, 43008\n"
"buffer_load_dwordx4 v[148:151], v232, s[80:83], s68 offen offset:1024 sc1\n"
"s_cmp_lg_u32 s64, 3\n"
"s_cbranch_scc1 LU3_%=\n"
"s_add_u32 s68, s63, 0\n"
"buffer_load_dwordx4 v[164:167], v228, s[80:83], s68 offen sc1\n"
"s_add_u32 s68, s63, 21504\n"
"buffer_load_dwordx4 v[168:171], v228, s[80:83], s68 offen sc1\n"
"s_add_u32 s68, s63, 43008\n"
"buffer_load_dwordx4 v[172:175], v228, s[80:83], s68 offen sc1\n"
"LU3_%=:\n"
"s_waitcnt vmcnt(0)\n"
"v_pk_add_f32 v[128:129], v[128:129], v[132:133]\n"
"v_pk_add_f32 v[130:131], v[130:131], v[134:135]\n"
"v_pk_add_f32 v[16:17], v[16:17], v[136:137]\n"
"v_pk_add_f32 v[18:19], v[18:19], v[138:139]\n"
"v_pk_add_f32 v[16:17], v[16:17], v[128:129]\n"
"v_pk_add_f32 v[18:19], v[18:19], v[130:131]\n"
"v_cvt_pk_bf16_f32 v16, v16, v17\n"
"v_cvt_pk_bf16_f32 v17, v18, v19\n"
"s_add_u32 s68, s60, s61\n"
"s_add_u32 s68, s68, s65\n"
"buffer_store_dwordx2 v[16:17], v231, s[84:87], s68 offen\n"
"v_pk_add_f32 v[140:141], v[140:141], v[144:145]\n"
"v_pk_add_f32 v[142:143], v[142:143], v[146:147]\n"
"v_pk_add_f32 v[20:21], v[20:21], v[148:149]\n"
"v_pk_add_f32 v[22:23], v[22:23], v[150:151]\n"
"v_pk_add_f32 v[20:21], v[20:21], v[140:141]\n"
"v_pk_add_f32 v[22:23], v[22:23], v[142:143]\n"
"v_cvt_pk_bf16_f32 v20, v20, v21\n"
"v_cvt_pk_bf16_f32 v21, v22, v23\n"
"s_xor_b32 s68, s65, 32\n"
"s_add_u32 s68, s68, s60\n"
"s_add_u32 s68, s68, s61\n"
"buffer_store_dwordx2 v[20:21], v231, s[84:87], s68 offen\n"
"s_cmp_lg_u32 s64, 3\n"
"s_cbranch_scc1 LW3_%=\n"
"v_pk_add_f32 v[164:165], v[164:165], v[168:169]\n"
"v_pk_add_f32 v[166:167], v[166:167], v[170:171]\n"
"v_pk_add_f32 v[48:49], v[48:49], v[172:173]\n"
"v_pk_add_f32 v[50:51], v[50:51], v[174:175]\n"
"v_pk_add_f32 v[48:49], v[48:49], v[164:165]\n"
"v_pk_add_f32 v[50:51], v[50:51], v[166:167]\n"
"v_cvt_pk_bf16_f32 v48, v48, v49\n"
"v_cvt_pk_bf16_f32 v49, v50, v51\n"
"s_add_u32 s68, s60, s62\n"
"buffer_store_dwordx2 v[48:49], v231, s[84:87], s68 offen\n"
"LW3_%=:\n"
"s_branch LEND_%=\n"
"LZZ3_%=:\n"
"LBAD_%=:\n"
"s_mov_b32 %[flag], 1\n"
"s_nop 7\n"
"s_nop 7\n"
"LEND_%=:\n"
  : [flag] "=&s"(flag)
  : [srdk] "s"(srdk), [srdv] "s"(srdv), [srdw] "s"(srdw), [srdx] "s"(srdx), [srdo] "s"(srdo), [wrot] "s"(wrot), [xrot] "s"(xrot),
    [role] "s"(role), [osoff] "s"(osoff), [isa] "s"(isA), [hb] "s"(hb), [kvs] "s"(kvs), [d1] "s"(d1),
    [pwo] "s"(pwo), [ppart] "s"(ppart), [pout] "s"(pout), [os1] "s"(os1), [os2] "s"(os2), [os3] "s"(os3), [kq64] "s"(kq64), [nb256] "s"(nb256), [pbase] "s"(pbase),
    [gin] "v"(gin), [gout] "v"(gout),
    [kb] "v"(kb), [vbe] "v"(vbe), [g0] "v"(g0), [g1] "v"(g1), [l16] "v"(l16), [wbe] "v"(wbe),
    [xoff] "v"(xoff), [xo9] "v"(xo9), [xb] "v"(xb)
  : "memory", "vcc", "scc", "v16","v17","v18","v19","v20","v21","v22","v23","v24","v25","v26","v27","v28","v29","v30","v31","v32","v33","v34","v35","v36","v37","v38","v39","v40","v41","v42","v43","v44","v45","v46","v47","v48","v49","v50","v51","v52","v53","v54","v55","v56","v57","v58","v59","v60","v61","v62","v63","v64","v65","v66","v67","v68","v69","v70","v71","v72","v73","v74","v75","v76","v77","v78","v79","v80","v81","v82","v83","v84","v85","v86","v87","v88","v89","v90","v91","v92","v93","v94","v95","v96","v97","v98","v99","v100","v101","v102","v103","v104","v105","v106","v107","v108","v109","v110","v111","v112","v113","v114","v115","v116","v117","v118","v119","v120","v121","v122","v123","v124","v125","v126","v127","v128","v129","v130","v131","v132","v133","v134","v135","v136","v137","v138","v139","v140","v141","v142","v143","v144","v145","v146","v147","v148","v149","v150","v151","v152","v153","v154","v155","v156","v157","v158","v159","v160","v161","v162","v163","v164","v165","v166","v167","v168","v169","v170","v171","v172","v173","v174","v175","v176","v177","v178","v179","v180","v181","v182","v183","v184","v185","v186","v187","v188","v189","v190","v191","v192","v193","v194","v195","v196","v197","v198","v199","v200","v201","v202","v203","v204","v205","v206","v207","v208","v209","v210","v211","v212","v213","v214","v215","v216","v217","v218","v219","v220","v221","v222","v223","v224","v225","v226","v227","v228","v229","v230","v231","v232","v233","v234","v235","v236","v237","v238","v239","v240","v241","v242","v243","v244","v245","v246","v247","v248","v249","v250","v251","v252","v253","v254","v255","s52","s53","s54","s55","s56","s57","s58","s59","s60","s61","s62","s63","s64","s65","s66","s67","s68","s69","s70","s71","s72","s73","s74","s75","s76","s77","s78","s79","s80","s81","s82","s83","s84","s85","s86","s87","s88","s89","s90","s91","s92","s93","s94","s95");

  if (!flag) break;
  {
    asm volatile("s_waitcnt vmcnt(0) lgkmcnt(0)\n s_barrier" ::: "memory");
  unsigned l;
  asm volatile("v_mbcnt_lo_u32_b32 %0, -1, 0\n v_mbcnt_hi_u32_b32 %0, -1, %0" : "=v"(l), "+s"(ka), "+s"(wg), "+s"(wv));
  __builtin_assume(l < 64u);
  const unsigned xcd = wg & 7u, jx = wg >> 3, jj = jx & 15u, isA = jx >> 4;
  const unsigned pair = (xcd & 3u) * 4u + (jj & 3u), b = (xcd >> 2) * 4u + (jj >> 2);
  const unsigned h = wv >> 2, w = (wv & 3u) ^ (h * 2u);
  const unsigned head = 2u * pair + h;
  const unsigned kvh = isA ? (b * 16u + pair) : (b * 32u + head);
  const unsigned P = isA ? 0u : 1u, pb = P * 8u + b;
  gcp xp = (gcp)ka[P * 5u] + (size_t)b * 144 * 576;
  gcp kp = (gcp)ka[P * 5u + 1u] + (size_t)kvh * 1024 * 128;
  gcp vp = (gcp)ka[P * 5u + 2u] + (size_t)kvh * 1024 * 128;
  gcp wp = (gcp)ka[P * 5u + 3u] + head * 128;
  GP u16* ofrag = (GP u16*)ka[10];
  GP u16* op = ofrag + (size_t)pb * 9 * 128 * 512;

    float* qs = (float*)(smem + h * 81920u);
    const unsigned t2 = (wv & 3u) * 64u + l;
    for (unsigned i = t2; i < 144u * 128u; i += 256u) {
      unsigned qr = i >> 7, d = i & 127u; float a = 0.f;
      for (unsigned c = 0; c < 576u; ++c) a += bf2f(xp[qr * 576u + c]) * bf2f(wp[(size_t)c * 4096u + d]);
      qs[qr * 129u + d] = bf2f(f2bf(a * 0.12751743f));
    }
    __syncthreads();
    if (t2 < 144u) {
      const float* q = qs + t2 * 129u;
      float mx = -__builtin_inff();
      for (unsigned k = 0; k < 1024u; ++k) {
        float s = 0.f;
        for (unsigned d = 0; d < 128u; ++d) s += q[d] * bf2f(kp[k * 128u + d]);
        mx = fmaxf(mx, s);
      }
      for (unsigned dc = 0; dc < 4u; ++dc) {
        float o[32]; float sum = 0.f;
#pragma unroll
        for (int d = 0; d < 32; ++d) o[d] = 0.f;
        for (unsigned k = 0; k < 1024u; ++k) {
          float s = 0.f;
          for (unsigned d = 0; d < 128u; ++d) s += q[d] * bf2f(kp[k * 128u + d]);
          float p = __builtin_amdgcn_exp2f(s - mx); sum += p;
          float pb = bf2f(f2bf(p));
#pragma unroll
          for (int d = 0; d < 32; ++d) o[d] += pb * bf2f(vp[k * 128u + dc * 32u + d]);
        }
        float rl = 1.f / sum;
        const unsigned mt = t2 >> 4;
        GP u16* dst = op + ((size_t)(head * 4u + dc) * 9u + (mt < 8u ? mt ^ h : 8u)) * 512u + (t2 & 15u) * 8u;
#pragma unroll
        for (int d = 0; d < 32; d += 4) {
          const unsigned long long pk = (unsigned long long)(f2bf(o[d] * rl) | ((unsigned)f2bf(o[d + 1] * rl) << 16)) |
                                        ((unsigned long long)(f2bf(o[d + 2] * rl) | ((unsigned)f2bf(o[d + 3] * rl) << 16)) << 32);
          __hip_atomic_store((GP unsigned long long*)(dst + ((d >> 2) & 3) * 128 + (d >> 4) * 4), pk, __ATOMIC_RELAXED, __HIP_MEMORY_SCOPE_AGENT);
        }
      }
    }
    asm volatile("s_waitcnt vmcnt(0) lgkmcnt(0)\n s_barrier" ::: "memory");
    mode = 4u;
  }
  }
}

extern "C" void kernel_launch(void* const* d_in, const int* in_sizes, int n_in, void* d_out, int out_size, void* d_ws, size_t ws_size, hipStream_t stream) {
  const u16* const* in = (const u16* const*)d_in;
  k1_attn<<<256, 512, 0, stream>>>(in[0], in[1], in[2], in[3], in[4], in[5], in[6], in[7], in[8], in[9], (u16*)d_ws, (u16*)d_out );
}
```
